```python
import torch

def _softmax(x, axis=-1):
    return torch.softmax(x, dim=axis)
def _gelu(x):
    return torch.nn.functional.gelu(x, approximate="tanh")
def _silu(x):
    return torch.nn.functional.silu(x)
def _sigmoid(x):
    return torch.sigmoid(x)
def _layernorm(x, axis=-1, eps=1e-5):
    m = x.mean(dim=axis, keepdim=True)
    v = x.var(dim=axis, keepdim=True, unbiased=False)
    return (x - m) / torch.sqrt(v + eps)
def _scatter_add(tgt, idx, src, axis=0):
    return tgt.clone().index_add_(axis, idx, src)
def _segment_sum(src, seg, n):
    out = torch.zeros((n,) + src.shape[1:], dtype=src.dtype)
    return out.index_add_(0, seg, src)
def _pad(x, pw):
    flat = tuple(v for pair in reversed(pw) for v in pair)
    return torch.nn.functional.pad(x, flat)

kernel_name = 'concat_heads__ssm_conv_h3__compressed_kv_cla__prompt'

def setup_inputs():

    _w = (lambda a: (lambda t: t.to(torch.bfloat16) if t.is_floating_point() else t)(torch.as_tensor(a)))
    def _A_setup():
        B, S, D = 1, 2048, 576
        ks = 4
        return {
            "q": _w((lambda *s: torch.randn(*s, dtype=torch.bfloat16) * 0.05)(B, S, D)),
            "k": _w((lambda *s: torch.randn(*s, dtype=torch.bfloat16) * 0.1)(B, S, D)),
            "v": _w((lambda *s: torch.randn(*s, dtype=torch.bfloat16) * 0.1)(B, S, D)),
            "shift_k": _w((lambda *s: torch.randn(*s, dtype=torch.bfloat16) * 0.1)(D, ks) * 0.5),
            "ssm_k": _w((lambda *s: torch.randn(*s, dtype=torch.bfloat16) * 0.1)(D, S) * (1.0 / S ** 0.5)),
            "ssm_D": _w((lambda *s: torch.randn(*s, dtype=torch.bfloat16) * 0.1)(D)),
        }
    _A_in = _A_setup()

    def _B_setup():
        B, H, Sq, Sk, D, M = 1, 32, 2048, 2048, 128, 576
        Dc = 4 * D
        return {
            "x": _w((lambda *s: torch.randn(*s, dtype=torch.bfloat16) * 3.2)(B, Sq, M)),
            "c_kv": _w((lambda *s: torch.randn(*s, dtype=torch.bfloat16) * 0.05)(B, Sk, Dc)),
            "w_q": _w((lambda *s: torch.randn(*s, dtype=torch.bfloat16) * 3.2)(M, H, D) * (M ** -0.5)),
            "w_uk": _w((lambda *s: torch.randn(*s, dtype=torch.bfloat16) * 3.2)(Dc, H, D) * (Dc ** -0.5)),
            "w_uv": _w((lambda *s: torch.randn(*s, dtype=torch.bfloat16) * 0.05)(Dc, H, D) * (Dc ** -0.5)),
            "w_o": _w((lambda *s: torch.randn(*s, dtype=torch.bfloat16) * 0.05)(H, D, M) * ((H * D) ** -0.5)),
        }
    _B_in = _B_setup()

    return {
        "A_q": _A_in["q"],
        "A_k": _A_in["k"],
        "A_v": _A_in["v"],
        "A_shift_k": _A_in["shift_k"],
        "A_ssm_k": _A_in["ssm_k"],
        "A_ssm_D": _A_in["ssm_D"],
        "B_x": _B_in["x"],
        "B_c_kv": _B_in["c_kv"],
        "B_w_q": _B_in["w_q"],
        "B_w_uk": _B_in["w_uk"],
        "B_w_uv": _B_in["w_uv"],
        "B_w_o": _B_in["w_o"],
    }

def reference(A_q, A_k, A_v, A_shift_k, A_ssm_k, A_ssm_D, B_x, B_c_kv, B_w_q, B_w_uk, B_w_uv, B_w_o):
    def _A_ref(q, k, v, shift_k, ssm_k, ssm_D):
        B, S, D = q.shape
        ks = shift_k.shape[-1]
        k_pad = _pad(k, ((0, 0), (ks - 1, 0), (0, 0)))
        k_shift = torch.zeros_like(k)
        for t in range(ks):
            k_shift = k_shift + k_pad[:, t:t + S, :] * shift_k[:, ks - 1 - t][None, None, :]
        kv = k_shift * v
        L = 2 * S
        kv_pad = _pad(kv, ((0, 0), (0, S), (0, 0)))
        kf_pad = _pad(ssm_k, ((0, 0), (0, S)))
        kv_f = (lambda x, axes=(-2,-1): torch.fft.rfft2(x.float(), dim=axes))(kv_pad[:, None, :, :], axes=(1, 2))
        kf_f = (lambda x, axes=(-2,-1): torch.fft.rfft2(x.float(), dim=axes))(kf_pad[:, None, :], axes=(1, 2))
        kf_f = (lambda x, *d: x.permute(*d))(kf_f, 1, 2, 0)
        y_f = kv_f * kf_f[None, :, :, :]
        y = (lambda x, s, axes=(-2,-1): torch.fft.irfft2(x, s=s, dim=axes).to(torch.bfloat16))(y_f, (1, L), axes=(1, 2))[:, 0, :S, :]
        y = y + kv * ssm_D[None, None, :]
        return q * y

    def _B_ref(x, c_kv, w_q, w_uk, w_uv, w_o):
        B, Sq, M = x.shape[0], x.shape[1], x.shape[2]
        Sk = c_kv.shape[1]
        H, D = w_q.shape[1], w_q.shape[2]
        q = torch.einsum("bqm,mhd->bhqd", x, w_q)
        k = torch.einsum("bkc,chd->bhkd", c_kv, w_uk)
        v = torch.einsum("bkc,chd->bhkd", c_kv, w_uv)
        scores = torch.einsum("bhqd,bhkd->bhqk", q, k) * (D ** -0.5)
        off = Sk - Sq
        qi = torch.arange(Sq)[:, None]
        kj = torch.arange(Sk)[None, :]
        cmask = kj <= (qi + off)
        scores = torch.where(cmask[None, None], scores, -1e9)
        p = _softmax(scores, axis=-1)
        ctx = torch.einsum("bhqk,bhkd->bhqd", p, v)
        return torch.einsum("bhqd,hdm->bqm", ctx, w_o)

    a = _A_ref(A_q, A_k, A_v, A_shift_k, A_ssm_k, A_ssm_D)
    b = _B_ref(B_x, B_c_kv, B_w_q, B_w_uk, B_w_uv, B_w_o)
    return torch.concatenate([a, b], axis=-1)
```

```cpp
#include <hip/hip_runtime.h>
#include <hip/hip_bf16.h>
#include <stdint.h>

typedef unsigned short u16;
namespace nv {
constexpr int S = 2048, DA = 576, H = 32, HD = 128, M = 576, DC = 512, NH = H * HD;

__device__ __forceinline__ float bf2f(u16 x) { return __uint_as_float(((unsigned)x) << 16); }
__device__ __forceinline__ u16 f2bf(float f) {
  unsigned u = __float_as_uint(f);
  return (u16)((u + 0x7FFF + ((u >> 16) & 1)) >> 16);
}
__device__ __forceinline__ float rbf(float f) { return bf2f(f2bf(f)); }

__global__ void k_kv(const u16* k, const u16* v, const u16* shk, u16* kv) {
  int i = blockIdx.x * 256 + threadIdx.x;
  if (i >= S * DA) return;
  int s = i / DA, d = i % DA;
  float acc = 0.f;
  for (int t = 0; t < 4; ++t) {
    int ss = s + t - 3;
    float kk = ss >= 0 ? bf2f(k[ss * DA + d]) : 0.f;
    float p = rbf(kk * bf2f(shk[d * 4 + 3 - t]));
    acc = rbf(acc + p);
  }
  kv[i] = f2bf(acc * bf2f(v[i]));
}

__global__ void k_conv(const u16* kv, const u16* hk, const u16* Dv, const u16* q, u16* out) {
  int i = blockIdx.x * 256 + threadIdx.x;
  if (i >= S * DA) return;
  int t = i / DA, d = i % DA;
  float acc = 0.f;
  for (int s = 0; s <= t; ++s) acc += bf2f(kv[s * DA + d]) * bf2f(hk[d * S + t - s]);
  float y = rbf(acc);
  float z = rbf(bf2f(kv[i]) * bf2f(Dv[d]));
  y = rbf(y + z);
  out[t * 1152 + d] = f2bf(bf2f(q[i]) * y);
}

__global__ void k_gemm(const u16* A, const u16* B, u16* C, int Mm, int N, int K, int ldc) {
  __shared__ float As[16][17], Bs[16][17];
  int tx = threadIdx.x & 15, ty = threadIdx.x >> 4;
  int m = blockIdx.y * 16 + ty, n = blockIdx.x * 16 + tx;
  float acc = 0.f;
  for (int k0 = 0; k0 < K; k0 += 16) {
    As[ty][tx] = bf2f(A[(size_t)m * K + k0 + tx]);
    Bs[ty][tx] = bf2f(B[(size_t)(k0 + ty) * N + n]);
    __syncthreads();
    for (int k = 0; k < 16; ++k) acc += As[ty][k] * Bs[k][tx];
    __syncthreads();
  }
  C[(size_t)m * ldc + n] = f2bf(acc);
}

__global__ void k_attn(const u16* Q, const u16* K, const u16* V, u16* ctx) {
  __shared__ float sc[S];
  __shared__ float qs[HD];
  __shared__ float red[128];
  int qi = blockIdx.x, h = blockIdx.y, t = threadIdx.x;
  qs[t] = bf2f(Q[(size_t)qi * NH + h * HD + t]);
  __syncthreads();
  float mx = -1e30f;
  for (int k = t; k <= qi; k += 128) {
    float a = 0.f;
    for (int d = 0; d < HD; ++d) a += qs[d] * bf2f(K[(size_t)k * NH + h * HD + d]);
    a = rbf(rbf(a) * 0.08838834764831845f);
    sc[k] = a;
    mx = fmaxf(mx, a);
  }
  red[t] = mx; __syncthreads();
  for (int o = 64; o > 0; o >>= 1) { if (t < o) red[t] = fmaxf(red[t], red[t + o]); __syncthreads(); }
  mx = red[0]; __syncthreads();
  float sm = 0.f;
  for (int k = t; k <= qi; k += 128) { float e = expf(sc[k] - mx); sc[k] = e; sm += e; }
  red[t] = sm; __syncthreads();
  for (int o = 64; o > 0; o >>= 1) { if (t < o) red[t] += red[t + o]; __syncthreads(); }
  sm = red[0]; __syncthreads();
  float acc = 0.f;
  for (int k = 0; k <= qi; ++k) acc += rbf(sc[k] / sm) * bf2f(V[(size_t)k * NH + h * HD + t]);
  ctx[(size_t)qi * NH + h * HD + t] = f2bf(acc);
}

inline void run(void* const* d_in, void* d_out, void* d_ws, hipStream_t stream) {
  const u16* Aq = (const u16*)d_in[0]; const u16* Ak = (const u16*)d_in[1]; const u16* Av = (const u16*)d_in[2];
  const u16* shk = (const u16*)d_in[3]; const u16* hk = (const u16*)d_in[4]; const u16* Dv = (const u16*)d_in[5];
  const u16* x = (const u16*)d_in[6]; const u16* ckv = (const u16*)d_in[7]; const u16* wq = (const u16*)d_in[8];
  const u16* wuk = (const u16*)d_in[9]; const u16* wuv = (const u16*)d_in[10]; const u16* wo = (const u16*)d_in[11];
  u16* out = (u16*)d_out;
  u16* ws = (u16*)d_ws;
  u16* kv = ws; ws += S * DA;
  u16* Q = ws; ws += S * NH;
  u16* K = ws; ws += S * NH;
  u16* V = ws; ws += S * NH;
  u16* ctx = ws; ws += S * NH;
  k_kv<<<(S * DA + 255) / 256, 256, 0, stream>>>(Ak, Av, shk, kv);
  k_conv<<<(S * DA + 255) / 256, 256, 0, stream>>>(kv, hk, Dv, Aq, out);
  k_gemm<<<dim3(NH / 16, S / 16), 256, 0, stream>>>(x, wq, Q, S, NH, M, NH);
  k_gemm<<<dim3(NH / 16, S / 16), 256, 0, stream>>>(ckv, wuk, K, S, NH, DC, NH);
  k_gemm<<<dim3(NH / 16, S / 16), 256, 0, stream>>>(ckv, wuv, V, S, NH, DC, NH);
  k_attn<<<dim3(S, H), 128, 0, stream>>>(Q, K, V, ctx);
  k_gemm<<<dim3(M / 16, S / 16), 256, 0, stream>>>(ctx, wo, out + 576, S, M, NH, 1152);
}
}
extern "C" void kernel_launch(void* const* d_in, const int* in_sizes, int n_in, void* d_out, int out_size,
                              void* d_ws, size_t ws_size, hipStream_t stream) {
  nv::run(d_in, d_out, d_ws, stream);
}
```

```cpp
#include <hip/hip_runtime.h>
#include <hip/hip_bf16.h>
#include <stdint.h>
struct KArgs { const void* in[12]; void* out; void* ws; };
constexpr unsigned WSB = 0u;

typedef unsigned short u16;
typedef unsigned long long u64;
typedef __attribute__((ext_vector_type(8))) __bf16 bf16x8;
typedef __attribute__((ext_vector_type(16))) float f32x16;
typedef __attribute__((ext_vector_type(4))) unsigned u32x4;
typedef __attribute__((ext_vector_type(2))) unsigned u32x2;
typedef __attribute__((address_space(4))) const KArgs* KP;
#define GAS __attribute__((address_space(1)))
typedef GAS const u16* cg16; typedef GAS u16* g16; typedef GAS unsigned* g32; typedef GAS u64* g64;
typedef GAS const u32x4* cg128; typedef GAS u32x4* g128;
template <class T> __device__ __forceinline__ T gp(const void* p, size_t off = 0) { return (T)((size_t)p + off); }
constexpr unsigned FL_A1 = 0x1000 / 4, FL_KV = 0x2000 / 4, FL_C = 0x3000 / 4, FL_A2 = 0x4000 / 4, FL_P = 0x5000 / 4;
constexpr unsigned WS_KVT = WSB + (72u << 20), WS_YT = WSB + (76u << 20), WS_FLAGS = WSB + (80u << 20);

__device__ __forceinline__ float bflo(unsigned x) { return __uint_as_float(x << 16); }
__device__ __forceinline__ float bfhi(unsigned x) { return __uint_as_float(x & 0xffff0000u); }
__device__ __forceinline__ unsigned pk2(float a, float b) {
  unsigned r; asm("v_cvt_pk_bf16_f32 %0, %1, %2" : "=v"(r) : "v"(a), "v"(b)); return r;
}
__device__ __forceinline__ float rbf(float a) { return bflo(pk2(a, a)); }
__device__ __forceinline__ void st128(g128 p, u32x4 v) { asm volatile("global_store_dwordx4 %0, %1, off sc1\n s_nop 1" : : "v"(p), "v"(v) : "memory"); }
__device__ __forceinline__ void drain() { asm volatile("s_waitcnt vmcnt(0)" : : : "memory"); }
__device__ __forceinline__ void flag_set(g32 f, unsigned tag, int tid) {
  if (tid == 0) __hip_atomic_store(f, tag, __ATOMIC_RELAXED, __HIP_MEMORY_SCOPE_AGENT);
}
__device__ __forceinline__ void flag_wait(g32 f, int n, unsigned tag, int tid) {
  if (tid < 64) {
    g32 p = f + (tid < n ? tid : n - 1);
    while (__any(__hip_atomic_load(p, __ATOMIC_RELAXED, __HIP_MEMORY_SCOPE_AGENT) != tag)) __builtin_amdgcn_s_sleep(2);
  }
  __syncthreads();
}

typedef __attribute__((ext_vector_type(2))) float f32x2;
__device__ __forceinline__ f32x2 unpk(unsigned x) { return (f32x2){bflo(x), bfhi(x)}; }
__device__ __forceinline__ void phase_a1a(const u32x4 (&kk)[7], const u32x4 (&vv)[4], const u32x4 (&sh)[4], char* lds, int tid) {
  u16* T = (u16*)lds;
  if (tid < 144) {
    const int c = tid % 9, sq = tid / 9;
#pragma unroll
    for (int p = 0; p < 4; ++p) {
      const f32x2 a01 = unpk(sh[p][0]), a23 = unpk(sh[p][1]), b01 = unpk(sh[p][2]), b23 = unpk(sh[p][3]);
      const f32x2 w[4] = {{a01[0], b01[0]}, {a01[1], b01[1]}, {a23[0], b23[0]}, {a23[1], b23[1]}};
      f32x2 kf[7], o[4];
#pragma unroll
      for (int i = 0; i < 7; ++i) kf[i] = unpk(kk[i][p]);
#pragma unroll
      for (int i = 0; i < 4; ++i) {
        f32x2 acc = kf[i] * w[3];
#pragma unroll
        for (int t = 1; t < 4; ++t) acc = __builtin_elementwise_fma(kf[i + t], w[3 - t], acc);
        o[i] = unpk(pk2(acc[0], acc[1])) * unpk(vv[i][p]);
      }
#pragma unroll
      for (int e = 0; e < 2; ++e)
        *(u32x2*)(T + (8 * c + 2 * p + e) * 72 + 4 * sq) = (u32x2){pk2(o[0][e], o[1][e]), pk2(o[2][e], o[3][e])};
    }
  }
}
__device__ __forceinline__ void phase_a1b(KP a, char* lds, int bid, int tid) {
  g16 kvT = gp<g16>(a->ws, WS_KVT);
  const int s0 = (bid >> 3) * 64, d0 = (bid & 7) * 72;
  const u16* T = (const u16*)lds;
#pragma unroll
  for (int n = 0; n < 3; ++n) {
    const int i = tid + 256 * n;
    if (i < 576) {
      int d = i >> 3, ch = i & 7;
      st128((g128)(kvT + (size_t)(d0 + d) * 2048 + s0 + 8 * ch), *(const u32x4*)(T + d * 72 + 8 * ch));
    }
  }
}

constexpr int A2_CS = 4384, A2_HB = 8 * A2_CS, A2_XS = 80, A2_CH = A2_HB + 96 * A2_XS;
__device__ __forceinline__ void phase_a2(KP a, char* lds, int bid, int tid, unsigned tag) {
  cg16 hk = gp<cg16>(a->in[4]), Dv = gp<cg16>(a->in[5]);
  cg16 kvT = gp<cg16>(a->ws, WS_KVT);
  g16 yT = gp<g16>(a->ws, WS_YT);
  g32 fl = gp<g32>(a->ws, WS_FLAGS);
  flag_wait(fl + FL_A1 + (bid >> 5) * 32, 32, tag, tid);
  const int g = bid >> 2, r = bid & 3, cb = 9 * g;
  const int cfirst = cb + (r == 0 ? 0 : r == 1 ? 2 : r == 2 ? 5 : 6);
  u32x4 h1[3], h0[3], xv[3];
#pragma unroll
  for (int c = 0; c < 3; ++c) {
    cg16 hr = hk + (size_t)(cfirst + c) * 2048;
    h1[c] = *(cg128)(hr + 8 * tid);
    h0[c] = tid ? *(cg128)(hr + 8 * tid - 8) : (u32x4){0, 0, 0, 0};
    asm volatile("global_load_dwordx4 %0, %1, off sc1" : "=v"(xv[c]) : "v"(kvT + (size_t)(cfirst + c) * 2048 + 8 * tid) : "memory");
  }
  asm volatile("s_waitcnt vmcnt(0)" : "+v"(xv[0]), "+v"(xv[1]), "+v"(xv[2]) : : "memory");
#pragma unroll
  for (int c = 0; c < 3; ++c) {
    char* base = lds + c * A2_CH;
    unsigned W[8];
#pragma unroll
    for (int i = 0; i < 4; ++i) { W[i] = __builtin_amdgcn_alignbit(h1[c][3 - i], h1[c][3 - i], 16); W[4 + i] = __builtin_amdgcn_alignbit(h0[c][3 - i], h0[c][3 - i], 16); }
    const int m = 256 - tid;
#pragma unroll
    for (int s = 0; s < 8; ++s) {
      const int st = 7 - s; u32x4 o;
#pragma unroll
      for (int i = 0; i < 4; ++i) o[i] = (st & 1) ? __builtin_amdgcn_alignbit(W[(st >> 1) + i + 1], W[(st >> 1) + i], 16) : W[(st >> 1) + i];
      *(u32x4*)(base + s * A2_CS + m * 16) = o;
    }
    if (tid < 32) *(u32x4*)(base + (tid >> 2) * A2_CS + (257 + (tid & 3)) * 16) = (u32x4){0, 0, 0, 0};
    char* X = base + A2_HB;
    *(u32x4*)(X + (32 + (tid >> 2)) * A2_XS + (tid & 3) * 16) = xv[c];
    if (tid < 128) *(u32x4*)(X + (tid >> 2) * A2_XS + (tid & 3) * 16) = (u32x4){0, 0, 0, 0};
  }
  __syncthreads();
  flag_set(fl + FL_C + (bid >> 5) * 32 + ((bid & 7) * 4 + ((bid >> 3) & 3)), tag, tid);
  const int w = __builtin_amdgcn_readfirstlane(tid >> 6), l = tid & 63, i = l & 31, hh = l >> 5;
  int ch0, ch1, big, two;
  if (r == 0)      { big = w < 3;  ch0 = w < 3 ? w : 0;                 ch1 = 1; two = (w == 3); }
  else if (r == 1) { big = w < 2;  ch0 = w < 2 ? w + 1 : (w == 2 ? 1 : 0); ch1 = 2; two = (w == 2); }
  else if (r == 2) { big = w < 3;  ch0 = w < 3 ? w : 0;                 ch1 = 0; two = 0; }
  else             { big = w == 0; ch0 = w < 2 ? 2 : w - 2;             ch1 = 0; two = 0; }
  f32x16 acc;
#pragma unroll
  for (int x = 0; x < 16; ++x) acc[x] = 0.f;
  for (int half = 0; half < 2; ++half) {
    if (half == 1 && !big && !two) break;
    const int ch = (half == 1 && two) ? ch1 : ch0, nt = big, d0 = big ? 32 * half : 0;
    const char* base = lds + ch * A2_CH;
    const char* pa = base + (i & 7) * A2_CS + 2 * (2048 - 32 * d0 - (i & ~7) + 8 * hh);
    const char* pb = base + A2_HB + (32 + 32 * nt + i - d0) * A2_XS + 16 * hh;
#pragma unroll 8
    for (int dl = 0; dl < 32; ++dl) {
      bf16x8 a0 = *(const bf16x8*)(pa - 64 * dl), a1 = *(const bf16x8*)(pa - 64 * dl + 32);
      bf16x8 b0 = *(const bf16x8*)(pb - A2_XS * dl), b1 = *(const bf16x8*)(pb - A2_XS * dl + 32);
      acc = __builtin_amdgcn_mfma_f32_32x32x16_bf16(a0, b0, acc, 0, 0, 0);
      acc = __builtin_amdgcn_mfma_f32_32x32x16_bf16(a1, b1, acc, 0, 0, 0);
    }
    if (!big || half == 1) {
      const int d = cfirst + ch;
      const float Dd = bflo(Dv[d]);
      const char* px = base + A2_HB + (32 + 32 * nt + i) * A2_XS + 8 * hh;
      g16 py = yT + (size_t)d * 2048 + 32 * (32 * nt + i) + 4 * hh;
#pragma unroll
      for (int q = 0; q < 4; ++q) {
        u32x2 x = *(const u32x2*)(px + 16 * q);
        float y0 = rbf(rbf(acc[4 * q]) + rbf(bflo(x[0]) * Dd)), y1 = rbf(rbf(acc[4 * q + 1]) + rbf(bfhi(x[0]) * Dd));
        float y2 = rbf(rbf(acc[4 * q + 2]) + rbf(bflo(x[1]) * Dd)), y3 = rbf(rbf(acc[4 * q + 3]) + rbf(bfhi(x[1]) * Dd));
        u64 o = (u64)pk2(y0, y1) | ((u64)pk2(y2, y3) << 32);
        __hip_atomic_store((g64)(py + 8 * q), o, __ATOMIC_RELAXED, __HIP_MEMORY_SCOPE_AGENT);
      }
#pragma unroll
      for (int x = 0; x < 16; ++x) acc[x] = 0.f;
    }
  }
}

__device__ __forceinline__ void phase_a3p(KP a, char* lds, int bid, int tid, unsigned tag, const u64 (&y)[5], const u32x4 (&qq)[3]) {
  g16 out = gp<g16>(a->out);
  g32 fl = gp<g32>(a->ws, WS_FLAGS);
  const int t0 = (bid >> 3) * 64, d0 = (bid & 7) * 72;
  u16* T = (u16*)lds;
#pragma unroll
  for (int n = 0; n < 5; ++n) {
    int i = tid + 256 * n;
    if (i < 1152) {
      int d = i >> 4, t = 4 * (i & 15);
#pragma unroll
      for (int e = 0; e < 4; ++e) T[(t + e) * 80 + d] = (u16)(y[n] >> (16 * e));
    }
  }
  drain();
  __syncthreads();
  {
    const int x = bid & 7, tt = (x >> 2) * 8 + ((bid >> 3) & 7), mt = bid >> 6;
    flag_set(fl + FL_P + (tt * 4 + mt) * 4 + (x & 3), tag, tid);
  }
#pragma unroll
  for (int n = 0; n < 3; ++n) {
    int i = tid + 256 * n;
    if (i < 576) {
      int t = i / 9, c = i % 9;
      u32x4 yy = *(const u32x4*)(T + t * 80 + 8 * c), o;
#pragma unroll
      for (int e = 0; e < 4; ++e) o[e] = pk2(bflo(qq[n][e]) * bflo(yy[e]), bfhi(qq[n][e]) * bfhi(yy[e]));
      *(g128)(out + (size_t)(t0 + t) * 1152 + d0 + 8 * c) = o;
    }
  }
}

__global__ __launch_bounds__(256, 1) void k_fused(KArgs args) {
  __shared__ __attribute__((aligned(1024))) char lds_[163840];
  KP kp = (KP)__builtin_amdgcn_kernarg_segment_ptr();
  const unsigned bid = blockIdx.x;
  const unsigned wid = __builtin_amdgcn_readfirstlane(threadIdx.x >> 6);
  unsigned pfd;
  { unsigned o = (((bid >> 3) * 4 + wid) * 12 + min(threadIdx.x & 15u, 11u)) * 128;
    asm volatile("s_getpc_b64 s[96:97]\n Lcode_begin:\n s_and_b32 s97, s97, 0xffff\n s_mov_b32 s98, Lcode_end-Lcode_begin\n s_mov_b32 s99, 0x20000\n s_nop 4\n"
                 "buffer_load_dword %0, %1, s[96:99], 0 offen" : "=v"(pfd) : "v"(o) : "s96", "s97", "s98", "s99", "scc"); }
  u32x4 kk[7], vv[4], sh[4];
  { int ln; asm volatile("v_mbcnt_lo_u32_b32 %0, -1, 0\n v_mbcnt_hi_u32_b32 %0, -1, %0" : "=v"(ln));
    const int ta = min((int)(wid * 64) + ln, 143), sa = (bid >> 3) * 64 + 4 * (ta / 9), da = (bid & 7) * 72 + 8 * (ta % 9);
    const unsigned oa = sa * 1152 + da * 2, o0 = sa >= 3 ? oa - 3456 : 0x80000000u, o1 = sa >= 2 ? oa - 2304 : 0x80000000u, o2 = sa >= 1 ? oa - 1152 : 0x80000000u, os = da * 8;
  asm volatile(
  "s_load_dwordx4 s[40:43], %15, 8\n"
  "s_load_dwordx2 s[48:49], %15, 24\n"
  "s_load_dwordx2 s[14:15], %15, 104\n"
  "s_load_dwordx2 s[32:33], %15, 56\n"
  "s_load_dwordx4 s[36:39], %15, 72\n"
  "s_waitcnt lgkmcnt(0)\n"
  "s_mov_b32 s44, s42\n"
  "s_and_b32 s45, s43, 0xffff\n"
  "s_mov_b32 s46, 2359296\n"
  "s_mov_b32 s47, 0x20000\n"
  "s_and_b32 s41, s41, 0xffff\n"
  "s_mov_b32 s42, 2359296\n"
  "s_mov_b32 s43, 0x20000\n"
  "s_and_b32 s49, s49, 0xffff\n"
  "s_mov_b32 s50, 4608\n"
  "s_mov_b32 s51, 0x20000\n"
  "s_nop 4\n"
  "buffer_load_dwordx4 %0, %19, s[40:43], 0 offen\n"
  "buffer_load_dwordx4 %1, %20, s[40:43], 0 offen\n"
  "buffer_load_dwordx4 %2, %21, s[40:43], 0 offen\n"
  "buffer_load_dwordx4 %3, %18, s[40:43], 0 offen offset:0\n"
  "buffer_load_dwordx4 %4, %18, s[40:43], 0 offen offset:1152\n"
  "buffer_load_dwordx4 %5, %18, s[40:43], 0 offen offset:2304\n"
  "buffer_load_dwordx4 %6, %18, s[40:43], 0 offen offset:3456\n"
  "buffer_load_dwordx4 %11, %22, s[48:51], 0 offen offset:0\n"
  "buffer_load_dwordx4 %12, %22, s[48:51], 0 offen offset:16\n"
  "buffer_load_dwordx4 %13, %22, s[48:51], 0 offen offset:32\n"
  "buffer_load_dwordx4 %14, %22, s[48:51], 0 offen offset:48\n"
  "buffer_load_dwordx4 %7, %18, s[44:47], 0 offen offset:0\n"
  "buffer_load_dwordx4 %8, %18, s[44:47], 0 offen offset:1152\n"
  "buffer_load_dwordx4 %9, %18, s[44:47], 0 offen offset:2304\n"
  "buffer_load_dwordx4 %10, %18, s[44:47], 0 offen offset:3456\n"
  "s_mov_b64 s[8:9], %15\n"
  "s_mov_b32 s10, %16\n"
  "s_mov_b32 s11, %17\n"
  "s_and_b32 s12, s10, 7\n"
  "s_lshl_b32 s12, s12, 2\n"
  "s_lshr_b32 s13, s10, 3\n"
  "s_and_b32 s13, s13, 3\n"
  "s_add_u32 s12, s12, s13\n"
  "s_lshr_b32 s13, s10, 5\n"
  "v_mbcnt_lo_u32_b32 v0, -1, 0\n"
  "v_mbcnt_hi_u32_b32 v0, -1, v0\n"
  "s_mov_b32 s16, s32\n"
  "s_mov_b32 s17, s33\n"
  "s_and_b32 s17, s17, 0xffff\n"
  "s_mov_b32 s18, 2097152\n"
  "s_mov_b32 s19, 131072\n"
  "s_cmp_ge_u32 s11, 2\n"
  "s_cselect_b32 s32, s38, s36\n"
  "s_cselect_b32 s33, s39, s37\n"
  "s_mov_b32 s20, s32\n"
  "s_mov_b32 s21, s33\n"
  "s_and_b32 s21, s21, 0xffff\n"
  "s_mov_b32 s22, 4194304\n"
  "s_mov_b32 s23, 131072\n"
  "s_cmp_ge_u32 s11, 2\n"
  "s_cselect_b32 s32, 16777216, 0\n"
  "s_add_u32 s34, s14, s32\n"
  "s_addc_u32 s35, s15, 0\n"
  "s_mov_b32 s24, s34\n"
  "s_mov_b32 s25, s35\n"
  "s_and_b32 s25, s25, 0xffff\n"
  "s_mov_b32 s26, 16777216\n"
  "s_mov_b32 s27, 131072\n"
  "s_lshl_b32 s30, s11, 13\n"
  "s_lshl_b32 s28, s13, 18\n"
  "s_lshl_b32 s32, s11, 16\n"
  "s_add_u32 s28, s28, s32\n"
  "s_and_b32 s32, s11, 1\n"
  "s_lshl_b32 s33, s32, 18\n"
  "s_lshl_b32 s29, s12, 8\n"
  "s_add_u32 s29, s29, s33\n"
  "s_lshl_b32 s31, s12, 5\n"
  "s_lshl_b32 s33, s13, 2\n"
  "s_add_u32 s31, s31, s33\n"
  "s_lshl_b32 s33, s32, 1\n"
  "s_add_u32 s31, s31, s33\n"
  "s_lshl_b32 s31, s31, 14\n"
  "v_lshrrev_b32 v34, 3, v0\n"
  "v_lshlrev_b32 v34, 10, v34\n"
  "v_and_b32 v35, 7, v0\n"
  "v_lshrrev_b32 v36, 4, v0\n"
  "v_add_u32 v37, 0, v36\n"
  "v_xor_b32 v37, v35, v37\n"
  "v_lshlrev_b32 v37, 4, v37\n"
  "v_add_u32 v37, v34, v37\n"
  "v_add_u32 v1, 0, v37\n"
  "v_add_u32 v3, 16384, v37\n"
  "v_add_u32 v5, 32768, v37\n"
  "v_add_u32 v7, 49152, v37\n"
  "v_add_u32 v37, 4, v36\n"
  "v_xor_b32 v37, v35, v37\n"
  "v_lshlrev_b32 v37, 4, v37\n"
  "v_add_u32 v37, v34, v37\n"
  "v_add_u32 v2, 8192, v37\n"
  "v_add_u32 v4, 24576, v37\n"
  "v_add_u32 v6, 40960, v37\n"
  "v_add_u32 v8, 57344, v37\n"
  "v_and_b32 v34, 31, v0\n"
  "v_lshrrev_b32 v34, 2, v34\n"
  "v_lshlrev_b32 v34, 13, v34\n"
  "v_lshrrev_b32 v35, 5, v0\n"
  "v_lshlrev_b32 v35, 6, v35\n"
  "v_and_b32 v36, 3, v0\n"
  "v_lshlrev_b32 v36, 4, v36\n"
  "v_add3_u32 v34, v34, v35, v36\n"
  "v_add_u32 v9, 0, v34\n"
  "v_add_u32 v10, 128, v34\n"
  "v_add_u32 v11, 65536, v34\n"
  "v_add_u32 v12, 65664, v34\n"
  "v_add_u32 v13, 131072, v34\n"
  "v_add_u32 v14, 131200, v34\n"
  "v_add_u32 v15, 196608, v34\n"
  "v_add_u32 v16, 196736, v34\n"
  "v_and_b32 v34, 31, v0\n"
  "v_lshrrev_b32 v35, 1, v34\n"
  "v_and_b32 v35, 7, v35\n"
  "v_lshrrev_b32 v36, 5, v0\n"
  "v_xor_b32 v35, v35, v36\n"
  "v_lshlrev_b32 v35, 4, v35\n"
  "v_lshlrev_b32 v34, 7, v34\n"
  "s_and_b32 s32, s11, 1\n"
  "s_lshl_b32 s32, s32, 14\n"
  "v_add3_u32 v34, v34, v35, s32\n"
  "v_xor_b32 v35, 0, v34\n"
  "v_add_u32 v17, 0, v35\n"
  "v_add_u32 v18, 65536, v35\n"
  "v_add_u32 v19, 131072, v35\n"
  "v_xor_b32 v35, 32, v34\n"
  "v_add_u32 v20, 0, v35\n"
  "v_add_u32 v21, 65536, v35\n"
  "v_add_u32 v22, 131072, v35\n"
  "v_xor_b32 v35, 64, v34\n"
  "v_add_u32 v23, 0, v35\n"
  "v_add_u32 v24, 65536, v35\n"
  "v_add_u32 v25, 131072, v35\n"
  "v_xor_b32 v35, 96, v34\n"
  "v_add_u32 v26, 0, v35\n"
  "v_add_u32 v27, 65536, v35\n"
  "v_add_u32 v28, 131072, v35\n"
  "v_lshlrev_b32 v34, 11, v36\n"
  "v_bfe_u32 v35, v0, 2, 2\n"
  "v_lshlrev_b32 v35, 6, v35\n"
  "v_bfe_u32 v37, v0, 4, 1\n"
  "v_lshlrev_b32 v37, 5, v37\n"
  "v_add3_u32 v34, v34, v35, v37\n"
  "v_and_b32 v35, 3, v0\n"
  "v_lshlrev_b32 v35, 3, v35\n"
  "s_lshr_b32 s32, s11, 1\n"
  "s_lshl_b32 s32, s32, 14\n"
  "v_add3_u32 v34, v34, v35, s32\n"
  "v_add_u32 v29, 0, v34\n"
  "v_add_u32 v30, 65536, v34\n"
  "v_add_u32 v31, 131072, v34\n"
  "v_lshlrev_b32 v32, 4, v0\n"
  "s_add_u32 m0, s30, 0\n"
  "s_nop 0\n"
  "buffer_load_dwordx4 v1, s[16:19], s28 offen lds\n"
  "s_add_u32 m0, s30, 1024\n"
  "s_nop 0\n"
  "buffer_load_dwordx4 v2, s[16:19], s28 offen lds\n"
  "s_add_u32 m0, s30, 2048\n"
  "s_nop 0\n"
  "buffer_load_dwordx4 v3, s[16:19], s28 offen lds\n"
  "s_add_u32 m0, s30, 3072\n"
  "s_nop 0\n"
  "buffer_load_dwordx4 v4, s[16:19], s28 offen lds\n"
  "s_add_u32 m0, s30, 4096\n"
  "s_nop 0\n"
  "buffer_load_dwordx4 v5, s[16:19], s28 offen lds\n"
  "s_add_u32 m0, s30, 5120\n"
  "s_nop 0\n"
  "buffer_load_dwordx4 v6, s[16:19], s28 offen lds\n"
  "s_add_u32 m0, s30, 6144\n"
  "s_nop 0\n"
  "buffer_load_dwordx4 v7, s[16:19], s28 offen lds\n"
  "s_add_u32 m0, s30, 7168\n"
  "s_nop 0\n"
  "buffer_load_dwordx4 v8, s[16:19], s28 offen lds\n"
  "s_add_u32 s28, s28, 128\n"
  "s_add_u32 m0, s30, 32768\n"
  "s_nop 0\n"
  "buffer_load_dwordx4 v9, s[20:23], s29 offen lds\n"
  "s_add_u32 m0, s30, 33792\n"
  "s_nop 0\n"
  "buffer_load_dwordx4 v10, s[20:23], s29 offen lds\n"
  "s_add_u32 m0, s30, 34816\n"
  "s_nop 0\n"
  "buffer_load_dwordx4 v11, s[20:23], s29 offen lds\n"
  "s_add_u32 m0, s30, 35840\n"
  "s_nop 0\n"
  "buffer_load_dwordx4 v12, s[20:23], s29 offen lds\n"
  "s_add_u32 m0, s30, 36864\n"
  "s_nop 0\n"
  "buffer_load_dwordx4 v13, s[20:23], s29 offen lds\n"
  "s_add_u32 m0, s30, 37888\n"
  "s_nop 0\n"
  "buffer_load_dwordx4 v14, s[20:23], s29 offen lds\n"
  "s_add_u32 m0, s30, 38912\n"
  "s_nop 0\n"
  "buffer_load_dwordx4 v15, s[20:23], s29 offen lds\n"
  "s_add_u32 m0, s30, 39936\n"
  "s_nop 0\n"
  "buffer_load_dwordx4 v16, s[20:23], s29 offen lds\n"
  "s_add_u32 s29, s29, 524288\n"
  "s_add_u32 m0, s30, 65536\n"
  "s_nop 0\n"
  "buffer_load_dwordx4 v1, s[16:19], s28 offen lds\n"
  "s_add_u32 m0, s30, 66560\n"
  "s_nop 0\n"
  "buffer_load_dwordx4 v2, s[16:19], s28 offen lds\n"
  "s_add_u32 m0, s30, 67584\n"
  "s_nop 0\n"
  "buffer_load_dwordx4 v3, s[16:19], s28 offen lds\n"
  "s_add_u32 m0, s30, 68608\n"
  "s_nop 0\n"
  "buffer_load_dwordx4 v4, s[16:19], s28 offen lds\n"
  "s_add_u32 m0, s30, 69632\n"
  "s_nop 0\n"
  "buffer_load_dwordx4 v5, s[16:19], s28 offen lds\n"
  "s_add_u32 m0, s30, 70656\n"
  "s_nop 0\n"
  "buffer_load_dwordx4 v6, s[16:19], s28 offen lds\n"
  "s_add_u32 m0, s30, 71680\n"
  "s_nop 0\n"
  "buffer_load_dwordx4 v7, s[16:19], s28 offen lds\n"
  "s_add_u32 m0, s30, 72704\n"
  "s_nop 0\n"
  "buffer_load_dwordx4 v8, s[16:19], s28 offen lds\n"
  "s_add_u32 s28, s28, 128\n"
  "s_add_u32 m0, s30, 98304\n"
  "s_nop 0\n"
  "buffer_load_dwordx4 v9, s[20:23], s29 offen lds\n"
  "s_add_u32 m0, s30, 99328\n"
  "s_nop 0\n"
  "buffer_load_dwordx4 v10, s[20:23], s29 offen lds\n"
  "s_add_u32 m0, s30, 100352\n"
  "s_nop 0\n"
  "buffer_load_dwordx4 v11, s[20:23], s29 offen lds\n"
  "s_add_u32 m0, s30, 101376\n"
  "s_nop 0\n"
  "buffer_load_dwordx4 v12, s[20:23], s29 offen lds\n"
  "s_add_u32 m0, s30, 102400\n"
  "s_nop 0\n"
  "buffer_load_dwordx4 v13, s[20:23], s29 offen lds\n"
  "s_add_u32 m0, s30, 103424\n"
  "s_nop 0\n"
  "buffer_load_dwordx4 v14, s[20:23], s29 offen lds\n"
  "s_add_u32 m0, s30, 104448\n"
  "s_nop 0\n"
  "buffer_load_dwordx4 v15, s[20:23], s29 offen lds\n"
  "s_add_u32 m0, s30, 105472\n"
  "s_nop 0\n"
  "buffer_load_dwordx4 v16, s[20:23], s29 offen lds\n"
  "s_add_u32 s29, s29, 524288\n"
  "s_waitcnt vmcnt(32)\n"
  : "=&v"(kk[0]), "=&v"(kk[1]), "=&v"(kk[2]), "=&v"(kk[3]), "=&v"(kk[4]), "=&v"(kk[5]), "=&v"(kk[6]), "=&v"(vv[0]), "=&v"(vv[1]), "=&v"(vv[2]), "=&v"(vv[3]), "=&v"(sh[0]), "=&v"(sh[1]), "=&v"(sh[2]), "=&v"(sh[3]) : "s"(kp), "s"(bid), "s"(wid), "v"(oa), "v"(o0), "v"(o1), "v"(o2), "v"(os) : "v0", "v1", "v2", "v3", "v4", "v5", "v6", "v7", "v8", "v9", "v10", "v11", "v12", "v13", "v14", "v15", "v16", "v17", "v18", "v19", "v20", "v21", "v22", "v23", "v24", "v25", "v26", "v27", "v28", "v29", "v30", "v31", "v32", "v33", "v34", "v35", "v36", "v37", "v38", "v39", "s8", "s9", "s10", "s11", "s12", "s13", "s14", "s15", "s16", "s17", "s18", "s19", "s20", "s21", "s22", "s23", "s24", "s25", "s26", "s27", "s28", "s29", "s30", "s31", "s32", "s33", "s34", "s35", "s36", "s37", "s38", "s39", "s40", "s41", "s42", "s43", "s44", "s45", "s46", "s47", "s48", "s49", "s50", "s51", "vcc", "scc", "memory"); }
  const unsigned eraw = __hip_atomic_load(gp<g32>(kp->ws, WS_FLAGS), __ATOMIC_RELAXED, __HIP_MEMORY_SCOPE_AGENT);
  { KP ap = kp; unsigned bb = bid, ww = wid; asm volatile("" : "+s"(ap), "+s"(bb), "+s"(ww)); int ln;
    asm volatile("v_mbcnt_lo_u32_b32 %0, -1, 0\n v_mbcnt_hi_u32_b32 %0, -1, %0" : "=v"(ln));
    phase_a1a(kk, vv, sh, lds_ + 131072, ww * 64 + ln); }
  asm volatile("s_waitcnt vmcnt(0) lgkmcnt(0)\n s_barrier" : "+v"(pfd) : : "memory");
  const unsigned tag = __builtin_amdgcn_readfirstlane(eraw) * 1664525u + 1013904223u;
  { KP ap = kp; unsigned bb = bid, ww = wid; asm volatile("" : "+s"(ap), "+s"(bb), "+s"(ww)); int ln;
    asm volatile("v_mbcnt_lo_u32_b32 %0, -1, 0\n v_mbcnt_hi_u32_b32 %0, -1, %0" : "=v"(ln));
    phase_a1b(ap, lds_ + 131072, bb, ww * 64 + ln); }
  asm volatile(
  "s_mov_b64 s[8:9], %0\n"
  "s_mov_b32 s10, %1\n"
  "s_mov_b32 s11, %2\n"
  "s_and_b32 s12, s10, 7\n"
  "s_lshl_b32 s12, s12, 2\n"
  "s_lshr_b32 s13, s10, 3\n"
  "s_and_b32 s13, s13, 3\n"
  "s_add_u32 s12, s12, s13\n"
  "s_lshr_b32 s13, s10, 5\n"
  "s_load_dwordx2 s[14:15], s[8:9], 104\n"
  "s_mov_b32 s71, %3\n"
  "s_waitcnt lgkmcnt(0)\n"
  "s_add_u32 s92, s14, 83886080\n"
  "s_addc_u32 s93, s15, 0\n"
  "s_and_b32 s93, s93, 0xffff\n"
  "s_mov_b32 s94, 65536\n"
  "s_mov_b32 s95, 131072\n"
  "s_load_dwordx2 s[32:33], s[8:9], 56\n"
  "s_load_dwordx4 s[36:39], s[8:9], 72\n"
  "v_mbcnt_lo_u32_b32 v0, -1, 0\n"
  "v_mbcnt_hi_u32_b32 v0, -1, v0\n"
  "s_waitcnt lgkmcnt(0)\n"
  "s_mov_b32 s16, s32\n"
  "s_mov_b32 s17, s33\n"
  "s_and_b32 s17, s17, 0xffff\n"
  "s_mov_b32 s18, 2097152\n"
  "s_mov_b32 s19, 131072\n"
  "s_cmp_ge_u32 s11, 2\n"
  "s_cselect_b32 s32, s38, s36\n"
  "s_cselect_b32 s33, s39, s37\n"
  "s_mov_b32 s20, s32\n"
  "s_mov_b32 s21, s33\n"
  "s_and_b32 s21, s21, 0xffff\n"
  "s_mov_b32 s22, 4194304\n"
  "s_mov_b32 s23, 131072\n"
  "s_cmp_ge_u32 s11, 2\n"
  "s_cselect_b32 s32, 16777216, 0\n"
  "s_add_u32 s34, s14, s32\n"
  "s_addc_u32 s35, s15, 0\n"
  "s_mov_b32 s24, s34\n"
  "s_mov_b32 s25, s35\n"
  "s_and_b32 s25, s25, 0xffff\n"
  "s_mov_b32 s26, 16777216\n"
  "s_mov_b32 s27, 131072\n"
  "s_lshl_b32 s30, s11, 13\n"
  "s_lshl_b32 s28, s13, 18\n"
  "s_lshl_b32 s32, s11, 16\n"
  "s_add_u32 s28, s28, s32\n"
  "s_and_b32 s32, s11, 1\n"
  "s_lshl_b32 s33, s32, 18\n"
  "s_lshl_b32 s29, s12, 8\n"
  "s_add_u32 s29, s29, s33\n"
  "s_lshl_b32 s31, s12, 5\n"
  "s_lshl_b32 s33, s13, 2\n"
  "s_add_u32 s31, s31, s33\n"
  "s_lshl_b32 s33, s32, 1\n"
  "s_add_u32 s31, s31, s33\n"
  "s_lshl_b32 s31, s31, 14\n"
  "v_lshrrev_b32 v34, 3, v0\n"
  "v_lshlrev_b32 v34, 10, v34\n"
  "v_and_b32 v35, 7, v0\n"
  "v_lshrrev_b32 v36, 4, v0\n"
  "v_add_u32 v37, 0, v36\n"
  "v_xor_b32 v37, v35, v37\n"
  "v_lshlrev_b32 v37, 4, v37\n"
  "v_add_u32 v37, v34, v37\n"
  "v_add_u32 v1, 0, v37\n"
  "v_add_u32 v3, 16384, v37\n"
  "v_add_u32 v5, 32768, v37\n"
  "v_add_u32 v7, 49152, v37\n"
  "v_add_u32 v37, 4, v36\n"
  "v_xor_b32 v37, v35, v37\n"
  "v_lshlrev_b32 v37, 4, v37\n"
  "v_add_u32 v37, v34, v37\n"
  "v_add_u32 v2, 8192, v37\n"
  "v_add_u32 v4, 24576, v37\n"
  "v_add_u32 v6, 40960, v37\n"
  "v_add_u32 v8, 57344, v37\n"
  "v_and_b32 v34, 31, v0\n"
  "v_lshrrev_b32 v34, 2, v34\n"
  "v_lshlrev_b32 v34, 13, v34\n"
  "v_lshrrev_b32 v35, 5, v0\n"
  "v_lshlrev_b32 v35, 6, v35\n"
  "v_and_b32 v36, 3, v0\n"
  "v_lshlrev_b32 v36, 4, v36\n"
  "v_add3_u32 v34, v34, v35, v36\n"
  "v_add_u32 v9, 0, v34\n"
  "v_add_u32 v10, 128, v34\n"
  "v_add_u32 v11, 65536, v34\n"
  "v_add_u32 v12, 65664, v34\n"
  "v_add_u32 v13, 131072, v34\n"
  "v_add_u32 v14, 131200, v34\n"
  "v_add_u32 v15, 196608, v34\n"
  "v_add_u32 v16, 196736, v34\n"
  "v_and_b32 v34, 31, v0\n"
  "v_lshrrev_b32 v35, 1, v34\n"
  "v_and_b32 v35, 7, v35\n"
  "v_lshrrev_b32 v36, 5, v0\n"
  "v_xor_b32 v35, v35, v36\n"
  "v_lshlrev_b32 v35, 4, v35\n"
  "v_lshlrev_b32 v34, 7, v34\n"
  "s_and_b32 s32, s11, 1\n"
  "s_lshl_b32 s32, s32, 14\n"
  "v_add3_u32 v34, v34, v35, s32\n"
  "v_xor_b32 v35, 0, v34\n"
  "v_add_u32 v17, 0, v35\n"
  "v_add_u32 v18, 65536, v35\n"
  "v_add_u32 v19, 131072, v35\n"
  "v_xor_b32 v35, 32, v34\n"
  "v_add_u32 v20, 0, v35\n"
  "v_add_u32 v21, 65536, v35\n"
  "v_add_u32 v22, 131072, v35\n"
  "v_xor_b32 v35, 64, v34\n"
  "v_add_u32 v23, 0, v35\n"
  "v_add_u32 v24, 65536, v35\n"
  "v_add_u32 v25, 131072, v35\n"
  "v_xor_b32 v35, 96, v34\n"
  "v_add_u32 v26, 0, v35\n"
  "v_add_u32 v27, 65536, v35\n"
  "v_add_u32 v28, 131072, v35\n"
  "v_lshlrev_b32 v34, 11, v36\n"
  "v_bfe_u32 v35, v0, 2, 2\n"
  "v_lshlrev_b32 v35, 6, v35\n"
  "v_bfe_u32 v37, v0, 4, 1\n"
  "v_lshlrev_b32 v37, 5, v37\n"
  "v_add3_u32 v34, v34, v35, v37\n"
  "v_and_b32 v35, 3, v0\n"
  "v_lshlrev_b32 v35, 3, v35\n"
  "s_lshr_b32 s32, s11, 1\n"
  "s_lshl_b32 s32, s32, 14\n"
  "v_add3_u32 v34, v34, v35, s32\n"
  "v_add_u32 v29, 0, v34\n"
  "v_add_u32 v30, 65536, v34\n"
  "v_add_u32 v31, 131072, v34\n"
  "v_lshlrev_b32 v32, 4, v0\n"
  "s_add_u32 s28, s28, 256\n"
  "s_add_u32 s29, s29, 1048576\n"
  "s_cmp_ge_u32 s11, 2\n"
  "s_cbranch_scc1 Lp1v\n"
  "s_barrier\n"
  "ds_read_b64_tr_b16 a[80:81], v29 offset:32768\n"
  "ds_read_b64_tr_b16 a[82:83], v29 offset:33024\n"
  "ds_read_b128 a[64:67], v17 offset:0\n"
  "ds_read_b128 a[68:71], v17 offset:4096\n"
  "ds_read_b128 a[72:75], v17 offset:8192\n"
  "ds_read_b128 a[76:79], v17 offset:12288\n"
  "ds_read_b64_tr_b16 a[84:85], v29 offset:33280\n"
  "ds_read_b64_tr_b16 a[86:87], v29 offset:33536\n"
  "ds_read_b64_tr_b16 a[88:89], v29 offset:33792\n"
  "ds_read_b64_tr_b16 a[90:91], v29 offset:34048\n"
  "ds_read_b64_tr_b16 a[92:93], v29 offset:34304\n"
  "ds_read_b64_tr_b16 a[94:95], v29 offset:34560\n"
  "s_waitcnt lgkmcnt(9)\n"
  "v_mfma_f32_32x32x16_bf16 v[64:79], a[80:83], a[64:67], 0\n"
  "ds_read_b64_tr_b16 a[112:113], v29 offset:36864\n"
  "s_add_u32 m0, s30, 131072\n"
  "s_waitcnt lgkmcnt(9)\n"
  "v_mfma_f32_32x32x16_bf16 v[80:95], a[80:83], a[68:71], 0\n"
  "ds_read_b64_tr_b16 a[114:115], v29 offset:37120\n"
  "buffer_load_dwordx4 v1, s[16:19], s28 offen lds\n"
  "s_waitcnt lgkmcnt(9)\n"
  "v_mfma_f32_32x32x16_bf16 v[96:111], a[80:83], a[72:75], 0\n"
  "ds_read_b128 a[96:99], v20 offset:0\n"
  "s_add_u32 m0, s30, 132096\n"
  "s_waitcnt lgkmcnt(9)\n"
  "v_mfma_f32_32x32x16_bf16 v[112:127], a[80:83], a[76:79], 0\n"
  "ds_read_b128 a[100:103], v20 offset:4096\n"
  "buffer_load_dwordx4 v2, s[16:19], s28 offen lds\n"
  "s_waitcnt lgkmcnt(8)\n"
  "v_mfma_f32_32x32x16_bf16 v[128:143], a[84:87], a[64:67], 0\n"
  "ds_read_b128 a[104:107], v20 offset:8192\n"
  "s_add_u32 m0, s30, 133120\n"
  "v_mfma_f32_32x32x16_bf16 v[144:159], a[84:87], a[68:71], 0\n"
  "ds_read_b128 a[108:111], v20 offset:12288\n"
  "buffer_load_dwordx4 v3, s[16:19], s28 offen lds\n"
  "v_mfma_f32_32x32x16_bf16 v[160:175], a[84:87], a[72:75], 0\n"
  "ds_read_b64_tr_b16 a[116:117], v29 offset:37376\n"
  "s_add_u32 m0, s30, 134144\n"
  "v_mfma_f32_32x32x16_bf16 v[176:191], a[84:87], a[76:79], 0\n"
  "ds_read_b64_tr_b16 a[118:119], v29 offset:37632\n"
  "buffer_load_dwordx4 v4, s[16:19], s28 offen lds\n"
  "s_waitcnt lgkmcnt(9)\n"
  "v_mfma_f32_32x32x16_bf16 v[192:207], a[88:91], a[64:67], 0\n"
  "ds_read_b64_tr_b16 a[120:121], v29 offset:37888\n"
  "s_add_u32 m0, s30, 135168\n"
  "v_mfma_f32_32x32x16_bf16 v[208:223], a[88:91], a[68:71], 0\n"
  "ds_read_b64_tr_b16 a[122:123], v29 offset:38144\n"
  "buffer_load_dwordx4 v5, s[16:19], s28 offen lds\n"
  "v_mfma_f32_32x32x16_bf16 v[224:239], a[88:91], a[72:75], 0\n"
  "ds_read_b64_tr_b16 a[124:125], v29 offset:38400\n"
  "s_add_u32 m0, s30, 136192\n"
  "v_mfma_f32_32x32x16_bf16 v[240:255], a[88:91], a[76:79], 0\n"
  "ds_read_b64_tr_b16 a[126:127], v29 offset:38656\n"
  "buffer_load_dwordx4 v6, s[16:19], s28 offen lds\n"
  "s_waitcnt lgkmcnt(8)\n"
  "v_mfma_f32_32x32x16_bf16 a[0:15], a[92:95], a[64:67], 0\n"
  "s_add_u32 m0, s30, 137216\n"
  "v_mfma_f32_32x32x16_bf16 a[16:31], a[92:95], a[68:71], 0\n"
  "buffer_load_dwordx4 v7, s[16:19], s28 offen lds\n"
  "v_mfma_f32_32x32x16_bf16 a[32:47], a[92:95], a[72:75], 0\n"
  "s_add_u32 m0, s30, 138240\n"
  "v_mfma_f32_32x32x16_bf16 a[48:63], a[92:95], a[76:79], 0\n"
  "buffer_load_dwordx4 v8, s[16:19], s28 offen lds\n"
  "s_add_u32 s28, s28, 128\n"
  "v_mfma_f32_32x32x16_bf16 v[64:79], a[112:115], a[96:99], v[64:79]\n"
  "ds_read_b64_tr_b16 a[80:81], v29 offset:40960\n"
  "v_mfma_f32_32x32x16_bf16 v[80:95], a[112:115], a[100:103], v[80:95]\n"
  "ds_read_b64_tr_b16 a[82:83], v29 offset:41216\n"
  "s_waitcnt lgkmcnt(6)\n"
  "v_mfma_f32_32x32x16_bf16 v[96:111], a[112:115], a[104:107], v[96:111]\n"
  "ds_read_b128 a[64:67], v23 offset:0\n"
  "v_mfma_f32_32x32x16_bf16 v[112:127], a[112:115], a[108:111], v[112:127]\n"
  "ds_read_b128 a[68:71], v23 offset:4096\n"
  "v_mfma_f32_32x32x16_bf16 v[128:143], a[116:119], a[96:99], v[128:143]\n"
  "ds_read_b128 a[72:75], v23 offset:8192\n"
  "v_mfma_f32_32x32x16_bf16 v[144:159], a[116:119], a[100:103], v[144:159]\n"
  "ds_read_b128 a[76:79], v23 offset:12288\n"
  "v_mfma_f32_32x32x16_bf16 v[160:175], a[116:119], a[104:107], v[160:175]\n"
  "ds_read_b64_tr_b16 a[84:85], v29 offset:41472\n"
  "v_mfma_f32_32x32x16_bf16 v[176:191], a[116:119], a[108:111], v[176:191]\n"
  "ds_read_b64_tr_b16 a[86:87], v29 offset:41728\n"
  "s_waitcnt lgkmcnt(8)\n"
  "v_mfma_f32_32x32x16_bf16 v[192:207], a[120:123], a[96:99], v[192:207]\n"
  "ds_read_b64_tr_b16 a[88:89], v29 offset:41984\n"
  "v_mfma_f32_32x32x16_bf16 v[208:223], a[120:123], a[100:103], v[208:223]\n"
  "ds_read_b64_tr_b16 a[90:91], v29 offset:42240\n"
  "v_mfma_f32_32x32x16_bf16 v[224:239], a[120:123], a[104:107], v[224:239]\n"
  "ds_read_b64_tr_b16 a[92:93], v29 offset:42496\n"
  "v_mfma_f32_32x32x16_bf16 v[240:255], a[120:123], a[108:111], v[240:255]\n"
  "ds_read_b64_tr_b16 a[94:95], v29 offset:42752\n"
  "v_mfma_f32_32x32x16_bf16 a[0:15], a[124:127], a[96:99], a[0:15]\n"
  "v_mfma_f32_32x32x16_bf16 a[16:31], a[124:127], a[100:103], a[16:31]\n"
  "v_mfma_f32_32x32x16_bf16 a[32:47], a[124:127], a[104:107], a[32:47]\n"
  "v_mfma_f32_32x32x16_bf16 a[48:63], a[124:127], a[108:111], a[48:63]\n"
  "s_waitcnt lgkmcnt(9)\n"
  "v_mfma_f32_32x32x16_bf16 v[64:79], a[80:83], a[64:67], v[64:79]\n"
  "ds_read_b64_tr_b16 a[112:113], v29 offset:45056\n"
  "s_waitcnt lgkmcnt(9)\n"
  "v_mfma_f32_32x32x16_bf16 v[80:95], a[80:83], a[68:71], v[80:95]\n"
  "ds_read_b64_tr_b16 a[114:115], v29 offset:45312\n"
  "s_waitcnt lgkmcnt(9)\n"
  "v_mfma_f32_32x32x16_bf16 v[96:111], a[80:83], a[72:75], v[96:111]\n"
  "ds_read_b128 a[96:99], v26 offset:0\n"
  "s_waitcnt lgkmcnt(9)\n"
  "v_mfma_f32_32x32x16_bf16 v[112:127], a[80:83], a[76:79], v[112:127]\n"
  "ds_read_b128 a[100:103], v26 offset:4096\n"
  "s_waitcnt lgkmcnt(8)\n"
  "v_mfma_f32_32x32x16_bf16 v[128:143], a[84:87], a[64:67], v[128:143]\n"
  "ds_read_b128 a[104:107], v26 offset:8192\n"
  "v_mfma_f32_32x32x16_bf16 v[144:159], a[84:87], a[68:71], v[144:159]\n"
  "ds_read_b128 a[108:111], v26 offset:12288\n"
  "v_mfma_f32_32x32x16_bf16 v[160:175], a[84:87], a[72:75], v[160:175]\n"
  "ds_read_b64_tr_b16 a[116:117], v29 offset:45568\n"
  "v_mfma_f32_32x32x16_bf16 v[176:191], a[84:87], a[76:79], v[176:191]\n"
  "ds_read_b64_tr_b16 a[118:119], v29 offset:45824\n"
  "s_waitcnt lgkmcnt(10)\n"
  "v_mfma_f32_32x32x16_bf16 v[192:207], a[88:91], a[64:67], v[192:207]\n"
  "ds_read_b64_tr_b16 a[120:121], v29 offset:46080\n"
  "v_mfma_f32_32x32x16_bf16 v[208:223], a[88:91], a[68:71], v[208:223]\n"
  "ds_read_b64_tr_b16 a[122:123], v29 offset:46336\n"
  "v_mfma_f32_32x32x16_bf16 v[224:239], a[88:91], a[72:75], v[224:239]\n"
  "ds_read_b64_tr_b16 a[124:125], v29 offset:46592\n"
  "v_mfma_f32_32x32x16_bf16 v[240:255], a[88:91], a[76:79], v[240:255]\n"
  "ds_read_b64_tr_b16 a[126:127], v29 offset:46848\n"
  "s_waitcnt lgkmcnt(12)\n"
  "v_mfma_f32_32x32x16_bf16 a[0:15], a[92:95], a[64:67], a[0:15]\n"
  "v_mfma_f32_32x32x16_bf16 a[16:31], a[92:95], a[68:71], a[16:31]\n"
  "v_mfma_f32_32x32x16_bf16 a[32:47], a[92:95], a[72:75], a[32:47]\n"
  "v_mfma_f32_32x32x16_bf16 a[48:63], a[92:95], a[76:79], a[48:63]\n"
  "s_waitcnt lgkmcnt(0)\n"
  "s_barrier\n"
  "v_mfma_f32_32x32x16_bf16 v[64:79], a[112:115], a[96:99], v[64:79]\n"
  "ds_read_b64_tr_b16 a[80:81], v30 offset:32768\n"
  "s_add_u32 m0, s30, 0\n"
  "v_mfma_f32_32x32x16_bf16 v[80:95], a[112:115], a[100:103], v[80:95]\n"
  "ds_read_b64_tr_b16 a[82:83], v30 offset:33024\n"
  "buffer_load_dwordx4 v9, s[20:23], s29 offen lds\n"
  "v_mfma_f32_32x32x16_bf16 v[96:111], a[112:115], a[104:107], v[96:111]\n"
  "ds_read_b128 a[64:67], v18 offset:0\n"
  "s_add_u32 m0, s30, 1024\n"
  "v_mfma_f32_32x32x16_bf16 v[112:127], a[112:115], a[108:111], v[112:127]\n"
  "ds_read_b128 a[68:71], v18 offset:4096\n"
  "buffer_load_dwordx4 v10, s[20:23], s29 offen lds\n"
  "v_mfma_f32_32x32x16_bf16 v[128:143], a[116:119], a[96:99], v[128:143]\n"
  "ds_read_b128 a[72:75], v18 offset:8192\n"
  "s_add_u32 m0, s30, 2048\n"
  "v_mfma_f32_32x32x16_bf16 v[144:159], a[116:119], a[100:103], v[144:159]\n"
  "ds_read_b128 a[76:79], v18 offset:12288\n"
  "buffer_load_dwordx4 v11, s[20:23], s29 offen lds\n"
  "v_mfma_f32_32x32x16_bf16 v[160:175], a[116:119], a[104:107], v[160:175]\n"
  "ds_read_b64_tr_b16 a[84:85], v30 offset:33280\n"
  "s_add_u32 m0, s30, 3072\n"
  "v_mfma_f32_32x32x16_bf16 v[176:191], a[116:119], a[108:111], v[176:191]\n"
  "ds_read_b64_tr_b16 a[86:87], v30 offset:33536\n"
  "buffer_load_dwordx4 v12, s[20:23], s29 offen lds\n"
  "v_mfma_f32_32x32x16_bf16 v[192:207], a[120:123], a[96:99], v[192:207]\n"
  "ds_read_b64_tr_b16 a[88:89], v30 offset:33792\n"
  "s_add_u32 m0, s30, 4096\n"
  "v_mfma_f32_32x32x16_bf16 v[208:223], a[120:123], a[100:103], v[208:223]\n"
  "ds_read_b64_tr_b16 a[90:91], v30 offset:34048\n"
  "buffer_load_dwordx4 v13, s[20:23], s29 offen lds\n"
  "v_mfma_f32_32x32x16_bf16 v[224:239], a[120:123], a[104:107], v[224:239]\n"
  "ds_read_b64_tr_b16 a[92:93], v30 offset:34304\n"
  "s_add_u32 m0, s30, 5120\n"
  "v_mfma_f32_32x32x16_bf16 v[240:255], a[120:123], a[108:111], v[240:255]\n"
  "ds_read_b64_tr_b16 a[94:95], v30 offset:34560\n"
  "buffer_load_dwordx4 v14, s[20:23], s29 offen lds\n"
  "v_mfma_f32_32x32x16_bf16 a[0:15], a[124:127], a[96:99], a[0:15]\n"
  "s_add_u32 m0, s30, 6144\n"
  "v_mfma_f32_32x32x16_bf16 a[16:31], a[124:127], a[100:103], a[16:31]\n"
  "buffer_load_dwordx4 v15, s[20:23], s29 offen lds\n"
  "v_mfma_f32_32x32x16_bf16 a[32:47], a[124:127], a[104:107], a[32:47]\n"
  "s_add_u32 m0, s30, 7168\n"
  "v_mfma_f32_32x32x16_bf16 a[48:63], a[124:127], a[108:111], a[48:63]\n"
  "buffer_load_dwordx4 v16, s[20:23], s29 offen lds\n"
  "s_add_u32 s29, s29, 524288\n"
  "s_waitcnt lgkmcnt(5)\n"
  "v_mfma_f32_32x32x16_bf16 v[64:79], a[80:83], a[64:67], v[64:79]\n"
  "ds_read_b64_tr_b16 a[112:113], v30 offset:36864\n"
  "v_mfma_f32_32x32x16_bf16 v[80:95], a[80:83], a[68:71], v[80:95]\n"
  "ds_read_b64_tr_b16 a[114:115], v30 offset:37120\n"
  "v_mfma_f32_32x32x16_bf16 v[96:111], a[80:83], a[72:75], v[96:111]\n"
  "ds_read_b128 a[96:99], v21 offset:0\n"
  "s_add_u32 m0, s30, 32768\n"
  "v_mfma_f32_32x32x16_bf16 v[112:127], a[80:83], a[76:79], v[112:127]\n"
  "ds_read_b128 a[100:103], v21 offset:4096\n"
  "buffer_load_dwordx4 v1, s[16:19], s28 offen lds\n"
  "s_waitcnt lgkmcnt(6)\n"
  "v_mfma_f32_32x32x16_bf16 v[128:143], a[84:87], a[64:67], v[128:143]\n"
  "ds_read_b128 a[104:107], v21 offset:8192\n"
  "v_mfma_f32_32x32x16_bf16 v[144:159], a[84:87], a[68:71], v[144:159]\n"
  "ds_read_b128 a[108:111], v21 offset:12288\n"
  "v_mfma_f32_32x32x16_bf16 v[160:175], a[84:87], a[72:75], v[160:175]\n"
  "ds_read_b64_tr_b16 a[116:117], v30 offset:37376\n"
  "s_add_u32 m0, s30, 33792\n"
  "v_mfma_f32_32x32x16_bf16 v[176:191], a[84:87], a[76:79], v[176:191]\n"
  "ds_read_b64_tr_b16 a[118:119], v30 offset:37632\n"
  "buffer_load_dwordx4 v2, s[16:19], s28 offen lds\n"
  "v_mfma_f32_32x32x16_bf16 v[192:207], a[88:91], a[64:67], v[192:207]\n"
  "ds_read_b64_tr_b16 a[120:121], v30 offset:37888\n"
  "v_mfma_f32_32x32x16_bf16 v[208:223], a[88:91], a[68:71], v[208:223]\n"
  "ds_read_b64_tr_b16 a[122:123], v30 offset:38144\n"
  "v_mfma_f32_32x32x16_bf16 v[224:239], a[88:91], a[72:75], v[224:239]\n"
  "ds_read_b64_tr_b16 a[124:125], v30 offset:38400\n"
  "s_add_u32 m0, s30, 34816\n"
  "v_mfma_f32_32x32x16_bf16 v[240:255], a[88:91], a[76:79], v[240:255]\n"
  "ds_read_b64_tr_b16 a[126:127], v30 offset:38656\n"
  "buffer_load_dwordx4 v3, s[16:19], s28 offen lds\n"
  "s_waitcnt lgkmcnt(10)\n"
  "v_mfma_f32_32x32x16_bf16 a[0:15], a[92:95], a[64:67], a[0:15]\n"
  "v_mfma_f32_32x32x16_bf16 a[16:31], a[92:95], a[68:71], a[16:31]\n"
  "v_mfma_f32_32x32x16_bf16 a[32:47], a[92:95], a[72:75], a[32:47]\n"
  "s_add_u32 m0, s30, 35840\n"
  "v_mfma_f32_32x32x16_bf16 a[48:63], a[92:95], a[76:79], a[48:63]\n"
  "buffer_load_dwordx4 v4, s[16:19], s28 offen lds\n"
  "s_waitcnt lgkmcnt(8)\n"
  "v_mfma_f32_32x32x16_bf16 v[64:79], a[112:115], a[96:99], v[64:79]\n"
  "ds_read_b64_tr_b16 a[80:81], v30 offset:40960\n"
  "v_mfma_f32_32x32x16_bf16 v[80:95], a[112:115], a[100:103], v[80:95]\n"
  "ds_read_b64_tr_b16 a[82:83], v30 offset:41216\n"
  "s_waitcnt lgkmcnt(8)\n"
  "v_mfma_f32_32x32x16_bf16 v[96:111], a[112:115], a[104:107], v[96:111]\n"
  "ds_read_b128 a[64:67], v24 offset:0\n"
  "s_add_u32 m0, s30, 36864\n"
  "v_mfma_f32_32x32x16_bf16 v[112:127], a[112:115], a[108:111], v[112:127]\n"
  "ds_read_b128 a[68:71], v24 offset:4096\n"
  "buffer_load_dwordx4 v5, s[16:19], s28 offen lds\n"
  "s_waitcnt lgkmcnt(8)\n"
  "v_mfma_f32_32x32x16_bf16 v[128:143], a[116:119], a[96:99], v[128:143]\n"
  "ds_read_b128 a[72:75], v24 offset:8192\n"
  "v_mfma_f32_32x32x16_bf16 v[144:159], a[116:119], a[100:103], v[144:159]\n"
  "ds_read_b128 a[76:79], v24 offset:12288\n"
  "v_mfma_f32_32x32x16_bf16 v[160:175], a[116:119], a[104:107], v[160:175]\n"
  "ds_read_b64_tr_b16 a[84:85], v30 offset:41472\n"
  "s_add_u32 m0, s30, 37888\n"
  "v_mfma_f32_32x32x16_bf16 v[176:191], a[116:119], a[108:111], v[176:191]\n"
  "ds_read_b64_tr_b16 a[86:87], v30 offset:41728\n"
  "buffer_load_dwordx4 v6, s[16:19], s28 offen lds\n"
  "s_waitcnt lgkmcnt(8)\n"
  "v_mfma_f32_32x32x16_bf16 v[192:207], a[120:123], a[96:99], v[192:207]\n"
  "ds_read_b64_tr_b16 a[88:89], v30 offset:41984\n"
  "v_mfma_f32_32x32x16_bf16 v[208:223], a[120:123], a[100:103], v[208:223]\n"
  "ds_read_b64_tr_b16 a[90:91], v30 offset:42240\n"
  "v_mfma_f32_32x32x16_bf16 v[224:239], a[120:123], a[104:107], v[224:239]\n"
  "ds_read_b64_tr_b16 a[92:93], v30 offset:42496\n"
  "s_add_u32 m0, s30, 38912\n"
  "v_mfma_f32_32x32x16_bf16 v[240:255], a[120:123], a[108:111], v[240:255]\n"
  "ds_read_b64_tr_b16 a[94:95], v30 offset:42752\n"
  "buffer_load_dwordx4 v7, s[16:19], s28 offen lds\n"
  "v_mfma_f32_32x32x16_bf16 a[0:15], a[124:127], a[96:99], a[0:15]\n"
  "v_mfma_f32_32x32x16_bf16 a[16:31], a[124:127], a[100:103], a[16:31]\n"
  "v_mfma_f32_32x32x16_bf16 a[32:47], a[124:127], a[104:107], a[32:47]\n"
  "s_add_u32 m0, s30, 39936\n"
  "v_mfma_f32_32x32x16_bf16 a[48:63], a[124:127], a[108:111], a[48:63]\n"
  "buffer_load_dwordx4 v8, s[16:19], s28 offen lds\n"
  "s_add_u32 s28, s28, 128\n"
  "s_waitcnt lgkmcnt(7)\n"
  "v_mfma_f32_32x32x16_bf16 v[64:79], a[80:83], a[64:67], v[64:79]\n"
  "ds_read_b64_tr_b16 a[112:113], v30 offset:45056\n"
  "v_mfma_f32_32x32x16_bf16 v[80:95], a[80:83], a[68:71], v[80:95]\n"
  "ds_read_b64_tr_b16 a[114:115], v30 offset:45312\n"
  "v_mfma_f32_32x32x16_bf16 v[96:111], a[80:83], a[72:75], v[96:111]\n"
  "ds_read_b128 a[96:99], v27 offset:0\n"
  "s_waitcnt lgkmcnt(8)\n"
  "v_mfma_f32_32x32x16_bf16 v[112:127], a[80:83], a[76:79], v[112:127]\n"
  "ds_read_b128 a[100:103], v27 offset:4096\n"
  "s_waitcnt lgkmcnt(8)\n"
  "v_mfma_f32_32x32x16_bf16 v[128:143], a[84:87], a[64:67], v[128:143]\n"
  "ds_read_b128 a[104:107], v27 offset:8192\n"
  "v_mfma_f32_32x32x16_bf16 v[144:159], a[84:87], a[68:71], v[144:159]\n"
  "ds_read_b128 a[108:111], v27 offset:12288\n"
  "v_mfma_f32_32x32x16_bf16 v[160:175], a[84:87], a[72:75], v[160:175]\n"
  "ds_read_b64_tr_b16 a[116:117], v30 offset:45568\n"
  "v_mfma_f32_32x32x16_bf16 v[176:191], a[84:87], a[76:79], v[176:191]\n"
  "ds_read_b64_tr_b16 a[118:119], v30 offset:45824\n"
  "s_waitcnt lgkmcnt(9)\n"
  "v_mfma_f32_32x32x16_bf16 v[192:207], a[88:91], a[64:67], v[192:207]\n"
  "ds_read_b64_tr_b16 a[120:121], v30 offset:46080\n"
  "v_mfma_f32_32x32x16_bf16 v[208:223], a[88:91], a[68:71], v[208:223]\n"
  "ds_read_b64_tr_b16 a[122:123], v30 offset:46336\n"
  "v_mfma_f32_32x32x16_bf16 v[224:239], a[88:91], a[72:75], v[224:239]\n"
  "ds_read_b64_tr_b16 a[124:125], v30 offset:46592\n"
  "v_mfma_f32_32x32x16_bf16 v[240:255], a[88:91], a[76:79], v[240:255]\n"
  "ds_read_b64_tr_b16 a[126:127], v30 offset:46848\n"
  "s_waitcnt lgkmcnt(12)\n"
  "v_mfma_f32_32x32x16_bf16 a[0:15], a[92:95], a[64:67], a[0:15]\n"
  "v_mfma_f32_32x32x16_bf16 a[16:31], a[92:95], a[68:71], a[16:31]\n"
  "v_mfma_f32_32x32x16_bf16 a[32:47], a[92:95], a[72:75], a[32:47]\n"
  "v_mfma_f32_32x32x16_bf16 a[48:63], a[92:95], a[76:79], a[48:63]\n"
  "s_waitcnt vmcnt(8)\n"
  "s_waitcnt lgkmcnt(0)\n"
  "s_barrier\n"
  "s_and_b32 s32, s10, 7\n"
  "s_lshl_b32 s32, s32, 5\n"
  "s_lshr_b32 s33, s10, 3\n"
  "s_add_u32 s32, s32, s33\n"
  "s_lshl_b32 s32, s32, 2\n"
  "s_add_u32 s32, s32, 4096\n"
  "v_mov_b32 v63, s71\n"
  "s_mov_b64 exec, 1\n"
  "buffer_store_dword v63, off, s[92:95], s32 sc1\n"
  "s_mov_b64 exec, -1\n"
  "v_mfma_f32_32x32x16_bf16 v[64:79], a[112:115], a[96:99], v[64:79]\n"
  "ds_read_b64_tr_b16 a[80:81], v29 offset:0\n"
  "s_add_u32 m0, s30, 65536\n"
  "v_mfma_f32_32x32x16_bf16 v[80:95], a[112:115], a[100:103], v[80:95]\n"
  "ds_read_b64_tr_b16 a[82:83], v29 offset:256\n"
  "buffer_load_dwordx4 v9, s[20:23], s29 offen lds\n"
  "v_mfma_f32_32x32x16_bf16 v[96:111], a[112:115], a[104:107], v[96:111]\n"
  "ds_read_b128 a[64:67], v19 offset:0\n"
  "s_add_u32 m0, s30, 66560\n"
  "v_mfma_f32_32x32x16_bf16 v[112:127], a[112:115], a[108:111], v[112:127]\n"
  "ds_read_b128 a[68:71], v19 offset:4096\n"
  "buffer_load_dwordx4 v10, s[20:23], s29 offen lds\n"
  "v_mfma_f32_32x32x16_bf16 v[128:143], a[116:119], a[96:99], v[128:143]\n"
  "ds_read_b128 a[72:75], v19 offset:8192\n"
  "s_add_u32 m0, s30, 67584\n"
  "v_mfma_f32_32x32x16_bf16 v[144:159], a[116:119], a[100:103], v[144:159]\n"
  "ds_read_b128 a[76:79], v19 offset:12288\n"
  "buffer_load_dwordx4 v11, s[20:23], s29 offen lds\n"
  "v_mfma_f32_32x32x16_bf16 v[160:175], a[116:119], a[104:107], v[160:175]\n"
  "ds_read_b64_tr_b16 a[84:85], v29 offset:512\n"
  "s_add_u32 m0, s30, 68608\n"
  "v_mfma_f32_32x32x16_bf16 v[176:191], a[116:119], a[108:111], v[176:191]\n"
  "ds_read_b64_tr_b16 a[86:87], v29 offset:768\n"
  "buffer_load_dwordx4 v12, s[20:23], s29 offen lds\n"
  "v_mfma_f32_32x32x16_bf16 v[192:207], a[120:123], a[96:99], v[192:207]\n"
  "ds_read_b64_tr_b16 a[88:89], v29 offset:1024\n"
  "s_add_u32 m0, s30, 69632\n"
  "v_mfma_f32_32x32x16_bf16 v[208:223], a[120:123], a[100:103], v[208:223]\n"
  "ds_read_b64_tr_b16 a[90:91], v29 offset:1280\n"
  "buffer_load_dwordx4 v13, s[20:23], s29 offen lds\n"
  "v_mfma_f32_32x32x16_bf16 v[224:239], a[120:123], a[104:107], v[224:239]\n"
  "ds_read_b64_tr_b16 a[92:93], v29 offset:1536\n"
  "s_add_u32 m0, s30, 70656\n"
  "v_mfma_f32_32x32x16_bf16 v[240:255], a[120:123], a[108:111], v[240:255]\n"
  "ds_read_b64_tr_b16 a[94:95], v29 offset:1792\n"
  "buffer_load_dwordx4 v14, s[20:23], s29 offen lds\n"
  "v_mfma_f32_32x32x16_bf16 a[0:15], a[124:127], a[96:99], a[0:15]\n"
  "s_add_u32 m0, s30, 71680\n"
  "v_mfma_f32_32x32x16_bf16 a[16:31], a[124:127], a[100:103], a[16:31]\n"
  "buffer_load_dwordx4 v15, s[20:23], s29 offen lds\n"
  "v_mfma_f32_32x32x16_bf16 a[32:47], a[124:127], a[104:107], a[32:47]\n"
  "s_add_u32 m0, s30, 72704\n"
  "v_mfma_f32_32x32x16_bf16 a[48:63], a[124:127], a[108:111], a[48:63]\n"
  "buffer_load_dwordx4 v16, s[20:23], s29 offen lds\n"
  "s_add_u32 s29, s29, 524288\n"
  "s_waitcnt lgkmcnt(5)\n"
  "v_mfma_f32_32x32x16_bf16 v[64:79], a[80:83], a[64:67], v[64:79]\n"
  "ds_read_b64_tr_b16 a[112:113], v29 offset:4096\n"
  "v_mfma_f32_32x32x16_bf16 v[80:95], a[80:83], a[68:71], v[80:95]\n"
  "ds_read_b64_tr_b16 a[114:115], v29 offset:4352\n"
  "v_mfma_f32_32x32x16_bf16 v[96:111], a[80:83], a[72:75], v[96:111]\n"
  "ds_read_b128 a[96:99], v22 offset:0\n"
  "s_add_u32 m0, s30, 98304\n"
  "v_mfma_f32_32x32x16_bf16 v[112:127], a[80:83], a[76:79], v[112:127]\n"
  "ds_read_b128 a[100:103], v22 offset:4096\n"
  "buffer_load_dwordx4 v1, s[16:19], s28 offen lds\n"
  "s_waitcnt lgkmcnt(6)\n"
  "v_mfma_f32_32x32x16_bf16 v[128:143], a[84:87], a[64:67], v[128:143]\n"
  "ds_read_b128 a[104:107], v22 offset:8192\n"
  "v_mfma_f32_32x32x16_bf16 v[144:159], a[84:87], a[68:71], v[144:159]\n"
  "ds_read_b128 a[108:111], v22 offset:12288\n"
  "v_mfma_f32_32x32x16_bf16 v[160:175], a[84:87], a[72:75], v[160:175]\n"
  "ds_read_b64_tr_b16 a[116:117], v29 offset:4608\n"
  "s_add_u32 m0, s30, 99328\n"
  "v_mfma_f32_32x32x16_bf16 v[176:191], a[84:87], a[76:79], v[176:191]\n"
  "ds_read_b64_tr_b16 a[118:119], v29 offset:4864\n"
  "buffer_load_dwordx4 v2, s[16:19], s28 offen lds\n"
  "v_mfma_f32_32x32x16_bf16 v[192:207], a[88:91], a[64:67], v[192:207]\n"
  "ds_read_b64_tr_b16 a[120:121], v29 offset:5120\n"
  "v_mfma_f32_32x32x16_bf16 v[208:223], a[88:91], a[68:71], v[208:223]\n"
  "ds_read_b64_tr_b16 a[122:123], v29 offset:5376\n"
  "v_mfma_f32_32x32x16_bf16 v[224:239], a[88:91], a[72:75], v[224:239]\n"
  "ds_read_b64_tr_b16 a[124:125], v29 offset:5632\n"
  "s_add_u32 m0, s30, 100352\n"
  "v_mfma_f32_32x32x16_bf16 v[240:255], a[88:91], a[76:79], v[240:255]\n"
  "ds_read_b64_tr_b16 a[126:127], v29 offset:5888\n"
  "buffer_load_dwordx4 v3, s[16:19], s28 offen lds\n"
  "s_waitcnt lgkmcnt(10)\n"
  "v_mfma_f32_32x32x16_bf16 a[0:15], a[92:95], a[64:67], a[0:15]\n"
  "v_mfma_f32_32x32x16_bf16 a[16:31], a[92:95], a[68:71], a[16:31]\n"
  "v_mfma_f32_32x32x16_bf16 a[32:47], a[92:95], a[72:75], a[32:47]\n"
  "s_add_u32 m0, s30, 101376\n"
  "v_mfma_f32_32x32x16_bf16 a[48:63], a[92:95], a[76:79], a[48:63]\n"
  "buffer_load_dwordx4 v4, s[16:19], s28 offen lds\n"
  "s_waitcnt lgkmcnt(8)\n"
  "v_mfma_f32_32x32x16_bf16 v[64:79], a[112:115], a[96:99], v[64:79]\n"
  "ds_read_b64_tr_b16 a[80:81], v29 offset:8192\n"
  "v_mfma_f32_32x32x16_bf16 v[80:95], a[112:115], a[100:103], v[80:95]\n"
  "ds_read_b64_tr_b16 a[82:83], v29 offset:8448\n"
  "s_waitcnt lgkmcnt(8)\n"
  "v_mfma_f32_32x32x16_bf16 v[96:111], a[112:115], a[104:107], v[96:111]\n"
  "ds_read_b128 a[64:67], v25 offset:0\n"
  "s_add_u32 m0, s30, 102400\n"
  "v_mfma_f32_32x32x16_bf16 v[112:127], a[112:115], a[108:111], v[112:127]\n"
  "ds_read_b128 a[68:71], v25 offset:4096\n"
  "buffer_load_dwordx4 v5, s[16:19], s28 offen lds\n"
  "s_waitcnt lgkmcnt(8)\n"
  "v_mfma_f32_32x32x16_bf16 v[128:143], a[116:119], a[96:99], v[128:143]\n"
  "ds_read_b128 a[72:75], v25 offset:8192\n"
  "v_mfma_f32_32x32x16_bf16 v[144:159], a[116:119], a[100:103], v[144:159]\n"
  "ds_read_b128 a[76:79], v25 offset:12288\n"
  "v_mfma_f32_32x32x16_bf16 v[160:175], a[116:119], a[104:107], v[160:175]\n"
  "ds_read_b64_tr_b16 a[84:85], v29 offset:8704\n"
  "s_add_u32 m0, s30, 103424\n"
  "v_mfma_f32_32x32x16_bf16 v[176:191], a[116:119], a[108:111], v[176:191]\n"
  "ds_read_b64_tr_b16 a[86:87], v29 offset:8960\n"
  "buffer_load_dwordx4 v6, s[16:19], s28 offen lds\n"
  "s_waitcnt lgkmcnt(8)\n"
  "v_mfma_f32_32x32x16_bf16 v[192:207], a[120:123], a[96:99], v[192:207]\n"
  "ds_read_b64_tr_b16 a[88:89], v29 offset:9216\n"
  "v_mfma_f32_32x32x16_bf16 v[208:223], a[120:123], a[100:103], v[208:223]\n"
  "ds_read_b64_tr_b16 a[90:91], v29 offset:9472\n"
  "v_mfma_f32_32x32x16_bf16 v[224:239], a[120:123], a[104:107], v[224:239]\n"
  "ds_read_b64_tr_b16 a[92:93], v29 offset:9728\n"
  "s_add_u32 m0, s30, 104448\n"
  "v_mfma_f32_32x32x16_bf16 v[240:255], a[120:123], a[108:111], v[240:255]\n"
  "ds_read_b64_tr_b16 a[94:95], v29 offset:9984\n"
  "buffer_load_dwordx4 v7, s[16:19], s28 offen lds\n"
  "v_mfma_f32_32x32x16_bf16 a[0:15], a[124:127], a[96:99], a[0:15]\n"
  "v_mfma_f32_32x32x16_bf16 a[16:31], a[124:127], a[100:103], a[16:31]\n"
  "v_mfma_f32_32x32x16_bf16 a[32:47], a[124:127], a[104:107], a[32:47]\n"
  "s_add_u32 m0, s30, 105472\n"
  "v_mfma_f32_32x32x16_bf16 a[48:63], a[124:127], a[108:111], a[48:63]\n"
  "buffer_load_dwordx4 v8, s[16:19], s28 offen lds\n"
  "s_add_u32 s28, s28, 128\n"
  "s_waitcnt lgkmcnt(7)\n"
  "v_mfma_f32_32x32x16_bf16 v[64:79], a[80:83], a[64:67], v[64:79]\n"
  "ds_read_b64_tr_b16 a[112:113], v29 offset:12288\n"
  "v_mfma_f32_32x32x16_bf16 v[80:95], a[80:83], a[68:71], v[80:95]\n"
  "ds_read_b64_tr_b16 a[114:115], v29 offset:12544\n"
  "v_mfma_f32_32x32x16_bf16 v[96:111], a[80:83], a[72:75], v[96:111]\n"
  "ds_read_b128 a[96:99], v28 offset:0\n"
  "s_waitcnt lgkmcnt(8)\n"
  "v_mfma_f32_32x32x16_bf16 v[112:127], a[80:83], a[76:79], v[112:127]\n"
  "ds_read_b128 a[100:103], v28 offset:4096\n"
  "s_waitcnt lgkmcnt(8)\n"
  "v_mfma_f32_32x32x16_bf16 v[128:143], a[84:87], a[64:67], v[128:143]\n"
  "ds_read_b128 a[104:107], v28 offset:8192\n"
  "v_mfma_f32_32x32x16_bf16 v[144:159], a[84:87], a[68:71], v[144:159]\n"
  "ds_read_b128 a[108:111], v28 offset:12288\n"
  "v_mfma_f32_32x32x16_bf16 v[160:175], a[84:87], a[72:75], v[160:175]\n"
  "ds_read_b64_tr_b16 a[116:117], v29 offset:12800\n"
  "v_mfma_f32_32x32x16_bf16 v[176:191], a[84:87], a[76:79], v[176:191]\n"
  "ds_read_b64_tr_b16 a[118:119], v29 offset:13056\n"
  "s_waitcnt lgkmcnt(9)\n"
  "v_mfma_f32_32x32x16_bf16 v[192:207], a[88:91], a[64:67], v[192:207]\n"
  "ds_read_b64_tr_b16 a[120:121], v29 offset:13312\n"
  "v_mfma_f32_32x32x16_bf16 v[208:223], a[88:91], a[68:71], v[208:223]\n"
  "ds_read_b64_tr_b16 a[122:123], v29 offset:13568\n"
  "v_mfma_f32_32x32x16_bf16 v[224:239], a[88:91], a[72:75], v[224:239]\n"
  "ds_read_b64_tr_b16 a[124:125], v29 offset:13824\n"
  "v_mfma_f32_32x32x16_bf16 v[240:255], a[88:91], a[76:79], v[240:255]\n"
  "ds_read_b64_tr_b16 a[126:127], v29 offset:14080\n"
  "s_waitcnt lgkmcnt(12)\n"
  "v_mfma_f32_32x32x16_bf16 a[0:15], a[92:95], a[64:67], a[0:15]\n"
  "v_mfma_f32_32x32x16_bf16 a[16:31], a[92:95], a[68:71], a[16:31]\n"
  "v_mfma_f32_32x32x16_bf16 a[32:47], a[92:95], a[72:75], a[32:47]\n"
  "v_mfma_f32_32x32x16_bf16 a[48:63], a[92:95], a[76:79], a[48:63]\n"
  "s_waitcnt vmcnt(8)\n"
  "s_waitcnt lgkmcnt(0)\n"
  "s_barrier\n"
  "v_mfma_f32_32x32x16_bf16 v[64:79], a[112:115], a[96:99], v[64:79]\n"
  "ds_read_b64_tr_b16 a[80:81], v30 offset:0\n"
  "s_add_u32 m0, s30, 131072\n"
  "v_mfma_f32_32x32x16_bf16 v[80:95], a[112:115], a[100:103], v[80:95]\n"
  "ds_read_b64_tr_b16 a[82:83], v30 offset:256\n"
  "buffer_load_dwordx4 v9, s[20:23], s29 offen lds\n"
  "v_mfma_f32_32x32x16_bf16 v[96:111], a[112:115], a[104:107], v[96:111]\n"
  "ds_read_b128 a[64:67], v17 offset:32768\n"
  "s_add_u32 m0, s30, 132096\n"
  "v_mfma_f32_32x32x16_bf16 v[112:127], a[112:115], a[108:111], v[112:127]\n"
  "ds_read_b128 a[68:71], v17 offset:36864\n"
  "buffer_load_dwordx4 v10, s[20:23], s29 offen lds\n"
  "v_mfma_f32_32x32x16_bf16 v[128:143], a[116:119], a[96:99], v[128:143]\n"
  "ds_read_b128 a[72:75], v17 offset:40960\n"
  "s_add_u32 m0, s30, 133120\n"
  "v_mfma_f32_32x32x16_bf16 v[144:159], a[116:119], a[100:103], v[144:159]\n"
  "ds_read_b128 a[76:79], v17 offset:45056\n"
  "buffer_load_dwordx4 v11, s[20:23], s29 offen lds\n"
  "v_mfma_f32_32x32x16_bf16 v[160:175], a[116:119], a[104:107], v[160:175]\n"
  "ds_read_b64_tr_b16 a[84:85], v30 offset:512\n"
  "s_add_u32 m0, s30, 134144\n"
  "v_mfma_f32_32x32x16_bf16 v[176:191], a[116:119], a[108:111], v[176:191]\n"
  "ds_read_b64_tr_b16 a[86:87], v30 offset:768\n"
  "buffer_load_dwordx4 v12, s[20:23], s29 offen lds\n"
  "v_mfma_f32_32x32x16_bf16 v[192:207], a[120:123], a[96:99], v[192:207]\n"
  "ds_read_b64_tr_b16 a[88:89], v30 offset:1024\n"
  "s_add_u32 m0, s30, 135168\n"
  "v_mfma_f32_32x32x16_bf16 v[208:223], a[120:123], a[100:103], v[208:223]\n"
  "ds_read_b64_tr_b16 a[90:91], v30 offset:1280\n"
  "buffer_load_dwordx4 v13, s[20:23], s29 offen lds\n"
  "v_mfma_f32_32x32x16_bf16 v[224:239], a[120:123], a[104:107], v[224:239]\n"
  "ds_read_b64_tr_b16 a[92:93], v30 offset:1536\n"
  "s_add_u32 m0, s30, 136192\n"
  "v_mfma_f32_32x32x16_bf16 v[240:255], a[120:123], a[108:111], v[240:255]\n"
  "ds_read_b64_tr_b16 a[94:95], v30 offset:1792\n"
  "buffer_load_dwordx4 v14, s[20:23], s29 offen lds\n"
  "v_mfma_f32_32x32x16_bf16 a[0:15], a[124:127], a[96:99], a[0:15]\n"
  "s_add_u32 m0, s30, 137216\n"
  "v_mfma_f32_32x32x16_bf16 a[16:31], a[124:127], a[100:103], a[16:31]\n"
  "buffer_load_dwordx4 v15, s[20:23], s29 offen lds\n"
  "v_mfma_f32_32x32x16_bf16 a[32:47], a[124:127], a[104:107], a[32:47]\n"
  "s_add_u32 m0, s30, 138240\n"
  "v_mfma_f32_32x32x16_bf16 a[48:63], a[124:127], a[108:111], a[48:63]\n"
  "buffer_load_dwordx4 v16, s[20:23], s29 offen lds\n"
  "s_add_u32 s29, s29, 524288\n"
  "s_waitcnt lgkmcnt(5)\n"
  "v_mfma_f32_32x32x16_bf16 v[64:79], a[80:83], a[64:67], v[64:79]\n"
  "ds_read_b64_tr_b16 a[112:113], v30 offset:4096\n"
  "v_mfma_f32_32x32x16_bf16 v[80:95], a[80:83], a[68:71], v[80:95]\n"
  "ds_read_b64_tr_b16 a[114:115], v30 offset:4352\n"
  "v_mfma_f32_32x32x16_bf16 v[96:111], a[80:83], a[72:75], v[96:111]\n"
  "ds_read_b128 a[96:99], v20 offset:32768\n"
  "s_add_u32 m0, s30, 0\n"
  "v_mfma_f32_32x32x16_bf16 v[112:127], a[80:83], a[76:79], v[112:127]\n"
  "ds_read_b128 a[100:103], v20 offset:36864\n"
  "buffer_load_dwordx4 v1, s[16:19], s28 offen lds\n"
  "s_waitcnt lgkmcnt(6)\n"
  "v_mfma_f32_32x32x16_bf16 v[128:143], a[84:87], a[64:67], v[128:143]\n"
  "ds_read_b128 a[104:107], v20 offset:40960\n"
  "v_mfma_f32_32x32x16_bf16 v[144:159], a[84:87], a[68:71], v[144:159]\n"
  "ds_read_b128 a[108:111], v20 offset:45056\n"
  "v_mfma_f32_32x32x16_bf16 v[160:175], a[84:87], a[72:75], v[160:175]\n"
  "ds_read_b64_tr_b16 a[116:117], v30 offset:4608\n"
  "s_add_u32 m0, s30, 1024\n"
  "v_mfma_f32_32x32x16_bf16 v[176:191], a[84:87], a[76:79], v[176:191]\n"
  "ds_read_b64_tr_b16 a[118:119], v30 offset:4864\n"
  "buffer_load_dwordx4 v2, s[16:19], s28 offen lds\n"
  "v_mfma_f32_32x32x16_bf16 v[192:207], a[88:91], a[64:67], v[192:207]\n"
  "ds_read_b64_tr_b16 a[120:121], v30 offset:5120\n"
  "v_mfma_f32_32x32x16_bf16 v[208:223], a[88:91], a[68:71], v[208:223]\n"
  "ds_read_b64_tr_b16 a[122:123], v30 offset:5376\n"
  "v_mfma_f32_32x32x16_bf16 v[224:239], a[88:91], a[72:75], v[224:239]\n"
  "ds_read_b64_tr_b16 a[124:125], v30 offset:5632\n"
  "s_add_u32 m0, s30, 2048\n"
  "v_mfma_f32_32x32x16_bf16 v[240:255], a[88:91], a[76:79], v[240:255]\n"
  "ds_read_b64_tr_b16 a[126:127], v30 offset:5888\n"
  "buffer_load_dwordx4 v3, s[16:19], s28 offen lds\n"
  "s_waitcnt lgkmcnt(10)\n"
  "v_mfma_f32_32x32x16_bf16 a[0:15], a[92:95], a[64:67], a[0:15]\n"
  "v_mfma_f32_32x32x16_bf16 a[16:31], a[92:95], a[68:71], a[16:31]\n"
  "v_mfma_f32_32x32x16_bf16 a[32:47], a[92:95], a[72:75], a[32:47]\n"
  "s_add_u32 m0, s30, 3072\n"
  "v_mfma_f32_32x32x16_bf16 a[48:63], a[92:95], a[76:79], a[48:63]\n"
  "buffer_load_dwordx4 v4, s[16:19], s28 offen lds\n"
  "s_waitcnt lgkmcnt(8)\n"
  "v_mfma_f32_32x32x16_bf16 v[64:79], a[112:115], a[96:99], v[64:79]\n"
  "ds_read_b64_tr_b16 a[80:81], v30 offset:8192\n"
  "v_mfma_f32_32x32x16_bf16 v[80:95], a[112:115], a[100:103], v[80:95]\n"
  "ds_read_b64_tr_b16 a[82:83], v30 offset:8448\n"
  "s_waitcnt lgkmcnt(8)\n"
  "v_mfma_f32_32x32x16_bf16 v[96:111], a[112:115], a[104:107], v[96:111]\n"
  "ds_read_b128 a[64:67], v23 offset:32768\n"
  "s_add_u32 m0, s30, 4096\n"
  "v_mfma_f32_32x32x16_bf16 v[112:127], a[112:115], a[108:111], v[112:127]\n"
  "ds_read_b128 a[68:71], v23 offset:36864\n"
  "buffer_load_dwordx4 v5, s[16:19], s28 offen lds\n"
  "s_waitcnt lgkmcnt(8)\n"
  "v_mfma_f32_32x32x16_bf16 v[128:143], a[116:119], a[96:99], v[128:143]\n"
  "ds_read_b128 a[72:75], v23 offset:40960\n"
  "v_mfma_f32_32x32x16_bf16 v[144:159], a[116:119], a[100:103], v[144:159]\n"
  "ds_read_b128 a[76:79], v23 offset:45056\n"
  "v_mfma_f32_32x32x16_bf16 v[160:175], a[116:119], a[104:107], v[160:175]\n"
  "ds_read_b64_tr_b16 a[84:85], v30 offset:8704\n"
  "s_add_u32 m0, s30, 5120\n"
  "v_mfma_f32_32x32x16_bf16 v[176:191], a[116:119], a[108:111], v[176:191]\n"
  "ds_read_b64_tr_b16 a[86:87], v30 offset:8960\n"
  "buffer_load_dwordx4 v6, s[16:19], s28 offen lds\n"
  "s_waitcnt lgkmcnt(8)\n"
  "v_mfma_f32_32x32x16_bf16 v[192:207], a[120:123], a[96:99], v[192:207]\n"
  "ds_read_b64_tr_b16 a[88:89], v30 offset:9216\n"
  "v_mfma_f32_32x32x16_bf16 v[208:223], a[120:123], a[100:103], v[208:223]\n"
  "ds_read_b64_tr_b16 a[90:91], v30 offset:9472\n"
  "v_mfma_f32_32x32x16_bf16 v[224:239], a[120:123], a[104:107], v[224:239]\n"
  "ds_read_b64_tr_b16 a[92:93], v30 offset:9728\n"
  "s_add_u32 m0, s30, 6144\n"
  "v_mfma_f32_32x32x16_bf16 v[240:255], a[120:123], a[108:111], v[240:255]\n"
  "ds_read_b64_tr_b16 a[94:95], v30 offset:9984\n"
  "buffer_load_dwordx4 v7, s[16:19], s28 offen lds\n"
  "v_mfma_f32_32x32x16_bf16 a[0:15], a[124:127], a[96:99], a[0:15]\n"
  "v_mfma_f32_32x32x16_bf16 a[16:31], a[124:127], a[100:103], a[16:31]\n"
  "v_mfma_f32_32x32x16_bf16 a[32:47], a[124:127], a[104:107], a[32:47]\n"
  "s_add_u32 m0, s30, 7168\n"
  "v_mfma_f32_32x32x16_bf16 a[48:63], a[124:127], a[108:111], a[48:63]\n"
  "buffer_load_dwordx4 v8, s[16:19], s28 offen lds\n"
  "s_add_u32 s28, s28, 128\n"
  "s_waitcnt lgkmcnt(7)\n"
  "v_mfma_f32_32x32x16_bf16 v[64:79], a[80:83], a[64:67], v[64:79]\n"
  "ds_read_b64_tr_b16 a[112:113], v30 offset:12288\n"
  "v_mfma_f32_32x32x16_bf16 v[80:95], a[80:83], a[68:71], v[80:95]\n"
  "ds_read_b64_tr_b16 a[114:115], v30 offset:12544\n"
  "v_mfma_f32_32x32x16_bf16 v[96:111], a[80:83], a[72:75], v[96:111]\n"
  "ds_read_b128 a[96:99], v26 offset:32768\n"
  "s_waitcnt lgkmcnt(8)\n"
  "v_mfma_f32_32x32x16_bf16 v[112:127], a[80:83], a[76:79], v[112:127]\n"
  "ds_read_b128 a[100:103], v26 offset:36864\n"
  "s_waitcnt lgkmcnt(8)\n"
  "v_mfma_f32_32x32x16_bf16 v[128:143], a[84:87], a[64:67], v[128:143]\n"
  "ds_read_b128 a[104:107], v26 offset:40960\n"
  "v_mfma_f32_32x32x16_bf16 v[144:159], a[84:87], a[68:71], v[144:159]\n"
  "ds_read_b128 a[108:111], v26 offset:45056\n"
  "v_mfma_f32_32x32x16_bf16 v[160:175], a[84:87], a[72:75], v[160:175]\n"
  "ds_read_b64_tr_b16 a[116:117], v30 offset:12800\n"
  "v_mfma_f32_32x32x16_bf16 v[176:191], a[84:87], a[76:79], v[176:191]\n"
  "ds_read_b64_tr_b16 a[118:119], v30 offset:13056\n"
  "s_waitcnt lgkmcnt(9)\n"
  "v_mfma_f32_32x32x16_bf16 v[192:207], a[88:91], a[64:67], v[192:207]\n"
  "ds_read_b64_tr_b16 a[120:121], v30 offset:13312\n"
  "v_mfma_f32_32x32x16_bf16 v[208:223], a[88:91], a[68:71], v[208:223]\n"
  "ds_read_b64_tr_b16 a[122:123], v30 offset:13568\n"
  "v_mfma_f32_32x32x16_bf16 v[224:239], a[88:91], a[72:75], v[224:239]\n"
  "ds_read_b64_tr_b16 a[124:125], v30 offset:13824\n"
  "v_mfma_f32_32x32x16_bf16 v[240:255], a[88:91], a[76:79], v[240:255]\n"
  "ds_read_b64_tr_b16 a[126:127], v30 offset:14080\n"
  "s_waitcnt lgkmcnt(12)\n"
  "v_mfma_f32_32x32x16_bf16 a[0:15], a[92:95], a[64:67], a[0:15]\n"
  "v_mfma_f32_32x32x16_bf16 a[16:31], a[92:95], a[68:71], a[16:31]\n"
  "v_mfma_f32_32x32x16_bf16 a[32:47], a[92:95], a[72:75], a[32:47]\n"
  "v_mfma_f32_32x32x16_bf16 a[48:63], a[92:95], a[76:79], a[48:63]\n"
  "s_waitcnt vmcnt(8)\n"
  "s_waitcnt lgkmcnt(0)\n"
  "s_barrier\n"
  "v_mfma_f32_32x32x16_bf16 v[64:79], a[112:115], a[96:99], v[64:79]\n"
  "ds_read_b64_tr_b16 a[80:81], v31 offset:0\n"
  "s_add_u32 m0, s30, 32768\n"
  "v_mfma_f32_32x32x16_bf16 v[80:95], a[112:115], a[100:103], v[80:95]\n"
  "ds_read_b64_tr_b16 a[82:83], v31 offset:256\n"
  "buffer_load_dwordx4 v9, s[20:23], s29 offen lds\n"
  "v_mfma_f32_32x32x16_bf16 v[96:111], a[112:115], a[104:107], v[96:111]\n"
  "ds_read_b128 a[64:67], v18 offset:32768\n"
  "s_add_u32 m0, s30, 33792\n"
  "v_mfma_f32_32x32x16_bf16 v[112:127], a[112:115], a[108:111], v[112:127]\n"
  "ds_read_b128 a[68:71], v18 offset:36864\n"
  "buffer_load_dwordx4 v10, s[20:23], s29 offen lds\n"
  "v_mfma_f32_32x32x16_bf16 v[128:143], a[116:119], a[96:99], v[128:143]\n"
  "ds_read_b128 a[72:75], v18 offset:40960\n"
  "s_add_u32 m0, s30, 34816\n"
  "v_mfma_f32_32x32x16_bf16 v[144:159], a[116:119], a[100:103], v[144:159]\n"
  "ds_read_b128 a[76:79], v18 offset:45056\n"
  "buffer_load_dwordx4 v11, s[20:23], s29 offen lds\n"
  "v_mfma_f32_32x32x16_bf16 v[160:175], a[116:119], a[104:107], v[160:175]\n"
  "ds_read_b64_tr_b16 a[84:85], v31 offset:512\n"
  "s_add_u32 m0, s30, 35840\n"
  "v_mfma_f32_32x32x16_bf16 v[176:191], a[116:119], a[108:111], v[176:191]\n"
  "ds_read_b64_tr_b16 a[86:87], v31 offset:768\n"
  "buffer_load_dwordx4 v12, s[20:23], s29 offen lds\n"
  "v_mfma_f32_32x32x16_bf16 v[192:207], a[120:123], a[96:99], v[192:207]\n"
  "ds_read_b64_tr_b16 a[88:89], v31 offset:1024\n"
  "s_add_u32 m0, s30, 36864\n"
  "v_mfma_f32_32x32x16_bf16 v[208:223], a[120:123], a[100:103], v[208:223]\n"
  "ds_read_b64_tr_b16 a[90:91], v31 offset:1280\n"
  "buffer_load_dwordx4 v13, s[20:23], s29 offen lds\n"
  "v_mfma_f32_32x32x16_bf16 v[224:239], a[120:123], a[104:107], v[224:239]\n"
  "ds_read_b64_tr_b16 a[92:93], v31 offset:1536\n"
  "s_add_u32 m0, s30, 37888\n"
  "v_mfma_f32_32x32x16_bf16 v[240:255], a[120:123], a[108:111], v[240:255]\n"
  "ds_read_b64_tr_b16 a[94:95], v31 offset:1792\n"
  "buffer_load_dwordx4 v14, s[20:23], s29 offen lds\n"
  "v_mfma_f32_32x32x16_bf16 a[0:15], a[124:127], a[96:99], a[0:15]\n"
  "s_add_u32 m0, s30, 38912\n"
  "v_mfma_f32_32x32x16_bf16 a[16:31], a[124:127], a[100:103], a[16:31]\n"
  "buffer_load_dwordx4 v15, s[20:23], s29 offen lds\n"
  "v_mfma_f32_32x32x16_bf16 a[32:47], a[124:127], a[104:107], a[32:47]\n"
  "s_add_u32 m0, s30, 39936\n"
  "v_mfma_f32_32x32x16_bf16 a[48:63], a[124:127], a[108:111], a[48:63]\n"
  "buffer_load_dwordx4 v16, s[20:23], s29 offen lds\n"
  "s_add_u32 s29, s29, 524288\n"
  "s_waitcnt lgkmcnt(5)\n"
  "v_mfma_f32_32x32x16_bf16 v[64:79], a[80:83], a[64:67], v[64:79]\n"
  "ds_read_b64_tr_b16 a[112:113], v31 offset:4096\n"
  "v_mfma_f32_32x32x16_bf16 v[80:95], a[80:83], a[68:71], v[80:95]\n"
  "ds_read_b64_tr_b16 a[114:115], v31 offset:4352\n"
  "v_mfma_f32_32x32x16_bf16 v[96:111], a[80:83], a[72:75], v[96:111]\n"
  "ds_read_b128 a[96:99], v21 offset:32768\n"
  "s_add_u32 m0, s30, 65536\n"
  "v_mfma_f32_32x32x16_bf16 v[112:127], a[80:83], a[76:79], v[112:127]\n"
  "ds_read_b128 a[100:103], v21 offset:36864\n"
  "buffer_load_dwordx4 v1, s[16:19], s28 offen lds\n"
  "s_waitcnt lgkmcnt(6)\n"
  "v_mfma_f32_32x32x16_bf16 v[128:143], a[84:87], a[64:67], v[128:143]\n"
  "ds_read_b128 a[104:107], v21 offset:40960\n"
  "v_mfma_f32_32x32x16_bf16 v[144:159], a[84:87], a[68:71], v[144:159]\n"
  "ds_read_b128 a[108:111], v21 offset:45056\n"
  "v_mfma_f32_32x32x16_bf16 v[160:175], a[84:87], a[72:75], v[160:175]\n"
  "ds_read_b64_tr_b16 a[116:117], v31 offset:4608\n"
  "s_add_u32 m0, s30, 66560\n"
  "v_mfma_f32_32x32x16_bf16 v[176:191], a[84:87], a[76:79], v[176:191]\n"
  "ds_read_b64_tr_b16 a[118:119], v31 offset:4864\n"
  "buffer_load_dwordx4 v2, s[16:19], s28 offen lds\n"
  "v_mfma_f32_32x32x16_bf16 v[192:207], a[88:91], a[64:67], v[192:207]\n"
  "ds_read_b64_tr_b16 a[120:121], v31 offset:5120\n"
  "v_mfma_f32_32x32x16_bf16 v[208:223], a[88:91], a[68:71], v[208:223]\n"
  "ds_read_b64_tr_b16 a[122:123], v31 offset:5376\n"
  "v_mfma_f32_32x32x16_bf16 v[224:239], a[88:91], a[72:75], v[224:239]\n"
  "ds_read_b64_tr_b16 a[124:125], v31 offset:5632\n"
  "s_add_u32 m0, s30, 67584\n"
  "v_mfma_f32_32x32x16_bf16 v[240:255], a[88:91], a[76:79], v[240:255]\n"
  "ds_read_b64_tr_b16 a[126:127], v31 offset:5888\n"
  "buffer_load_dwordx4 v3, s[16:19], s28 offen lds\n"
  "s_waitcnt lgkmcnt(10)\n"
  "v_mfma_f32_32x32x16_bf16 a[0:15], a[92:95], a[64:67], a[0:15]\n"
  "v_mfma_f32_32x32x16_bf16 a[16:31], a[92:95], a[68:71], a[16:31]\n"
  "v_mfma_f32_32x32x16_bf16 a[32:47], a[92:95], a[72:75], a[32:47]\n"
  "s_add_u32 m0, s30, 68608\n"
  "v_mfma_f32_32x32x16_bf16 a[48:63], a[92:95], a[76:79], a[48:63]\n"
  "buffer_load_dwordx4 v4, s[16:19], s28 offen lds\n"
  "s_waitcnt lgkmcnt(8)\n"
  "v_mfma_f32_32x32x16_bf16 v[64:79], a[112:115], a[96:99], v[64:79]\n"
  "ds_read_b64_tr_b16 a[80:81], v31 offset:8192\n"
  "v_mfma_f32_32x32x16_bf16 v[80:95], a[112:115], a[100:103], v[80:95]\n"
  "ds_read_b64_tr_b16 a[82:83], v31 offset:8448\n"
  "s_waitcnt lgkmcnt(8)\n"
  "v_mfma_f32_32x32x16_bf16 v[96:111], a[112:115], a[104:107], v[96:111]\n"
  "ds_read_b128 a[64:67], v24 offset:32768\n"
  "s_add_u32 m0, s30, 69632\n"
  "v_mfma_f32_32x32x16_bf16 v[112:127], a[112:115], a[108:111], v[112:127]\n"
  "ds_read_b128 a[68:71], v24 offset:36864\n"
  "buffer_load_dwordx4 v5, s[16:19], s28 offen lds\n"
  "s_waitcnt lgkmcnt(8)\n"
  "v_mfma_f32_32x32x16_bf16 v[128:143], a[116:119], a[96:99], v[128:143]\n"
  "ds_read_b128 a[72:75], v24 offset:40960\n"
  "v_mfma_f32_32x32x16_bf16 v[144:159], a[116:119], a[100:103], v[144:159]\n"
  "ds_read_b128 a[76:79], v24 offset:45056\n"
  "v_mfma_f32_32x32x16_bf16 v[160:175], a[116:119], a[104:107], v[160:175]\n"
  "ds_read_b64_tr_b16 a[84:85], v31 offset:8704\n"
  "s_add_u32 m0, s30, 70656\n"
  "v_mfma_f32_32x32x16_bf16 v[176:191], a[116:119], a[108:111], v[176:191]\n"
  "ds_read_b64_tr_b16 a[86:87], v31 offset:8960\n"
  "buffer_load_dwordx4 v6, s[16:19], s28 offen lds\n"
  "s_waitcnt lgkmcnt(8)\n"
  "v_mfma_f32_32x32x16_bf16 v[192:207], a[120:123], a[96:99], v[192:207]\n"
  "ds_read_b64_tr_b16 a[88:89], v31 offset:9216\n"
  "v_mfma_f32_32x32x16_bf16 v[208:223], a[120:123], a[100:103], v[208:223]\n"
  "ds_read_b64_tr_b16 a[90:91], v31 offset:9472\n"
  "v_mfma_f32_32x32x16_bf16 v[224:239], a[120:123], a[104:107], v[224:239]\n"
  "ds_read_b64_tr_b16 a[92:93], v31 offset:9728\n"
  "s_add_u32 m0, s30, 71680\n"
  "v_mfma_f32_32x32x16_bf16 v[240:255], a[120:123], a[108:111], v[240:255]\n"
  "ds_read_b64_tr_b16 a[94:95], v31 offset:9984\n"
  "buffer_load_dwordx4 v7, s[16:19], s28 offen lds\n"
  "v_mfma_f32_32x32x16_bf16 a[0:15], a[124:127], a[96:99], a[0:15]\n"
  "v_mfma_f32_32x32x16_bf16 a[16:31], a[124:127], a[100:103], a[16:31]\n"
  "v_mfma_f32_32x32x16_bf16 a[32:47], a[124:127], a[104:107], a[32:47]\n"
  "s_add_u32 m0, s30, 72704\n"
  "v_mfma_f32_32x32x16_bf16 a[48:63], a[124:127], a[108:111], a[48:63]\n"
  "buffer_load_dwordx4 v8, s[16:19], s28 offen lds\n"
  "s_add_u32 s28, s28, 128\n"
  "s_waitcnt lgkmcnt(7)\n"
  "v_mfma_f32_32x32x16_bf16 v[64:79], a[80:83], a[64:67], v[64:79]\n"
  "ds_read_b64_tr_b16 a[112:113], v31 offset:12288\n"
  "v_mfma_f32_32x32x16_bf16 v[80:95], a[80:83], a[68:71], v[80:95]\n"
  "ds_read_b64_tr_b16 a[114:115], v31 offset:12544\n"
  "v_mfma_f32_32x32x16_bf16 v[96:111], a[80:83], a[72:75], v[96:111]\n"
  "ds_read_b128 a[96:99], v27 offset:32768\n"
  "s_waitcnt lgkmcnt(8)\n"
  "v_mfma_f32_32x32x16_bf16 v[112:127], a[80:83], a[76:79], v[112:127]\n"
  "ds_read_b128 a[100:103], v27 offset:36864\n"
  "s_waitcnt lgkmcnt(8)\n"
  "v_mfma_f32_32x32x16_bf16 v[128:143], a[84:87], a[64:67], v[128:143]\n"
  "ds_read_b128 a[104:107], v27 offset:40960\n"
  "v_mfma_f32_32x32x16_bf16 v[144:159], a[84:87], a[68:71], v[144:159]\n"
  "ds_read_b128 a[108:111], v27 offset:45056\n"
  "v_mfma_f32_32x32x16_bf16 v[160:175], a[84:87], a[72:75], v[160:175]\n"
  "ds_read_b64_tr_b16 a[116:117], v31 offset:12800\n"
  "v_mfma_f32_32x32x16_bf16 v[176:191], a[84:87], a[76:79], v[176:191]\n"
  "ds_read_b64_tr_b16 a[118:119], v31 offset:13056\n"
  "s_waitcnt lgkmcnt(9)\n"
  "v_mfma_f32_32x32x16_bf16 v[192:207], a[88:91], a[64:67], v[192:207]\n"
  "ds_read_b64_tr_b16 a[120:121], v31 offset:13312\n"
  "v_mfma_f32_32x32x16_bf16 v[208:223], a[88:91], a[68:71], v[208:223]\n"
  "ds_read_b64_tr_b16 a[122:123], v31 offset:13568\n"
  "v_mfma_f32_32x32x16_bf16 v[224:239], a[88:91], a[72:75], v[224:239]\n"
  "ds_read_b64_tr_b16 a[124:125], v31 offset:13824\n"
  "v_mfma_f32_32x32x16_bf16 v[240:255], a[88:91], a[76:79], v[240:255]\n"
  "ds_read_b64_tr_b16 a[126:127], v31 offset:14080\n"
  "s_waitcnt lgkmcnt(12)\n"
  "v_mfma_f32_32x32x16_bf16 a[0:15], a[92:95], a[64:67], a[0:15]\n"
  "v_mfma_f32_32x32x16_bf16 a[16:31], a[92:95], a[68:71], a[16:31]\n"
  "v_mfma_f32_32x32x16_bf16 a[32:47], a[92:95], a[72:75], a[32:47]\n"
  "v_mfma_f32_32x32x16_bf16 a[48:63], a[92:95], a[76:79], a[48:63]\n"
  "s_waitcnt vmcnt(8)\n"
  "s_waitcnt lgkmcnt(0)\n"
  "s_barrier\n"
  "s_lshl_b32 s32, s10, 2\n"
  "s_add_u32 s32, s32, s11\n"
  "s_load_dwordx2 s[40:41], s[8:9], 48\n"
  "s_load_dwordx2 s[42:43], s[8:9], 64\n"
  "v_min_u32 v55, 17, v0\n"
  "s_mul_i32 s33, s32, 18\n"
  "v_add_u32 v55, s33, v55\n"
  "v_lshlrev_b32 v55, 7, v55\n"
  "v_min_u32 v56, 35, v0\n"
  "s_mul_i32 s33, s32, 36\n"
  "v_add_u32 v56, s33, v56\n"
  "v_lshlrev_b32 v56, 7, v56\n"
  "s_waitcnt lgkmcnt(0)\n"
  "global_load_dword v61, v55, s[40:41]\n"
  "global_load_dword v61, v56, s[42:43]\n"
  "v_mfma_f32_32x32x16_bf16 v[64:79], a[112:115], a[96:99], v[64:79]\n"
  "ds_read_b64_tr_b16 a[80:81], v29 offset:32768\n"
  "s_add_u32 m0, s30, 98304\n"
  "v_mfma_f32_32x32x16_bf16 v[80:95], a[112:115], a[100:103], v[80:95]\n"
  "ds_read_b64_tr_b16 a[82:83], v29 offset:33024\n"
  "buffer_load_dwordx4 v9, s[20:23], s29 offen lds\n"
  "v_mfma_f32_32x32x16_bf16 v[96:111], a[112:115], a[104:107], v[96:111]\n"
  "ds_read_b128 a[64:67], v17 offset:0\n"
  "s_add_u32 m0, s30, 99328\n"
  "v_mfma_f32_32x32x16_bf16 v[112:127], a[112:115], a[108:111], v[112:127]\n"
  "ds_read_b128 a[68:71], v17 offset:4096\n"
  "buffer_load_dwordx4 v10, s[20:23], s29 offen lds\n"
  "v_mfma_f32_32x32x16_bf16 v[128:143], a[116:119], a[96:99], v[128:143]\n"
  "ds_read_b128 a[72:75], v17 offset:8192\n"
  "s_add_u32 m0, s30, 100352\n"
  "v_mfma_f32_32x32x16_bf16 v[144:159], a[116:119], a[100:103], v[144:159]\n"
  "ds_read_b128 a[76:79], v17 offset:12288\n"
  "buffer_load_dwordx4 v11, s[20:23], s29 offen lds\n"
  "v_mfma_f32_32x32x16_bf16 v[160:175], a[116:119], a[104:107], v[160:175]\n"
  "ds_read_b64_tr_b16 a[84:85], v29 offset:33280\n"
  "s_add_u32 m0, s30, 101376\n"
  "v_mfma_f32_32x32x16_bf16 v[176:191], a[116:119], a[108:111], v[176:191]\n"
  "ds_read_b64_tr_b16 a[86:87], v29 offset:33536\n"
  "buffer_load_dwordx4 v12, s[20:23], s29 offen lds\n"
  "v_mfma_f32_32x32x16_bf16 v[192:207], a[120:123], a[96:99], v[192:207]\n"
  "ds_read_b64_tr_b16 a[88:89], v29 offset:33792\n"
  "s_add_u32 m0, s30, 102400\n"
  "v_mfma_f32_32x32x16_bf16 v[208:223], a[120:123], a[100:103], v[208:223]\n"
  "ds_read_b64_tr_b16 a[90:91], v29 offset:34048\n"
  "buffer_load_dwordx4 v13, s[20:23], s29 offen lds\n"
  "v_mfma_f32_32x32x16_bf16 v[224:239], a[120:123], a[104:107], v[224:239]\n"
  "ds_read_b64_tr_b16 a[92:93], v29 offset:34304\n"
  "s_add_u32 m0, s30, 103424\n"
  "v_mfma_f32_32x32x16_bf16 v[240:255], a[120:123], a[108:111], v[240:255]\n"
  "ds_read_b64_tr_b16 a[94:95], v29 offset:34560\n"
  "buffer_load_dwordx4 v14, s[20:23], s29 offen lds\n"
  "v_mfma_f32_32x32x16_bf16 a[0:15], a[124:127], a[96:99], a[0:15]\n"
  "s_add_u32 m0, s30, 104448\n"
  "v_mfma_f32_32x32x16_bf16 a[16:31], a[124:127], a[100:103], a[16:31]\n"
  "buffer_load_dwordx4 v15, s[20:23], s29 offen lds\n"
  "v_mfma_f32_32x32x16_bf16 a[32:47], a[124:127], a[104:107], a[32:47]\n"
  "s_add_u32 m0, s30, 105472\n"
  "v_mfma_f32_32x32x16_bf16 a[48:63], a[124:127], a[108:111], a[48:63]\n"
  "buffer_load_dwordx4 v16, s[20:23], s29 offen lds\n"
  "s_add_u32 s29, s29, 524288\n"
  "s_waitcnt lgkmcnt(5)\n"
  "v_mfma_f32_32x32x16_bf16 v[64:79], a[80:83], a[64:67], v[64:79]\n"
  "ds_read_b64_tr_b16 a[112:113], v29 offset:36864\n"
  "v_mfma_f32_32x32x16_bf16 v[80:95], a[80:83], a[68:71], v[80:95]\n"
  "ds_read_b64_tr_b16 a[114:115], v29 offset:37120\n"
  "v_mfma_f32_32x32x16_bf16 v[96:111], a[80:83], a[72:75], v[96:111]\n"
  "ds_read_b128 a[96:99], v20 offset:0\n"
  "s_add_u32 m0, s30, 131072\n"
  "v_mfma_f32_32x32x16_bf16 v[112:127], a[80:83], a[76:79], v[112:127]\n"
  "ds_read_b128 a[100:103], v20 offset:4096\n"
  "buffer_load_dwordx4 v1, s[16:19], s28 offen lds\n"
  "s_waitcnt lgkmcnt(6)\n"
  "v_mfma_f32_32x32x16_bf16 v[128:143], a[84:87], a[64:67], v[128:143]\n"
  "ds_read_b128 a[104:107], v20 offset:8192\n"
  "v_mfma_f32_32x32x16_bf16 v[144:159], a[84:87], a[68:71], v[144:159]\n"
  "ds_read_b128 a[108:111], v20 offset:12288\n"
  "v_mfma_f32_32x32x16_bf16 v[160:175], a[84:87], a[72:75], v[160:175]\n"
  "ds_read_b64_tr_b16 a[116:117], v29 offset:37376\n"
  "s_add_u32 m0, s30, 132096\n"
  "v_mfma_f32_32x32x16_bf16 v[176:191], a[84:87], a[76:79], v[176:191]\n"
  "ds_read_b64_tr_b16 a[118:119], v29 offset:37632\n"
  "buffer_load_dwordx4 v2, s[16:19], s28 offen lds\n"
  "v_mfma_f32_32x32x16_bf16 v[192:207], a[88:91], a[64:67], v[192:207]\n"
  "ds_read_b64_tr_b16 a[120:121], v29 offset:37888\n"
  "v_mfma_f32_32x32x16_bf16 v[208:223], a[88:91], a[68:71], v[208:223]\n"
  "ds_read_b64_tr_b16 a[122:123], v29 offset:38144\n"
  "v_mfma_f32_32x32x16_bf16 v[224:239], a[88:91], a[72:75], v[224:239]\n"
  "ds_read_b64_tr_b16 a[124:125], v29 offset:38400\n"
  "s_add_u32 m0, s30, 133120\n"
  "v_mfma_f32_32x32x16_bf16 v[240:255], a[88:91], a[76:79], v[240:255]\n"
  "ds_read_b64_tr_b16 a[126:127], v29 offset:38656\n"
  "buffer_load_dwordx4 v3, s[16:19], s28 offen lds\n"
  "s_waitcnt lgkmcnt(10)\n"
  "v_mfma_f32_32x32x16_bf16 a[0:15], a[92:95], a[64:67], a[0:15]\n"
  "v_mfma_f32_32x32x16_bf16 a[16:31], a[92:95], a[68:71], a[16:31]\n"
  "v_mfma_f32_32x32x16_bf16 a[32:47], a[92:95], a[72:75], a[32:47]\n"
  "s_add_u32 m0, s30, 134144\n"
  "v_mfma_f32_32x32x16_bf16 a[48:63], a[92:95], a[76:79], a[48:63]\n"
  "buffer_load_dwordx4 v4, s[16:19], s28 offen lds\n"
  "s_waitcnt lgkmcnt(8)\n"
  "v_mfma_f32_32x32x16_bf16 v[64:79], a[112:115], a[96:99], v[64:79]\n"
  "ds_read_b64_tr_b16 a[80:81], v29 offset:40960\n"
  "v_mfma_f32_32x32x16_bf16 v[80:95], a[112:115], a[100:103], v[80:95]\n"
  "ds_read_b64_tr_b16 a[82:83], v29 offset:41216\n"
  "s_waitcnt lgkmcnt(8)\n"
  "v_mfma_f32_32x32x16_bf16 v[96:111], a[112:115], a[104:107], v[96:111]\n"
  "ds_read_b128 a[64:67], v23 offset:0\n"
  "s_add_u32 m0, s30, 135168\n"
  "v_mfma_f32_32x32x16_bf16 v[112:127], a[112:115], a[108:111], v[112:127]\n"
  "ds_read_b128 a[68:71], v23 offset:4096\n"
  "buffer_load_dwordx4 v5, s[16:19], s28 offen lds\n"
  "s_waitcnt lgkmcnt(8)\n"
  "v_mfma_f32_32x32x16_bf16 v[128:143], a[116:119], a[96:99], v[128:143]\n"
  "ds_read_b128 a[72:75], v23 offset:8192\n"
  "v_mfma_f32_32x32x16_bf16 v[144:159], a[116:119], a[100:103], v[144:159]\n"
  "ds_read_b128 a[76:79], v23 offset:12288\n"
  "v_mfma_f32_32x32x16_bf16 v[160:175], a[116:119], a[104:107], v[160:175]\n"
  "ds_read_b64_tr_b16 a[84:85], v29 offset:41472\n"
  "s_add_u32 m0, s30, 136192\n"
  "v_mfma_f32_32x32x16_bf16 v[176:191], a[116:119], a[108:111], v[176:191]\n"
  "ds_read_b64_tr_b16 a[86:87], v29 offset:41728\n"
  "buffer_load_dwordx4 v6, s[16:19], s28 offen lds\n"
  "s_waitcnt lgkmcnt(8)\n"
  "v_mfma_f32_32x32x16_bf16 v[192:207], a[120:123], a[96:99], v[192:207]\n"
  "ds_read_b64_tr_b16 a[88:89], v29 offset:41984\n"
  "v_mfma_f32_32x32x16_bf16 v[208:223], a[120:123], a[100:103], v[208:223]\n"
  "ds_read_b64_tr_b16 a[90:91], v29 offset:42240\n"
  "v_mfma_f32_32x32x16_bf16 v[224:239], a[120:123], a[104:107], v[224:239]\n"
  "ds_read_b64_tr_b16 a[92:93], v29 offset:42496\n"
  "s_add_u32 m0, s30, 137216\n"
  "v_mfma_f32_32x32x16_bf16 v[240:255], a[120:123], a[108:111], v[240:255]\n"
  "ds_read_b64_tr_b16 a[94:95], v29 offset:42752\n"
  "buffer_load_dwordx4 v7, s[16:19], s28 offen lds\n"
  "v_mfma_f32_32x32x16_bf16 a[0:15], a[124:127], a[96:99], a[0:15]\n"
  "v_mfma_f32_32x32x16_bf16 a[16:31], a[124:127], a[100:103], a[16:31]\n"
  "v_mfma_f32_32x32x16_bf16 a[32:47], a[124:127], a[104:107], a[32:47]\n"
  "s_add_u32 m0, s30, 138240\n"
  "v_mfma_f32_32x32x16_bf16 a[48:63], a[124:127], a[108:111], a[48:63]\n"
  "buffer_load_dwordx4 v8, s[16:19], s28 offen lds\n"
  "s_add_u32 s28, s28, 128\n"
  "s_waitcnt lgkmcnt(7)\n"
  "v_mfma_f32_32x32x16_bf16 v[64:79], a[80:83], a[64:67], v[64:79]\n"
  "ds_read_b64_tr_b16 a[112:113], v29 offset:45056\n"
  "v_mfma_f32_32x32x16_bf16 v[80:95], a[80:83], a[68:71], v[80:95]\n"
  "ds_read_b64_tr_b16 a[114:115], v29 offset:45312\n"
  "v_mfma_f32_32x32x16_bf16 v[96:111], a[80:83], a[72:75], v[96:111]\n"
  "ds_read_b128 a[96:99], v26 offset:0\n"
  "s_waitcnt lgkmcnt(8)\n"
  "v_mfma_f32_32x32x16_bf16 v[112:127], a[80:83], a[76:79], v[112:127]\n"
  "ds_read_b128 a[100:103], v26 offset:4096\n"
  "s_waitcnt lgkmcnt(8)\n"
  "v_mfma_f32_32x32x16_bf16 v[128:143], a[84:87], a[64:67], v[128:143]\n"
  "ds_read_b128 a[104:107], v26 offset:8192\n"
  "v_mfma_f32_32x32x16_bf16 v[144:159], a[84:87], a[68:71], v[144:159]\n"
  "ds_read_b128 a[108:111], v26 offset:12288\n"
  "v_mfma_f32_32x32x16_bf16 v[160:175], a[84:87], a[72:75], v[160:175]\n"
  "ds_read_b64_tr_b16 a[116:117], v29 offset:45568\n"
  "v_mfma_f32_32x32x16_bf16 v[176:191], a[84:87], a[76:79], v[176:191]\n"
  "ds_read_b64_tr_b16 a[118:119], v29 offset:45824\n"
  "s_waitcnt lgkmcnt(9)\n"
  "v_mfma_f32_32x32x16_bf16 v[192:207], a[88:91], a[64:67], v[192:207]\n"
  "ds_read_b64_tr_b16 a[120:121], v29 offset:46080\n"
  "v_mfma_f32_32x32x16_bf16 v[208:223], a[88:91], a[68:71], v[208:223]\n"
  "ds_read_b64_tr_b16 a[122:123], v29 offset:46336\n"
  "v_mfma_f32_32x32x16_bf16 v[224:239], a[88:91], a[72:75], v[224:239]\n"
  "ds_read_b64_tr_b16 a[124:125], v29 offset:46592\n"
  "v_mfma_f32_32x32x16_bf16 v[240:255], a[88:91], a[76:79], v[240:255]\n"
  "ds_read_b64_tr_b16 a[126:127], v29 offset:46848\n"
  "s_waitcnt lgkmcnt(12)\n"
  "v_mfma_f32_32x32x16_bf16 a[0:15], a[92:95], a[64:67], a[0:15]\n"
  "v_mfma_f32_32x32x16_bf16 a[16:31], a[92:95], a[68:71], a[16:31]\n"
  "v_mfma_f32_32x32x16_bf16 a[32:47], a[92:95], a[72:75], a[32:47]\n"
  "v_mfma_f32_32x32x16_bf16 a[48:63], a[92:95], a[76:79], a[48:63]\n"
  "s_waitcnt vmcnt(8)\n"
  "s_waitcnt lgkmcnt(0)\n"
  "s_barrier\n"
  "v_mfma_f32_32x32x16_bf16 v[64:79], a[112:115], a[96:99], v[64:79]\n"
  "ds_read_b64_tr_b16 a[80:81], v30 offset:32768\n"
  "s_add_u32 m0, s30, 0\n"
  "v_mfma_f32_32x32x16_bf16 v[80:95], a[112:115], a[100:103], v[80:95]\n"
  "ds_read_b64_tr_b16 a[82:83], v30 offset:33024\n"
  "buffer_load_dwordx4 v9, s[20:23], s29 offen lds\n"
  "v_mfma_f32_32x32x16_bf16 v[96:111], a[112:115], a[104:107], v[96:111]\n"
  "ds_read_b128 a[64:67], v18 offset:0\n"
  "s_add_u32 m0, s30, 1024\n"
  "v_mfma_f32_32x32x16_bf16 v[112:127], a[112:115], a[108:111], v[112:127]\n"
  "ds_read_b128 a[68:71], v18 offset:4096\n"
  "buffer_load_dwordx4 v10, s[20:23], s29 offen lds\n"
  "v_mfma_f32_32x32x16_bf16 v[128:143], a[116:119], a[96:99], v[128:143]\n"
  "ds_read_b128 a[72:75], v18 offset:8192\n"
  "s_add_u32 m0, s30, 2048\n"
  "v_mfma_f32_32x32x16_bf16 v[144:159], a[116:119], a[100:103], v[144:159]\n"
  "ds_read_b128 a[76:79], v18 offset:12288\n"
  "buffer_load_dwordx4 v11, s[20:23], s29 offen lds\n"
  "v_mfma_f32_32x32x16_bf16 v[160:175], a[116:119], a[104:107], v[160:175]\n"
  "ds_read_b64_tr_b16 a[84:85], v30 offset:33280\n"
  "s_add_u32 m0, s30, 3072\n"
  "v_mfma_f32_32x32x16_bf16 v[176:191], a[116:119], a[108:111], v[176:191]\n"
  "ds_read_b64_tr_b16 a[86:87], v30 offset:33536\n"
  "buffer_load_dwordx4 v12, s[20:23], s29 offen lds\n"
  "v_mfma_f32_32x32x16_bf16 v[192:207], a[120:123], a[96:99], v[192:207]\n"
  "ds_read_b64_tr_b16 a[88:89], v30 offset:33792\n"
  "s_add_u32 m0, s30, 4096\n"
  "v_mfma_f32_32x32x16_bf16 v[208:223], a[120:123], a[100:103], v[208:223]\n"
  "ds_read_b64_tr_b16 a[90:91], v30 offset:34048\n"
  "buffer_load_dwordx4 v13, s[20:23], s29 offen lds\n"
  "v_mfma_f32_32x32x16_bf16 v[224:239], a[120:123], a[104:107], v[224:239]\n"
  "ds_read_b64_tr_b16 a[92:93], v30 offset:34304\n"
  "s_add_u32 m0, s30, 5120\n"
  "v_mfma_f32_32x32x16_bf16 v[240:255], a[120:123], a[108:111], v[240:255]\n"
  "ds_read_b64_tr_b16 a[94:95], v30 offset:34560\n"
  "buffer_load_dwordx4 v14, s[20:23], s29 offen lds\n"
  "v_mfma_f32_32x32x16_bf16 a[0:15], a[124:127], a[96:99], a[0:15]\n"
  "s_add_u32 m0, s30, 6144\n"
  "v_mfma_f32_32x32x16_bf16 a[16:31], a[124:127], a[100:103], a[16:31]\n"
  "buffer_load_dwordx4 v15, s[20:23], s29 offen lds\n"
  "v_mfma_f32_32x32x16_bf16 a[32:47], a[124:127], a[104:107], a[32:47]\n"
  "s_add_u32 m0, s30, 7168\n"
  "v_mfma_f32_32x32x16_bf16 a[48:63], a[124:127], a[108:111], a[48:63]\n"
  "buffer_load_dwordx4 v16, s[20:23], s29 offen lds\n"
  "s_add_u32 s29, s29, 524288\n"
  "s_waitcnt lgkmcnt(5)\n"
  "v_mfma_f32_32x32x16_bf16 v[64:79], a[80:83], a[64:67], v[64:79]\n"
  "ds_read_b64_tr_b16 a[112:113], v30 offset:36864\n"
  "v_mfma_f32_32x32x16_bf16 v[80:95], a[80:83], a[68:71], v[80:95]\n"
  "ds_read_b64_tr_b16 a[114:115], v30 offset:37120\n"
  "v_mfma_f32_32x32x16_bf16 v[96:111], a[80:83], a[72:75], v[96:111]\n"
  "ds_read_b128 a[96:99], v21 offset:0\n"
  "v_mfma_f32_32x32x16_bf16 v[112:127], a[80:83], a[76:79], v[112:127]\n"
  "ds_read_b128 a[100:103], v21 offset:4096\n"
  "s_waitcnt lgkmcnt(7)\n"
  "v_mfma_f32_32x32x16_bf16 v[128:143], a[84:87], a[64:67], v[128:143]\n"
  "ds_read_b128 a[104:107], v21 offset:8192\n"
  "v_mfma_f32_32x32x16_bf16 v[144:159], a[84:87], a[68:71], v[144:159]\n"
  "ds_read_b128 a[108:111], v21 offset:12288\n"
  "v_mfma_f32_32x32x16_bf16 v[160:175], a[84:87], a[72:75], v[160:175]\n"
  "ds_read_b64_tr_b16 a[116:117], v30 offset:37376\n"
  "v_mfma_f32_32x32x16_bf16 v[176:191], a[84:87], a[76:79], v[176:191]\n"
  "ds_read_b64_tr_b16 a[118:119], v30 offset:37632\n"
  "s_waitcnt lgkmcnt(8)\n"
  "v_mfma_f32_32x32x16_bf16 v[192:207], a[88:91], a[64:67], v[192:207]\n"
  "ds_read_b64_tr_b16 a[120:121], v30 offset:37888\n"
  "v_mfma_f32_32x32x16_bf16 v[208:223], a[88:91], a[68:71], v[208:223]\n"
  "ds_read_b64_tr_b16 a[122:123], v30 offset:38144\n"
  "v_mfma_f32_32x32x16_bf16 v[224:239], a[88:91], a[72:75], v[224:239]\n"
  "ds_read_b64_tr_b16 a[124:125], v30 offset:38400\n"
  "v_mfma_f32_32x32x16_bf16 v[240:255], a[88:91], a[76:79], v[240:255]\n"
  "ds_read_b64_tr_b16 a[126:127], v30 offset:38656\n"
  "v_mfma_f32_32x32x16_bf16 a[0:15], a[92:95], a[64:67], a[0:15]\n"
  "v_mfma_f32_32x32x16_bf16 a[16:31], a[92:95], a[68:71], a[16:31]\n"
  "v_mfma_f32_32x32x16_bf16 a[32:47], a[92:95], a[72:75], a[32:47]\n"
  "v_mfma_f32_32x32x16_bf16 a[48:63], a[92:95], a[76:79], a[48:63]\n"
  "s_waitcnt lgkmcnt(9)\n"
  "v_mfma_f32_32x32x16_bf16 v[64:79], a[112:115], a[96:99], v[64:79]\n"
  "ds_read_b64_tr_b16 a[80:81], v30 offset:40960\n"
  "s_waitcnt lgkmcnt(9)\n"
  "v_mfma_f32_32x32x16_bf16 v[80:95], a[112:115], a[100:103], v[80:95]\n"
  "ds_read_b64_tr_b16 a[82:83], v30 offset:41216\n"
  "s_waitcnt lgkmcnt(9)\n"
  "v_mfma_f32_32x32x16_bf16 v[96:111], a[112:115], a[104:107], v[96:111]\n"
  "ds_read_b128 a[64:67], v24 offset:0\n"
  "s_waitcnt lgkmcnt(9)\n"
  "v_mfma_f32_32x32x16_bf16 v[112:127], a[112:115], a[108:111], v[112:127]\n"
  "ds_read_b128 a[68:71], v24 offset:4096\n"
  "s_waitcnt lgkmcnt(8)\n"
  "v_mfma_f32_32x32x16_bf16 v[128:143], a[116:119], a[96:99], v[128:143]\n"
  "ds_read_b128 a[72:75], v24 offset:8192\n"
  "v_mfma_f32_32x32x16_bf16 v[144:159], a[116:119], a[100:103], v[144:159]\n"
  "ds_read_b128 a[76:79], v24 offset:12288\n"
  "v_mfma_f32_32x32x16_bf16 v[160:175], a[116:119], a[104:107], v[160:175]\n"
  "ds_read_b64_tr_b16 a[84:85], v30 offset:41472\n"
  "v_mfma_f32_32x32x16_bf16 v[176:191], a[116:119], a[108:111], v[176:191]\n"
  "ds_read_b64_tr_b16 a[86:87], v30 offset:41728\n"
  "s_waitcnt lgkmcnt(10)\n"
  "v_mfma_f32_32x32x16_bf16 v[192:207], a[120:123], a[96:99], v[192:207]\n"
  "ds_read_b64_tr_b16 a[88:89], v30 offset:41984\n"
  "v_mfma_f32_32x32x16_bf16 v[208:223], a[120:123], a[100:103], v[208:223]\n"
  "ds_read_b64_tr_b16 a[90:91], v30 offset:42240\n"
  "v_mfma_f32_32x32x16_bf16 v[224:239], a[120:123], a[104:107], v[224:239]\n"
  "ds_read_b64_tr_b16 a[92:93], v30 offset:42496\n"
  "v_mfma_f32_32x32x16_bf16 v[240:255], a[120:123], a[108:111], v[240:255]\n"
  "ds_read_b64_tr_b16 a[94:95], v30 offset:42752\n"
  "s_waitcnt lgkmcnt(12)\n"
  "v_mfma_f32_32x32x16_bf16 a[0:15], a[124:127], a[96:99], a[0:15]\n"
  "v_mfma_f32_32x32x16_bf16 a[16:31], a[124:127], a[100:103], a[16:31]\n"
  "v_mfma_f32_32x32x16_bf16 a[32:47], a[124:127], a[104:107], a[32:47]\n"
  "v_mfma_f32_32x32x16_bf16 a[48:63], a[124:127], a[108:111], a[48:63]\n"
  "s_waitcnt lgkmcnt(9)\n"
  "v_mfma_f32_32x32x16_bf16 v[64:79], a[80:83], a[64:67], v[64:79]\n"
  "ds_read_b64_tr_b16 a[112:113], v30 offset:45056\n"
  "s_waitcnt lgkmcnt(9)\n"
  "v_mfma_f32_32x32x16_bf16 v[80:95], a[80:83], a[68:71], v[80:95]\n"
  "ds_read_b64_tr_b16 a[114:115], v30 offset:45312\n"
  "s_waitcnt lgkmcnt(9)\n"
  "v_mfma_f32_32x32x16_bf16 v[96:111], a[80:83], a[72:75], v[96:111]\n"
  "ds_read_b128 a[96:99], v27 offset:0\n"
  "s_waitcnt lgkmcnt(9)\n"
  "v_mfma_f32_32x32x16_bf16 v[112:127], a[80:83], a[76:79], v[112:127]\n"
  "ds_read_b128 a[100:103], v27 offset:4096\n"
  "s_waitcnt lgkmcnt(8)\n"
  "v_mfma_f32_32x32x16_bf16 v[128:143], a[84:87], a[64:67], v[128:143]\n"
  "ds_read_b128 a[104:107], v27 offset:8192\n"
  "v_mfma_f32_32x32x16_bf16 v[144:159], a[84:87], a[68:71], v[144:159]\n"
  "ds_read_b128 a[108:111], v27 offset:12288\n"
  "v_mfma_f32_32x32x16_bf16 v[160:175], a[84:87], a[72:75], v[160:175]\n"
  "ds_read_b64_tr_b16 a[116:117], v30 offset:45568\n"
  "v_mfma_f32_32x32x16_bf16 v[176:191], a[84:87], a[76:79], v[176:191]\n"
  "ds_read_b64_tr_b16 a[118:119], v30 offset:45824\n"
  "s_waitcnt lgkmcnt(10)\n"
  "v_mfma_f32_32x32x16_bf16 v[192:207], a[88:91], a[64:67], v[192:207]\n"
  "ds_read_b64_tr_b16 a[120:121], v30 offset:46080\n"
  "v_mfma_f32_32x32x16_bf16 v[208:223], a[88:91], a[68:71], v[208:223]\n"
  "ds_read_b64_tr_b16 a[122:123], v30 offset:46336\n"
  "v_mfma_f32_32x32x16_bf16 v[224:239], a[88:91], a[72:75], v[224:239]\n"
  "ds_read_b64_tr_b16 a[124:125], v30 offset:46592\n"
  "v_mfma_f32_32x32x16_bf16 v[240:255], a[88:91], a[76:79], v[240:255]\n"
  "ds_read_b64_tr_b16 a[126:127], v30 offset:46848\n"
  "s_waitcnt lgkmcnt(12)\n"
  "v_mfma_f32_32x32x16_bf16 a[0:15], a[92:95], a[64:67], a[0:15]\n"
  "v_mfma_f32_32x32x16_bf16 a[16:31], a[92:95], a[68:71], a[16:31]\n"
  "v_mfma_f32_32x32x16_bf16 a[32:47], a[92:95], a[72:75], a[32:47]\n"
  "v_mfma_f32_32x32x16_bf16 a[48:63], a[92:95], a[76:79], a[48:63]\n"
  "s_waitcnt vmcnt(0)\n"
  "s_waitcnt lgkmcnt(0)\n"
  "s_barrier\n"
  "v_mfma_f32_32x32x16_bf16 v[64:79], a[112:115], a[96:99], v[64:79]\n"
  "ds_read_b64_tr_b16 a[80:81], v29 offset:0\n"
  "v_mfma_f32_32x32x16_bf16 v[80:95], a[112:115], a[100:103], v[80:95]\n"
  "ds_read_b64_tr_b16 a[82:83], v29 offset:256\n"
  "v_mfma_f32_32x32x16_bf16 v[96:111], a[112:115], a[104:107], v[96:111]\n"
  "ds_read_b128 a[64:67], v19 offset:0\n"
  "v_mfma_f32_32x32x16_bf16 v[112:127], a[112:115], a[108:111], v[112:127]\n"
  "ds_read_b128 a[68:71], v19 offset:4096\n"
  "v_mfma_f32_32x32x16_bf16 v[128:143], a[116:119], a[96:99], v[128:143]\n"
  "ds_read_b128 a[72:75], v19 offset:8192\n"
  "v_mfma_f32_32x32x16_bf16 v[144:159], a[116:119], a[100:103], v[144:159]\n"
  "ds_read_b128 a[76:79], v19 offset:12288\n"
  "v_mfma_f32_32x32x16_bf16 v[160:175], a[116:119], a[104:107], v[160:175]\n"
  "ds_read_b64_tr_b16 a[84:85], v29 offset:512\n"
  "v_mfma_f32_32x32x16_bf16 v[176:191], a[116:119], a[108:111], v[176:191]\n"
  "ds_read_b64_tr_b16 a[86:87], v29 offset:768\n"
  "v_mfma_f32_32x32x16_bf16 v[192:207], a[120:123], a[96:99], v[192:207]\n"
  "ds_read_b64_tr_b16 a[88:89], v29 offset:1024\n"
  "v_mfma_f32_32x32x16_bf16 v[208:223], a[120:123], a[100:103], v[208:223]\n"
  "ds_read_b64_tr_b16 a[90:91], v29 offset:1280\n"
  "v_mfma_f32_32x32x16_bf16 v[224:239], a[120:123], a[104:107], v[224:239]\n"
  "ds_read_b64_tr_b16 a[92:93], v29 offset:1536\n"
  "v_mfma_f32_32x32x16_bf16 v[240:255], a[120:123], a[108:111], v[240:255]\n"
  "ds_read_b64_tr_b16 a[94:95], v29 offset:1792\n"
  "v_mfma_f32_32x32x16_bf16 a[0:15], a[124:127], a[96:99], a[0:15]\n"
  "v_mfma_f32_32x32x16_bf16 a[16:31], a[124:127], a[100:103], a[16:31]\n"
  "v_mfma_f32_32x32x16_bf16 a[32:47], a[124:127], a[104:107], a[32:47]\n"
  "v_mfma_f32_32x32x16_bf16 a[48:63], a[124:127], a[108:111], a[48:63]\n"
  "s_waitcnt lgkmcnt(9)\n"
  "v_mfma_f32_32x32x16_bf16 v[64:79], a[80:83], a[64:67], v[64:79]\n"
  "ds_read_b64_tr_b16 a[112:113], v29 offset:4096\n"
  "s_waitcnt lgkmcnt(9)\n"
  "v_mfma_f32_32x32x16_bf16 v[80:95], a[80:83], a[68:71], v[80:95]\n"
  "ds_read_b64_tr_b16 a[114:115], v29 offset:4352\n"
  "s_waitcnt lgkmcnt(9)\n"
  "v_mfma_f32_32x32x16_bf16 v[96:111], a[80:83], a[72:75], v[96:111]\n"
  "ds_read_b128 a[96:99], v22 offset:0\n"
  "s_waitcnt lgkmcnt(9)\n"
  "v_mfma_f32_32x32x16_bf16 v[112:127], a[80:83], a[76:79], v[112:127]\n"
  "ds_read_b128 a[100:103], v22 offset:4096\n"
  "s_waitcnt lgkmcnt(8)\n"
  "v_mfma_f32_32x32x16_bf16 v[128:143], a[84:87], a[64:67], v[128:143]\n"
  "ds_read_b128 a[104:107], v22 offset:8192\n"
  "v_mfma_f32_32x32x16_bf16 v[144:159], a[84:87], a[68:71], v[144:159]\n"
  "ds_read_b128 a[108:111], v22 offset:12288\n"
  "v_mfma_f32_32x32x16_bf16 v[160:175], a[84:87], a[72:75], v[160:175]\n"
  "ds_read_b64_tr_b16 a[116:117], v29 offset:4608\n"
  "v_mfma_f32_32x32x16_bf16 v[176:191], a[84:87], a[76:79], v[176:191]\n"
  "ds_read_b64_tr_b16 a[118:119], v29 offset:4864\n"
  "s_waitcnt lgkmcnt(10)\n"
  "v_mfma_f32_32x32x16_bf16 v[192:207], a[88:91], a[64:67], v[192:207]\n"
  "ds_read_b64_tr_b16 a[120:121], v29 offset:5120\n"
  "v_mfma_f32_32x32x16_bf16 v[208:223], a[88:91], a[68:71], v[208:223]\n"
  "ds_read_b64_tr_b16 a[122:123], v29 offset:5376\n"
  "v_mfma_f32_32x32x16_bf16 v[224:239], a[88:91], a[72:75], v[224:239]\n"
  "ds_read_b64_tr_b16 a[124:125], v29 offset:5632\n"
  "v_mfma_f32_32x32x16_bf16 v[240:255], a[88:91], a[76:79], v[240:255]\n"
  "ds_read_b64_tr_b16 a[126:127], v29 offset:5888\n"
  "s_waitcnt lgkmcnt(12)\n"
  "v_mfma_f32_32x32x16_bf16 a[0:15], a[92:95], a[64:67], a[0:15]\n"
  "v_mfma_f32_32x32x16_bf16 a[16:31], a[92:95], a[68:71], a[16:31]\n"
  "v_mfma_f32_32x32x16_bf16 a[32:47], a[92:95], a[72:75], a[32:47]\n"
  "v_mfma_f32_32x32x16_bf16 a[48:63], a[92:95], a[76:79], a[48:63]\n"
  "s_waitcnt lgkmcnt(9)\n"
  "v_mfma_f32_32x32x16_bf16 v[64:79], a[112:115], a[96:99], v[64:79]\n"
  "ds_read_b64_tr_b16 a[80:81], v29 offset:8192\n"
  "s_waitcnt lgkmcnt(9)\n"
  "v_mfma_f32_32x32x16_bf16 v[80:95], a[112:115], a[100:103], v[80:95]\n"
  "ds_read_b64_tr_b16 a[82:83], v29 offset:8448\n"
  "s_waitcnt lgkmcnt(9)\n"
  "v_mfma_f32_32x32x16_bf16 v[96:111], a[112:115], a[104:107], v[96:111]\n"
  "ds_read_b128 a[64:67], v25 offset:0\n"
  "s_waitcnt lgkmcnt(9)\n"
  "v_mfma_f32_32x32x16_bf16 v[112:127], a[112:115], a[108:111], v[112:127]\n"
  "ds_read_b128 a[68:71], v25 offset:4096\n"
  "s_waitcnt lgkmcnt(8)\n"
  "v_mfma_f32_32x32x16_bf16 v[128:143], a[116:119], a[96:99], v[128:143]\n"
  "ds_read_b128 a[72:75], v25 offset:8192\n"
  "v_mfma_f32_32x32x16_bf16 v[144:159], a[116:119], a[100:103], v[144:159]\n"
  "ds_read_b128 a[76:79], v25 offset:12288\n"
  "v_mfma_f32_32x32x16_bf16 v[160:175], a[116:119], a[104:107], v[160:175]\n"
  "ds_read_b64_tr_b16 a[84:85], v29 offset:8704\n"
  "v_mfma_f32_32x32x16_bf16 v[176:191], a[116:119], a[108:111], v[176:191]\n"
  "ds_read_b64_tr_b16 a[86:87], v29 offset:8960\n"
  "s_waitcnt lgkmcnt(10)\n"
  "v_mfma_f32_32x32x16_bf16 v[192:207], a[120:123], a[96:99], v[192:207]\n"
  "ds_read_b64_tr_b16 a[88:89], v29 offset:9216\n"
  "v_mfma_f32_32x32x16_bf16 v[208:223], a[120:123], a[100:103], v[208:223]\n"
  "ds_read_b64_tr_b16 a[90:91], v29 offset:9472\n"
  "v_mfma_f32_32x32x16_bf16 v[224:239], a[120:123], a[104:107], v[224:239]\n"
  "ds_read_b64_tr_b16 a[92:93], v29 offset:9728\n"
  "v_mfma_f32_32x32x16_bf16 v[240:255], a[120:123], a[108:111], v[240:255]\n"
  "ds_read_b64_tr_b16 a[94:95], v29 offset:9984\n"
  "s_waitcnt lgkmcnt(12)\n"
  "v_mfma_f32_32x32x16_bf16 a[0:15], a[124:127], a[96:99], a[0:15]\n"
  "v_mfma_f32_32x32x16_bf16 a[16:31], a[124:127], a[100:103], a[16:31]\n"
  "v_mfma_f32_32x32x16_bf16 a[32:47], a[124:127], a[104:107], a[32:47]\n"
  "v_mfma_f32_32x32x16_bf16 a[48:63], a[124:127], a[108:111], a[48:63]\n"
  "s_waitcnt lgkmcnt(9)\n"
  "v_mfma_f32_32x32x16_bf16 v[64:79], a[80:83], a[64:67], v[64:79]\n"
  "ds_read_b64_tr_b16 a[112:113], v29 offset:12288\n"
  "s_waitcnt lgkmcnt(9)\n"
  "v_mfma_f32_32x32x16_bf16 v[80:95], a[80:83], a[68:71], v[80:95]\n"
  "ds_read_b64_tr_b16 a[114:115], v29 offset:12544\n"
  "s_waitcnt lgkmcnt(9)\n"
  "v_mfma_f32_32x32x16_bf16 v[96:111], a[80:83], a[72:75], v[96:111]\n"
  "ds_read_b128 a[96:99], v28 offset:0\n"
  "s_waitcnt lgkmcnt(9)\n"
  "v_mfma_f32_32x32x16_bf16 v[112:127], a[80:83], a[76:79], v[112:127]\n"
  "ds_read_b128 a[100:103], v28 offset:4096\n"
  "s_waitcnt lgkmcnt(8)\n"
  "v_mfma_f32_32x32x16_bf16 v[128:143], a[84:87], a[64:67], v[128:143]\n"
  "ds_read_b128 a[104:107], v28 offset:8192\n"
  "v_mfma_f32_32x32x16_bf16 v[144:159], a[84:87], a[68:71], v[144:159]\n"
  "ds_read_b128 a[108:111], v28 offset:12288\n"
  "v_mfma_f32_32x32x16_bf16 v[160:175], a[84:87], a[72:75], v[160:175]\n"
  "ds_read_b64_tr_b16 a[116:117], v29 offset:12800\n"
  "v_mfma_f32_32x32x16_bf16 v[176:191], a[84:87], a[76:79], v[176:191]\n"
  "ds_read_b64_tr_b16 a[118:119], v29 offset:13056\n"
  "s_waitcnt lgkmcnt(10)\n"
  "v_mfma_f32_32x32x16_bf16 v[192:207], a[88:91], a[64:67], v[192:207]\n"
  "ds_read_b64_tr_b16 a[120:121], v29 offset:13312\n"
  "v_mfma_f32_32x32x16_bf16 v[208:223], a[88:91], a[68:71], v[208:223]\n"
  "ds_read_b64_tr_b16 a[122:123], v29 offset:13568\n"
  "v_mfma_f32_32x32x16_bf16 v[224:239], a[88:91], a[72:75], v[224:239]\n"
  "ds_read_b64_tr_b16 a[124:125], v29 offset:13824\n"
  "v_mfma_f32_32x32x16_bf16 v[240:255], a[88:91], a[76:79], v[240:255]\n"
  "ds_read_b64_tr_b16 a[126:127], v29 offset:14080\n"
  "s_waitcnt lgkmcnt(12)\n"
  "v_mfma_f32_32x32x16_bf16 a[0:15], a[92:95], a[64:67], a[0:15]\n"
  "v_mfma_f32_32x32x16_bf16 a[16:31], a[92:95], a[68:71], a[16:31]\n"
  "v_mfma_f32_32x32x16_bf16 a[32:47], a[92:95], a[72:75], a[32:47]\n"
  "v_mfma_f32_32x32x16_bf16 a[48:63], a[92:95], a[76:79], a[48:63]\n"
  "s_waitcnt lgkmcnt(9)\n"
  "v_mfma_f32_32x32x16_bf16 v[64:79], a[112:115], a[96:99], v[64:79]\n"
  "s_waitcnt lgkmcnt(8)\n"
  "v_mfma_f32_32x32x16_bf16 v[80:95], a[112:115], a[100:103], v[80:95]\n"
  "s_waitcnt lgkmcnt(7)\n"
  "v_mfma_f32_32x32x16_bf16 v[96:111], a[112:115], a[104:107], v[96:111]\n"
  "s_waitcnt lgkmcnt(6)\n"
  "v_mfma_f32_32x32x16_bf16 v[112:127], a[112:115], a[108:111], v[112:127]\n"
  "s_waitcnt lgkmcnt(4)\n"
  "v_mfma_f32_32x32x16_bf16 v[128:143], a[116:119], a[96:99], v[128:143]\n"
  "v_mfma_f32_32x32x16_bf16 v[144:159], a[116:119], a[100:103], v[144:159]\n"
  "v_mfma_f32_32x32x16_bf16 v[160:175], a[116:119], a[104:107], v[160:175]\n"
  "v_mfma_f32_32x32x16_bf16 v[176:191], a[116:119], a[108:111], v[176:191]\n"
  "s_waitcnt lgkmcnt(2)\n"
  "v_mfma_f32_32x32x16_bf16 v[192:207], a[120:123], a[96:99], v[192:207]\n"
  "v_mfma_f32_32x32x16_bf16 v[208:223], a[120:123], a[100:103], v[208:223]\n"
  "v_mfma_f32_32x32x16_bf16 v[224:239], a[120:123], a[104:107], v[224:239]\n"
  "v_mfma_f32_32x32x16_bf16 v[240:255], a[120:123], a[108:111], v[240:255]\n"
  "s_waitcnt lgkmcnt(0)\n"
  "v_mfma_f32_32x32x16_bf16 a[0:15], a[124:127], a[96:99], a[0:15]\n"
  "v_mfma_f32_32x32x16_bf16 a[16:31], a[124:127], a[100:103], a[16:31]\n"
  "v_mfma_f32_32x32x16_bf16 a[32:47], a[124:127], a[104:107], a[32:47]\n"
  "v_mfma_f32_32x32x16_bf16 a[48:63], a[124:127], a[108:111], a[48:63]\n"
  "s_barrier\n"
  "s_branch Lp1end\n"
  "Lp1v:\n"
  "s_barrier\n"
  "ds_read_b128 a[64:67], v17 offset:0\n"
  "ds_read_b64_tr_b16 a[80:81], v29 offset:32768\n"
  "ds_read_b64_tr_b16 a[82:83], v29 offset:33024\n"
  "ds_read_b64_tr_b16 a[84:85], v29 offset:33280\n"
  "ds_read_b64_tr_b16 a[86:87], v29 offset:33536\n"
  "ds_read_b64_tr_b16 a[88:89], v29 offset:33792\n"
  "ds_read_b64_tr_b16 a[90:91], v29 offset:34048\n"
  "ds_read_b64_tr_b16 a[92:93], v29 offset:34304\n"
  "ds_read_b64_tr_b16 a[94:95], v29 offset:34560\n"
  "ds_read_b128 a[68:71], v17 offset:4096\n"
  "ds_read_b128 a[72:75], v17 offset:8192\n"
  "ds_read_b128 a[76:79], v17 offset:12288\n"
  "s_waitcnt lgkmcnt(9)\n"
  "v_mfma_f32_32x32x16_bf16 v[64:79], a[64:67], a[80:83], 0\n"
  "ds_read_b128 a[96:99], v20 offset:0\n"
  "s_add_u32 m0, s30, 131072\n"
  "s_waitcnt lgkmcnt(8)\n"
  "v_mfma_f32_32x32x16_bf16 v[80:95], a[64:67], a[84:87], 0\n"
  "ds_read_b64_tr_b16 a[112:113], v29 offset:36864\n"
  "buffer_load_dwordx4 v1, s[16:19], s28 offen lds\n"
  "s_waitcnt lgkmcnt(7)\n"
  "v_mfma_f32_32x32x16_bf16 v[96:111], a[64:67], a[88:91], 0\n"
  "ds_read_b64_tr_b16 a[114:115], v29 offset:37120\n"
  "s_add_u32 m0, s30, 132096\n"
  "s_waitcnt lgkmcnt(6)\n"
  "v_mfma_f32_32x32x16_bf16 v[112:127], a[64:67], a[92:95], 0\n"
  "ds_read_b64_tr_b16 a[116:117], v29 offset:37376\n"
  "buffer_load_dwordx4 v2, s[16:19], s28 offen lds\n"
  "s_waitcnt lgkmcnt(6)\n"
  "v_mfma_f32_32x32x16_bf16 v[128:143], a[68:71], a[80:83], 0\n"
  "ds_read_b64_tr_b16 a[118:119], v29 offset:37632\n"
  "s_add_u32 m0, s30, 133120\n"
  "v_mfma_f32_32x32x16_bf16 v[144:159], a[68:71], a[84:87], 0\n"
  "ds_read_b64_tr_b16 a[120:121], v29 offset:37888\n"
  "buffer_load_dwordx4 v3, s[16:19], s28 offen lds\n"
  "v_mfma_f32_32x32x16_bf16 v[160:175], a[68:71], a[88:91], 0\n"
  "ds_read_b64_tr_b16 a[122:123], v29 offset:38144\n"
  "s_add_u32 m0, s30, 134144\n"
  "v_mfma_f32_32x32x16_bf16 v[176:191], a[68:71], a[92:95], 0\n"
  "ds_read_b64_tr_b16 a[124:125], v29 offset:38400\n"
  "buffer_load_dwordx4 v4, s[16:19], s28 offen lds\n"
  "s_waitcnt lgkmcnt(9)\n"
  "v_mfma_f32_32x32x16_bf16 v[192:207], a[72:75], a[80:83], 0\n"
  "ds_read_b64_tr_b16 a[126:127], v29 offset:38656\n"
  "s_add_u32 m0, s30, 135168\n"
  "v_mfma_f32_32x32x16_bf16 v[208:223], a[72:75], a[84:87], 0\n"
  "ds_read_b128 a[100:103], v20 offset:4096\n"
  "buffer_load_dwordx4 v5, s[16:19], s28 offen lds\n"
  "v_mfma_f32_32x32x16_bf16 v[224:239], a[72:75], a[88:91], 0\n"
  "ds_read_b128 a[104:107], v20 offset:8192\n"
  "s_add_u32 m0, s30, 136192\n"
  "v_mfma_f32_32x32x16_bf16 v[240:255], a[72:75], a[92:95], 0\n"
  "ds_read_b128 a[108:111], v20 offset:12288\n"
  "buffer_load_dwordx4 v6, s[16:19], s28 offen lds\n"
  "s_waitcnt lgkmcnt(8)\n"
  "v_mfma_f32_32x32x16_bf16 a[0:15], a[76:79], a[80:83], 0\n"
  "s_add_u32 m0, s30, 137216\n"
  "v_mfma_f32_32x32x16_bf16 a[16:31], a[76:79], a[84:87], 0\n"
  "buffer_load_dwordx4 v7, s[16:19], s28 offen lds\n"
  "v_mfma_f32_32x32x16_bf16 a[32:47], a[76:79], a[88:91], 0\n"
  "s_add_u32 m0, s30, 138240\n"
  "v_mfma_f32_32x32x16_bf16 a[48:63], a[76:79], a[92:95], 0\n"
  "buffer_load_dwordx4 v8, s[16:19], s28 offen lds\n"
  "s_add_u32 s28, s28, 128\n"
  "v_mfma_f32_32x32x16_bf16 v[64:79], a[96:99], a[112:115], v[64:79]\n"
  "ds_read_b128 a[64:67], v23 offset:0\n"
  "s_waitcnt lgkmcnt(6)\n"
  "v_mfma_f32_32x32x16_bf16 v[80:95], a[96:99], a[116:119], v[80:95]\n"
  "ds_read_b64_tr_b16 a[80:81], v29 offset:40960\n"
  "v_mfma_f32_32x32x16_bf16 v[96:111], a[96:99], a[120:123], v[96:111]\n"
  "ds_read_b64_tr_b16 a[82:83], v29 offset:41216\n"
  "s_waitcnt lgkmcnt(6)\n"
  "v_mfma_f32_32x32x16_bf16 v[112:127], a[96:99], a[124:127], v[112:127]\n"
  "ds_read_b64_tr_b16 a[84:85], v29 offset:41472\n"
  "s_waitcnt lgkmcnt(6)\n"
  "v_mfma_f32_32x32x16_bf16 v[128:143], a[100:103], a[112:115], v[128:143]\n"
  "ds_read_b64_tr_b16 a[86:87], v29 offset:41728\n"
  "v_mfma_f32_32x32x16_bf16 v[144:159], a[100:103], a[116:119], v[144:159]\n"
  "ds_read_b64_tr_b16 a[88:89], v29 offset:41984\n"
  "v_mfma_f32_32x32x16_bf16 v[160:175], a[100:103], a[120:123], v[160:175]\n"
  "ds_read_b64_tr_b16 a[90:91], v29 offset:42240\n"
  "v_mfma_f32_32x32x16_bf16 v[176:191], a[100:103], a[124:127], v[176:191]\n"
  "ds_read_b64_tr_b16 a[92:93], v29 offset:42496\n"
  "s_waitcnt lgkmcnt(8)\n"
  "v_mfma_f32_32x32x16_bf16 v[192:207], a[104:107], a[112:115], v[192:207]\n"
  "ds_read_b64_tr_b16 a[94:95], v29 offset:42752\n"
  "v_mfma_f32_32x32x16_bf16 v[208:223], a[104:107], a[116:119], v[208:223]\n"
  "ds_read_b128 a[68:71], v23 offset:4096\n"
  "v_mfma_f32_32x32x16_bf16 v[224:239], a[104:107], a[120:123], v[224:239]\n"
  "ds_read_b128 a[72:75], v23 offset:8192\n"
  "v_mfma_f32_32x32x16_bf16 v[240:255], a[104:107], a[124:127], v[240:255]\n"
  "ds_read_b128 a[76:79], v23 offset:12288\n"
  "v_mfma_f32_32x32x16_bf16 a[0:15], a[108:111], a[112:115], a[0:15]\n"
  "v_mfma_f32_32x32x16_bf16 a[16:31], a[108:111], a[116:119], a[16:31]\n"
  "v_mfma_f32_32x32x16_bf16 a[32:47], a[108:111], a[120:123], a[32:47]\n"
  "v_mfma_f32_32x32x16_bf16 a[48:63], a[108:111], a[124:127], a[48:63]\n"
  "s_waitcnt lgkmcnt(9)\n"
  "v_mfma_f32_32x32x16_bf16 v[64:79], a[64:67], a[80:83], v[64:79]\n"
  "ds_read_b128 a[96:99], v26 offset:0\n"
  "s_waitcnt lgkmcnt(8)\n"
  "v_mfma_f32_32x32x16_bf16 v[80:95], a[64:67], a[84:87], v[80:95]\n"
  "ds_read_b64_tr_b16 a[112:113], v29 offset:45056\n"
  "s_waitcnt lgkmcnt(7)\n"
  "v_mfma_f32_32x32x16_bf16 v[96:111], a[64:67], a[88:91], v[96:111]\n"
  "ds_read_b64_tr_b16 a[114:115], v29 offset:45312\n"
  "s_waitcnt lgkmcnt(6)\n"
  "v_mfma_f32_32x32x16_bf16 v[112:127], a[64:67], a[92:95], v[112:127]\n"
  "ds_read_b64_tr_b16 a[116:117], v29 offset:45568\n"
  "s_waitcnt lgkmcnt(6)\n"
  "v_mfma_f32_32x32x16_bf16 v[128:143], a[68:71], a[80:83], v[128:143]\n"
  "ds_read_b64_tr_b16 a[118:119], v29 offset:45824\n"
  "v_mfma_f32_32x32x16_bf16 v[144:159], a[68:71], a[84:87], v[144:159]\n"
  "ds_read_b64_tr_b16 a[120:121], v29 offset:46080\n"
  "v_mfma_f32_32x32x16_bf16 v[160:175], a[68:71], a[88:91], v[160:175]\n"
  "ds_read_b64_tr_b16 a[122:123], v29 offset:46336\n"
  "v_mfma_f32_32x32x16_bf16 v[176:191], a[68:71], a[92:95], v[176:191]\n"
  "ds_read_b64_tr_b16 a[124:125], v29 offset:46592\n"
  "s_waitcnt lgkmcnt(9)\n"
  "v_mfma_f32_32x32x16_bf16 v[192:207], a[72:75], a[80:83], v[192:207]\n"
  "ds_read_b64_tr_b16 a[126:127], v29 offset:46848\n"
  "v_mfma_f32_32x32x16_bf16 v[208:223], a[72:75], a[84:87], v[208:223]\n"
  "ds_read_b128 a[100:103], v26 offset:4096\n"
  "v_mfma_f32_32x32x16_bf16 v[224:239], a[72:75], a[88:91], v[224:239]\n"
  "ds_read_b128 a[104:107], v26 offset:8192\n"
  "v_mfma_f32_32x32x16_bf16 v[240:255], a[72:75], a[92:95], v[240:255]\n"
  "ds_read_b128 a[108:111], v26 offset:12288\n"
  "s_waitcnt lgkmcnt(12)\n"
  "v_mfma_f32_32x32x16_bf16 a[0:15], a[76:79], a[80:83], a[0:15]\n"
  "v_mfma_f32_32x32x16_bf16 a[16:31], a[76:79], a[84:87], a[16:31]\n"
  "v_mfma_f32_32x32x16_bf16 a[32:47], a[76:79], a[88:91], a[32:47]\n"
  "v_mfma_f32_32x32x16_bf16 a[48:63], a[76:79], a[92:95], a[48:63]\n"
  "s_waitcnt lgkmcnt(0)\n"
  "s_barrier\n"
  "v_mfma_f32_32x32x16_bf16 v[64:79], a[96:99], a[112:115], v[64:79]\n"
  "ds_read_b128 a[64:67], v18 offset:0\n"
  "s_add_u32 m0, s30, 0\n"
  "v_mfma_f32_32x32x16_bf16 v[80:95], a[96:99], a[116:119], v[80:95]\n"
  "ds_read_b64_tr_b16 a[80:81], v30 offset:32768\n"
  "buffer_load_dwordx4 v9, s[20:23], s29 offen lds\n"
  "v_mfma_f32_32x32x16_bf16 v[96:111], a[96:99], a[120:123], v[96:111]\n"
  "ds_read_b64_tr_b16 a[82:83], v30 offset:33024\n"
  "s_add_u32 m0, s30, 1024\n"
  "v_mfma_f32_32x32x16_bf16 v[112:127], a[96:99], a[124:127], v[112:127]\n"
  "ds_read_b64_tr_b16 a[84:85], v30 offset:33280\n"
  "buffer_load_dwordx4 v10, s[20:23], s29 offen lds\n"
  "v_mfma_f32_32x32x16_bf16 v[128:143], a[100:103], a[112:115], v[128:143]\n"
  "ds_read_b64_tr_b16 a[86:87], v30 offset:33536\n"
  "s_add_u32 m0, s30, 2048\n"
  "v_mfma_f32_32x32x16_bf16 v[144:159], a[100:103], a[116:119], v[144:159]\n"
  "ds_read_b64_tr_b16 a[88:89], v30 offset:33792\n"
  "buffer_load_dwordx4 v11, s[20:23], s29 offen lds\n"
  "v_mfma_f32_32x32x16_bf16 v[160:175], a[100:103], a[120:123], v[160:175]\n"
  "ds_read_b64_tr_b16 a[90:91], v30 offset:34048\n"
  "s_add_u32 m0, s30, 3072\n"
  "v_mfma_f32_32x32x16_bf16 v[176:191], a[100:103], a[124:127], v[176:191]\n"
  "ds_read_b64_tr_b16 a[92:93], v30 offset:34304\n"
  "buffer_load_dwordx4 v12, s[20:23], s29 offen lds\n"
  "v_mfma_f32_32x32x16_bf16 v[192:207], a[104:107], a[112:115], v[192:207]\n"
  "ds_read_b64_tr_b16 a[94:95], v30 offset:34560\n"
  "s_add_u32 m0, s30, 4096\n"
  "v_mfma_f32_32x32x16_bf16 v[208:223], a[104:107], a[116:119], v[208:223]\n"
  "ds_read_b128 a[68:71], v18 offset:4096\n"
  "buffer_load_dwordx4 v13, s[20:23], s29 offen lds\n"
  "v_mfma_f32_32x32x16_bf16 v[224:239], a[104:107], a[120:123], v[224:239]\n"
  "ds_read_b128 a[72:75], v18 offset:8192\n"
  "s_add_u32 m0, s30, 5120\n"
  "v_mfma_f32_32x32x16_bf16 v[240:255], a[104:107], a[124:127], v[240:255]\n"
  "ds_read_b128 a[76:79], v18 offset:12288\n"
  "buffer_load_dwordx4 v14, s[20:23], s29 offen lds\n"
  "v_mfma_f32_32x32x16_bf16 a[0:15], a[108:111], a[112:115], a[0:15]\n"
  "s_add_u32 m0, s30, 6144\n"
  "v_mfma_f32_32x32x16_bf16 a[16:31], a[108:111], a[116:119], a[16:31]\n"
  "buffer_load_dwordx4 v15, s[20:23], s29 offen lds\n"
  "v_mfma_f32_32x32x16_bf16 a[32:47], a[108:111], a[120:123], a[32:47]\n"
  "s_add_u32 m0, s30, 7168\n"
  "v_mfma_f32_32x32x16_bf16 a[48:63], a[108:111], a[124:127], a[48:63]\n"
  "buffer_load_dwordx4 v16, s[20:23], s29 offen lds\n"
  "s_add_u32 s29, s29, 524288\n"
  "s_waitcnt lgkmcnt(5)\n"
  "v_mfma_f32_32x32x16_bf16 v[64:79], a[64:67], a[80:83], v[64:79]\n"
  "ds_read_b128 a[96:99], v21 offset:0\n"
  "v_mfma_f32_32x32x16_bf16 v[80:95], a[64:67], a[84:87], v[80:95]\n"
  "ds_read_b64_tr_b16 a[112:113], v30 offset:36864\n"
  "v_mfma_f32_32x32x16_bf16 v[96:111], a[64:67], a[88:91], v[96:111]\n"
  "ds_read_b64_tr_b16 a[114:115], v30 offset:37120\n"
  "s_add_u32 m0, s30, 32768\n"
  "s_waitcnt lgkmcnt(6)\n"
  "v_mfma_f32_32x32x16_bf16 v[112:127], a[64:67], a[92:95], v[112:127]\n"
  "ds_read_b64_tr_b16 a[116:117], v30 offset:37376\n"
  "buffer_load_dwordx4 v1, s[16:19], s28 offen lds\n"
  "s_waitcnt lgkmcnt(6)\n"
  "v_mfma_f32_32x32x16_bf16 v[128:143], a[68:71], a[80:83], v[128:143]\n"
  "ds_read_b64_tr_b16 a[118:119], v30 offset:37632\n"
  "v_mfma_f32_32x32x16_bf16 v[144:159], a[68:71], a[84:87], v[144:159]\n"
  "ds_read_b64_tr_b16 a[120:121], v30 offset:37888\n"
  "v_mfma_f32_32x32x16_bf16 v[160:175], a[68:71], a[88:91], v[160:175]\n"
  "ds_read_b64_tr_b16 a[122:123], v30 offset:38144\n"
  "s_add_u32 m0, s30, 33792\n"
  "v_mfma_f32_32x32x16_bf16 v[176:191], a[68:71], a[92:95], v[176:191]\n"
  "ds_read_b64_tr_b16 a[124:125], v30 offset:38400\n"
  "buffer_load_dwordx4 v2, s[16:19], s28 offen lds\n"
  "s_waitcnt lgkmcnt(8)\n"
  "v_mfma_f32_32x32x16_bf16 v[192:207], a[72:75], a[80:83], v[192:207]\n"
  "ds_read_b64_tr_b16 a[126:127], v30 offset:38656\n"
  "v_mfma_f32_32x32x16_bf16 v[208:223], a[72:75], a[84:87], v[208:223]\n"
  "ds_read_b128 a[100:103], v21 offset:4096\n"
  "v_mfma_f32_32x32x16_bf16 v[224:239], a[72:75], a[88:91], v[224:239]\n"
  "ds_read_b128 a[104:107], v21 offset:8192\n"
  "s_add_u32 m0, s30, 34816\n"
  "v_mfma_f32_32x32x16_bf16 v[240:255], a[72:75], a[92:95], v[240:255]\n"
  "ds_read_b128 a[108:111], v21 offset:12288\n"
  "buffer_load_dwordx4 v3, s[16:19], s28 offen lds\n"
  "v_mfma_f32_32x32x16_bf16 a[0:15], a[76:79], a[80:83], a[0:15]\n"
  "v_mfma_f32_32x32x16_bf16 a[16:31], a[76:79], a[84:87], a[16:31]\n"
  "v_mfma_f32_32x32x16_bf16 a[32:47], a[76:79], a[88:91], a[32:47]\n"
  "s_add_u32 m0, s30, 35840\n"
  "v_mfma_f32_32x32x16_bf16 a[48:63], a[76:79], a[92:95], a[48:63]\n"
  "buffer_load_dwordx4 v4, s[16:19], s28 offen lds\n"
  "s_waitcnt lgkmcnt(8)\n"
  "v_mfma_f32_32x32x16_bf16 v[64:79], a[96:99], a[112:115], v[64:79]\n"
  "ds_read_b128 a[64:67], v24 offset:0\n"
  "s_waitcnt lgkmcnt(8)\n"
  "v_mfma_f32_32x32x16_bf16 v[80:95], a[96:99], a[116:119], v[80:95]\n"
  "ds_read_b64_tr_b16 a[80:81], v30 offset:40960\n"
  "s_waitcnt lgkmcnt(7)\n"
  "v_mfma_f32_32x32x16_bf16 v[96:111], a[96:99], a[120:123], v[96:111]\n"
  "ds_read_b64_tr_b16 a[82:83], v30 offset:41216\n"
  "s_add_u32 m0, s30, 36864\n"
  "s_waitcnt lgkmcnt(6)\n"
  "v_mfma_f32_32x32x16_bf16 v[112:127], a[96:99], a[124:127], v[112:127]\n"
  "ds_read_b64_tr_b16 a[84:85], v30 offset:41472\n"
  "buffer_load_dwordx4 v5, s[16:19], s28 offen lds\n"
  "s_waitcnt lgkmcnt(6)\n"
  "v_mfma_f32_32x32x16_bf16 v[128:143], a[100:103], a[112:115], v[128:143]\n"
  "ds_read_b64_tr_b16 a[86:87], v30 offset:41728\n"
  "v_mfma_f32_32x32x16_bf16 v[144:159], a[100:103], a[116:119], v[144:159]\n"
  "ds_read_b64_tr_b16 a[88:89], v30 offset:41984\n"
  "v_mfma_f32_32x32x16_bf16 v[160:175], a[100:103], a[120:123], v[160:175]\n"
  "ds_read_b64_tr_b16 a[90:91], v30 offset:42240\n"
  "s_add_u32 m0, s30, 37888\n"
  "v_mfma_f32_32x32x16_bf16 v[176:191], a[100:103], a[124:127], v[176:191]\n"
  "ds_read_b64_tr_b16 a[92:93], v30 offset:42496\n"
  "buffer_load_dwordx4 v6, s[16:19], s28 offen lds\n"
  "s_waitcnt lgkmcnt(8)\n"
  "v_mfma_f32_32x32x16_bf16 v[192:207], a[104:107], a[112:115], v[192:207]\n"
  "ds_read_b64_tr_b16 a[94:95], v30 offset:42752\n"
  "v_mfma_f32_32x32x16_bf16 v[208:223], a[104:107], a[116:119], v[208:223]\n"
  "ds_read_b128 a[68:71], v24 offset:4096\n"
  "v_mfma_f32_32x32x16_bf16 v[224:239], a[104:107], a[120:123], v[224:239]\n"
  "ds_read_b128 a[72:75], v24 offset:8192\n"
  "s_add_u32 m0, s30, 38912\n"
  "v_mfma_f32_32x32x16_bf16 v[240:255], a[104:107], a[124:127], v[240:255]\n"
  "ds_read_b128 a[76:79], v24 offset:12288\n"
  "buffer_load_dwordx4 v7, s[16:19], s28 offen lds\n"
  "v_mfma_f32_32x32x16_bf16 a[0:15], a[108:111], a[112:115], a[0:15]\n"
  "v_mfma_f32_32x32x16_bf16 a[16:31], a[108:111], a[116:119], a[16:31]\n"
  "v_mfma_f32_32x32x16_bf16 a[32:47], a[108:111], a[120:123], a[32:47]\n"
  "s_add_u32 m0, s30, 39936\n"
  "v_mfma_f32_32x32x16_bf16 a[48:63], a[108:111], a[124:127], a[48:63]\n"
  "buffer_load_dwordx4 v8, s[16:19], s28 offen lds\n"
  "s_add_u32 s28, s28, 128\n"
  "s_waitcnt lgkmcnt(7)\n"
  "v_mfma_f32_32x32x16_bf16 v[64:79], a[64:67], a[80:83], v[64:79]\n"
  "ds_read_b128 a[96:99], v27 offset:0\n"
  "v_mfma_f32_32x32x16_bf16 v[80:95], a[64:67], a[84:87], v[80:95]\n"
  "ds_read_b64_tr_b16 a[112:113], v30 offset:45056\n"
  "s_waitcnt lgkmcnt(7)\n"
  "v_mfma_f32_32x32x16_bf16 v[96:111], a[64:67], a[88:91], v[96:111]\n"
  "ds_read_b64_tr_b16 a[114:115], v30 offset:45312\n"
  "s_waitcnt lgkmcnt(6)\n"
  "v_mfma_f32_32x32x16_bf16 v[112:127], a[64:67], a[92:95], v[112:127]\n"
  "ds_read_b64_tr_b16 a[116:117], v30 offset:45568\n"
  "s_waitcnt lgkmcnt(6)\n"
  "v_mfma_f32_32x32x16_bf16 v[128:143], a[68:71], a[80:83], v[128:143]\n"
  "ds_read_b64_tr_b16 a[118:119], v30 offset:45824\n"
  "v_mfma_f32_32x32x16_bf16 v[144:159], a[68:71], a[84:87], v[144:159]\n"
  "ds_read_b64_tr_b16 a[120:121], v30 offset:46080\n"
  "v_mfma_f32_32x32x16_bf16 v[160:175], a[68:71], a[88:91], v[160:175]\n"
  "ds_read_b64_tr_b16 a[122:123], v30 offset:46336\n"
  "v_mfma_f32_32x32x16_bf16 v[176:191], a[68:71], a[92:95], v[176:191]\n"
  "ds_read_b64_tr_b16 a[124:125], v30 offset:46592\n"
  "s_waitcnt lgkmcnt(9)\n"
  "v_mfma_f32_32x32x16_bf16 v[192:207], a[72:75], a[80:83], v[192:207]\n"
  "ds_read_b64_tr_b16 a[126:127], v30 offset:46848\n"
  "v_mfma_f32_32x32x16_bf16 v[208:223], a[72:75], a[84:87], v[208:223]\n"
  "ds_read_b128 a[100:103], v27 offset:4096\n"
  "v_mfma_f32_32x32x16_bf16 v[224:239], a[72:75], a[88:91], v[224:239]\n"
  "ds_read_b128 a[104:107], v27 offset:8192\n"
  "v_mfma_f32_32x32x16_bf16 v[240:255], a[72:75], a[92:95], v[240:255]\n"
  "ds_read_b128 a[108:111], v27 offset:12288\n"
  "s_waitcnt lgkmcnt(12)\n"
  "v_mfma_f32_32x32x16_bf16 a[0:15], a[76:79], a[80:83], a[0:15]\n"
  "v_mfma_f32_32x32x16_bf16 a[16:31], a[76:79], a[84:87], a[16:31]\n"
  "v_mfma_f32_32x32x16_bf16 a[32:47], a[76:79], a[88:91], a[32:47]\n"
  "v_mfma_f32_32x32x16_bf16 a[48:63], a[76:79], a[92:95], a[48:63]\n"
  "s_waitcnt vmcnt(8)\n"
  "s_waitcnt lgkmcnt(0)\n"
  "s_barrier\n"
  "s_and_b32 s32, s10, 7\n"
  "s_lshl_b32 s32, s32, 5\n"
  "s_lshr_b32 s33, s10, 3\n"
  "s_add_u32 s32, s32, s33\n"
  "s_lshl_b32 s32, s32, 2\n"
  "s_add_u32 s32, s32, 4096\n"
  "v_mov_b32 v63, s71\n"
  "s_mov_b64 exec, 1\n"
  "buffer_store_dword v63, off, s[92:95], s32 sc1\n"
  "s_mov_b64 exec, -1\n"
  "v_mfma_f32_32x32x16_bf16 v[64:79], a[96:99], a[112:115], v[64:79]\n"
  "ds_read_b128 a[64:67], v19 offset:0\n"
  "s_add_u32 m0, s30, 65536\n"
  "v_mfma_f32_32x32x16_bf16 v[80:95], a[96:99], a[116:119], v[80:95]\n"
  "ds_read_b64_tr_b16 a[80:81], v29 offset:0\n"
  "buffer_load_dwordx4 v9, s[20:23], s29 offen lds\n"
  "v_mfma_f32_32x32x16_bf16 v[96:111], a[96:99], a[120:123], v[96:111]\n"
  "ds_read_b64_tr_b16 a[82:83], v29 offset:256\n"
  "s_add_u32 m0, s30, 66560\n"
  "v_mfma_f32_32x32x16_bf16 v[112:127], a[96:99], a[124:127], v[112:127]\n"
  "ds_read_b64_tr_b16 a[84:85], v29 offset:512\n"
  "buffer_load_dwordx4 v10, s[20:23], s29 offen lds\n"
  "v_mfma_f32_32x32x16_bf16 v[128:143], a[100:103], a[112:115], v[128:143]\n"
  "ds_read_b64_tr_b16 a[86:87], v29 offset:768\n"
  "s_add_u32 m0, s30, 67584\n"
  "v_mfma_f32_32x32x16_bf16 v[144:159], a[100:103], a[116:119], v[144:159]\n"
  "ds_read_b64_tr_b16 a[88:89], v29 offset:1024\n"
  "buffer_load_dwordx4 v11, s[20:23], s29 offen lds\n"
  "v_mfma_f32_32x32x16_bf16 v[160:175], a[100:103], a[120:123], v[160:175]\n"
  "ds_read_b64_tr_b16 a[90:91], v29 offset:1280\n"
  "s_add_u32 m0, s30, 68608\n"
  "v_mfma_f32_32x32x16_bf16 v[176:191], a[100:103], a[124:127], v[176:191]\n"
  "ds_read_b64_tr_b16 a[92:93], v29 offset:1536\n"
  "buffer_load_dwordx4 v12, s[20:23], s29 offen lds\n"
  "v_mfma_f32_32x32x16_bf16 v[192:207], a[104:107], a[112:115], v[192:207]\n"
  "ds_read_b64_tr_b16 a[94:95], v29 offset:1792\n"
  "s_add_u32 m0, s30, 69632\n"
  "v_mfma_f32_32x32x16_bf16 v[208:223], a[104:107], a[116:119], v[208:223]\n"
  "ds_read_b128 a[68:71], v19 offset:4096\n"
  "buffer_load_dwordx4 v13, s[20:23], s29 offen lds\n"
  "v_mfma_f32_32x32x16_bf16 v[224:239], a[104:107], a[120:123], v[224:239]\n"
  "ds_read_b128 a[72:75], v19 offset:8192\n"
  "s_add_u32 m0, s30, 70656\n"
  "v_mfma_f32_32x32x16_bf16 v[240:255], a[104:107], a[124:127], v[240:255]\n"
  "ds_read_b128 a[76:79], v19 offset:12288\n"
  "buffer_load_dwordx4 v14, s[20:23], s29 offen lds\n"
  "v_mfma_f32_32x32x16_bf16 a[0:15], a[108:111], a[112:115], a[0:15]\n"
  "s_add_u32 m0, s30, 71680\n"
  "v_mfma_f32_32x32x16_bf16 a[16:31], a[108:111], a[116:119], a[16:31]\n"
  "buffer_load_dwordx4 v15, s[20:23], s29 offen lds\n"
  "v_mfma_f32_32x32x16_bf16 a[32:47], a[108:111], a[120:123], a[32:47]\n"
  "s_add_u32 m0, s30, 72704\n"
  "v_mfma_f32_32x32x16_bf16 a[48:63], a[108:111], a[124:127], a[48:63]\n"
  "buffer_load_dwordx4 v16, s[20:23], s29 offen lds\n"
  "s_add_u32 s29, s29, 524288\n"
  "s_waitcnt lgkmcnt(5)\n"
  "v_mfma_f32_32x32x16_bf16 v[64:79], a[64:67], a[80:83], v[64:79]\n"
  "ds_read_b128 a[96:99], v22 offset:0\n"
  "v_mfma_f32_32x32x16_bf16 v[80:95], a[64:67], a[84:87], v[80:95]\n"
  "ds_read_b64_tr_b16 a[112:113], v29 offset:4096\n"
  "v_mfma_f32_32x32x16_bf16 v[96:111], a[64:67], a[88:91], v[96:111]\n"
  "ds_read_b64_tr_b16 a[114:115], v29 offset:4352\n"
  "s_add_u32 m0, s30, 98304\n"
  "s_waitcnt lgkmcnt(6)\n"
  "v_mfma_f32_32x32x16_bf16 v[112:127], a[64:67], a[92:95], v[112:127]\n"
  "ds_read_b64_tr_b16 a[116:117], v29 offset:4608\n"
  "buffer_load_dwordx4 v1, s[16:19], s28 offen lds\n"
  "s_waitcnt lgkmcnt(6)\n"
  "v_mfma_f32_32x32x16_bf16 v[128:143], a[68:71], a[80:83], v[128:143]\n"
  "ds_read_b64_tr_b16 a[118:119], v29 offset:4864\n"
  "v_mfma_f32_32x32x16_bf16 v[144:159], a[68:71], a[84:87], v[144:159]\n"
  "ds_read_b64_tr_b16 a[120:121], v29 offset:5120\n"
  "v_mfma_f32_32x32x16_bf16 v[160:175], a[68:71], a[88:91], v[160:175]\n"
  "ds_read_b64_tr_b16 a[122:123], v29 offset:5376\n"
  "s_add_u32 m0, s30, 99328\n"
  "v_mfma_f32_32x32x16_bf16 v[176:191], a[68:71], a[92:95], v[176:191]\n"
  "ds_read_b64_tr_b16 a[124:125], v29 offset:5632\n"
  "buffer_load_dwordx4 v2, s[16:19], s28 offen lds\n"
  "s_waitcnt lgkmcnt(8)\n"
  "v_mfma_f32_32x32x16_bf16 v[192:207], a[72:75], a[80:83], v[192:207]\n"
  "ds_read_b64_tr_b16 a[126:127], v29 offset:5888\n"
  "v_mfma_f32_32x32x16_bf16 v[208:223], a[72:75], a[84:87], v[208:223]\n"
  "ds_read_b128 a[100:103], v22 offset:4096\n"
  "v_mfma_f32_32x32x16_bf16 v[224:239], a[72:75], a[88:91], v[224:239]\n"
  "ds_read_b128 a[104:107], v22 offset:8192\n"
  "s_add_u32 m0, s30, 100352\n"
  "v_mfma_f32_32x32x16_bf16 v[240:255], a[72:75], a[92:95], v[240:255]\n"
  "ds_read_b128 a[108:111], v22 offset:12288\n"
  "buffer_load_dwordx4 v3, s[16:19], s28 offen lds\n"
  "v_mfma_f32_32x32x16_bf16 a[0:15], a[76:79], a[80:83], a[0:15]\n"
  "v_mfma_f32_32x32x16_bf16 a[16:31], a[76:79], a[84:87], a[16:31]\n"
  "v_mfma_f32_32x32x16_bf16 a[32:47], a[76:79], a[88:91], a[32:47]\n"
  "s_add_u32 m0, s30, 101376\n"
  "v_mfma_f32_32x32x16_bf16 a[48:63], a[76:79], a[92:95], a[48:63]\n"
  "buffer_load_dwordx4 v4, s[16:19], s28 offen lds\n"
  "s_waitcnt lgkmcnt(8)\n"
  "v_mfma_f32_32x32x16_bf16 v[64:79], a[96:99], a[112:115], v[64:79]\n"
  "ds_read_b128 a[64:67], v25 offset:0\n"
  "s_waitcnt lgkmcnt(8)\n"
  "v_mfma_f32_32x32x16_bf16 v[80:95], a[96:99], a[116:119], v[80:95]\n"
  "ds_read_b64_tr_b16 a[80:81], v29 offset:8192\n"
  "s_waitcnt lgkmcnt(7)\n"
  "v_mfma_f32_32x32x16_bf16 v[96:111], a[96:99], a[120:123], v[96:111]\n"
  "ds_read_b64_tr_b16 a[82:83], v29 offset:8448\n"
  "s_add_u32 m0, s30, 102400\n"
  "s_waitcnt lgkmcnt(6)\n"
  "v_mfma_f32_32x32x16_bf16 v[112:127], a[96:99], a[124:127], v[112:127]\n"
  "ds_read_b64_tr_b16 a[84:85], v29 offset:8704\n"
  "buffer_load_dwordx4 v5, s[16:19], s28 offen lds\n"
  "s_waitcnt lgkmcnt(6)\n"
  "v_mfma_f32_32x32x16_bf16 v[128:143], a[100:103], a[112:115], v[128:143]\n"
  "ds_read_b64_tr_b16 a[86:87], v29 offset:8960\n"
  "v_mfma_f32_32x32x16_bf16 v[144:159], a[100:103], a[116:119], v[144:159]\n"
  "ds_read_b64_tr_b16 a[88:89], v29 offset:9216\n"
  "v_mfma_f32_32x32x16_bf16 v[160:175], a[100:103], a[120:123], v[160:175]\n"
  "ds_read_b64_tr_b16 a[90:91], v29 offset:9472\n"
  "s_add_u32 m0, s30, 103424\n"
  "v_mfma_f32_32x32x16_bf16 v[176:191], a[100:103], a[124:127], v[176:191]\n"
  "ds_read_b64_tr_b16 a[92:93], v29 offset:9728\n"
  "buffer_load_dwordx4 v6, s[16:19], s28 offen lds\n"
  "s_waitcnt lgkmcnt(8)\n"
  "v_mfma_f32_32x32x16_bf16 v[192:207], a[104:107], a[112:115], v[192:207]\n"
  "ds_read_b64_tr_b16 a[94:95], v29 offset:9984\n"
  "v_mfma_f32_32x32x16_bf16 v[208:223], a[104:107], a[116:119], v[208:223]\n"
  "ds_read_b128 a[68:71], v25 offset:4096\n"
  "v_mfma_f32_32x32x16_bf16 v[224:239], a[104:107], a[120:123], v[224:239]\n"
  "ds_read_b128 a[72:75], v25 offset:8192\n"
  "s_add_u32 m0, s30, 104448\n"
  "v_mfma_f32_32x32x16_bf16 v[240:255], a[104:107], a[124:127], v[240:255]\n"
  "ds_read_b128 a[76:79], v25 offset:12288\n"
  "buffer_load_dwordx4 v7, s[16:19], s28 offen lds\n"
  "v_mfma_f32_32x32x16_bf16 a[0:15], a[108:111], a[112:115], a[0:15]\n"
  "v_mfma_f32_32x32x16_bf16 a[16:31], a[108:111], a[116:119], a[16:31]\n"
  "v_mfma_f32_32x32x16_bf16 a[32:47], a[108:111], a[120:123], a[32:47]\n"
  "s_add_u32 m0, s30, 105472\n"
  "v_mfma_f32_32x32x16_bf16 a[48:63], a[108:111], a[124:127], a[48:63]\n"
  "buffer_load_dwordx4 v8, s[16:19], s28 offen lds\n"
  "s_add_u32 s28, s28, 128\n"
  "s_waitcnt lgkmcnt(7)\n"
  "v_mfma_f32_32x32x16_bf16 v[64:79], a[64:67], a[80:83], v[64:79]\n"
  "ds_read_b128 a[96:99], v28 offset:0\n"
  "v_mfma_f32_32x32x16_bf16 v[80:95], a[64:67], a[84:87], v[80:95]\n"
  "ds_read_b64_tr_b16 a[112:113], v29 offset:12288\n"
  "s_waitcnt lgkmcnt(7)\n"
  "v_mfma_f32_32x32x16_bf16 v[96:111], a[64:67], a[88:91], v[96:111]\n"
  "ds_read_b64_tr_b16 a[114:115], v29 offset:12544\n"
  "s_waitcnt lgkmcnt(6)\n"
  "v_mfma_f32_32x32x16_bf16 v[112:127], a[64:67], a[92:95], v[112:127]\n"
  "ds_read_b64_tr_b16 a[116:117], v29 offset:12800\n"
  "s_waitcnt lgkmcnt(6)\n"
  "v_mfma_f32_32x32x16_bf16 v[128:143], a[68:71], a[80:83], v[128:143]\n"
  "ds_read_b64_tr_b16 a[118:119], v29 offset:13056\n"
  "v_mfma_f32_32x32x16_bf16 v[144:159], a[68:71], a[84:87], v[144:159]\n"
  "ds_read_b64_tr_b16 a[120:121], v29 offset:13312\n"
  "v_mfma_f32_32x32x16_bf16 v[160:175], a[68:71], a[88:91], v[160:175]\n"
  "ds_read_b64_tr_b16 a[122:123], v29 offset:13568\n"
  "v_mfma_f32_32x32x16_bf16 v[176:191], a[68:71], a[92:95], v[176:191]\n"
  "ds_read_b64_tr_b16 a[124:125], v29 offset:13824\n"
  "s_waitcnt lgkmcnt(9)\n"
  "v_mfma_f32_32x32x16_bf16 v[192:207], a[72:75], a[80:83], v[192:207]\n"
  "ds_read_b64_tr_b16 a[126:127], v29 offset:14080\n"
  "v_mfma_f32_32x32x16_bf16 v[208:223], a[72:75], a[84:87], v[208:223]\n"
  "ds_read_b128 a[100:103], v28 offset:4096\n"
  "v_mfma_f32_32x32x16_bf16 v[224:239], a[72:75], a[88:91], v[224:239]\n"
  "ds_read_b128 a[104:107], v28 offset:8192\n"
  "v_mfma_f32_32x32x16_bf16 v[240:255], a[72:75], a[92:95], v[240:255]\n"
  "ds_read_b128 a[108:111], v28 offset:12288\n"
  "s_waitcnt lgkmcnt(12)\n"
  "v_mfma_f32_32x32x16_bf16 a[0:15], a[76:79], a[80:83], a[0:15]\n"
  "v_mfma_f32_32x32x16_bf16 a[16:31], a[76:79], a[84:87], a[16:31]\n"
  "v_mfma_f32_32x32x16_bf16 a[32:47], a[76:79], a[88:91], a[32:47]\n"
  "v_mfma_f32_32x32x16_bf16 a[48:63], a[76:79], a[92:95], a[48:63]\n"
  "s_waitcnt vmcnt(8)\n"
  "s_waitcnt lgkmcnt(0)\n"
  "s_barrier\n"
  "v_mfma_f32_32x32x16_bf16 v[64:79], a[96:99], a[112:115], v[64:79]\n"
  "ds_read_b128 a[64:67], v17 offset:32768\n"
  "s_add_u32 m0, s30, 131072\n"
  "v_mfma_f32_32x32x16_bf16 v[80:95], a[96:99], a[116:119], v[80:95]\n"
  "ds_read_b64_tr_b16 a[80:81], v30 offset:0\n"
  "buffer_load_dwordx4 v9, s[20:23], s29 offen lds\n"
  "v_mfma_f32_32x32x16_bf16 v[96:111], a[96:99], a[120:123], v[96:111]\n"
  "ds_read_b64_tr_b16 a[82:83], v30 offset:256\n"
  "s_add_u32 m0, s30, 132096\n"
  "v_mfma_f32_32x32x16_bf16 v[112:127], a[96:99], a[124:127], v[112:127]\n"
  "ds_read_b64_tr_b16 a[84:85], v30 offset:512\n"
  "buffer_load_dwordx4 v10, s[20:23], s29 offen lds\n"
  "v_mfma_f32_32x32x16_bf16 v[128:143], a[100:103], a[112:115], v[128:143]\n"
  "ds_read_b64_tr_b16 a[86:87], v30 offset:768\n"
  "s_add_u32 m0, s30, 133120\n"
  "v_mfma_f32_32x32x16_bf16 v[144:159], a[100:103], a[116:119], v[144:159]\n"
  "ds_read_b64_tr_b16 a[88:89], v30 offset:1024\n"
  "buffer_load_dwordx4 v11, s[20:23], s29 offen lds\n"
  "v_mfma_f32_32x32x16_bf16 v[160:175], a[100:103], a[120:123], v[160:175]\n"
  "ds_read_b64_tr_b16 a[90:91], v30 offset:1280\n"
  "s_add_u32 m0, s30, 134144\n"
  "v_mfma_f32_32x32x16_bf16 v[176:191], a[100:103], a[124:127], v[176:191]\n"
  "ds_read_b64_tr_b16 a[92:93], v30 offset:1536\n"
  "buffer_load_dwordx4 v12, s[20:23], s29 offen lds\n"
  "v_mfma_f32_32x32x16_bf16 v[192:207], a[104:107], a[112:115], v[192:207]\n"
  "ds_read_b64_tr_b16 a[94:95], v30 offset:1792\n"
  "s_add_u32 m0, s30, 135168\n"
  "v_mfma_f32_32x32x16_bf16 v[208:223], a[104:107], a[116:119], v[208:223]\n"
  "ds_read_b128 a[68:71], v17 offset:36864\n"
  "buffer_load_dwordx4 v13, s[20:23], s29 offen lds\n"
  "v_mfma_f32_32x32x16_bf16 v[224:239], a[104:107], a[120:123], v[224:239]\n"
  "ds_read_b128 a[72:75], v17 offset:40960\n"
  "s_add_u32 m0, s30, 136192\n"
  "v_mfma_f32_32x32x16_bf16 v[240:255], a[104:107], a[124:127], v[240:255]\n"
  "ds_read_b128 a[76:79], v17 offset:45056\n"
  "buffer_load_dwordx4 v14, s[20:23], s29 offen lds\n"
  "v_mfma_f32_32x32x16_bf16 a[0:15], a[108:111], a[112:115], a[0:15]\n"
  "s_add_u32 m0, s30, 137216\n"
  "v_mfma_f32_32x32x16_bf16 a[16:31], a[108:111], a[116:119], a[16:31]\n"
  "buffer_load_dwordx4 v15, s[20:23], s29 offen lds\n"
  "v_mfma_f32_32x32x16_bf16 a[32:47], a[108:111], a[120:123], a[32:47]\n"
  "s_add_u32 m0, s30, 138240\n"
  "v_mfma_f32_32x32x16_bf16 a[48:63], a[108:111], a[124:127], a[48:63]\n"
  "buffer_load_dwordx4 v16, s[20:23], s29 offen lds\n"
  "s_add_u32 s29, s29, 524288\n"
  "s_waitcnt lgkmcnt(5)\n"
  "v_mfma_f32_32x32x16_bf16 v[64:79], a[64:67], a[80:83], v[64:79]\n"
  "ds_read_b128 a[96:99], v20 offset:32768\n"
  "v_mfma_f32_32x32x16_bf16 v[80:95], a[64:67], a[84:87], v[80:95]\n"
  "ds_read_b64_tr_b16 a[112:113], v30 offset:4096\n"
  "v_mfma_f32_32x32x16_bf16 v[96:111], a[64:67], a[88:91], v[96:111]\n"
  "ds_read_b64_tr_b16 a[114:115], v30 offset:4352\n"
  "s_add_u32 m0, s30, 0\n"
  "s_waitcnt lgkmcnt(6)\n"
  "v_mfma_f32_32x32x16_bf16 v[112:127], a[64:67], a[92:95], v[112:127]\n"
  "ds_read_b64_tr_b16 a[116:117], v30 offset:4608\n"
  "buffer_load_dwordx4 v1, s[16:19], s28 offen lds\n"
  "s_waitcnt lgkmcnt(6)\n"
  "v_mfma_f32_32x32x16_bf16 v[128:143], a[68:71], a[80:83], v[128:143]\n"
  "ds_read_b64_tr_b16 a[118:119], v30 offset:4864\n"
  "v_mfma_f32_32x32x16_bf16 v[144:159], a[68:71], a[84:87], v[144:159]\n"
  "ds_read_b64_tr_b16 a[120:121], v30 offset:5120\n"
  "v_mfma_f32_32x32x16_bf16 v[160:175], a[68:71], a[88:91], v[160:175]\n"
  "ds_read_b64_tr_b16 a[122:123], v30 offset:5376\n"
  "s_add_u32 m0, s30, 1024\n"
  "v_mfma_f32_32x32x16_bf16 v[176:191], a[68:71], a[92:95], v[176:191]\n"
  "ds_read_b64_tr_b16 a[124:125], v30 offset:5632\n"
  "buffer_load_dwordx4 v2, s[16:19], s28 offen lds\n"
  "s_waitcnt lgkmcnt(8)\n"
  "v_mfma_f32_32x32x16_bf16 v[192:207], a[72:75], a[80:83], v[192:207]\n"
  "ds_read_b64_tr_b16 a[126:127], v30 offset:5888\n"
  "v_mfma_f32_32x32x16_bf16 v[208:223], a[72:75], a[84:87], v[208:223]\n"
  "ds_read_b128 a[100:103], v20 offset:36864\n"
  "v_mfma_f32_32x32x16_bf16 v[224:239], a[72:75], a[88:91], v[224:239]\n"
  "ds_read_b128 a[104:107], v20 offset:40960\n"
  "s_add_u32 m0, s30, 2048\n"
  "v_mfma_f32_32x32x16_bf16 v[240:255], a[72:75], a[92:95], v[240:255]\n"
  "ds_read_b128 a[108:111], v20 offset:45056\n"
  "buffer_load_dwordx4 v3, s[16:19], s28 offen lds\n"
  "v_mfma_f32_32x32x16_bf16 a[0:15], a[76:79], a[80:83], a[0:15]\n"
  "v_mfma_f32_32x32x16_bf16 a[16:31], a[76:79], a[84:87], a[16:31]\n"
  "v_mfma_f32_32x32x16_bf16 a[32:47], a[76:79], a[88:91], a[32:47]\n"
  "s_add_u32 m0, s30, 3072\n"
  "v_mfma_f32_32x32x16_bf16 a[48:63], a[76:79], a[92:95], a[48:63]\n"
  "buffer_load_dwordx4 v4, s[16:19], s28 offen lds\n"
  "s_waitcnt lgkmcnt(8)\n"
  "v_mfma_f32_32x32x16_bf16 v[64:79], a[96:99], a[112:115], v[64:79]\n"
  "ds_read_b128 a[64:67], v23 offset:32768\n"
  "s_waitcnt lgkmcnt(8)\n"
  "v_mfma_f32_32x32x16_bf16 v[80:95], a[96:99], a[116:119], v[80:95]\n"
  "ds_read_b64_tr_b16 a[80:81], v30 offset:8192\n"
  "s_waitcnt lgkmcnt(7)\n"
  "v_mfma_f32_32x32x16_bf16 v[96:111], a[96:99], a[120:123], v[96:111]\n"
  "ds_read_b64_tr_b16 a[82:83], v30 offset:8448\n"
  "s_add_u32 m0, s30, 4096\n"
  "s_waitcnt lgkmcnt(6)\n"
  "v_mfma_f32_32x32x16_bf16 v[112:127], a[96:99], a[124:127], v[112:127]\n"
  "ds_read_b64_tr_b16 a[84:85], v30 offset:8704\n"
  "buffer_load_dwordx4 v5, s[16:19], s28 offen lds\n"
  "s_waitcnt lgkmcnt(6)\n"
  "v_mfma_f32_32x32x16_bf16 v[128:143], a[100:103], a[112:115], v[128:143]\n"
  "ds_read_b64_tr_b16 a[86:87], v30 offset:8960\n"
  "v_mfma_f32_32x32x16_bf16 v[144:159], a[100:103], a[116:119], v[144:159]\n"
  "ds_read_b64_tr_b16 a[88:89], v30 offset:9216\n"
  "v_mfma_f32_32x32x16_bf16 v[160:175], a[100:103], a[120:123], v[160:175]\n"
  "ds_read_b64_tr_b16 a[90:91], v30 offset:9472\n"
  "s_add_u32 m0, s30, 5120\n"
  "v_mfma_f32_32x32x16_bf16 v[176:191], a[100:103], a[124:127], v[176:191]\n"
  "ds_read_b64_tr_b16 a[92:93], v30 offset:9728\n"
  "buffer_load_dwordx4 v6, s[16:19], s28 offen lds\n"
  "s_waitcnt lgkmcnt(8)\n"
  "v_mfma_f32_32x32x16_bf16 v[192:207], a[104:107], a[112:115], v[192:207]\n"
  "ds_read_b64_tr_b16 a[94:95], v30 offset:9984\n"
  "v_mfma_f32_32x32x16_bf16 v[208:223], a[104:107], a[116:119], v[208:223]\n"
  "ds_read_b128 a[68:71], v23 offset:36864\n"
  "v_mfma_f32_32x32x16_bf16 v[224:239], a[104:107], a[120:123], v[224:239]\n"
  "ds_read_b128 a[72:75], v23 offset:40960\n"
  "s_add_u32 m0, s30, 6144\n"
  "v_mfma_f32_32x32x16_bf16 v[240:255], a[104:107], a[124:127], v[240:255]\n"
  "ds_read_b128 a[76:79], v23 offset:45056\n"
  "buffer_load_dwordx4 v7, s[16:19], s28 offen lds\n"
  "v_mfma_f32_32x32x16_bf16 a[0:15], a[108:111], a[112:115], a[0:15]\n"
  "v_mfma_f32_32x32x16_bf16 a[16:31], a[108:111], a[116:119], a[16:31]\n"
  "v_mfma_f32_32x32x16_bf16 a[32:47], a[108:111], a[120:123], a[32:47]\n"
  "s_add_u32 m0, s30, 7168\n"
  "v_mfma_f32_32x32x16_bf16 a[48:63], a[108:111], a[124:127], a[48:63]\n"
  "buffer_load_dwordx4 v8, s[16:19], s28 offen lds\n"
  "s_add_u32 s28, s28, 128\n"
  "s_waitcnt lgkmcnt(7)\n"
  "v_mfma_f32_32x32x16_bf16 v[64:79], a[64:67], a[80:83], v[64:79]\n"
  "ds_read_b128 a[96:99], v26 offset:32768\n"
  "v_mfma_f32_32x32x16_bf16 v[80:95], a[64:67], a[84:87], v[80:95]\n"
  "ds_read_b64_tr_b16 a[112:113], v30 offset:12288\n"
  "s_waitcnt lgkmcnt(7)\n"
  "v_mfma_f32_32x32x16_bf16 v[96:111], a[64:67], a[88:91], v[96:111]\n"
  "ds_read_b64_tr_b16 a[114:115], v30 offset:12544\n"
  "s_waitcnt lgkmcnt(6)\n"
  "v_mfma_f32_32x32x16_bf16 v[112:127], a[64:67], a[92:95], v[112:127]\n"
  "ds_read_b64_tr_b16 a[116:117], v30 offset:12800\n"
  "s_waitcnt lgkmcnt(6)\n"
  "v_mfma_f32_32x32x16_bf16 v[128:143], a[68:71], a[80:83], v[128:143]\n"
  "ds_read_b64_tr_b16 a[118:119], v30 offset:13056\n"
  "v_mfma_f32_32x32x16_bf16 v[144:159], a[68:71], a[84:87], v[144:159]\n"
  "ds_read_b64_tr_b16 a[120:121], v30 offset:13312\n"
  "v_mfma_f32_32x32x16_bf16 v[160:175], a[68:71], a[88:91], v[160:175]\n"
  "ds_read_b64_tr_b16 a[122:123], v30 offset:13568\n"
  "v_mfma_f32_32x32x16_bf16 v[176:191], a[68:71], a[92:95], v[176:191]\n"
  "ds_read_b64_tr_b16 a[124:125], v30 offset:13824\n"
  "s_waitcnt lgkmcnt(9)\n"
  "v_mfma_f32_32x32x16_bf16 v[192:207], a[72:75], a[80:83], v[192:207]\n"
  "ds_read_b64_tr_b16 a[126:127], v30 offset:14080\n"
  "v_mfma_f32_32x32x16_bf16 v[208:223], a[72:75], a[84:87], v[208:223]\n"
  "ds_read_b128 a[100:103], v26 offset:36864\n"
  "v_mfma_f32_32x32x16_bf16 v[224:239], a[72:75], a[88:91], v[224:239]\n"
  "ds_read_b128 a[104:107], v26 offset:40960\n"
  "v_mfma_f32_32x32x16_bf16 v[240:255], a[72:75], a[92:95], v[240:255]\n"
  "ds_read_b128 a[108:111], v26 offset:45056\n"
  "s_waitcnt lgkmcnt(12)\n"
  "v_mfma_f32_32x32x16_bf16 a[0:15], a[76:79], a[80:83], a[0:15]\n"
  "v_mfma_f32_32x32x16_bf16 a[16:31], a[76:79], a[84:87], a[16:31]\n"
  "v_mfma_f32_32x32x16_bf16 a[32:47], a[76:79], a[88:91], a[32:47]\n"
  "v_mfma_f32_32x32x16_bf16 a[48:63], a[76:79], a[92:95], a[48:63]\n"
  "s_waitcnt vmcnt(8)\n"
  "s_waitcnt lgkmcnt(0)\n"
  "s_barrier\n"
  "v_mfma_f32_32x32x16_bf16 v[64:79], a[96:99], a[112:115], v[64:79]\n"
  "ds_read_b128 a[64:67], v18 offset:32768\n"
  "s_add_u32 m0, s30, 32768\n"
  "v_mfma_f32_32x32x16_bf16 v[80:95], a[96:99], a[116:119], v[80:95]\n"
  "ds_read_b64_tr_b16 a[80:81], v31 offset:0\n"
  "buffer_load_dwordx4 v9, s[20:23], s29 offen lds\n"
  "v_mfma_f32_32x32x16_bf16 v[96:111], a[96:99], a[120:123], v[96:111]\n"
  "ds_read_b64_tr_b16 a[82:83], v31 offset:256\n"
  "s_add_u32 m0, s30, 33792\n"
  "v_mfma_f32_32x32x16_bf16 v[112:127], a[96:99], a[124:127], v[112:127]\n"
  "ds_read_b64_tr_b16 a[84:85], v31 offset:512\n"
  "buffer_load_dwordx4 v10, s[20:23], s29 offen lds\n"
  "v_mfma_f32_32x32x16_bf16 v[128:143], a[100:103], a[112:115], v[128:143]\n"
  "ds_read_b64_tr_b16 a[86:87], v31 offset:768\n"
  "s_add_u32 m0, s30, 34816\n"
  "v_mfma_f32_32x32x16_bf16 v[144:159], a[100:103], a[116:119], v[144:159]\n"
  "ds_read_b64_tr_b16 a[88:89], v31 offset:1024\n"
  "buffer_load_dwordx4 v11, s[20:23], s29 offen lds\n"
  "v_mfma_f32_32x32x16_bf16 v[160:175], a[100:103], a[120:123], v[160:175]\n"
  "ds_read_b64_tr_b16 a[90:91], v31 offset:1280\n"
  "s_add_u32 m0, s30, 35840\n"
  "v_mfma_f32_32x32x16_bf16 v[176:191], a[100:103], a[124:127], v[176:191]\n"
  "ds_read_b64_tr_b16 a[92:93], v31 offset:1536\n"
  "buffer_load_dwordx4 v12, s[20:23], s29 offen lds\n"
  "v_mfma_f32_32x32x16_bf16 v[192:207], a[104:107], a[112:115], v[192:207]\n"
  "ds_read_b64_tr_b16 a[94:95], v31 offset:1792\n"
  "s_add_u32 m0, s30, 36864\n"
  "v_mfma_f32_32x32x16_bf16 v[208:223], a[104:107], a[116:119], v[208:223]\n"
  "ds_read_b128 a[68:71], v18 offset:36864\n"
  "buffer_load_dwordx4 v13, s[20:23], s29 offen lds\n"
  "v_mfma_f32_32x32x16_bf16 v[224:239], a[104:107], a[120:123], v[224:239]\n"
  "ds_read_b128 a[72:75], v18 offset:40960\n"
  "s_add_u32 m0, s30, 37888\n"
  "v_mfma_f32_32x32x16_bf16 v[240:255], a[104:107], a[124:127], v[240:255]\n"
  "ds_read_b128 a[76:79], v18 offset:45056\n"
  "buffer_load_dwordx4 v14, s[20:23], s29 offen lds\n"
  "v_mfma_f32_32x32x16_bf16 a[0:15], a[108:111], a[112:115], a[0:15]\n"
  "s_add_u32 m0, s30, 38912\n"
  "v_mfma_f32_32x32x16_bf16 a[16:31], a[108:111], a[116:119], a[16:31]\n"
  "buffer_load_dwordx4 v15, s[20:23], s29 offen lds\n"
  "v_mfma_f32_32x32x16_bf16 a[32:47], a[108:111], a[120:123], a[32:47]\n"
  "s_add_u32 m0, s30, 39936\n"
  "v_mfma_f32_32x32x16_bf16 a[48:63], a[108:111], a[124:127], a[48:63]\n"
  "buffer_load_dwordx4 v16, s[20:23], s29 offen lds\n"
  "s_add_u32 s29, s29, 524288\n"
  "s_waitcnt lgkmcnt(5)\n"
  "v_mfma_f32_32x32x16_bf16 v[64:79], a[64:67], a[80:83], v[64:79]\n"
  "ds_read_b128 a[96:99], v21 offset:32768\n"
  "v_mfma_f32_32x32x16_bf16 v[80:95], a[64:67], a[84:87], v[80:95]\n"
  "ds_read_b64_tr_b16 a[112:113], v31 offset:4096\n"
  "v_mfma_f32_32x32x16_bf16 v[96:111], a[64:67], a[88:91], v[96:111]\n"
  "ds_read_b64_tr_b16 a[114:115], v31 offset:4352\n"
  "s_add_u32 m0, s30, 65536\n"
  "s_waitcnt lgkmcnt(6)\n"
  "v_mfma_f32_32x32x16_bf16 v[112:127], a[64:67], a[92:95], v[112:127]\n"
  "ds_read_b64_tr_b16 a[116:117], v31 offset:4608\n"
  "buffer_load_dwordx4 v1, s[16:19], s28 offen lds\n"
  "s_waitcnt lgkmcnt(6)\n"
  "v_mfma_f32_32x32x16_bf16 v[128:143], a[68:71], a[80:83], v[128:143]\n"
  "ds_read_b64_tr_b16 a[118:119], v31 offset:4864\n"
  "v_mfma_f32_32x32x16_bf16 v[144:159], a[68:71], a[84:87], v[144:159]\n"
  "ds_read_b64_tr_b16 a[120:121], v31 offset:5120\n"
  "v_mfma_f32_32x32x16_bf16 v[160:175], a[68:71], a[88:91], v[160:175]\n"
  "ds_read_b64_tr_b16 a[122:123], v31 offset:5376\n"
  "s_add_u32 m0, s30, 66560\n"
  "v_mfma_f32_32x32x16_bf16 v[176:191], a[68:71], a[92:95], v[176:191]\n"
  "ds_read_b64_tr_b16 a[124:125], v31 offset:5632\n"
  "buffer_load_dwordx4 v2, s[16:19], s28 offen lds\n"
  "s_waitcnt lgkmcnt(8)\n"
  "v_mfma_f32_32x32x16_bf16 v[192:207], a[72:75], a[80:83], v[192:207]\n"
  "ds_read_b64_tr_b16 a[126:127], v31 offset:5888\n"
  "v_mfma_f32_32x32x16_bf16 v[208:223], a[72:75], a[84:87], v[208:223]\n"
  "ds_read_b128 a[100:103], v21 offset:36864\n"
  "v_mfma_f32_32x32x16_bf16 v[224:239], a[72:75], a[88:91], v[224:239]\n"
  "ds_read_b128 a[104:107], v21 offset:40960\n"
  "s_add_u32 m0, s30, 67584\n"
  "v_mfma_f32_32x32x16_bf16 v[240:255], a[72:75], a[92:95], v[240:255]\n"
  "ds_read_b128 a[108:111], v21 offset:45056\n"
  "buffer_load_dwordx4 v3, s[16:19], s28 offen lds\n"
  "v_mfma_f32_32x32x16_bf16 a[0:15], a[76:79], a[80:83], a[0:15]\n"
  "v_mfma_f32_32x32x16_bf16 a[16:31], a[76:79], a[84:87], a[16:31]\n"
  "v_mfma_f32_32x32x16_bf16 a[32:47], a[76:79], a[88:91], a[32:47]\n"
  "s_add_u32 m0, s30, 68608\n"
  "v_mfma_f32_32x32x16_bf16 a[48:63], a[76:79], a[92:95], a[48:63]\n"
  "buffer_load_dwordx4 v4, s[16:19], s28 offen lds\n"
  "s_waitcnt lgkmcnt(8)\n"
  "v_mfma_f32_32x32x16_bf16 v[64:79], a[96:99], a[112:115], v[64:79]\n"
  "ds_read_b128 a[64:67], v24 offset:32768\n"
  "s_waitcnt lgkmcnt(8)\n"
  "v_mfma_f32_32x32x16_bf16 v[80:95], a[96:99], a[116:119], v[80:95]\n"
  "ds_read_b64_tr_b16 a[80:81], v31 offset:8192\n"
  "s_waitcnt lgkmcnt(7)\n"
  "v_mfma_f32_32x32x16_bf16 v[96:111], a[96:99], a[120:123], v[96:111]\n"
  "ds_read_b64_tr_b16 a[82:83], v31 offset:8448\n"
  "s_add_u32 m0, s30, 69632\n"
  "s_waitcnt lgkmcnt(6)\n"
  "v_mfma_f32_32x32x16_bf16 v[112:127], a[96:99], a[124:127], v[112:127]\n"
  "ds_read_b64_tr_b16 a[84:85], v31 offset:8704\n"
  "buffer_load_dwordx4 v5, s[16:19], s28 offen lds\n"
  "s_waitcnt lgkmcnt(6)\n"
  "v_mfma_f32_32x32x16_bf16 v[128:143], a[100:103], a[112:115], v[128:143]\n"
  "ds_read_b64_tr_b16 a[86:87], v31 offset:8960\n"
  "v_mfma_f32_32x32x16_bf16 v[144:159], a[100:103], a[116:119], v[144:159]\n"
  "ds_read_b64_tr_b16 a[88:89], v31 offset:9216\n"
  "v_mfma_f32_32x32x16_bf16 v[160:175], a[100:103], a[120:123], v[160:175]\n"
  "ds_read_b64_tr_b16 a[90:91], v31 offset:9472\n"
  "s_add_u32 m0, s30, 70656\n"
  "v_mfma_f32_32x32x16_bf16 v[176:191], a[100:103], a[124:127], v[176:191]\n"
  "ds_read_b64_tr_b16 a[92:93], v31 offset:9728\n"
  "buffer_load_dwordx4 v6, s[16:19], s28 offen lds\n"
  "s_waitcnt lgkmcnt(8)\n"
  "v_mfma_f32_32x32x16_bf16 v[192:207], a[104:107], a[112:115], v[192:207]\n"
  "ds_read_b64_tr_b16 a[94:95], v31 offset:9984\n"
  "v_mfma_f32_32x32x16_bf16 v[208:223], a[104:107], a[116:119], v[208:223]\n"
  "ds_read_b128 a[68:71], v24 offset:36864\n"
  "v_mfma_f32_32x32x16_bf16 v[224:239], a[104:107], a[120:123], v[224:239]\n"
  "ds_read_b128 a[72:75], v24 offset:40960\n"
  "s_add_u32 m0, s30, 71680\n"
  "v_mfma_f32_32x32x16_bf16 v[240:255], a[104:107], a[124:127], v[240:255]\n"
  "ds_read_b128 a[76:79], v24 offset:45056\n"
  "buffer_load_dwordx4 v7, s[16:19], s28 offen lds\n"
  "v_mfma_f32_32x32x16_bf16 a[0:15], a[108:111], a[112:115], a[0:15]\n"
  "v_mfma_f32_32x32x16_bf16 a[16:31], a[108:111], a[116:119], a[16:31]\n"
  "v_mfma_f32_32x32x16_bf16 a[32:47], a[108:111], a[120:123], a[32:47]\n"
  "s_add_u32 m0, s30, 72704\n"
  "v_mfma_f32_32x32x16_bf16 a[48:63], a[108:111], a[124:127], a[48:63]\n"
  "buffer_load_dwordx4 v8, s[16:19], s28 offen lds\n"
  "s_add_u32 s28, s28, 128\n"
  "s_waitcnt lgkmcnt(7)\n"
  "v_mfma_f32_32x32x16_bf16 v[64:79], a[64:67], a[80:83], v[64:79]\n"
  "ds_read_b128 a[96:99], v27 offset:32768\n"
  "v_mfma_f32_32x32x16_bf16 v[80:95], a[64:67], a[84:87], v[80:95]\n"
  "ds_read_b64_tr_b16 a[112:113], v31 offset:12288\n"
  "s_waitcnt lgkmcnt(7)\n"
  "v_mfma_f32_32x32x16_bf16 v[96:111], a[64:67], a[88:91], v[96:111]\n"
  "ds_read_b64_tr_b16 a[114:115], v31 offset:12544\n"
  "s_waitcnt lgkmcnt(6)\n"
  "v_mfma_f32_32x32x16_bf16 v[112:127], a[64:67], a[92:95], v[112:127]\n"
  "ds_read_b64_tr_b16 a[116:117], v31 offset:12800\n"
  "s_waitcnt lgkmcnt(6)\n"
  "v_mfma_f32_32x32x16_bf16 v[128:143], a[68:71], a[80:83], v[128:143]\n"
  "ds_read_b64_tr_b16 a[118:119], v31 offset:13056\n"
  "v_mfma_f32_32x32x16_bf16 v[144:159], a[68:71], a[84:87], v[144:159]\n"
  "ds_read_b64_tr_b16 a[120:121], v31 offset:13312\n"
  "v_mfma_f32_32x32x16_bf16 v[160:175], a[68:71], a[88:91], v[160:175]\n"
  "ds_read_b64_tr_b16 a[122:123], v31 offset:13568\n"
  "v_mfma_f32_32x32x16_bf16 v[176:191], a[68:71], a[92:95], v[176:191]\n"
  "ds_read_b64_tr_b16 a[124:125], v31 offset:13824\n"
  "s_waitcnt lgkmcnt(9)\n"
  "v_mfma_f32_32x32x16_bf16 v[192:207], a[72:75], a[80:83], v[192:207]\n"
  "ds_read_b64_tr_b16 a[126:127], v31 offset:14080\n"
  "v_mfma_f32_32x32x16_bf16 v[208:223], a[72:75], a[84:87], v[208:223]\n"
  "ds_read_b128 a[100:103], v27 offset:36864\n"
  "v_mfma_f32_32x32x16_bf16 v[224:239], a[72:75], a[88:91], v[224:239]\n"
  "ds_read_b128 a[104:107], v27 offset:40960\n"
  "v_mfma_f32_32x32x16_bf16 v[240:255], a[72:75], a[92:95], v[240:255]\n"
  "ds_read_b128 a[108:111], v27 offset:45056\n"
  "s_waitcnt lgkmcnt(12)\n"
  "v_mfma_f32_32x32x16_bf16 a[0:15], a[76:79], a[80:83], a[0:15]\n"
  "v_mfma_f32_32x32x16_bf16 a[16:31], a[76:79], a[84:87], a[16:31]\n"
  "v_mfma_f32_32x32x16_bf16 a[32:47], a[76:79], a[88:91], a[32:47]\n"
  "v_mfma_f32_32x32x16_bf16 a[48:63], a[76:79], a[92:95], a[48:63]\n"
  "s_waitcnt vmcnt(8)\n"
  "s_waitcnt lgkmcnt(0)\n"
  "s_barrier\n"
  "s_lshl_b32 s32, s10, 2\n"
  "s_add_u32 s32, s32, s11\n"
  "s_load_dwordx2 s[40:41], s[8:9], 48\n"
  "s_load_dwordx2 s[42:43], s[8:9], 64\n"
  "v_min_u32 v55, 17, v0\n"
  "s_mul_i32 s33, s32, 18\n"
  "v_add_u32 v55, s33, v55\n"
  "v_lshlrev_b32 v55, 7, v55\n"
  "v_min_u32 v56, 35, v0\n"
  "s_mul_i32 s33, s32, 36\n"
  "v_add_u32 v56, s33, v56\n"
  "v_lshlrev_b32 v56, 7, v56\n"
  "s_waitcnt lgkmcnt(0)\n"
  "global_load_dword v61, v55, s[40:41]\n"
  "global_load_dword v61, v56, s[42:43]\n"
  "v_mfma_f32_32x32x16_bf16 v[64:79], a[96:99], a[112:115], v[64:79]\n"
  "ds_read_b128 a[64:67], v17 offset:0\n"
  "s_add_u32 m0, s30, 98304\n"
  "v_mfma_f32_32x32x16_bf16 v[80:95], a[96:99], a[116:119], v[80:95]\n"
  "ds_read_b64_tr_b16 a[80:81], v29 offset:32768\n"
  "buffer_load_dwordx4 v9, s[20:23], s29 offen lds\n"
  "v_mfma_f32_32x32x16_bf16 v[96:111], a[96:99], a[120:123], v[96:111]\n"
  "ds_read_b64_tr_b16 a[82:83], v29 offset:33024\n"
  "s_add_u32 m0, s30, 99328\n"
  "v_mfma_f32_32x32x16_bf16 v[112:127], a[96:99], a[124:127], v[112:127]\n"
  "ds_read_b64_tr_b16 a[84:85], v29 offset:33280\n"
  "buffer_load_dwordx4 v10, s[20:23], s29 offen lds\n"
  "v_mfma_f32_32x32x16_bf16 v[128:143], a[100:103], a[112:115], v[128:143]\n"
  "ds_read_b64_tr_b16 a[86:87], v29 offset:33536\n"
  "s_add_u32 m0, s30, 100352\n"
  "v_mfma_f32_32x32x16_bf16 v[144:159], a[100:103], a[116:119], v[144:159]\n"
  "ds_read_b64_tr_b16 a[88:89], v29 offset:33792\n"
  "buffer_load_dwordx4 v11, s[20:23], s29 offen lds\n"
  "v_mfma_f32_32x32x16_bf16 v[160:175], a[100:103], a[120:123], v[160:175]\n"
  "ds_read_b64_tr_b16 a[90:91], v29 offset:34048\n"
  "s_add_u32 m0, s30, 101376\n"
  "v_mfma_f32_32x32x16_bf16 v[176:191], a[100:103], a[124:127], v[176:191]\n"
  "ds_read_b64_tr_b16 a[92:93], v29 offset:34304\n"
  "buffer_load_dwordx4 v12, s[20:23], s29 offen lds\n"
  "v_mfma_f32_32x32x16_bf16 v[192:207], a[104:107], a[112:115], v[192:207]\n"
  "ds_read_b64_tr_b16 a[94:95], v29 offset:34560\n"
  "s_add_u32 m0, s30, 102400\n"
  "v_mfma_f32_32x32x16_bf16 v[208:223], a[104:107], a[116:119], v[208:223]\n"
  "ds_read_b128 a[68:71], v17 offset:4096\n"
  "buffer_load_dwordx4 v13, s[20:23], s29 offen lds\n"
  "v_mfma_f32_32x32x16_bf16 v[224:239], a[104:107], a[120:123], v[224:239]\n"
  "ds_read_b128 a[72:75], v17 offset:8192\n"
  "s_add_u32 m0, s30, 103424\n"
  "v_mfma_f32_32x32x16_bf16 v[240:255], a[104:107], a[124:127], v[240:255]\n"
  "ds_read_b128 a[76:79], v17 offset:12288\n"
  "buffer_load_dwordx4 v14, s[20:23], s29 offen lds\n"
  "v_mfma_f32_32x32x16_bf16 a[0:15], a[108:111], a[112:115], a[0:15]\n"
  "s_add_u32 m0, s30, 104448\n"
  "v_mfma_f32_32x32x16_bf16 a[16:31], a[108:111], a[116:119], a[16:31]\n"
  "buffer_load_dwordx4 v15, s[20:23], s29 offen lds\n"
  "v_mfma_f32_32x32x16_bf16 a[32:47], a[108:111], a[120:123], a[32:47]\n"
  "s_add_u32 m0, s30, 105472\n"
  "v_mfma_f32_32x32x16_bf16 a[48:63], a[108:111], a[124:127], a[48:63]\n"
  "buffer_load_dwordx4 v16, s[20:23], s29 offen lds\n"
  "s_add_u32 s29, s29, 524288\n"
  "s_waitcnt lgkmcnt(5)\n"
  "v_mfma_f32_32x32x16_bf16 v[64:79], a[64:67], a[80:83], v[64:79]\n"
  "ds_read_b128 a[96:99], v20 offset:0\n"
  "v_mfma_f32_32x32x16_bf16 v[80:95], a[64:67], a[84:87], v[80:95]\n"
  "ds_read_b64_tr_b16 a[112:113], v29 offset:36864\n"
  "v_mfma_f32_32x32x16_bf16 v[96:111], a[64:67], a[88:91], v[96:111]\n"
  "ds_read_b64_tr_b16 a[114:115], v29 offset:37120\n"
  "s_add_u32 m0, s30, 131072\n"
  "s_waitcnt lgkmcnt(6)\n"
  "v_mfma_f32_32x32x16_bf16 v[112:127], a[64:67], a[92:95], v[112:127]\n"
  "ds_read_b64_tr_b16 a[116:117], v29 offset:37376\n"
  "buffer_load_dwordx4 v1, s[16:19], s28 offen lds\n"
  "s_waitcnt lgkmcnt(6)\n"
  "v_mfma_f32_32x32x16_bf16 v[128:143], a[68:71], a[80:83], v[128:143]\n"
  "ds_read_b64_tr_b16 a[118:119], v29 offset:37632\n"
  "v_mfma_f32_32x32x16_bf16 v[144:159], a[68:71], a[84:87], v[144:159]\n"
  "ds_read_b64_tr_b16 a[120:121], v29 offset:37888\n"
  "v_mfma_f32_32x32x16_bf16 v[160:175], a[68:71], a[88:91], v[160:175]\n"
  "ds_read_b64_tr_b16 a[122:123], v29 offset:38144\n"
  "s_add_u32 m0, s30, 132096\n"
  "v_mfma_f32_32x32x16_bf16 v[176:191], a[68:71], a[92:95], v[176:191]\n"
  "ds_read_b64_tr_b16 a[124:125], v29 offset:38400\n"
  "buffer_load_dwordx4 v2, s[16:19], s28 offen lds\n"
  "s_waitcnt lgkmcnt(8)\n"
  "v_mfma_f32_32x32x16_bf16 v[192:207], a[72:75], a[80:83], v[192:207]\n"
  "ds_read_b64_tr_b16 a[126:127], v29 offset:38656\n"
  "v_mfma_f32_32x32x16_bf16 v[208:223], a[72:75], a[84:87], v[208:223]\n"
  "ds_read_b128 a[100:103], v20 offset:4096\n"
  "v_mfma_f32_32x32x16_bf16 v[224:239], a[72:75], a[88:91], v[224:239]\n"
  "ds_read_b128 a[104:107], v20 offset:8192\n"
  "s_add_u32 m0, s30, 133120\n"
  "v_mfma_f32_32x32x16_bf16 v[240:255], a[72:75], a[92:95], v[240:255]\n"
  "ds_read_b128 a[108:111], v20 offset:12288\n"
  "buffer_load_dwordx4 v3, s[16:19], s28 offen lds\n"
  "v_mfma_f32_32x32x16_bf16 a[0:15], a[76:79], a[80:83], a[0:15]\n"
  "v_mfma_f32_32x32x16_bf16 a[16:31], a[76:79], a[84:87], a[16:31]\n"
  "v_mfma_f32_32x32x16_bf16 a[32:47], a[76:79], a[88:91], a[32:47]\n"
  "s_add_u32 m0, s30, 134144\n"
  "v_mfma_f32_32x32x16_bf16 a[48:63], a[76:79], a[92:95], a[48:63]\n"
  "buffer_load_dwordx4 v4, s[16:19], s28 offen lds\n"
  "s_waitcnt lgkmcnt(8)\n"
  "v_mfma_f32_32x32x16_bf16 v[64:79], a[96:99], a[112:115], v[64:79]\n"
  "ds_read_b128 a[64:67], v23 offset:0\n"
  "s_waitcnt lgkmcnt(8)\n"
  "v_mfma_f32_32x32x16_bf16 v[80:95], a[96:99], a[116:119], v[80:95]\n"
  "ds_read_b64_tr_b16 a[80:81], v29 offset:40960\n"
  "s_waitcnt lgkmcnt(7)\n"
  "v_mfma_f32_32x32x16_bf16 v[96:111], a[96:99], a[120:123], v[96:111]\n"
  "ds_read_b64_tr_b16 a[82:83], v29 offset:41216\n"
  "s_add_u32 m0, s30, 135168\n"
  "s_waitcnt lgkmcnt(6)\n"
  "v_mfma_f32_32x32x16_bf16 v[112:127], a[96:99], a[124:127], v[112:127]\n"
  "ds_read_b64_tr_b16 a[84:85], v29 offset:41472\n"
  "buffer_load_dwordx4 v5, s[16:19], s28 offen lds\n"
  "s_waitcnt lgkmcnt(6)\n"
  "v_mfma_f32_32x32x16_bf16 v[128:143], a[100:103], a[112:115], v[128:143]\n"
  "ds_read_b64_tr_b16 a[86:87], v29 offset:41728\n"
  "v_mfma_f32_32x32x16_bf16 v[144:159], a[100:103], a[116:119], v[144:159]\n"
  "ds_read_b64_tr_b16 a[88:89], v29 offset:41984\n"
  "v_mfma_f32_32x32x16_bf16 v[160:175], a[100:103], a[120:123], v[160:175]\n"
  "ds_read_b64_tr_b16 a[90:91], v29 offset:42240\n"
  "s_add_u32 m0, s30, 136192\n"
  "v_mfma_f32_32x32x16_bf16 v[176:191], a[100:103], a[124:127], v[176:191]\n"
  "ds_read_b64_tr_b16 a[92:93], v29 offset:42496\n"
  "buffer_load_dwordx4 v6, s[16:19], s28 offen lds\n"
  "s_waitcnt lgkmcnt(8)\n"
  "v_mfma_f32_32x32x16_bf16 v[192:207], a[104:107], a[112:115], v[192:207]\n"
  "ds_read_b64_tr_b16 a[94:95], v29 offset:42752\n"
  "v_mfma_f32_32x32x16_bf16 v[208:223], a[104:107], a[116:119], v[208:223]\n"
  "ds_read_b128 a[68:71], v23 offset:4096\n"
  "v_mfma_f32_32x32x16_bf16 v[224:239], a[104:107], a[120:123], v[224:239]\n"
  "ds_read_b128 a[72:75], v23 offset:8192\n"
  "s_add_u32 m0, s30, 137216\n"
  "v_mfma_f32_32x32x16_bf16 v[240:255], a[104:107], a[124:127], v[240:255]\n"
  "ds_read_b128 a[76:79], v23 offset:12288\n"
  "buffer_load_dwordx4 v7, s[16:19], s28 offen lds\n"
  "v_mfma_f32_32x32x16_bf16 a[0:15], a[108:111], a[112:115], a[0:15]\n"
  "v_mfma_f32_32x32x16_bf16 a[16:31], a[108:111], a[116:119], a[16:31]\n"
  "v_mfma_f32_32x32x16_bf16 a[32:47], a[108:111], a[120:123], a[32:47]\n"
  "s_add_u32 m0, s30, 138240\n"
  "v_mfma_f32_32x32x16_bf16 a[48:63], a[108:111], a[124:127], a[48:63]\n"
  "buffer_load_dwordx4 v8, s[16:19], s28 offen lds\n"
  "s_add_u32 s28, s28, 128\n"
  "s_waitcnt lgkmcnt(7)\n"
  "v_mfma_f32_32x32x16_bf16 v[64:79], a[64:67], a[80:83], v[64:79]\n"
  "ds_read_b128 a[96:99], v26 offset:0\n"
  "v_mfma_f32_32x32x16_bf16 v[80:95], a[64:67], a[84:87], v[80:95]\n"
  "ds_read_b64_tr_b16 a[112:113], v29 offset:45056\n"
  "s_waitcnt lgkmcnt(7)\n"
  "v_mfma_f32_32x32x16_bf16 v[96:111], a[64:67], a[88:91], v[96:111]\n"
  "ds_read_b64_tr_b16 a[114:115], v29 offset:45312\n"
  "s_waitcnt lgkmcnt(6)\n"
  "v_mfma_f32_32x32x16_bf16 v[112:127], a[64:67], a[92:95], v[112:127]\n"
  "ds_read_b64_tr_b16 a[116:117], v29 offset:45568\n"
  "s_waitcnt lgkmcnt(6)\n"
  "v_mfma_f32_32x32x16_bf16 v[128:143], a[68:71], a[80:83], v[128:143]\n"
  "ds_read_b64_tr_b16 a[118:119], v29 offset:45824\n"
  "v_mfma_f32_32x32x16_bf16 v[144:159], a[68:71], a[84:87], v[144:159]\n"
  "ds_read_b64_tr_b16 a[120:121], v29 offset:46080\n"
  "v_mfma_f32_32x32x16_bf16 v[160:175], a[68:71], a[88:91], v[160:175]\n"
  "ds_read_b64_tr_b16 a[122:123], v29 offset:46336\n"
  "v_mfma_f32_32x32x16_bf16 v[176:191], a[68:71], a[92:95], v[176:191]\n"
  "ds_read_b64_tr_b16 a[124:125], v29 offset:46592\n"
  "s_waitcnt lgkmcnt(9)\n"
  "v_mfma_f32_32x32x16_bf16 v[192:207], a[72:75], a[80:83], v[192:207]\n"
  "ds_read_b64_tr_b16 a[126:127], v29 offset:46848\n"
  "v_mfma_f32_32x32x16_bf16 v[208:223], a[72:75], a[84:87], v[208:223]\n"
  "ds_read_b128 a[100:103], v26 offset:4096\n"
  "v_mfma_f32_32x32x16_bf16 v[224:239], a[72:75], a[88:91], v[224:239]\n"
  "ds_read_b128 a[104:107], v26 offset:8192\n"
  "v_mfma_f32_32x32x16_bf16 v[240:255], a[72:75], a[92:95], v[240:255]\n"
  "ds_read_b128 a[108:111], v26 offset:12288\n"
  "s_waitcnt lgkmcnt(12)\n"
  "v_mfma_f32_32x32x16_bf16 a[0:15], a[76:79], a[80:83], a[0:15]\n"
  "v_mfma_f32_32x32x16_bf16 a[16:31], a[76:79], a[84:87], a[16:31]\n"
  "v_mfma_f32_32x32x16_bf16 a[32:47], a[76:79], a[88:91], a[32:47]\n"
  "v_mfma_f32_32x32x16_bf16 a[48:63], a[76:79], a[92:95], a[48:63]\n"
  "s_waitcnt vmcnt(8)\n"
  "s_waitcnt lgkmcnt(0)\n"
  "s_barrier\n"
  "v_mfma_f32_32x32x16_bf16 v[64:79], a[96:99], a[112:115], v[64:79]\n"
  "ds_read_b128 a[64:67], v18 offset:0\n"
  "s_add_u32 m0, s30, 0\n"
  "v_mfma_f32_32x32x16_bf16 v[80:95], a[96:99], a[116:119], v[80:95]\n"
  "ds_read_b64_tr_b16 a[80:81], v30 offset:32768\n"
  "buffer_load_dwordx4 v9, s[20:23], s29 offen lds\n"
  "v_mfma_f32_32x32x16_bf16 v[96:111], a[96:99], a[120:123], v[96:111]\n"
  "ds_read_b64_tr_b16 a[82:83], v30 offset:33024\n"
  "s_add_u32 m0, s30, 1024\n"
  "v_mfma_f32_32x32x16_bf16 v[112:127], a[96:99], a[124:127], v[112:127]\n"
  "ds_read_b64_tr_b16 a[84:85], v30 offset:33280\n"
  "buffer_load_dwordx4 v10, s[20:23], s29 offen lds\n"
  "v_mfma_f32_32x32x16_bf16 v[128:143], a[100:103], a[112:115], v[128:143]\n"
  "ds_read_b64_tr_b16 a[86:87], v30 offset:33536\n"
  "s_add_u32 m0, s30, 2048\n"
  "v_mfma_f32_32x32x16_bf16 v[144:159], a[100:103], a[116:119], v[144:159]\n"
  "ds_read_b64_tr_b16 a[88:89], v30 offset:33792\n"
  "buffer_load_dwordx4 v11, s[20:23], s29 offen lds\n"
  "v_mfma_f32_32x32x16_bf16 v[160:175], a[100:103], a[120:123], v[160:175]\n"
  "ds_read_b64_tr_b16 a[90:91], v30 offset:34048\n"
  "s_add_u32 m0, s30, 3072\n"
  "v_mfma_f32_32x32x16_bf16 v[176:191], a[100:103], a[124:127], v[176:191]\n"
  "ds_read_b64_tr_b16 a[92:93], v30 offset:34304\n"
  "buffer_load_dwordx4 v12, s[20:23], s29 offen lds\n"
  "v_mfma_f32_32x32x16_bf16 v[192:207], a[104:107], a[112:115], v[192:207]\n"
  "ds_read_b64_tr_b16 a[94:95], v30 offset:34560\n"
  "s_add_u32 m0, s30, 4096\n"
  "v_mfma_f32_32x32x16_bf16 v[208:223], a[104:107], a[116:119], v[208:223]\n"
  "ds_read_b128 a[68:71], v18 offset:4096\n"
  "buffer_load_dwordx4 v13, s[20:23], s29 offen lds\n"
  "v_mfma_f32_32x32x16_bf16 v[224:239], a[104:107], a[120:123], v[224:239]\n"
  "ds_read_b128 a[72:75], v18 offset:8192\n"
  "s_add_u32 m0, s30, 5120\n"
  "v_mfma_f32_32x32x16_bf16 v[240:255], a[104:107], a[124:127], v[240:255]\n"
  "ds_read_b128 a[76:79], v18 offset:12288\n"
  "buffer_load_dwordx4 v14, s[20:23], s29 offen lds\n"
  "v_mfma_f32_32x32x16_bf16 a[0:15], a[108:111], a[112:115], a[0:15]\n"
  "s_add_u32 m0, s30, 6144\n"
  "v_mfma_f32_32x32x16_bf16 a[16:31], a[108:111], a[116:119], a[16:31]\n"
  "buffer_load_dwordx4 v15, s[20:23], s29 offen lds\n"
  "v_mfma_f32_32x32x16_bf16 a[32:47], a[108:111], a[120:123], a[32:47]\n"
  "s_add_u32 m0, s30, 7168\n"
  "v_mfma_f32_32x32x16_bf16 a[48:63], a[108:111], a[124:127], a[48:63]\n"
  "buffer_load_dwordx4 v16, s[20:23], s29 offen lds\n"
  "s_add_u32 s29, s29, 524288\n"
  "s_waitcnt lgkmcnt(5)\n"
  "v_mfma_f32_32x32x16_bf16 v[64:79], a[64:67], a[80:83], v[64:79]\n"
  "ds_read_b128 a[96:99], v21 offset:0\n"
  "v_mfma_f32_32x32x16_bf16 v[80:95], a[64:67], a[84:87], v[80:95]\n"
  "ds_read_b64_tr_b16 a[112:113], v30 offset:36864\n"
  "v_mfma_f32_32x32x16_bf16 v[96:111], a[64:67], a[88:91], v[96:111]\n"
  "ds_read_b64_tr_b16 a[114:115], v30 offset:37120\n"
  "s_waitcnt lgkmcnt(6)\n"
  "v_mfma_f32_32x32x16_bf16 v[112:127], a[64:67], a[92:95], v[112:127]\n"
  "ds_read_b64_tr_b16 a[116:117], v30 offset:37376\n"
  "s_waitcnt lgkmcnt(6)\n"
  "v_mfma_f32_32x32x16_bf16 v[128:143], a[68:71], a[80:83], v[128:143]\n"
  "ds_read_b64_tr_b16 a[118:119], v30 offset:37632\n"
  "v_mfma_f32_32x32x16_bf16 v[144:159], a[68:71], a[84:87], v[144:159]\n"
  "ds_read_b64_tr_b16 a[120:121], v30 offset:37888\n"
  "v_mfma_f32_32x32x16_bf16 v[160:175], a[68:71], a[88:91], v[160:175]\n"
  "ds_read_b64_tr_b16 a[122:123], v30 offset:38144\n"
  "v_mfma_f32_32x32x16_bf16 v[176:191], a[68:71], a[92:95], v[176:191]\n"
  "ds_read_b64_tr_b16 a[124:125], v30 offset:38400\n"
  "s_waitcnt lgkmcnt(8)\n"
  "v_mfma_f32_32x32x16_bf16 v[192:207], a[72:75], a[80:83], v[192:207]\n"
  "ds_read_b64_tr_b16 a[126:127], v30 offset:38656\n"
  "v_mfma_f32_32x32x16_bf16 v[208:223], a[72:75], a[84:87], v[208:223]\n"
  "ds_read_b128 a[100:103], v21 offset:4096\n"
  "v_mfma_f32_32x32x16_bf16 v[224:239], a[72:75], a[88:91], v[224:239]\n"
  "ds_read_b128 a[104:107], v21 offset:8192\n"
  "v_mfma_f32_32x32x16_bf16 v[240:255], a[72:75], a[92:95], v[240:255]\n"
  "ds_read_b128 a[108:111], v21 offset:12288\n"
  "v_mfma_f32_32x32x16_bf16 a[0:15], a[76:79], a[80:83], a[0:15]\n"
  "v_mfma_f32_32x32x16_bf16 a[16:31], a[76:79], a[84:87], a[16:31]\n"
  "v_mfma_f32_32x32x16_bf16 a[32:47], a[76:79], a[88:91], a[32:47]\n"
  "v_mfma_f32_32x32x16_bf16 a[48:63], a[76:79], a[92:95], a[48:63]\n"
  "s_waitcnt lgkmcnt(9)\n"
  "v_mfma_f32_32x32x16_bf16 v[64:79], a[96:99], a[112:115], v[64:79]\n"
  "ds_read_b128 a[64:67], v24 offset:0\n"
  "s_waitcnt lgkmcnt(8)\n"
  "v_mfma_f32_32x32x16_bf16 v[80:95], a[96:99], a[116:119], v[80:95]\n"
  "ds_read_b64_tr_b16 a[80:81], v30 offset:40960\n"
  "s_waitcnt lgkmcnt(7)\n"
  "v_mfma_f32_32x32x16_bf16 v[96:111], a[96:99], a[120:123], v[96:111]\n"
  "ds_read_b64_tr_b16 a[82:83], v30 offset:41216\n"
  "s_waitcnt lgkmcnt(6)\n"
  "v_mfma_f32_32x32x16_bf16 v[112:127], a[96:99], a[124:127], v[112:127]\n"
  "ds_read_b64_tr_b16 a[84:85], v30 offset:41472\n"
  "s_waitcnt lgkmcnt(6)\n"
  "v_mfma_f32_32x32x16_bf16 v[128:143], a[100:103], a[112:115], v[128:143]\n"
  "ds_read_b64_tr_b16 a[86:87], v30 offset:41728\n"
  "v_mfma_f32_32x32x16_bf16 v[144:159], a[100:103], a[116:119], v[144:159]\n"
  "ds_read_b64_tr_b16 a[88:89], v30 offset:41984\n"
  "v_mfma_f32_32x32x16_bf16 v[160:175], a[100:103], a[120:123], v[160:175]\n"
  "ds_read_b64_tr_b16 a[90:91], v30 offset:42240\n"
  "v_mfma_f32_32x32x16_bf16 v[176:191], a[100:103], a[124:127], v[176:191]\n"
  "ds_read_b64_tr_b16 a[92:93], v30 offset:42496\n"
  "s_waitcnt lgkmcnt(9)\n"
  "v_mfma_f32_32x32x16_bf16 v[192:207], a[104:107], a[112:115], v[192:207]\n"
  "ds_read_b64_tr_b16 a[94:95], v30 offset:42752\n"
  "v_mfma_f32_32x32x16_bf16 v[208:223], a[104:107], a[116:119], v[208:223]\n"
  "ds_read_b128 a[68:71], v24 offset:4096\n"
  "v_mfma_f32_32x32x16_bf16 v[224:239], a[104:107], a[120:123], v[224:239]\n"
  "ds_read_b128 a[72:75], v24 offset:8192\n"
  "v_mfma_f32_32x32x16_bf16 v[240:255], a[104:107], a[124:127], v[240:255]\n"
  "ds_read_b128 a[76:79], v24 offset:12288\n"
  "s_waitcnt lgkmcnt(12)\n"
  "v_mfma_f32_32x32x16_bf16 a[0:15], a[108:111], a[112:115], a[0:15]\n"
  "v_mfma_f32_32x32x16_bf16 a[16:31], a[108:111], a[116:119], a[16:31]\n"
  "v_mfma_f32_32x32x16_bf16 a[32:47], a[108:111], a[120:123], a[32:47]\n"
  "v_mfma_f32_32x32x16_bf16 a[48:63], a[108:111], a[124:127], a[48:63]\n"
  "s_waitcnt lgkmcnt(9)\n"
  "v_mfma_f32_32x32x16_bf16 v[64:79], a[64:67], a[80:83], v[64:79]\n"
  "ds_read_b128 a[96:99], v27 offset:0\n"
  "s_waitcnt lgkmcnt(8)\n"
  "v_mfma_f32_32x32x16_bf16 v[80:95], a[64:67], a[84:87], v[80:95]\n"
  "ds_read_b64_tr_b16 a[112:113], v30 offset:45056\n"
  "s_waitcnt lgkmcnt(7)\n"
  "v_mfma_f32_32x32x16_bf16 v[96:111], a[64:67], a[88:91], v[96:111]\n"
  "ds_read_b64_tr_b16 a[114:115], v30 offset:45312\n"
  "s_waitcnt lgkmcnt(6)\n"
  "v_mfma_f32_32x32x16_bf16 v[112:127], a[64:67], a[92:95], v[112:127]\n"
  "ds_read_b64_tr_b16 a[116:117], v30 offset:45568\n"
  "s_waitcnt lgkmcnt(6)\n"
  "v_mfma_f32_32x32x16_bf16 v[128:143], a[68:71], a[80:83], v[128:143]\n"
  "ds_read_b64_tr_b16 a[118:119], v30 offset:45824\n"
  "v_mfma_f32_32x32x16_bf16 v[144:159], a[68:71], a[84:87], v[144:159]\n"
  "ds_read_b64_tr_b16 a[120:121], v30 offset:46080\n"
  "v_mfma_f32_32x32x16_bf16 v[160:175], a[68:71], a[88:91], v[160:175]\n"
  "ds_read_b64_tr_b16 a[122:123], v30 offset:46336\n"
  "v_mfma_f32_32x32x16_bf16 v[176:191], a[68:71], a[92:95], v[176:191]\n"
  "ds_read_b64_tr_b16 a[124:125], v30 offset:46592\n"
  "s_waitcnt lgkmcnt(9)\n"
  "v_mfma_f32_32x32x16_bf16 v[192:207], a[72:75], a[80:83], v[192:207]\n"
  "ds_read_b64_tr_b16 a[126:127], v30 offset:46848\n"
  "v_mfma_f32_32x32x16_bf16 v[208:223], a[72:75], a[84:87], v[208:223]\n"
  "ds_read_b128 a[100:103], v27 offset:4096\n"
  "v_mfma_f32_32x32x16_bf16 v[224:239], a[72:75], a[88:91], v[224:239]\n"
  "ds_read_b128 a[104:107], v27 offset:8192\n"
  "v_mfma_f32_32x32x16_bf16 v[240:255], a[72:75], a[92:95], v[240:255]\n"
  "ds_read_b128 a[108:111], v27 offset:12288\n"
  "s_waitcnt lgkmcnt(12)\n"
  "v_mfma_f32_32x32x16_bf16 a[0:15], a[76:79], a[80:83], a[0:15]\n"
  "v_mfma_f32_32x32x16_bf16 a[16:31], a[76:79], a[84:87], a[16:31]\n"
  "v_mfma_f32_32x32x16_bf16 a[32:47], a[76:79], a[88:91], a[32:47]\n"
  "v_mfma_f32_32x32x16_bf16 a[48:63], a[76:79], a[92:95], a[48:63]\n"
  "s_waitcnt vmcnt(0)\n"
  "s_waitcnt lgkmcnt(0)\n"
  "s_barrier\n"
  "v_mfma_f32_32x32x16_bf16 v[64:79], a[96:99], a[112:115], v[64:79]\n"
  "ds_read_b128 a[64:67], v19 offset:0\n"
  "v_mfma_f32_32x32x16_bf16 v[80:95], a[96:99], a[116:119], v[80:95]\n"
  "ds_read_b64_tr_b16 a[80:81], v29 offset:0\n"
  "v_mfma_f32_32x32x16_bf16 v[96:111], a[96:99], a[120:123], v[96:111]\n"
  "ds_read_b64_tr_b16 a[82:83], v29 offset:256\n"
  "v_mfma_f32_32x32x16_bf16 v[112:127], a[96:99], a[124:127], v[112:127]\n"
  "ds_read_b64_tr_b16 a[84:85], v29 offset:512\n"
  "v_mfma_f32_32x32x16_bf16 v[128:143], a[100:103], a[112:115], v[128:143]\n"
  "ds_read_b64_tr_b16 a[86:87], v29 offset:768\n"
  "v_mfma_f32_32x32x16_bf16 v[144:159], a[100:103], a[116:119], v[144:159]\n"
  "ds_read_b64_tr_b16 a[88:89], v29 offset:1024\n"
  "v_mfma_f32_32x32x16_bf16 v[160:175], a[100:103], a[120:123], v[160:175]\n"
  "ds_read_b64_tr_b16 a[90:91], v29 offset:1280\n"
  "v_mfma_f32_32x32x16_bf16 v[176:191], a[100:103], a[124:127], v[176:191]\n"
  "ds_read_b64_tr_b16 a[92:93], v29 offset:1536\n"
  "v_mfma_f32_32x32x16_bf16 v[192:207], a[104:107], a[112:115], v[192:207]\n"
  "ds_read_b64_tr_b16 a[94:95], v29 offset:1792\n"
  "v_mfma_f32_32x32x16_bf16 v[208:223], a[104:107], a[116:119], v[208:223]\n"
  "ds_read_b128 a[68:71], v19 offset:4096\n"
  "v_mfma_f32_32x32x16_bf16 v[224:239], a[104:107], a[120:123], v[224:239]\n"
  "ds_read_b128 a[72:75], v19 offset:8192\n"
  "v_mfma_f32_32x32x16_bf16 v[240:255], a[104:107], a[124:127], v[240:255]\n"
  "ds_read_b128 a[76:79], v19 offset:12288\n"
  "v_mfma_f32_32x32x16_bf16 a[0:15], a[108:111], a[112:115], a[0:15]\n"
  "v_mfma_f32_32x32x16_bf16 a[16:31], a[108:111], a[116:119], a[16:31]\n"
  "v_mfma_f32_32x32x16_bf16 a[32:47], a[108:111], a[120:123], a[32:47]\n"
  "v_mfma_f32_32x32x16_bf16 a[48:63], a[108:111], a[124:127], a[48:63]\n"
  "s_waitcnt lgkmcnt(9)\n"
  "v_mfma_f32_32x32x16_bf16 v[64:79], a[64:67], a[80:83], v[64:79]\n"
  "ds_read_b128 a[96:99], v22 offset:0\n"
  "s_waitcnt lgkmcnt(8)\n"
  "v_mfma_f32_32x32x16_bf16 v[80:95], a[64:67], a[84:87], v[80:95]\n"
  "ds_read_b64_tr_b16 a[112:113], v29 offset:4096\n"
  "s_waitcnt lgkmcnt(7)\n"
  "v_mfma_f32_32x32x16_bf16 v[96:111], a[64:67], a[88:91], v[96:111]\n"
  "ds_read_b64_tr_b16 a[114:115], v29 offset:4352\n"
  "s_waitcnt lgkmcnt(6)\n"
  "v_mfma_f32_32x32x16_bf16 v[112:127], a[64:67], a[92:95], v[112:127]\n"
  "ds_read_b64_tr_b16 a[116:117], v29 offset:4608\n"
  "s_waitcnt lgkmcnt(6)\n"
  "v_mfma_f32_32x32x16_bf16 v[128:143], a[68:71], a[80:83], v[128:143]\n"
  "ds_read_b64_tr_b16 a[118:119], v29 offset:4864\n"
  "v_mfma_f32_32x32x16_bf16 v[144:159], a[68:71], a[84:87], v[144:159]\n"
  "ds_read_b64_tr_b16 a[120:121], v29 offset:5120\n"
  "v_mfma_f32_32x32x16_bf16 v[160:175], a[68:71], a[88:91], v[160:175]\n"
  "ds_read_b64_tr_b16 a[122:123], v29 offset:5376\n"
  "v_mfma_f32_32x32x16_bf16 v[176:191], a[68:71], a[92:95], v[176:191]\n"
  "ds_read_b64_tr_b16 a[124:125], v29 offset:5632\n"
  "s_waitcnt lgkmcnt(9)\n"
  "v_mfma_f32_32x32x16_bf16 v[192:207], a[72:75], a[80:83], v[192:207]\n"
  "ds_read_b64_tr_b16 a[126:127], v29 offset:5888\n"
  "v_mfma_f32_32x32x16_bf16 v[208:223], a[72:75], a[84:87], v[208:223]\n"
  "ds_read_b128 a[100:103], v22 offset:4096\n"
  "v_mfma_f32_32x32x16_bf16 v[224:239], a[72:75], a[88:91], v[224:239]\n"
  "ds_read_b128 a[104:107], v22 offset:8192\n"
  "v_mfma_f32_32x32x16_bf16 v[240:255], a[72:75], a[92:95], v[240:255]\n"
  "ds_read_b128 a[108:111], v22 offset:12288\n"
  "s_waitcnt lgkmcnt(12)\n"
  "v_mfma_f32_32x32x16_bf16 a[0:15], a[76:79], a[80:83], a[0:15]\n"
  "v_mfma_f32_32x32x16_bf16 a[16:31], a[76:79], a[84:87], a[16:31]\n"
  "v_mfma_f32_32x32x16_bf16 a[32:47], a[76:79], a[88:91], a[32:47]\n"
  "v_mfma_f32_32x32x16_bf16 a[48:63], a[76:79], a[92:95], a[48:63]\n"
  "s_waitcnt lgkmcnt(9)\n"
  "v_mfma_f32_32x32x16_bf16 v[64:79], a[96:99], a[112:115], v[64:79]\n"
  "ds_read_b128 a[64:67], v25 offset:0\n"
  "s_waitcnt lgkmcnt(8)\n"
  "v_mfma_f32_32x32x16_bf16 v[80:95], a[96:99], a[116:119], v[80:95]\n"
  "ds_read_b64_tr_b16 a[80:81], v29 offset:8192\n"
  "s_waitcnt lgkmcnt(7)\n"
  "v_mfma_f32_32x32x16_bf16 v[96:111], a[96:99], a[120:123], v[96:111]\n"
  "ds_read_b64_tr_b16 a[82:83], v29 offset:8448\n"
  "s_waitcnt lgkmcnt(6)\n"
  "v_mfma_f32_32x32x16_bf16 v[112:127], a[96:99], a[124:127], v[112:127]\n"
  "ds_read_b64_tr_b16 a[84:85], v29 offset:8704\n"
  "s_waitcnt lgkmcnt(6)\n"
  "v_mfma_f32_32x32x16_bf16 v[128:143], a[100:103], a[112:115], v[128:143]\n"
  "ds_read_b64_tr_b16 a[86:87], v29 offset:8960\n"
  "v_mfma_f32_32x32x16_bf16 v[144:159], a[100:103], a[116:119], v[144:159]\n"
  "ds_read_b64_tr_b16 a[88:89], v29 offset:9216\n"
  "v_mfma_f32_32x32x16_bf16 v[160:175], a[100:103], a[120:123], v[160:175]\n"
  "ds_read_b64_tr_b16 a[90:91], v29 offset:9472\n"
  "v_mfma_f32_32x32x16_bf16 v[176:191], a[100:103], a[124:127], v[176:191]\n"
  "ds_read_b64_tr_b16 a[92:93], v29 offset:9728\n"
  "s_waitcnt lgkmcnt(9)\n"
  "v_mfma_f32_32x32x16_bf16 v[192:207], a[104:107], a[112:115], v[192:207]\n"
  "ds_read_b64_tr_b16 a[94:95], v29 offset:9984\n"
  "v_mfma_f32_32x32x16_bf16 v[208:223], a[104:107], a[116:119], v[208:223]\n"
  "ds_read_b128 a[68:71], v25 offset:4096\n"
  "v_mfma_f32_32x32x16_bf16 v[224:239], a[104:107], a[120:123], v[224:239]\n"
  "ds_read_b128 a[72:75], v25 offset:8192\n"
  "v_mfma_f32_32x32x16_bf16 v[240:255], a[104:107], a[124:127], v[240:255]\n"
  "ds_read_b128 a[76:79], v25 offset:12288\n"
  "s_waitcnt lgkmcnt(12)\n"
  "v_mfma_f32_32x32x16_bf16 a[0:15], a[108:111], a[112:115], a[0:15]\n"
  "v_mfma_f32_32x32x16_bf16 a[16:31], a[108:111], a[116:119], a[16:31]\n"
  "v_mfma_f32_32x32x16_bf16 a[32:47], a[108:111], a[120:123], a[32:47]\n"
  "v_mfma_f32_32x32x16_bf16 a[48:63], a[108:111], a[124:127], a[48:63]\n"
  "s_waitcnt lgkmcnt(9)\n"
  "v_mfma_f32_32x32x16_bf16 v[64:79], a[64:67], a[80:83], v[64:79]\n"
  "ds_read_b128 a[96:99], v28 offset:0\n"
  "s_waitcnt lgkmcnt(8)\n"
  "v_mfma_f32_32x32x16_bf16 v[80:95], a[64:67], a[84:87], v[80:95]\n"
  "ds_read_b64_tr_b16 a[112:113], v29 offset:12288\n"
  "s_waitcnt lgkmcnt(7)\n"
  "v_mfma_f32_32x32x16_bf16 v[96:111], a[64:67], a[88:91], v[96:111]\n"
  "ds_read_b64_tr_b16 a[114:115], v29 offset:12544\n"
  "s_waitcnt lgkmcnt(6)\n"
  "v_mfma_f32_32x32x16_bf16 v[112:127], a[64:67], a[92:95], v[112:127]\n"
  "ds_read_b64_tr_b16 a[116:117], v29 offset:12800\n"
  "s_waitcnt lgkmcnt(6)\n"
  "v_mfma_f32_32x32x16_bf16 v[128:143], a[68:71], a[80:83], v[128:143]\n"
  "ds_read_b64_tr_b16 a[118:119], v29 offset:13056\n"
  "v_mfma_f32_32x32x16_bf16 v[144:159], a[68:71], a[84:87], v[144:159]\n"
  "ds_read_b64_tr_b16 a[120:121], v29 offset:13312\n"
  "v_mfma_f32_32x32x16_bf16 v[160:175], a[68:71], a[88:91], v[160:175]\n"
  "ds_read_b64_tr_b16 a[122:123], v29 offset:13568\n"
  "v_mfma_f32_32x32x16_bf16 v[176:191], a[68:71], a[92:95], v[176:191]\n"
  "ds_read_b64_tr_b16 a[124:125], v29 offset:13824\n"
  "s_waitcnt lgkmcnt(9)\n"
  "v_mfma_f32_32x32x16_bf16 v[192:207], a[72:75], a[80:83], v[192:207]\n"
  "ds_read_b64_tr_b16 a[126:127], v29 offset:14080\n"
  "v_mfma_f32_32x32x16_bf16 v[208:223], a[72:75], a[84:87], v[208:223]\n"
  "ds_read_b128 a[100:103], v28 offset:4096\n"
  "v_mfma_f32_32x32x16_bf16 v[224:239], a[72:75], a[88:91], v[224:239]\n"
  "ds_read_b128 a[104:107], v28 offset:8192\n"
  "v_mfma_f32_32x32x16_bf16 v[240:255], a[72:75], a[92:95], v[240:255]\n"
  "ds_read_b128 a[108:111], v28 offset:12288\n"
  "s_waitcnt lgkmcnt(12)\n"
  "v_mfma_f32_32x32x16_bf16 a[0:15], a[76:79], a[80:83], a[0:15]\n"
  "v_mfma_f32_32x32x16_bf16 a[16:31], a[76:79], a[84:87], a[16:31]\n"
  "v_mfma_f32_32x32x16_bf16 a[32:47], a[76:79], a[88:91], a[32:47]\n"
  "v_mfma_f32_32x32x16_bf16 a[48:63], a[76:79], a[92:95], a[48:63]\n"
  "s_waitcnt lgkmcnt(9)\n"
  "v_mfma_f32_32x32x16_bf16 v[64:79], a[96:99], a[112:115], v[64:79]\n"
  "s_waitcnt lgkmcnt(7)\n"
  "v_mfma_f32_32x32x16_bf16 v[80:95], a[96:99], a[116:119], v[80:95]\n"
  "s_waitcnt lgkmcnt(5)\n"
  "v_mfma_f32_32x32x16_bf16 v[96:111], a[96:99], a[120:123], v[96:111]\n"
  "s_waitcnt lgkmcnt(3)\n"
  "v_mfma_f32_32x32x16_bf16 v[112:127], a[96:99], a[124:127], v[112:127]\n"
  "s_waitcnt lgkmcnt(2)\n"
  "v_mfma_f32_32x32x16_bf16 v[128:143], a[100:103], a[112:115], v[128:143]\n"
  "v_mfma_f32_32x32x16_bf16 v[144:159], a[100:103], a[116:119], v[144:159]\n"
  "v_mfma_f32_32x32x16_bf16 v[160:175], a[100:103], a[120:123], v[160:175]\n"
  "v_mfma_f32_32x32x16_bf16 v[176:191], a[100:103], a[124:127], v[176:191]\n"
  "s_waitcnt lgkmcnt(1)\n"
  "v_mfma_f32_32x32x16_bf16 v[192:207], a[104:107], a[112:115], v[192:207]\n"
  "v_mfma_f32_32x32x16_bf16 v[208:223], a[104:107], a[116:119], v[208:223]\n"
  "v_mfma_f32_32x32x16_bf16 v[224:239], a[104:107], a[120:123], v[224:239]\n"
  "v_mfma_f32_32x32x16_bf16 v[240:255], a[104:107], a[124:127], v[240:255]\n"
  "s_waitcnt lgkmcnt(0)\n"
  "v_mfma_f32_32x32x16_bf16 a[0:15], a[108:111], a[112:115], a[0:15]\n"
  "v_mfma_f32_32x32x16_bf16 a[16:31], a[108:111], a[116:119], a[16:31]\n"
  "v_mfma_f32_32x32x16_bf16 a[32:47], a[108:111], a[120:123], a[32:47]\n"
  "v_mfma_f32_32x32x16_bf16 a[48:63], a[108:111], a[124:127], a[48:63]\n"
  "s_barrier\n"
  "Lp1end:\n"
  "s_mov_b64 s[8:9], %0\n"
  "s_mov_b32 s10, %1\n"
  "s_mov_b32 s11, %2\n"
  "s_and_b32 s12, s10, 7\n"
  "s_lshl_b32 s12, s12, 2\n"
  "s_lshr_b32 s13, s10, 3\n"
  "s_and_b32 s13, s13, 3\n"
  "s_add_u32 s12, s12, s13\n"
  "s_lshr_b32 s13, s10, 5\n"
  "s_load_dwordx2 s[14:15], s[8:9], 104\n"
  "s_mov_b32 s71, %3\n"
  "s_waitcnt lgkmcnt(0)\n"
  "s_add_u32 s92, s14, 83886080\n"
  "s_addc_u32 s93, s15, 0\n"
  "s_and_b32 s93, s93, 0xffff\n"
  "s_mov_b32 s94, 65536\n"
  "s_mov_b32 s95, 131072\n"
  "s_load_dwordx2 s[58:59], s[8:9], 48\n"
  "s_load_dwordx2 s[60:61], s[8:9], 64\n"
  "v_mbcnt_lo_u32_b32 v0, -1, 0\n"
  "v_mbcnt_hi_u32_b32 v0, -1, v0\n"
  "s_waitcnt lgkmcnt(0)\n"
  "s_mov_b32 s16, s58\n"
  "s_mov_b32 s17, s59\n"
  "s_and_b32 s17, s17, 0xffff\n"
  "s_mov_b32 s18, 2359296\n"
  "s_mov_b32 s19, 131072\n"
  "s_mov_b32 s20, s60\n"
  "s_mov_b32 s21, s61\n"
  "s_and_b32 s21, s21, 0xffff\n"
  "s_mov_b32 s22, 4718592\n"
  "s_mov_b32 s23, 131072\n"
  "s_mov_b32 s24, s14\n"
  "s_mov_b32 s25, s15\n"
  "s_and_b32 s25, s25, 0xffff\n"
  "s_mov_b32 s26, 33554432\n"
  "s_mov_b32 s27, 131072\n"
  "s_add_u32 s28, s14, 16777216\n"
  "s_addc_u32 s29, s15, 0\n"
  "s_and_b32 s29, s29, 0xffff\n"
  "s_mov_b32 s30, 16777216\n"
  "s_mov_b32 s31, 131072\n"
  "s_add_u32 s32, s14, 33554432\n"
  "s_addc_u32 s33, s15, 0\n"
  "s_and_b32 s33, s33, 0xffff\n"
  "s_mov_b32 s34, 16777216\n"
  "s_mov_b32 s35, 131072\n"
  "s_lshl_b32 s38, s11, 13\n"
  "s_lshl_b32 s39, s11, 12\n"
  "s_lshl_b32 s58, s13, 2\n"
  "s_add_u32 s58, s58, s11\n"
  "s_mul_i32 s36, s58, 36864\n"
  "s_lshl_b32 s37, s12, 8\n"
  "s_lshl_b32 s58, s11, 17\n"
  "s_add_u32 s37, s37, s58\n"
  "v_lshlrev_b32 v1, 4, v0\n"
  "s_lshl_b32 s58, s13, 1\n"
  "s_sub_u32 s58, 15, s58\n"
  "s_mul_i32 s58, s58, 147456\n"
  "v_lshrrev_b32 v26, 3, v0\n"
  "v_mul_u32_u24 v26, 1152, v26\n"
  "v_and_b32 v27, 7, v0\n"
  "v_lshrrev_b32 v28, 4, v0\n"
  "v_add_u32 v29, 0, v28\n"
  "v_xor_b32 v29, v27, v29\n"
  "v_lshlrev_b32 v29, 4, v29\n"
  "v_add_u32 v29, v26, v29\n"
  "v_add_u32 v4, 0, v29\n"
  "v_add_u32 v8, s58, v4\n"
  "v_add_u32 v6, 18432, v29\n"
  "v_add_u32 v10, s58, v6\n"
  "v_add_u32 v29, 4, v28\n"
  "v_xor_b32 v29, v27, v29\n"
  "v_lshlrev_b32 v29, 4, v29\n"
  "v_add_u32 v29, v26, v29\n"
  "v_add_u32 v5, 9216, v29\n"
  "v_add_u32 v9, s58, v5\n"
  "v_add_u32 v7, 27648, v29\n"
  "v_add_u32 v11, s58, v7\n"
  "v_and_b32 v26, 31, v0\n"
  "v_lshrrev_b32 v26, 2, v26\n"
  "v_lshlrev_b32 v26, 13, v26\n"
  "v_lshrrev_b32 v27, 5, v0\n"
  "v_lshlrev_b32 v27, 6, v27\n"
  "v_and_b32 v28, 3, v0\n"
  "v_lshlrev_b32 v28, 4, v28\n"
  "v_add3_u32 v26, v26, v27, v28\n"
  "v_add_u32 v12, 0, v26\n"
  "v_add_u32 v13, 128, v26\n"
  "v_add_u32 v14, 65536, v26\n"
  "v_add_u32 v15, 65664, v26\n"
  "v_and_b32 v26, 31, v0\n"
  "v_lshrrev_b32 v27, 1, v26\n"
  "v_and_b32 v27, 7, v27\n"
  "v_lshrrev_b32 v28, 5, v0\n"
  "v_xor_b32 v27, v27, v28\n"
  "v_lshlrev_b32 v27, 4, v27\n"
  "v_lshlrev_b32 v26, 7, v26\n"
  "v_add3_u32 v26, v26, v27, s38\n"
  "v_xor_b32 v16, 0, v26\n"
  "v_add_u32 v17, 98304, v16\n"
  "v_xor_b32 v18, 32, v26\n"
  "v_add_u32 v19, 98304, v18\n"
  "v_xor_b32 v20, 64, v26\n"
  "v_add_u32 v21, 98304, v20\n"
  "v_xor_b32 v22, 96, v26\n"
  "v_add_u32 v23, 98304, v22\n"
  "v_lshlrev_b32 v26, 11, v28\n"
  "v_bfe_u32 v27, v0, 2, 2\n"
  "v_lshlrev_b32 v27, 6, v27\n"
  "v_bfe_u32 v29, v0, 4, 1\n"
  "v_lshlrev_b32 v29, 5, v29\n"
  "v_add3_u32 v26, v26, v27, v29\n"
  "v_and_b32 v27, 3, v0\n"
  "v_lshlrev_b32 v27, 3, v27\n"
  "v_add_u32 v26, v26, v27\n"
  "v_add_u32 v24, 65536, v26\n"
  "v_add_u32 v25, 131072, v26\n"
  "s_mov_b32 s66, 0x3e0293ee\n"
  "s_mov_b32 s67, 0x3e0293ee\n"
  "s_lshl_b32 s58, s12, 19\n"
  "s_add_u32 s58, s58, s39\n"
  "s_mov_b32 s40, s58\n"
  "s_mov_b32 s41, s58\n"
  "s_lshl_b32 s59, s13, 1\n"
  "s_sub_u32 s59, 31, s59\n"
  "s_lshl_b32 s59, s59, 14\n"
  "s_add_u32 s42, s58, s59\n"
  "s_mov_b32 s44, s39\n"
  "s_mov_b32 s45, s39\n"
  "s_mov_b32 s47, 0x53800000\n"
  "s_mov_b32 s68, 0xffff\n"
  "s_add_u32 m0, s39, 65536\n"
  "s_nop 0\n"
  "buffer_load_dwordx4 v12, s[20:23], s37 offen lds\n"
  "s_add_u32 m0, s39, 66560\n"
  "s_nop 0\n"
  "buffer_load_dwordx4 v13, s[20:23], s37 offen lds\n"
  "s_add_u32 m0, s39, 67584\n"
  "s_nop 0\n"
  "buffer_load_dwordx4 v14, s[20:23], s37 offen lds\n"
  "s_add_u32 m0, s39, 68608\n"
  "s_nop 0\n"
  "buffer_load_dwordx4 v15, s[20:23], s37 offen lds\n"
  "s_add_u32 s37, s37, 524288\n"
  "s_add_u32 m0, s38, 0\n"
  "s_nop 0\n"
  "buffer_load_dwordx4 v4, s[16:19], s36 offen lds\n"
  "s_add_u32 m0, s38, 1024\n"
  "s_nop 0\n"
  "buffer_load_dwordx4 v5, s[16:19], s36 offen lds\n"
  "s_add_u32 m0, s38, 2048\n"
  "s_nop 0\n"
  "buffer_load_dwordx4 v6, s[16:19], s36 offen lds\n"
  "s_add_u32 m0, s38, 3072\n"
  "s_nop 0\n"
  "buffer_load_dwordx4 v7, s[16:19], s36 offen lds\n"
  "s_add_u32 m0, s38, 4096\n"
  "s_nop 0\n"
  "buffer_load_dwordx4 v8, s[16:19], s36 offen lds\n"
  "s_add_u32 m0, s38, 5120\n"
  "s_nop 0\n"
  "buffer_load_dwordx4 v9, s[16:19], s36 offen lds\n"
  "s_add_u32 m0, s38, 6144\n"
  "s_nop 0\n"
  "buffer_load_dwordx4 v10, s[16:19], s36 offen lds\n"
  "s_add_u32 m0, s38, 7168\n"
  "s_nop 0\n"
  "buffer_load_dwordx4 v11, s[16:19], s36 offen lds\n"
  "s_add_u32 s36, s36, 128\n"
  "s_add_u32 m0, s39, 81920\n"
  "s_nop 0\n"
  "buffer_load_dwordx4 v12, s[20:23], s37 offen lds\n"
  "s_add_u32 m0, s39, 82944\n"
  "s_nop 0\n"
  "buffer_load_dwordx4 v13, s[20:23], s37 offen lds\n"
  "s_add_u32 m0, s39, 83968\n"
  "s_nop 0\n"
  "buffer_load_dwordx4 v14, s[20:23], s37 offen lds\n"
  "s_add_u32 m0, s39, 84992\n"
  "s_nop 0\n"
  "buffer_load_dwordx4 v15, s[20:23], s37 offen lds\n"
  "s_add_u32 s37, s37, 524288\n"
  "s_add_u32 m0, s38, 32768\n"
  "s_nop 0\n"
  "buffer_load_dwordx4 v4, s[16:19], s36 offen lds\n"
  "s_add_u32 m0, s38, 33792\n"
  "s_nop 0\n"
  "buffer_load_dwordx4 v5, s[16:19], s36 offen lds\n"
  "s_add_u32 m0, s38, 34816\n"
  "s_nop 0\n"
  "buffer_load_dwordx4 v6, s[16:19], s36 offen lds\n"
  "s_add_u32 m0, s38, 35840\n"
  "s_nop 0\n"
  "buffer_load_dwordx4 v7, s[16:19], s36 offen lds\n"
  "s_add_u32 m0, s38, 36864\n"
  "s_nop 0\n"
  "buffer_load_dwordx4 v8, s[16:19], s36 offen lds\n"
  "s_add_u32 m0, s38, 37888\n"
  "s_nop 0\n"
  "buffer_load_dwordx4 v9, s[16:19], s36 offen lds\n"
  "s_add_u32 m0, s38, 38912\n"
  "s_nop 0\n"
  "buffer_load_dwordx4 v10, s[16:19], s36 offen lds\n"
  "s_add_u32 m0, s38, 39936\n"
  "s_nop 0\n"
  "buffer_load_dwordx4 v11, s[16:19], s36 offen lds\n"
  "s_add_u32 s36, s36, 128\n"
  "s_and_b32 s58, s11, 1\n"
  "s_lshl_b32 s58, s58, 1\n"
  "s_lshl_b32 s59, s12, 5\n"
  "s_add_u32 s58, s58, s59\n"
  "s_lshl_b32 s59, s13, 2\n"
  "s_add_u32 s58, s58, s59\n"
  "s_lshl_b32 s72, s58, 14\n"
  "s_mov_b32 s58, 0\n"
  "s_mov_b32 s73, 1024\n"
  "s_mov_b32 s74, 7168\n"
  "s_mov_b32 s75, 4294943744\n"
  "s_cmp_ge_u32 s11, 2\n"
  "s_cmov_b32 s58, 16777216\n"
  "s_cmov_b32 s73, 4096\n"
  "s_cmov_b32 s74, 4294964224\n"
  "s_cmov_b32 s75, 1024\n"
  "s_add_u32 s72, s72, s58\n"
  "v_cvt_pk_bf16_f32 v64, v64, v65\n"
  "v_cvt_pk_bf16_f32 v65, v66, v67\n"
  "v_cvt_pk_bf16_f32 v66, v68, v69\n"
  "v_cvt_pk_bf16_f32 v67, v70, v71\n"
  "v_cvt_pk_bf16_f32 v68, v72, v73\n"
  "v_cvt_pk_bf16_f32 v69, v74, v75\n"
  "v_cvt_pk_bf16_f32 v70, v76, v77\n"
  "v_cvt_pk_bf16_f32 v71, v78, v79\n"
  "buffer_store_dwordx4 v[64:67], v1, s[24:27], s72 offen sc1\n"
  "s_add_u32 s72, s72, s73\n"
  "buffer_store_dwordx4 v[68:71], v1, s[24:27], s72 offen sc1\n"
  "s_add_u32 s72, s72, s74\n"
  "v_cvt_pk_bf16_f32 v160, v160, v161\n"
  "v_cvt_pk_bf16_f32 v161, v162, v163\n"
  "v_cvt_pk_bf16_f32 v162, v164, v165\n"
  "v_cvt_pk_bf16_f32 v163, v166, v167\n"
  "v_cvt_pk_bf16_f32 v164, v168, v169\n"
  "v_cvt_pk_bf16_f32 v165, v170, v171\n"
  "v_cvt_pk_bf16_f32 v166, v172, v173\n"
  "v_cvt_pk_bf16_f32 v167, v174, v175\n"
  "v_cvt_pk_bf16_f32 v176, v176, v177\n"
  "v_cvt_pk_bf16_f32 v177, v178, v179\n"
  "v_cvt_pk_bf16_f32 v178, v180, v181\n"
  "v_cvt_pk_bf16_f32 v179, v182, v183\n"
  "v_cvt_pk_bf16_f32 v180, v184, v185\n"
  "v_cvt_pk_bf16_f32 v181, v186, v187\n"
  "v_cvt_pk_bf16_f32 v182, v188, v189\n"
  "v_cvt_pk_bf16_f32 v183, v190, v191\n"
  "v_cvt_pk_bf16_f32 v192, v192, v193\n"
  "v_cvt_pk_bf16_f32 v193, v194, v195\n"
  "v_cvt_pk_bf16_f32 v194, v196, v197\n"
  "v_cvt_pk_bf16_f32 v195, v198, v199\n"
  "v_cvt_pk_bf16_f32 v196, v200, v201\n"
  "v_cvt_pk_bf16_f32 v197, v202, v203\n"
  "v_cvt_pk_bf16_f32 v198, v204, v205\n"
  "v_cvt_pk_bf16_f32 v199, v206, v207\n"
  "v_cvt_pk_bf16_f32 v80, v80, v81\n"
  "v_cvt_pk_bf16_f32 v81, v82, v83\n"
  "v_cvt_pk_bf16_f32 v82, v84, v85\n"
  "v_cvt_pk_bf16_f32 v83, v86, v87\n"
  "v_cvt_pk_bf16_f32 v84, v88, v89\n"
  "v_cvt_pk_bf16_f32 v85, v90, v91\n"
  "v_cvt_pk_bf16_f32 v86, v92, v93\n"
  "v_cvt_pk_bf16_f32 v87, v94, v95\n"
  "buffer_store_dwordx4 v[80:83], v1, s[24:27], s72 offen sc1\n"
  "s_add_u32 s72, s72, s73\n"
  "buffer_store_dwordx4 v[84:87], v1, s[24:27], s72 offen sc1\n"
  "s_add_u32 s72, s72, s74\n"
  "v_cvt_pk_bf16_f32 v208, v208, v209\n"
  "v_cvt_pk_bf16_f32 v209, v210, v211\n"
  "v_cvt_pk_bf16_f32 v210, v212, v213\n"
  "v_cvt_pk_bf16_f32 v211, v214, v215\n"
  "v_cvt_pk_bf16_f32 v212, v216, v217\n"
  "v_cvt_pk_bf16_f32 v213, v218, v219\n"
  "v_cvt_pk_bf16_f32 v214, v220, v221\n"
  "v_cvt_pk_bf16_f32 v215, v222, v223\n"
  "v_cvt_pk_bf16_f32 v224, v224, v225\n"
  "v_cvt_pk_bf16_f32 v225, v226, v227\n"
  "v_cvt_pk_bf16_f32 v226, v228, v229\n"
  "v_cvt_pk_bf16_f32 v227, v230, v231\n"
  "v_cvt_pk_bf16_f32 v228, v232, v233\n"
  "v_cvt_pk_bf16_f32 v229, v234, v235\n"
  "v_cvt_pk_bf16_f32 v230, v236, v237\n"
  "v_cvt_pk_bf16_f32 v231, v238, v239\n"
  "v_cvt_pk_bf16_f32 v240, v240, v241\n"
  "v_cvt_pk_bf16_f32 v241, v242, v243\n"
  "v_cvt_pk_bf16_f32 v242, v244, v245\n"
  "v_cvt_pk_bf16_f32 v243, v246, v247\n"
  "v_cvt_pk_bf16_f32 v244, v248, v249\n"
  "v_cvt_pk_bf16_f32 v245, v250, v251\n"
  "v_cvt_pk_bf16_f32 v246, v252, v253\n"
  "v_cvt_pk_bf16_f32 v247, v254, v255\n"
  "v_cvt_pk_bf16_f32 v96, v96, v97\n"
  "v_cvt_pk_bf16_f32 v97, v98, v99\n"
  "v_cvt_pk_bf16_f32 v98, v100, v101\n"
  "v_cvt_pk_bf16_f32 v99, v102, v103\n"
  "v_cvt_pk_bf16_f32 v100, v104, v105\n"
  "v_cvt_pk_bf16_f32 v101, v106, v107\n"
  "v_cvt_pk_bf16_f32 v102, v108, v109\n"
  "v_cvt_pk_bf16_f32 v103, v110, v111\n"
  "buffer_store_dwordx4 v[96:99], v1, s[24:27], s72 offen sc1\n"
  "s_add_u32 s72, s72, s73\n"
  "buffer_store_dwordx4 v[100:103], v1, s[24:27], s72 offen sc1\n"
  "s_add_u32 s72, s72, s74\n"
  "v_accvgpr_read_b32 v34, a0\n"
  "v_accvgpr_read_b32 v35, a1\n"
  "v_accvgpr_read_b32 v36, a2\n"
  "v_accvgpr_read_b32 v37, a3\n"
  "v_accvgpr_read_b32 v38, a4\n"
  "v_accvgpr_read_b32 v39, a5\n"
  "v_accvgpr_read_b32 v40, a6\n"
  "v_accvgpr_read_b32 v41, a7\n"
  "v_cvt_pk_bf16_f32 v168, v34, v35\n"
  "v_cvt_pk_bf16_f32 v169, v36, v37\n"
  "v_cvt_pk_bf16_f32 v170, v38, v39\n"
  "v_cvt_pk_bf16_f32 v171, v40, v41\n"
  "v_accvgpr_read_b32 v42, a8\n"
  "v_accvgpr_read_b32 v43, a9\n"
  "v_accvgpr_read_b32 v44, a10\n"
  "v_accvgpr_read_b32 v45, a11\n"
  "v_accvgpr_read_b32 v46, a12\n"
  "v_accvgpr_read_b32 v47, a13\n"
  "v_accvgpr_read_b32 v48, a14\n"
  "v_accvgpr_read_b32 v49, a15\n"
  "v_cvt_pk_bf16_f32 v172, v42, v43\n"
  "v_cvt_pk_bf16_f32 v173, v44, v45\n"
  "v_cvt_pk_bf16_f32 v174, v46, v47\n"
  "v_cvt_pk_bf16_f32 v175, v48, v49\n"
  "v_accvgpr_read_b32 v34, a16\n"
  "v_accvgpr_read_b32 v35, a17\n"
  "v_accvgpr_read_b32 v36, a18\n"
  "v_accvgpr_read_b32 v37, a19\n"
  "v_accvgpr_read_b32 v38, a20\n"
  "v_accvgpr_read_b32 v39, a21\n"
  "v_accvgpr_read_b32 v40, a22\n"
  "v_accvgpr_read_b32 v41, a23\n"
  "v_cvt_pk_bf16_f32 v184, v34, v35\n"
  "v_cvt_pk_bf16_f32 v185, v36, v37\n"
  "v_cvt_pk_bf16_f32 v186, v38, v39\n"
  "v_cvt_pk_bf16_f32 v187, v40, v41\n"
  "v_accvgpr_read_b32 v42, a24\n"
  "v_accvgpr_read_b32 v43, a25\n"
  "v_accvgpr_read_b32 v44, a26\n"
  "v_accvgpr_read_b32 v45, a27\n"
  "v_accvgpr_read_b32 v46, a28\n"
  "v_accvgpr_read_b32 v47, a29\n"
  "v_accvgpr_read_b32 v48, a30\n"
  "v_accvgpr_read_b32 v49, a31\n"
  "v_cvt_pk_bf16_f32 v188, v42, v43\n"
  "v_cvt_pk_bf16_f32 v189, v44, v45\n"
  "v_cvt_pk_bf16_f32 v190, v46, v47\n"
  "v_cvt_pk_bf16_f32 v191, v48, v49\n"
  "v_accvgpr_read_b32 v34, a32\n"
  "v_accvgpr_read_b32 v35, a33\n"
  "v_accvgpr_read_b32 v36, a34\n"
  "v_accvgpr_read_b32 v37, a35\n"
  "v_accvgpr_read_b32 v38, a36\n"
  "v_accvgpr_read_b32 v39, a37\n"
  "v_accvgpr_read_b32 v40, a38\n"
  "v_accvgpr_read_b32 v41, a39\n"
  "v_cvt_pk_bf16_f32 v200, v34, v35\n"
  "v_cvt_pk_bf16_f32 v201, v36, v37\n"
  "v_cvt_pk_bf16_f32 v202, v38, v39\n"
  "v_cvt_pk_bf16_f32 v203, v40, v41\n"
  "v_accvgpr_read_b32 v42, a40\n"
  "v_accvgpr_read_b32 v43, a41\n"
  "v_accvgpr_read_b32 v44, a42\n"
  "v_accvgpr_read_b32 v45, a43\n"
  "v_accvgpr_read_b32 v46, a44\n"
  "v_accvgpr_read_b32 v47, a45\n"
  "v_accvgpr_read_b32 v48, a46\n"
  "v_accvgpr_read_b32 v49, a47\n"
  "v_cvt_pk_bf16_f32 v204, v42, v43\n"
  "v_cvt_pk_bf16_f32 v205, v44, v45\n"
  "v_cvt_pk_bf16_f32 v206, v46, v47\n"
  "v_cvt_pk_bf16_f32 v207, v48, v49\n"
  "v_cvt_pk_bf16_f32 v112, v112, v113\n"
  "v_cvt_pk_bf16_f32 v113, v114, v115\n"
  "v_cvt_pk_bf16_f32 v114, v116, v117\n"
  "v_cvt_pk_bf16_f32 v115, v118, v119\n"
  "v_cvt_pk_bf16_f32 v116, v120, v121\n"
  "v_cvt_pk_bf16_f32 v117, v122, v123\n"
  "v_cvt_pk_bf16_f32 v118, v124, v125\n"
  "v_cvt_pk_bf16_f32 v119, v126, v127\n"
  "buffer_store_dwordx4 v[112:115], v1, s[24:27], s72 offen sc1\n"
  "s_add_u32 s72, s72, s73\n"
  "buffer_store_dwordx4 v[116:119], v1, s[24:27], s72 offen sc1\n"
  "s_add_u32 s72, s72, s75\n"
  "v_accvgpr_read_b32 v34, a48\n"
  "v_accvgpr_read_b32 v35, a49\n"
  "v_accvgpr_read_b32 v36, a50\n"
  "v_accvgpr_read_b32 v37, a51\n"
  "v_accvgpr_read_b32 v38, a52\n"
  "v_accvgpr_read_b32 v39, a53\n"
  "v_accvgpr_read_b32 v40, a54\n"
  "v_accvgpr_read_b32 v41, a55\n"
  "v_cvt_pk_bf16_f32 v216, v34, v35\n"
  "v_cvt_pk_bf16_f32 v217, v36, v37\n"
  "v_cvt_pk_bf16_f32 v218, v38, v39\n"
  "v_cvt_pk_bf16_f32 v219, v40, v41\n"
  "v_accvgpr_read_b32 v42, a56\n"
  "v_accvgpr_read_b32 v43, a57\n"
  "v_accvgpr_read_b32 v44, a58\n"
  "v_accvgpr_read_b32 v45, a59\n"
  "v_accvgpr_read_b32 v46, a60\n"
  "v_accvgpr_read_b32 v47, a61\n"
  "v_accvgpr_read_b32 v48, a62\n"
  "v_accvgpr_read_b32 v49, a63\n"
  "v_cvt_pk_bf16_f32 v220, v42, v43\n"
  "v_cvt_pk_bf16_f32 v221, v44, v45\n"
  "v_cvt_pk_bf16_f32 v222, v46, v47\n"
  "v_cvt_pk_bf16_f32 v223, v48, v49\n"
  "v_cvt_pk_bf16_f32 v232, v128, v129\n"
  "v_cvt_pk_bf16_f32 v233, v130, v131\n"
  "v_cvt_pk_bf16_f32 v234, v132, v133\n"
  "v_cvt_pk_bf16_f32 v235, v134, v135\n"
  "v_cvt_pk_bf16_f32 v236, v136, v137\n"
  "v_cvt_pk_bf16_f32 v237, v138, v139\n"
  "v_cvt_pk_bf16_f32 v238, v140, v141\n"
  "v_cvt_pk_bf16_f32 v239, v142, v143\n"
  "v_cvt_pk_bf16_f32 v248, v144, v145\n"
  "v_cvt_pk_bf16_f32 v249, v146, v147\n"
  "v_cvt_pk_bf16_f32 v250, v148, v149\n"
  "v_cvt_pk_bf16_f32 v251, v150, v151\n"
  "v_cvt_pk_bf16_f32 v252, v152, v153\n"
  "v_cvt_pk_bf16_f32 v253, v154, v155\n"
  "v_cvt_pk_bf16_f32 v254, v156, v157\n"
  "v_cvt_pk_bf16_f32 v255, v158, v159\n"
  "s_waitcnt vmcnt(20)\n"
  "s_barrier\n"
  "ds_read_b64_tr_b16 a[136:137], v24 offset:0\n"
  "ds_read_b64_tr_b16 a[138:139], v24 offset:256\n"
  "ds_read_b128 a[128:131], v16 offset:0\n"
  "ds_read_b128 a[132:135], v16 offset:4096\n"
  "ds_read_b64_tr_b16 a[140:141], v24 offset:512\n"
  "ds_read_b64_tr_b16 a[142:143], v24 offset:768\n"
  "ds_read_b64_tr_b16 a[144:145], v24 offset:1024\n"
  "ds_read_b64_tr_b16 a[146:147], v24 offset:1280\n"
  "ds_read_b64_tr_b16 a[148:149], v24 offset:1536\n"
  "ds_read_b64_tr_b16 a[150:151], v24 offset:1792\n"
  "buffer_store_dwordx4 v[232:235], v1, s[24:27], s72 offen sc1\n"
  "s_add_u32 s72, s72, s73\n"
  "s_add_u32 m0, s39, 131072\n"
  "s_waitcnt lgkmcnt(7)\n"
  "v_mfma_f32_32x32x16_bf16 v[32:47], a[136:139], a[128:131], 0\n"
  "ds_read_b64_tr_b16 a[160:161], v24 offset:4096\n"
  "ds_read_b64_tr_b16 a[162:163], v24 offset:4352\n"
  "buffer_load_dwordx4 v12, s[20:23], s37 offen lds\n"
  "s_waitcnt lgkmcnt(8)\n"
  "v_mfma_f32_32x32x16_bf16 v[96:111], a[136:139], a[132:135], 0\n"
  "ds_read_b128 a[152:155], v18 offset:0\n"
  "ds_read_b128 a[156:159], v18 offset:4096\n"
  "v_accvgpr_write_b32 a0, 0\n"
  "s_add_u32 m0, s39, 132096\n"
  "s_waitcnt lgkmcnt(8)\n"
  "v_mfma_f32_32x32x16_bf16 v[48:63], a[140:143], a[128:131], 0\n"
  "ds_read_b64_tr_b16 a[164:165], v24 offset:4608\n"
  "buffer_load_dwordx4 v13, s[20:23], s37 offen lds\n"
  "v_mfma_f32_32x32x16_bf16 v[112:127], a[140:143], a[132:135], 0\n"
  "ds_read_b64_tr_b16 a[166:167], v24 offset:4864\n"
  "v_accvgpr_write_b32 a1, 0\n"
  "s_add_u32 m0, s39, 133120\n"
  "s_waitcnt lgkmcnt(8)\n"
  "v_mfma_f32_32x32x16_bf16 v[64:79], a[144:147], a[128:131], 0\n"
  "ds_read_b64_tr_b16 a[168:169], v24 offset:5120\n"
  "buffer_load_dwordx4 v14, s[20:23], s37 offen lds\n"
  "v_mfma_f32_32x32x16_bf16 v[128:143], a[144:147], a[132:135], 0\n"
  "ds_read_b64_tr_b16 a[170:171], v24 offset:5376\n"
  "v_accvgpr_write_b32 a2, 0\n"
  "s_add_u32 m0, s39, 134144\n"
  "s_waitcnt lgkmcnt(8)\n"
  "v_mfma_f32_32x32x16_bf16 v[80:95], a[148:151], a[128:131], 0\n"
  "ds_read_b64_tr_b16 a[172:173], v24 offset:5632\n"
  "buffer_load_dwordx4 v15, s[20:23], s37 offen lds\n"
  "s_add_u32 s37, s37, 524288\n"
  "v_mfma_f32_32x32x16_bf16 v[144:159], a[148:151], a[132:135], 0\n"
  "ds_read_b64_tr_b16 a[174:175], v24 offset:5888\n"
  "buffer_store_dwordx4 v[236:239], v1, s[24:27], s72 offen sc1\n"
  "s_add_u32 s72, s72, s74\n"
  "s_add_u32 m0, s38, 98304\n"
  "s_waitcnt lgkmcnt(6)\n"
  "v_mfma_f32_32x32x16_bf16 v[32:47], a[160:163], a[152:155], v[32:47]\n"
  "ds_read_b64_tr_b16 a[136:137], v24 offset:8192\n"
  "ds_read_b64_tr_b16 a[138:139], v24 offset:8448\n"
  "buffer_load_dwordx4 v4, s[16:19], s36 offen lds\n"
  "v_mfma_f32_32x32x16_bf16 v[96:111], a[160:163], a[156:159], v[96:111]\n"
  "ds_read_b128 a[128:131], v20 offset:0\n"
  "ds_read_b128 a[132:135], v20 offset:4096\n"
  "v_accvgpr_write_b32 a3, 0\n"
  "s_add_u32 m0, s38, 99328\n"
  "s_waitcnt lgkmcnt(8)\n"
  "v_mfma_f32_32x32x16_bf16 v[48:63], a[164:167], a[152:155], v[48:63]\n"
  "ds_read_b64_tr_b16 a[140:141], v24 offset:8704\n"
  "buffer_load_dwordx4 v5, s[16:19], s36 offen lds\n"
  "s_add_u32 m0, s38, 100352\n"
  "v_mfma_f32_32x32x16_bf16 v[112:127], a[164:167], a[156:159], v[112:127]\n"
  "ds_read_b64_tr_b16 a[142:143], v24 offset:8960\n"
  "buffer_load_dwordx4 v6, s[16:19], s36 offen lds\n"
  "s_waitcnt lgkmcnt(8)\n"
  "v_mfma_f32_32x32x16_bf16 v[64:79], a[168:171], a[152:155], v[64:79]\n"
  "ds_read_b64_tr_b16 a[144:145], v24 offset:9216\n"
  "v_accvgpr_write_b32 a4, 0\n"
  "s_add_u32 m0, s38, 101376\n"
  "v_mfma_f32_32x32x16_bf16 v[128:143], a[168:171], a[156:159], v[128:143]\n"
  "ds_read_b64_tr_b16 a[146:147], v24 offset:9472\n"
  "buffer_load_dwordx4 v7, s[16:19], s36 offen lds\n"
  "s_waitcnt lgkmcnt(8)\n"
  "v_mfma_f32_32x32x16_bf16 v[80:95], a[172:175], a[152:155], v[80:95]\n"
  "ds_read_b64_tr_b16 a[148:149], v24 offset:9728\n"
  "v_accvgpr_write_b32 a5, 0\n"
  "v_mfma_f32_32x32x16_bf16 v[144:159], a[172:175], a[156:159], v[144:159]\n"
  "ds_read_b64_tr_b16 a[150:151], v24 offset:9984\n"
  "buffer_store_dwordx4 v[248:251], v1, s[24:27], s72 offen sc1\n"
  "s_add_u32 s72, s72, s73\n"
  "s_add_u32 m0, s38, 102400\n"
  "s_waitcnt lgkmcnt(7)\n"
  "v_mfma_f32_32x32x16_bf16 v[32:47], a[136:139], a[128:131], v[32:47]\n"
  "ds_read_b64_tr_b16 a[160:161], v24 offset:12288\n"
  "ds_read_b64_tr_b16 a[162:163], v24 offset:12544\n"
  "buffer_load_dwordx4 v8, s[16:19], s36 offen lds\n"
  "s_waitcnt lgkmcnt(8)\n"
  "v_mfma_f32_32x32x16_bf16 v[96:111], a[136:139], a[132:135], v[96:111]\n"
  "ds_read_b128 a[152:155], v22 offset:0\n"
  "ds_read_b128 a[156:159], v22 offset:4096\n"
  "v_accvgpr_write_b32 a6, 0\n"
  "s_waitcnt lgkmcnt(8)\n"
  "v_mfma_f32_32x32x16_bf16 v[48:63], a[140:143], a[128:131], v[48:63]\n"
  "ds_read_b64_tr_b16 a[164:165], v24 offset:12800\n"
  "v_accvgpr_write_b32 a7, 0\n"
  "s_add_u32 m0, s38, 103424\n"
  "v_mfma_f32_32x32x16_bf16 v[112:127], a[140:143], a[132:135], v[112:127]\n"
  "ds_read_b64_tr_b16 a[166:167], v24 offset:13056\n"
  "buffer_load_dwordx4 v9, s[16:19], s36 offen lds\n"
  "s_waitcnt lgkmcnt(8)\n"
  "v_mfma_f32_32x32x16_bf16 v[64:79], a[144:147], a[128:131], v[64:79]\n"
  "ds_read_b64_tr_b16 a[168:169], v24 offset:13312\n"
  "v_accvgpr_write_b32 a8, 0\n"
  "s_add_u32 m0, s38, 104448\n"
  "v_mfma_f32_32x32x16_bf16 v[128:143], a[144:147], a[132:135], v[128:143]\n"
  "ds_read_b64_tr_b16 a[170:171], v24 offset:13568\n"
  "buffer_load_dwordx4 v10, s[16:19], s36 offen lds\n"
  "s_waitcnt lgkmcnt(8)\n"
  "v_mfma_f32_32x32x16_bf16 v[80:95], a[148:151], a[128:131], v[80:95]\n"
  "ds_read_b64_tr_b16 a[172:173], v24 offset:13824\n"
  "v_accvgpr_write_b32 a9, 0\n"
  "v_mfma_f32_32x32x16_bf16 v[144:159], a[148:151], a[132:135], v[144:159]\n"
  "ds_read_b64_tr_b16 a[174:175], v24 offset:14080\n"
  "buffer_store_dwordx4 v[252:255], v1, s[24:27], s72 offen sc1\n"
  "s_add_u32 s72, s72, s74\n"
  "s_waitcnt vmcnt(23)\n"
  "s_waitcnt lgkmcnt(0)\n"
  "s_barrier\n"
  "s_add_u32 m0, s38, 105472\n"
  "v_mfma_f32_32x32x16_bf16 v[32:47], a[160:163], a[152:155], v[32:47]\n"
  "ds_read_b64_tr_b16 a[136:137], v24 offset:16384\n"
  "ds_read_b64_tr_b16 a[138:139], v24 offset:16640\n"
  "buffer_load_dwordx4 v11, s[16:19], s36 offen lds\n"
  "s_add_u32 s36, s36, 128\n"
  "v_mfma_f32_32x32x16_bf16 v[96:111], a[160:163], a[156:159], v[96:111]\n"
  "ds_read_b128 a[128:131], v16 offset:32768\n"
  "ds_read_b128 a[132:135], v16 offset:36864\n"
  "v_accvgpr_write_b32 a10, 0\n"
  "s_add_u32 m0, s39, 65536\n"
  "v_mfma_f32_32x32x16_bf16 v[48:63], a[164:167], a[152:155], v[48:63]\n"
  "ds_read_b64_tr_b16 a[140:141], v24 offset:16896\n"
  "buffer_load_dwordx4 v12, s[20:23], s37 offen lds\n"
  "v_mfma_f32_32x32x16_bf16 v[112:127], a[164:167], a[156:159], v[112:127]\n"
  "ds_read_b64_tr_b16 a[142:143], v24 offset:17152\n"
  "v_accvgpr_write_b32 a11, 0\n"
  "s_add_u32 m0, s39, 66560\n"
  "v_mfma_f32_32x32x16_bf16 v[64:79], a[168:171], a[152:155], v[64:79]\n"
  "ds_read_b64_tr_b16 a[144:145], v24 offset:17408\n"
  "buffer_load_dwordx4 v13, s[20:23], s37 offen lds\n"
  "v_mfma_f32_32x32x16_bf16 v[128:143], a[168:171], a[156:159], v[128:143]\n"
  "ds_read_b64_tr_b16 a[146:147], v24 offset:17664\n"
  "v_accvgpr_write_b32 a12, 0\n"
  "s_add_u32 m0, s39, 67584\n"
  "v_mfma_f32_32x32x16_bf16 v[80:95], a[172:175], a[152:155], v[80:95]\n"
  "ds_read_b64_tr_b16 a[148:149], v24 offset:17920\n"
  "buffer_load_dwordx4 v14, s[20:23], s37 offen lds\n"
  "v_mfma_f32_32x32x16_bf16 v[144:159], a[172:175], a[156:159], v[144:159]\n"
  "ds_read_b64_tr_b16 a[150:151], v24 offset:18176\n"
  "buffer_store_dwordx4 v[160:163], v1, s[24:27], s72 offen sc1\n"
  "s_add_u32 s72, s72, s73\n"
  "s_add_u32 m0, s39, 68608\n"
  "s_waitcnt lgkmcnt(7)\n"
  "v_mfma_f32_32x32x16_bf16 v[32:47], a[136:139], a[128:131], v[32:47]\n"
  "ds_read_b64_tr_b16 a[160:161], v24 offset:20480\n"
  "ds_read_b64_tr_b16 a[162:163], v24 offset:20736\n"
  "buffer_load_dwordx4 v15, s[20:23], s37 offen lds\n"
  "s_add_u32 s37, s37, 524288\n"
  "s_waitcnt lgkmcnt(7)\n"
  "v_mfma_f32_32x32x16_bf16 v[96:111], a[136:139], a[132:135], v[96:111]\n"
  "ds_read_b128 a[152:155], v18 offset:32768\n"
  "ds_read_b128 a[156:159], v18 offset:36864\n"
  "v_accvgpr_write_b32 a13, 0\n"
  "s_add_u32 m0, s38, 0\n"
  "s_waitcnt lgkmcnt(8)\n"
  "v_mfma_f32_32x32x16_bf16 v[48:63], a[140:143], a[128:131], v[48:63]\n"
  "ds_read_b64_tr_b16 a[164:165], v24 offset:20992\n"
  "buffer_load_dwordx4 v4, s[16:19], s36 offen lds\n"
  "v_mfma_f32_32x32x16_bf16 v[112:127], a[140:143], a[132:135], v[112:127]\n"
  "ds_read_b64_tr_b16 a[166:167], v24 offset:21248\n"
  "v_accvgpr_write_b32 a14, 0\n"
  "s_add_u32 m0, s38, 1024\n"
  "s_waitcnt lgkmcnt(8)\n"
  "v_mfma_f32_32x32x16_bf16 v[64:79], a[144:147], a[128:131], v[64:79]\n"
  "ds_read_b64_tr_b16 a[168:169], v24 offset:21504\n"
  "buffer_load_dwordx4 v5, s[16:19], s36 offen lds\n"
  "s_add_u32 m0, s38, 2048\n"
  "v_mfma_f32_32x32x16_bf16 v[128:143], a[144:147], a[132:135], v[128:143]\n"
  "ds_read_b64_tr_b16 a[170:171], v24 offset:21760\n"
  "buffer_load_dwordx4 v6, s[16:19], s36 offen lds\n"
  "s_waitcnt lgkmcnt(8)\n"
  "v_mfma_f32_32x32x16_bf16 v[80:95], a[148:151], a[128:131], v[80:95]\n"
  "ds_read_b64_tr_b16 a[172:173], v24 offset:22016\n"
  "v_accvgpr_write_b32 a15, 0\n"
  "v_mfma_f32_32x32x16_bf16 v[144:159], a[148:151], a[132:135], v[144:159]\n"
  "ds_read_b64_tr_b16 a[174:175], v24 offset:22272\n"
  "buffer_store_dwordx4 v[164:167], v1, s[24:27], s72 offen sc1\n"
  "s_add_u32 s72, s72, s74\n"
  "s_add_u32 m0, s38, 3072\n"
  "s_waitcnt lgkmcnt(7)\n"
  "v_mfma_f32_32x32x16_bf16 v[32:47], a[160:163], a[152:155], v[32:47]\n"
  "ds_read_b64_tr_b16 a[136:137], v24 offset:24576\n"
  "ds_read_b64_tr_b16 a[138:139], v24 offset:24832\n"
  "buffer_load_dwordx4 v7, s[16:19], s36 offen lds\n"
  "s_waitcnt lgkmcnt(8)\n"
  "v_mfma_f32_32x32x16_bf16 v[96:111], a[160:163], a[156:159], v[96:111]\n"
  "ds_read_b128 a[128:131], v20 offset:32768\n"
  "ds_read_b128 a[132:135], v20 offset:36864\n"
  "v_accvgpr_write_b32 a16, 0\n"
  "s_waitcnt lgkmcnt(8)\n"
  "v_mfma_f32_32x32x16_bf16 v[48:63], a[164:167], a[152:155], v[48:63]\n"
  "ds_read_b64_tr_b16 a[140:141], v24 offset:25088\n"
  "v_accvgpr_write_b32 a17, 0\n"
  "s_add_u32 m0, s38, 4096\n"
  "v_mfma_f32_32x32x16_bf16 v[112:127], a[164:167], a[156:159], v[112:127]\n"
  "ds_read_b64_tr_b16 a[142:143], v24 offset:25344\n"
  "buffer_load_dwordx4 v8, s[16:19], s36 offen lds\n"
  "s_waitcnt lgkmcnt(8)\n"
  "v_mfma_f32_32x32x16_bf16 v[64:79], a[168:171], a[152:155], v[64:79]\n"
  "ds_read_b64_tr_b16 a[144:145], v24 offset:25600\n"
  "v_accvgpr_write_b32 a18, 0\n"
  "s_add_u32 m0, s38, 5120\n"
  "v_mfma_f32_32x32x16_bf16 v[128:143], a[168:171], a[156:159], v[128:143]\n"
  "ds_read_b64_tr_b16 a[146:147], v24 offset:25856\n"
  "buffer_load_dwordx4 v9, s[16:19], s36 offen lds\n"
  "s_waitcnt lgkmcnt(8)\n"
  "v_mfma_f32_32x32x16_bf16 v[80:95], a[172:175], a[152:155], v[80:95]\n"
  "ds_read_b64_tr_b16 a[148:149], v24 offset:26112\n"
  "v_accvgpr_write_b32 a19, 0\n"
  "v_mfma_f32_32x32x16_bf16 v[144:159], a[172:175], a[156:159], v[144:159]\n"
  "ds_read_b64_tr_b16 a[150:151], v24 offset:26368\n"
  "buffer_store_dwordx4 v[176:179], v1, s[24:27], s72 offen sc1\n"
  "s_add_u32 s72, s72, s73\n"
  "s_add_u32 m0, s38, 6144\n"
  "s_waitcnt lgkmcnt(7)\n"
  "v_mfma_f32_32x32x16_bf16 v[32:47], a[136:139], a[128:131], v[32:47]\n"
  "ds_read_b64_tr_b16 a[160:161], v24 offset:28672\n"
  "ds_read_b64_tr_b16 a[162:163], v24 offset:28928\n"
  "buffer_load_dwordx4 v10, s[16:19], s36 offen lds\n"
  "s_waitcnt lgkmcnt(8)\n"
  "v_mfma_f32_32x32x16_bf16 v[96:111], a[136:139], a[132:135], v[96:111]\n"
  "ds_read_b128 a[152:155], v22 offset:32768\n"
  "ds_read_b128 a[156:159], v22 offset:36864\n"
  "v_accvgpr_write_b32 a20, 0\n"
  "s_waitcnt lgkmcnt(8)\n"
  "v_mfma_f32_32x32x16_bf16 v[48:63], a[140:143], a[128:131], v[48:63]\n"
  "ds_read_b64_tr_b16 a[164:165], v24 offset:29184\n"
  "v_accvgpr_write_b32 a21, 0\n"
  "s_add_u32 m0, s38, 7168\n"
  "v_mfma_f32_32x32x16_bf16 v[112:127], a[140:143], a[132:135], v[112:127]\n"
  "ds_read_b64_tr_b16 a[166:167], v24 offset:29440\n"
  "buffer_load_dwordx4 v11, s[16:19], s36 offen lds\n"
  "s_add_u32 s36, s36, 128\n"
  "s_waitcnt lgkmcnt(8)\n"
  "v_mfma_f32_32x32x16_bf16 v[64:79], a[144:147], a[128:131], v[64:79]\n"
  "ds_read_b64_tr_b16 a[168:169], v24 offset:29696\n"
  "v_accvgpr_write_b32 a22, 0\n"
  "v_mfma_f32_32x32x16_bf16 v[128:143], a[144:147], a[132:135], v[128:143]\n"
  "ds_read_b64_tr_b16 a[170:171], v24 offset:29952\n"
  "v_accvgpr_write_b32 a23, 0\n"
  "s_waitcnt lgkmcnt(8)\n"
  "v_mfma_f32_32x32x16_bf16 v[80:95], a[148:151], a[128:131], v[80:95]\n"
  "ds_read_b64_tr_b16 a[172:173], v24 offset:30208\n"
  "v_accvgpr_write_b32 a24, 0\n"
  "v_mfma_f32_32x32x16_bf16 v[144:159], a[148:151], a[132:135], v[144:159]\n"
  "ds_read_b64_tr_b16 a[174:175], v24 offset:30464\n"
  "buffer_store_dwordx4 v[180:183], v1, s[24:27], s72 offen sc1\n"
  "s_add_u32 s72, s72, s75\n"
  "s_waitcnt vmcnt(16)\n"
  "s_waitcnt lgkmcnt(0)\n"
  "s_barrier\n"
  "s_add_u32 m0, s39, 81920\n"
  "v_mfma_f32_32x32x16_bf16 v[32:47], a[160:163], a[152:155], v[32:47]\n"
  "ds_read_b64_tr_b16 a[136:137], v25 offset:0\n"
  "ds_read_b64_tr_b16 a[138:139], v25 offset:256\n"
  "buffer_load_dwordx4 v12, s[20:23], s37 offen lds\n"
  "v_mfma_f32_32x32x16_bf16 v[96:111], a[160:163], a[156:159], v[96:111]\n"
  "ds_read_b128 a[128:131], v17 offset:0\n"
  "ds_read_b128 a[132:135], v17 offset:4096\n"
  "v_accvgpr_write_b32 a25, 0\n"
  "s_add_u32 m0, s39, 82944\n"
  "v_mfma_f32_32x32x16_bf16 v[48:63], a[164:167], a[152:155], v[48:63]\n"
  "ds_read_b64_tr_b16 a[140:141], v25 offset:512\n"
  "buffer_load_dwordx4 v13, s[20:23], s37 offen lds\n"
  "v_mfma_f32_32x32x16_bf16 v[112:127], a[164:167], a[156:159], v[112:127]\n"
  "ds_read_b64_tr_b16 a[142:143], v25 offset:768\n"
  "v_accvgpr_write_b32 a26, 0\n"
  "s_add_u32 m0, s39, 83968\n"
  "v_mfma_f32_32x32x16_bf16 v[64:79], a[168:171], a[152:155], v[64:79]\n"
  "ds_read_b64_tr_b16 a[144:145], v25 offset:1024\n"
  "buffer_load_dwordx4 v14, s[20:23], s37 offen lds\n"
  "v_mfma_f32_32x32x16_bf16 v[128:143], a[168:171], a[156:159], v[128:143]\n"
  "ds_read_b64_tr_b16 a[146:147], v25 offset:1280\n"
  "v_accvgpr_write_b32 a27, 0\n"
  "s_add_u32 m0, s39, 84992\n"
  "v_mfma_f32_32x32x16_bf16 v[80:95], a[172:175], a[152:155], v[80:95]\n"
  "ds_read_b64_tr_b16 a[148:149], v25 offset:1536\n"
  "buffer_load_dwordx4 v15, s[20:23], s37 offen lds\n"
  "s_add_u32 s37, s37, 524288\n"
  "v_mfma_f32_32x32x16_bf16 v[144:159], a[172:175], a[156:159], v[144:159]\n"
  "ds_read_b64_tr_b16 a[150:151], v25 offset:1792\n"
  "buffer_store_dwordx4 v[192:195], v1, s[24:27], s72 offen sc1\n"
  "s_add_u32 s72, s72, s73\n"
  "s_add_u32 m0, s38, 32768\n"
  "s_waitcnt lgkmcnt(6)\n"
  "v_mfma_f32_32x32x16_bf16 v[32:47], a[136:139], a[128:131], v[32:47]\n"
  "ds_read_b64_tr_b16 a[160:161], v25 offset:4096\n"
  "ds_read_b64_tr_b16 a[162:163], v25 offset:4352\n"
  "buffer_load_dwordx4 v4, s[16:19], s36 offen lds\n"
  "v_mfma_f32_32x32x16_bf16 v[96:111], a[136:139], a[132:135], v[96:111]\n"
  "ds_read_b128 a[152:155], v19 offset:0\n"
  "ds_read_b128 a[156:159], v19 offset:4096\n"
  "v_accvgpr_write_b32 a28, 0\n"
  "s_add_u32 m0, s38, 33792\n"
  "s_waitcnt lgkmcnt(8)\n"
  "v_mfma_f32_32x32x16_bf16 v[48:63], a[140:143], a[128:131], v[48:63]\n"
  "ds_read_b64_tr_b16 a[164:165], v25 offset:4608\n"
  "buffer_load_dwordx4 v5, s[16:19], s36 offen lds\n"
  "s_add_u32 m0, s38, 34816\n"
  "v_mfma_f32_32x32x16_bf16 v[112:127], a[140:143], a[132:135], v[112:127]\n"
  "ds_read_b64_tr_b16 a[166:167], v25 offset:4864\n"
  "buffer_load_dwordx4 v6, s[16:19], s36 offen lds\n"
  "s_waitcnt lgkmcnt(8)\n"
  "v_mfma_f32_32x32x16_bf16 v[64:79], a[144:147], a[128:131], v[64:79]\n"
  "ds_read_b64_tr_b16 a[168:169], v25 offset:5120\n"
  "v_accvgpr_write_b32 a29, 0\n"
  "s_add_u32 m0, s38, 35840\n"
  "v_mfma_f32_32x32x16_bf16 v[128:143], a[144:147], a[132:135], v[128:143]\n"
  "ds_read_b64_tr_b16 a[170:171], v25 offset:5376\n"
  "buffer_load_dwordx4 v7, s[16:19], s36 offen lds\n"
  "s_waitcnt lgkmcnt(8)\n"
  "v_mfma_f32_32x32x16_bf16 v[80:95], a[148:151], a[128:131], v[80:95]\n"
  "ds_read_b64_tr_b16 a[172:173], v25 offset:5632\n"
  "v_accvgpr_write_b32 a30, 0\n"
  "v_mfma_f32_32x32x16_bf16 v[144:159], a[148:151], a[132:135], v[144:159]\n"
  "ds_read_b64_tr_b16 a[174:175], v25 offset:5888\n"
  "buffer_store_dwordx4 v[196:199], v1, s[24:27], s72 offen sc1\n"
  "s_add_u32 s72, s72, s74\n"
  "s_add_u32 m0, s38, 36864\n"
  "s_waitcnt lgkmcnt(7)\n"
  "v_mfma_f32_32x32x16_bf16 v[32:47], a[160:163], a[152:155], v[32:47]\n"
  "ds_read_b64_tr_b16 a[136:137], v25 offset:8192\n"
  "ds_read_b64_tr_b16 a[138:139], v25 offset:8448\n"
  "buffer_load_dwordx4 v8, s[16:19], s36 offen lds\n"
  "s_waitcnt lgkmcnt(8)\n"
  "v_mfma_f32_32x32x16_bf16 v[96:111], a[160:163], a[156:159], v[96:111]\n"
  "ds_read_b128 a[128:131], v21 offset:0\n"
  "ds_read_b128 a[132:135], v21 offset:4096\n"
  "v_accvgpr_write_b32 a31, 0\n"
  "s_waitcnt lgkmcnt(8)\n"
  "v_mfma_f32_32x32x16_bf16 v[48:63], a[164:167], a[152:155], v[48:63]\n"
  "ds_read_b64_tr_b16 a[140:141], v25 offset:8704\n"
  "v_accvgpr_write_b32 a32, 0\n"
  "s_add_u32 m0, s38, 37888\n"
  "v_mfma_f32_32x32x16_bf16 v[112:127], a[164:167], a[156:159], v[112:127]\n"
  "ds_read_b64_tr_b16 a[142:143], v25 offset:8960\n"
  "buffer_load_dwordx4 v9, s[16:19], s36 offen lds\n"
  "s_waitcnt lgkmcnt(8)\n"
  "v_mfma_f32_32x32x16_bf16 v[64:79], a[168:171], a[152:155], v[64:79]\n"
  "ds_read_b64_tr_b16 a[144:145], v25 offset:9216\n"
  "v_accvgpr_write_b32 a33, 0\n"
  "s_add_u32 m0, s38, 38912\n"
  "v_mfma_f32_32x32x16_bf16 v[128:143], a[168:171], a[156:159], v[128:143]\n"
  "ds_read_b64_tr_b16 a[146:147], v25 offset:9472\n"
  "buffer_load_dwordx4 v10, s[16:19], s36 offen lds\n"
  "s_waitcnt lgkmcnt(8)\n"
  "v_mfma_f32_32x32x16_bf16 v[80:95], a[172:175], a[152:155], v[80:95]\n"
  "ds_read_b64_tr_b16 a[148:149], v25 offset:9728\n"
  "v_accvgpr_write_b32 a34, 0\n"
  "v_mfma_f32_32x32x16_bf16 v[144:159], a[172:175], a[156:159], v[144:159]\n"
  "ds_read_b64_tr_b16 a[150:151], v25 offset:9984\n"
  "buffer_store_dwordx4 v[208:211], v1, s[24:27], s72 offen sc1\n"
  "s_add_u32 s72, s72, s73\n"
  "s_add_u32 m0, s38, 39936\n"
  "s_waitcnt lgkmcnt(7)\n"
  "v_mfma_f32_32x32x16_bf16 v[32:47], a[136:139], a[128:131], v[32:47]\n"
  "ds_read_b64_tr_b16 a[160:161], v25 offset:12288\n"
  "ds_read_b64_tr_b16 a[162:163], v25 offset:12544\n"
  "buffer_load_dwordx4 v11, s[16:19], s36 offen lds\n"
  "s_add_u32 s36, s36, 128\n"
  "s_waitcnt lgkmcnt(7)\n"
  "v_mfma_f32_32x32x16_bf16 v[96:111], a[136:139], a[132:135], v[96:111]\n"
  "ds_read_b128 a[152:155], v23 offset:0\n"
  "ds_read_b128 a[156:159], v23 offset:4096\n"
  "v_accvgpr_write_b32 a35, 0\n"
  "s_waitcnt lgkmcnt(8)\n"
  "v_mfma_f32_32x32x16_bf16 v[48:63], a[140:143], a[128:131], v[48:63]\n"
  "ds_read_b64_tr_b16 a[164:165], v25 offset:12800\n"
  "v_accvgpr_write_b32 a36, 0\n"
  "v_mfma_f32_32x32x16_bf16 v[112:127], a[140:143], a[132:135], v[112:127]\n"
  "ds_read_b64_tr_b16 a[166:167], v25 offset:13056\n"
  "v_accvgpr_write_b32 a37, 0\n"
  "s_waitcnt lgkmcnt(8)\n"
  "v_mfma_f32_32x32x16_bf16 v[64:79], a[144:147], a[128:131], v[64:79]\n"
  "ds_read_b64_tr_b16 a[168:169], v25 offset:13312\n"
  "v_accvgpr_write_b32 a38, 0\n"
  "v_mfma_f32_32x32x16_bf16 v[128:143], a[144:147], a[132:135], v[128:143]\n"
  "ds_read_b64_tr_b16 a[170:171], v25 offset:13568\n"
  "v_accvgpr_write_b32 a39, 0\n"
  "s_waitcnt lgkmcnt(8)\n"
  "v_mfma_f32_32x32x16_bf16 v[80:95], a[148:151], a[128:131], v[80:95]\n"
  "ds_read_b64_tr_b16 a[172:173], v25 offset:13824\n"
  "v_accvgpr_write_b32 a40, 0\n"
  "v_mfma_f32_32x32x16_bf16 v[144:159], a[148:151], a[132:135], v[144:159]\n"
  "ds_read_b64_tr_b16 a[174:175], v25 offset:14080\n"
  "buffer_store_dwordx4 v[212:215], v1, s[24:27], s72 offen sc1\n"
  "s_add_u32 s72, s72, s74\n"
  "s_waitcnt vmcnt(17)\n"
  "s_waitcnt lgkmcnt(0)\n"
  "s_barrier\n"
  "s_add_u32 m0, s39, 131072\n"
  "v_mfma_f32_32x32x16_bf16 v[32:47], a[160:163], a[152:155], v[32:47]\n"
  "ds_read_b64_tr_b16 a[136:137], v24 offset:0\n"
  "ds_read_b64_tr_b16 a[138:139], v24 offset:256\n"
  "buffer_load_dwordx4 v12, s[20:23], s37 offen lds\n"
  "v_mfma_f32_32x32x16_bf16 v[96:111], a[160:163], a[156:159], v[96:111]\n"
  "ds_read_b128 a[128:131], v16 offset:0\n"
  "ds_read_b128 a[132:135], v16 offset:4096\n"
  "v_accvgpr_write_b32 a41, 0\n"
  "s_add_u32 m0, s39, 132096\n"
  "v_mfma_f32_32x32x16_bf16 v[48:63], a[164:167], a[152:155], v[48:63]\n"
  "ds_read_b64_tr_b16 a[140:141], v24 offset:512\n"
  "buffer_load_dwordx4 v13, s[20:23], s37 offen lds\n"
  "v_mfma_f32_32x32x16_bf16 v[112:127], a[164:167], a[156:159], v[112:127]\n"
  "ds_read_b64_tr_b16 a[142:143], v24 offset:768\n"
  "v_accvgpr_write_b32 a42, 0\n"
  "s_add_u32 m0, s39, 133120\n"
  "v_mfma_f32_32x32x16_bf16 v[64:79], a[168:171], a[152:155], v[64:79]\n"
  "ds_read_b64_tr_b16 a[144:145], v24 offset:1024\n"
  "buffer_load_dwordx4 v14, s[20:23], s37 offen lds\n"
  "v_mfma_f32_32x32x16_bf16 v[128:143], a[168:171], a[156:159], v[128:143]\n"
  "ds_read_b64_tr_b16 a[146:147], v24 offset:1280\n"
  "v_accvgpr_write_b32 a43, 0\n"
  "s_add_u32 m0, s39, 134144\n"
  "v_mfma_f32_32x32x16_bf16 v[80:95], a[172:175], a[152:155], v[80:95]\n"
  "ds_read_b64_tr_b16 a[148:149], v24 offset:1536\n"
  "buffer_load_dwordx4 v15, s[20:23], s37 offen lds\n"
  "s_add_u32 s37, s37, 524288\n"
  "v_mfma_f32_32x32x16_bf16 v[144:159], a[172:175], a[156:159], v[144:159]\n"
  "ds_read_b64_tr_b16 a[150:151], v24 offset:1792\n"
  "buffer_store_dwordx4 v[224:227], v1, s[24:27], s72 offen sc1\n"
  "s_add_u32 s72, s72, s73\n"
  "s_add_u32 m0, s38, 98304\n"
  "s_waitcnt lgkmcnt(6)\n"
  "v_mfma_f32_32x32x16_bf16 v[32:47], a[136:139], a[128:131], v[32:47]\n"
  "ds_read_b64_tr_b16 a[160:161], v24 offset:4096\n"
  "ds_read_b64_tr_b16 a[162:163], v24 offset:4352\n"
  "buffer_load_dwordx4 v4, s[16:19], s36 offen lds\n"
  "v_mfma_f32_32x32x16_bf16 v[96:111], a[136:139], a[132:135], v[96:111]\n"
  "ds_read_b128 a[152:155], v18 offset:0\n"
  "ds_read_b128 a[156:159], v18 offset:4096\n"
  "v_accvgpr_write_b32 a44, 0\n"
  "s_add_u32 m0, s38, 99328\n"
  "s_waitcnt lgkmcnt(8)\n"
  "v_mfma_f32_32x32x16_bf16 v[48:63], a[140:143], a[128:131], v[48:63]\n"
  "ds_read_b64_tr_b16 a[164:165], v24 offset:4608\n"
  "buffer_load_dwordx4 v5, s[16:19], s36 offen lds\n"
  "s_add_u32 m0, s38, 100352\n"
  "v_mfma_f32_32x32x16_bf16 v[112:127], a[140:143], a[132:135], v[112:127]\n"
  "ds_read_b64_tr_b16 a[166:167], v24 offset:4864\n"
  "buffer_load_dwordx4 v6, s[16:19], s36 offen lds\n"
  "s_waitcnt lgkmcnt(8)\n"
  "v_mfma_f32_32x32x16_bf16 v[64:79], a[144:147], a[128:131], v[64:79]\n"
  "ds_read_b64_tr_b16 a[168:169], v24 offset:5120\n"
  "v_accvgpr_write_b32 a45, 0\n"
  "s_add_u32 m0, s38, 101376\n"
  "v_mfma_f32_32x32x16_bf16 v[128:143], a[144:147], a[132:135], v[128:143]\n"
  "ds_read_b64_tr_b16 a[170:171], v24 offset:5376\n"
  "buffer_load_dwordx4 v7, s[16:19], s36 offen lds\n"
  "s_waitcnt lgkmcnt(8)\n"
  "v_mfma_f32_32x32x16_bf16 v[80:95], a[148:151], a[128:131], v[80:95]\n"
  "ds_read_b64_tr_b16 a[172:173], v24 offset:5632\n"
  "v_accvgpr_write_b32 a46, 0\n"
  "v_mfma_f32_32x32x16_bf16 v[144:159], a[148:151], a[132:135], v[144:159]\n"
  "ds_read_b64_tr_b16 a[174:175], v24 offset:5888\n"
  "buffer_store_dwordx4 v[228:231], v1, s[24:27], s72 offen sc1\n"
  "s_add_u32 s72, s72, s74\n"
  "s_add_u32 m0, s38, 102400\n"
  "s_waitcnt lgkmcnt(7)\n"
  "v_mfma_f32_32x32x16_bf16 v[32:47], a[160:163], a[152:155], v[32:47]\n"
  "ds_read_b64_tr_b16 a[136:137], v24 offset:8192\n"
  "ds_read_b64_tr_b16 a[138:139], v24 offset:8448\n"
  "buffer_load_dwordx4 v8, s[16:19], s36 offen lds\n"
  "s_waitcnt lgkmcnt(8)\n"
  "v_mfma_f32_32x32x16_bf16 v[96:111], a[160:163], a[156:159], v[96:111]\n"
  "ds_read_b128 a[128:131], v20 offset:0\n"
  "ds_read_b128 a[132:135], v20 offset:4096\n"
  "v_accvgpr_write_b32 a47, 0\n"
  "s_waitcnt lgkmcnt(8)\n"
  "v_mfma_f32_32x32x16_bf16 v[48:63], a[164:167], a[152:155], v[48:63]\n"
  "ds_read_b64_tr_b16 a[140:141], v24 offset:8704\n"
  "v_accvgpr_write_b32 a48, 0\n"
  "s_add_u32 m0, s38, 103424\n"
  "v_mfma_f32_32x32x16_bf16 v[112:127], a[164:167], a[156:159], v[112:127]\n"
  "ds_read_b64_tr_b16 a[142:143], v24 offset:8960\n"
  "buffer_load_dwordx4 v9, s[16:19], s36 offen lds\n"
  "s_waitcnt lgkmcnt(8)\n"
  "v_mfma_f32_32x32x16_bf16 v[64:79], a[168:171], a[152:155], v[64:79]\n"
  "ds_read_b64_tr_b16 a[144:145], v24 offset:9216\n"
  "v_accvgpr_write_b32 a49, 0\n"
  "s_add_u32 m0, s38, 104448\n"
  "v_mfma_f32_32x32x16_bf16 v[128:143], a[168:171], a[156:159], v[128:143]\n"
  "ds_read_b64_tr_b16 a[146:147], v24 offset:9472\n"
  "buffer_load_dwordx4 v10, s[16:19], s36 offen lds\n"
  "s_waitcnt lgkmcnt(8)\n"
  "v_mfma_f32_32x32x16_bf16 v[80:95], a[172:175], a[152:155], v[80:95]\n"
  "ds_read_b64_tr_b16 a[148:149], v24 offset:9728\n"
  "v_accvgpr_write_b32 a50, 0\n"
  "v_mfma_f32_32x32x16_bf16 v[144:159], a[172:175], a[156:159], v[144:159]\n"
  "ds_read_b64_tr_b16 a[150:151], v24 offset:9984\n"
  "buffer_store_dwordx4 v[240:243], v1, s[24:27], s72 offen sc1\n"
  "s_add_u32 s72, s72, s73\n"
  "s_add_u32 m0, s38, 105472\n"
  "s_waitcnt lgkmcnt(7)\n"
  "v_mfma_f32_32x32x16_bf16 v[32:47], a[136:139], a[128:131], v[32:47]\n"
  "ds_read_b64_tr_b16 a[160:161], v24 offset:12288\n"
  "ds_read_b64_tr_b16 a[162:163], v24 offset:12544\n"
  "buffer_load_dwordx4 v11, s[16:19], s36 offen lds\n"
  "s_add_u32 s36, s36, 128\n"
  "s_waitcnt lgkmcnt(7)\n"
  "v_mfma_f32_32x32x16_bf16 v[96:111], a[136:139], a[132:135], v[96:111]\n"
  "ds_read_b128 a[152:155], v22 offset:0\n"
  "ds_read_b128 a[156:159], v22 offset:4096\n"
  "v_accvgpr_write_b32 a51, 0\n"
  "s_waitcnt lgkmcnt(8)\n"
  "v_mfma_f32_32x32x16_bf16 v[48:63], a[140:143], a[128:131], v[48:63]\n"
  "ds_read_b64_tr_b16 a[164:165], v24 offset:12800\n"
  "v_accvgpr_write_b32 a52, 0\n"
  "v_mfma_f32_32x32x16_bf16 v[112:127], a[140:143], a[132:135], v[112:127]\n"
  "ds_read_b64_tr_b16 a[166:167], v24 offset:13056\n"
  "v_accvgpr_write_b32 a53, 0\n"
  "s_waitcnt lgkmcnt(8)\n"
  "v_mfma_f32_32x32x16_bf16 v[64:79], a[144:147], a[128:131], v[64:79]\n"
  "ds_read_b64_tr_b16 a[168:169], v24 offset:13312\n"
  "v_accvgpr_write_b32 a54, 0\n"
  "v_mfma_f32_32x32x16_bf16 v[128:143], a[144:147], a[132:135], v[128:143]\n"
  "ds_read_b64_tr_b16 a[170:171], v24 offset:13568\n"
  "v_accvgpr_write_b32 a55, 0\n"
  "s_waitcnt lgkmcnt(8)\n"
  "v_mfma_f32_32x32x16_bf16 v[80:95], a[148:151], a[128:131], v[80:95]\n"
  "ds_read_b64_tr_b16 a[172:173], v24 offset:13824\n"
  "v_accvgpr_write_b32 a56, 0\n"
  "v_mfma_f32_32x32x16_bf16 v[144:159], a[148:151], a[132:135], v[144:159]\n"
  "ds_read_b64_tr_b16 a[174:175], v24 offset:14080\n"
  "buffer_store_dwordx4 v[244:247], v1, s[24:27], s72 offen sc1\n"
  "s_add_u32 s72, s72, s75\n"
  "s_waitcnt vmcnt(17)\n"
  "s_waitcnt lgkmcnt(0)\n"
  "s_barrier\n"
  "s_add_u32 m0, s39, 65536\n"
  "v_mfma_f32_32x32x16_bf16 v[32:47], a[160:163], a[152:155], v[32:47]\n"
  "ds_read_b64_tr_b16 a[136:137], v24 offset:16384\n"
  "ds_read_b64_tr_b16 a[138:139], v24 offset:16640\n"
  "buffer_load_dwordx4 v12, s[20:23], s37 offen lds\n"
  "v_mfma_f32_32x32x16_bf16 v[96:111], a[160:163], a[156:159], v[96:111]\n"
  "ds_read_b128 a[128:131], v16 offset:32768\n"
  "ds_read_b128 a[132:135], v16 offset:36864\n"
  "v_accvgpr_write_b32 a57, 0\n"
  "s_add_u32 m0, s39, 66560\n"
  "v_mfma_f32_32x32x16_bf16 v[48:63], a[164:167], a[152:155], v[48:63]\n"
  "ds_read_b64_tr_b16 a[140:141], v24 offset:16896\n"
  "buffer_load_dwordx4 v13, s[20:23], s37 offen lds\n"
  "v_mfma_f32_32x32x16_bf16 v[112:127], a[164:167], a[156:159], v[112:127]\n"
  "ds_read_b64_tr_b16 a[142:143], v24 offset:17152\n"
  "v_accvgpr_write_b32 a58, 0\n"
  "s_add_u32 m0, s39, 67584\n"
  "v_mfma_f32_32x32x16_bf16 v[64:79], a[168:171], a[152:155], v[64:79]\n"
  "ds_read_b64_tr_b16 a[144:145], v24 offset:17408\n"
  "buffer_load_dwordx4 v14, s[20:23], s37 offen lds\n"
  "v_mfma_f32_32x32x16_bf16 v[128:143], a[168:171], a[156:159], v[128:143]\n"
  "ds_read_b64_tr_b16 a[146:147], v24 offset:17664\n"
  "v_accvgpr_write_b32 a59, 0\n"
  "s_add_u32 m0, s39, 68608\n"
  "v_mfma_f32_32x32x16_bf16 v[80:95], a[172:175], a[152:155], v[80:95]\n"
  "ds_read_b64_tr_b16 a[148:149], v24 offset:17920\n"
  "buffer_load_dwordx4 v15, s[20:23], s37 offen lds\n"
  "s_add_u32 s37, s37, 524288\n"
  "v_mfma_f32_32x32x16_bf16 v[144:159], a[172:175], a[156:159], v[144:159]\n"
  "ds_read_b64_tr_b16 a[150:151], v24 offset:18176\n"
  "buffer_store_dwordx4 v[168:171], v1, s[24:27], s72 offen sc1\n"
  "s_add_u32 s72, s72, s73\n"
  "s_add_u32 m0, s38, 0\n"
  "s_waitcnt lgkmcnt(6)\n"
  "v_mfma_f32_32x32x16_bf16 v[32:47], a[136:139], a[128:131], v[32:47]\n"
  "ds_read_b64_tr_b16 a[160:161], v24 offset:20480\n"
  "ds_read_b64_tr_b16 a[162:163], v24 offset:20736\n"
  "buffer_load_dwordx4 v4, s[16:19], s36 offen lds\n"
  "v_mfma_f32_32x32x16_bf16 v[96:111], a[136:139], a[132:135], v[96:111]\n"
  "ds_read_b128 a[152:155], v18 offset:32768\n"
  "ds_read_b128 a[156:159], v18 offset:36864\n"
  "v_accvgpr_write_b32 a60, 0\n"
  "s_add_u32 m0, s38, 1024\n"
  "s_waitcnt lgkmcnt(8)\n"
  "v_mfma_f32_32x32x16_bf16 v[48:63], a[140:143], a[128:131], v[48:63]\n"
  "ds_read_b64_tr_b16 a[164:165], v24 offset:20992\n"
  "buffer_load_dwordx4 v5, s[16:19], s36 offen lds\n"
  "s_add_u32 m0, s38, 2048\n"
  "v_mfma_f32_32x32x16_bf16 v[112:127], a[140:143], a[132:135], v[112:127]\n"
  "ds_read_b64_tr_b16 a[166:167], v24 offset:21248\n"
  "buffer_load_dwordx4 v6, s[16:19], s36 offen lds\n"
  "s_waitcnt lgkmcnt(8)\n"
  "v_mfma_f32_32x32x16_bf16 v[64:79], a[144:147], a[128:131], v[64:79]\n"
  "ds_read_b64_tr_b16 a[168:169], v24 offset:21504\n"
  "v_accvgpr_write_b32 a61, 0\n"
  "s_add_u32 m0, s38, 3072\n"
  "v_mfma_f32_32x32x16_bf16 v[128:143], a[144:147], a[132:135], v[128:143]\n"
  "ds_read_b64_tr_b16 a[170:171], v24 offset:21760\n"
  "buffer_load_dwordx4 v7, s[16:19], s36 offen lds\n"
  "s_waitcnt lgkmcnt(8)\n"
  "v_mfma_f32_32x32x16_bf16 v[80:95], a[148:151], a[128:131], v[80:95]\n"
  "ds_read_b64_tr_b16 a[172:173], v24 offset:22016\n"
  "v_accvgpr_write_b32 a62, 0\n"
  "v_mfma_f32_32x32x16_bf16 v[144:159], a[148:151], a[132:135], v[144:159]\n"
  "ds_read_b64_tr_b16 a[174:175], v24 offset:22272\n"
  "buffer_store_dwordx4 v[172:175], v1, s[24:27], s72 offen sc1\n"
  "s_add_u32 s72, s72, s74\n"
  "s_add_u32 m0, s38, 4096\n"
  "s_waitcnt lgkmcnt(7)\n"
  "v_mfma_f32_32x32x16_bf16 v[32:47], a[160:163], a[152:155], v[32:47]\n"
  "ds_read_b64_tr_b16 a[136:137], v24 offset:24576\n"
  "ds_read_b64_tr_b16 a[138:139], v24 offset:24832\n"
  "buffer_load_dwordx4 v8, s[16:19], s36 offen lds\n"
  "s_waitcnt lgkmcnt(8)\n"
  "v_mfma_f32_32x32x16_bf16 v[96:111], a[160:163], a[156:159], v[96:111]\n"
  "ds_read_b128 a[128:131], v20 offset:32768\n"
  "ds_read_b128 a[132:135], v20 offset:36864\n"
  "v_accvgpr_write_b32 a63, 0\n"
  "s_waitcnt lgkmcnt(8)\n"
  "v_mfma_f32_32x32x16_bf16 v[48:63], a[164:167], a[152:155], v[48:63]\n"
  "ds_read_b64_tr_b16 a[140:141], v24 offset:25088\n"
  "v_accvgpr_write_b32 a64, 0\n"
  "s_add_u32 m0, s38, 5120\n"
  "v_mfma_f32_32x32x16_bf16 v[112:127], a[164:167], a[156:159], v[112:127]\n"
  "ds_read_b64_tr_b16 a[142:143], v24 offset:25344\n"
  "buffer_load_dwordx4 v9, s[16:19], s36 offen lds\n"
  "s_waitcnt lgkmcnt(8)\n"
  "v_mfma_f32_32x32x16_bf16 v[64:79], a[168:171], a[152:155], v[64:79]\n"
  "ds_read_b64_tr_b16 a[144:145], v24 offset:25600\n"
  "v_accvgpr_write_b32 a65, 0\n"
  "s_add_u32 m0, s38, 6144\n"
  "v_mfma_f32_32x32x16_bf16 v[128:143], a[168:171], a[156:159], v[128:143]\n"
  "ds_read_b64_tr_b16 a[146:147], v24 offset:25856\n"
  "buffer_load_dwordx4 v10, s[16:19], s36 offen lds\n"
  "s_waitcnt lgkmcnt(8)\n"
  "v_mfma_f32_32x32x16_bf16 v[80:95], a[172:175], a[152:155], v[80:95]\n"
  "ds_read_b64_tr_b16 a[148:149], v24 offset:26112\n"
  "v_accvgpr_write_b32 a66, 0\n"
  "v_mfma_f32_32x32x16_bf16 v[144:159], a[172:175], a[156:159], v[144:159]\n"
  "ds_read_b64_tr_b16 a[150:151], v24 offset:26368\n"
  "buffer_store_dwordx4 v[184:187], v1, s[24:27], s72 offen sc1\n"
  "s_add_u32 s72, s72, s73\n"
  "s_add_u32 m0, s38, 7168\n"
  "s_waitcnt lgkmcnt(7)\n"
  "v_mfma_f32_32x32x16_bf16 v[32:47], a[136:139], a[128:131], v[32:47]\n"
  "ds_read_b64_tr_b16 a[160:161], v24 offset:28672\n"
  "ds_read_b64_tr_b16 a[162:163], v24 offset:28928\n"
  "buffer_load_dwordx4 v11, s[16:19], s36 offen lds\n"
  "s_add_u32 s36, s36, 128\n"
  "s_waitcnt lgkmcnt(7)\n"
  "v_mfma_f32_32x32x16_bf16 v[96:111], a[136:139], a[132:135], v[96:111]\n"
  "ds_read_b128 a[152:155], v22 offset:32768\n"
  "ds_read_b128 a[156:159], v22 offset:36864\n"
  "v_accvgpr_write_b32 a67, 0\n"
  "s_waitcnt lgkmcnt(8)\n"
  "v_mfma_f32_32x32x16_bf16 v[48:63], a[140:143], a[128:131], v[48:63]\n"
  "ds_read_b64_tr_b16 a[164:165], v24 offset:29184\n"
  "v_accvgpr_write_b32 a68, 0\n"
  "v_mfma_f32_32x32x16_bf16 v[112:127], a[140:143], a[132:135], v[112:127]\n"
  "ds_read_b64_tr_b16 a[166:167], v24 offset:29440\n"
  "v_accvgpr_write_b32 a69, 0\n"
  "s_waitcnt lgkmcnt(8)\n"
  "v_mfma_f32_32x32x16_bf16 v[64:79], a[144:147], a[128:131], v[64:79]\n"
  "ds_read_b64_tr_b16 a[168:169], v24 offset:29696\n"
  "v_accvgpr_write_b32 a70, 0\n"
  "v_mfma_f32_32x32x16_bf16 v[128:143], a[144:147], a[132:135], v[128:143]\n"
  "ds_read_b64_tr_b16 a[170:171], v24 offset:29952\n"
  "v_accvgpr_write_b32 a71, 0\n"
  "s_waitcnt lgkmcnt(8)\n"
  "v_mfma_f32_32x32x16_bf16 v[80:95], a[148:151], a[128:131], v[80:95]\n"
  "ds_read_b64_tr_b16 a[172:173], v24 offset:30208\n"
  "v_accvgpr_write_b32 a72, 0\n"
  "v_mfma_f32_32x32x16_bf16 v[144:159], a[148:151], a[132:135], v[144:159]\n"
  "ds_read_b64_tr_b16 a[174:175], v24 offset:30464\n"
  "buffer_store_dwordx4 v[188:191], v1, s[24:27], s72 offen sc1\n"
  "s_add_u32 s72, s72, s74\n"
  "s_waitcnt vmcnt(17)\n"
  "s_waitcnt lgkmcnt(0)\n"
  "s_barrier\n"
  "s_add_u32 m0, s39, 81920\n"
  "v_mfma_f32_32x32x16_bf16 v[32:47], a[160:163], a[152:155], v[32:47]\n"
  "ds_read_b64_tr_b16 a[136:137], v25 offset:0\n"
  "ds_read_b64_tr_b16 a[138:139], v25 offset:256\n"
  "buffer_load_dwordx4 v12, s[20:23], s37 offen lds\n"
  "v_mfma_f32_32x32x16_bf16 v[96:111], a[160:163], a[156:159], v[96:111]\n"
  "ds_read_b128 a[128:131], v17 offset:0\n"
  "ds_read_b128 a[132:135], v17 offset:4096\n"
  "v_accvgpr_write_b32 a73, 0\n"
  "s_add_u32 m0, s39, 82944\n"
  "v_mfma_f32_32x32x16_bf16 v[48:63], a[164:167], a[152:155], v[48:63]\n"
  "ds_read_b64_tr_b16 a[140:141], v25 offset:512\n"
  "buffer_load_dwordx4 v13, s[20:23], s37 offen lds\n"
  "v_mfma_f32_32x32x16_bf16 v[112:127], a[164:167], a[156:159], v[112:127]\n"
  "ds_read_b64_tr_b16 a[142:143], v25 offset:768\n"
  "v_accvgpr_write_b32 a74, 0\n"
  "s_add_u32 m0, s39, 83968\n"
  "v_mfma_f32_32x32x16_bf16 v[64:79], a[168:171], a[152:155], v[64:79]\n"
  "ds_read_b64_tr_b16 a[144:145], v25 offset:1024\n"
  "buffer_load_dwordx4 v14, s[20:23], s37 offen lds\n"
  "v_mfma_f32_32x32x16_bf16 v[128:143], a[168:171], a[156:159], v[128:143]\n"
  "ds_read_b64_tr_b16 a[146:147], v25 offset:1280\n"
  "v_accvgpr_write_b32 a75, 0\n"
  "s_add_u32 m0, s39, 84992\n"
  "v_mfma_f32_32x32x16_bf16 v[80:95], a[172:175], a[152:155], v[80:95]\n"
  "ds_read_b64_tr_b16 a[148:149], v25 offset:1536\n"
  "buffer_load_dwordx4 v15, s[20:23], s37 offen lds\n"
  "s_add_u32 s37, s37, 524288\n"
  "v_mfma_f32_32x32x16_bf16 v[144:159], a[172:175], a[156:159], v[144:159]\n"
  "ds_read_b64_tr_b16 a[150:151], v25 offset:1792\n"
  "buffer_store_dwordx4 v[200:203], v1, s[24:27], s72 offen sc1\n"
  "s_add_u32 s72, s72, s73\n"
  "s_add_u32 m0, s38, 32768\n"
  "s_waitcnt lgkmcnt(6)\n"
  "v_mfma_f32_32x32x16_bf16 v[32:47], a[136:139], a[128:131], v[32:47]\n"
  "ds_read_b64_tr_b16 a[160:161], v25 offset:4096\n"
  "ds_read_b64_tr_b16 a[162:163], v25 offset:4352\n"
  "buffer_load_dwordx4 v4, s[16:19], s36 offen lds\n"
  "v_mfma_f32_32x32x16_bf16 v[96:111], a[136:139], a[132:135], v[96:111]\n"
  "ds_read_b128 a[152:155], v19 offset:0\n"
  "ds_read_b128 a[156:159], v19 offset:4096\n"
  "v_accvgpr_write_b32 a76, 0\n"
  "s_add_u32 m0, s38, 33792\n"
  "s_waitcnt lgkmcnt(8)\n"
  "v_mfma_f32_32x32x16_bf16 v[48:63], a[140:143], a[128:131], v[48:63]\n"
  "ds_read_b64_tr_b16 a[164:165], v25 offset:4608\n"
  "buffer_load_dwordx4 v5, s[16:19], s36 offen lds\n"
  "s_add_u32 m0, s38, 34816\n"
  "v_mfma_f32_32x32x16_bf16 v[112:127], a[140:143], a[132:135], v[112:127]\n"
  "ds_read_b64_tr_b16 a[166:167], v25 offset:4864\n"
  "buffer_load_dwordx4 v6, s[16:19], s36 offen lds\n"
  "s_waitcnt lgkmcnt(8)\n"
  "v_mfma_f32_32x32x16_bf16 v[64:79], a[144:147], a[128:131], v[64:79]\n"
  "ds_read_b64_tr_b16 a[168:169], v25 offset:5120\n"
  "v_accvgpr_write_b32 a77, 0\n"
  "s_add_u32 m0, s38, 35840\n"
  "v_mfma_f32_32x32x16_bf16 v[128:143], a[144:147], a[132:135], v[128:143]\n"
  "ds_read_b64_tr_b16 a[170:171], v25 offset:5376\n"
  "buffer_load_dwordx4 v7, s[16:19], s36 offen lds\n"
  "s_waitcnt lgkmcnt(8)\n"
  "v_mfma_f32_32x32x16_bf16 v[80:95], a[148:151], a[128:131], v[80:95]\n"
  "ds_read_b64_tr_b16 a[172:173], v25 offset:5632\n"
  "v_accvgpr_write_b32 a78, 0\n"
  "v_mfma_f32_32x32x16_bf16 v[144:159], a[148:151], a[132:135], v[144:159]\n"
  "ds_read_b64_tr_b16 a[174:175], v25 offset:5888\n"
  "buffer_store_dwordx4 v[204:207], v1, s[24:27], s72 offen sc1\n"
  "s_add_u32 s72, s72, s74\n"
  "s_add_u32 m0, s38, 36864\n"
  "s_waitcnt lgkmcnt(7)\n"
  "v_mfma_f32_32x32x16_bf16 v[32:47], a[160:163], a[152:155], v[32:47]\n"
  "ds_read_b64_tr_b16 a[136:137], v25 offset:8192\n"
  "ds_read_b64_tr_b16 a[138:139], v25 offset:8448\n"
  "buffer_load_dwordx4 v8, s[16:19], s36 offen lds\n"
  "s_waitcnt lgkmcnt(8)\n"
  "v_mfma_f32_32x32x16_bf16 v[96:111], a[160:163], a[156:159], v[96:111]\n"
  "ds_read_b128 a[128:131], v21 offset:0\n"
  "ds_read_b128 a[132:135], v21 offset:4096\n"
  "v_accvgpr_write_b32 a79, 0\n"
  "s_waitcnt lgkmcnt(8)\n"
  "v_mfma_f32_32x32x16_bf16 v[48:63], a[164:167], a[152:155], v[48:63]\n"
  "ds_read_b64_tr_b16 a[140:141], v25 offset:8704\n"
  "v_accvgpr_write_b32 a80, 0\n"
  "s_add_u32 m0, s38, 37888\n"
  "v_mfma_f32_32x32x16_bf16 v[112:127], a[164:167], a[156:159], v[112:127]\n"
  "ds_read_b64_tr_b16 a[142:143], v25 offset:8960\n"
  "buffer_load_dwordx4 v9, s[16:19], s36 offen lds\n"
  "s_waitcnt lgkmcnt(8)\n"
  "v_mfma_f32_32x32x16_bf16 v[64:79], a[168:171], a[152:155], v[64:79]\n"
  "ds_read_b64_tr_b16 a[144:145], v25 offset:9216\n"
  "v_accvgpr_write_b32 a81, 0\n"
  "s_add_u32 m0, s38, 38912\n"
  "v_mfma_f32_32x32x16_bf16 v[128:143], a[168:171], a[156:159], v[128:143]\n"
  "ds_read_b64_tr_b16 a[146:147], v25 offset:9472\n"
  "buffer_load_dwordx4 v10, s[16:19], s36 offen lds\n"
  "s_waitcnt lgkmcnt(8)\n"
  "v_mfma_f32_32x32x16_bf16 v[80:95], a[172:175], a[152:155], v[80:95]\n"
  "ds_read_b64_tr_b16 a[148:149], v25 offset:9728\n"
  "v_accvgpr_write_b32 a82, 0\n"
  "v_mfma_f32_32x32x16_bf16 v[144:159], a[172:175], a[156:159], v[144:159]\n"
  "ds_read_b64_tr_b16 a[150:151], v25 offset:9984\n"
  "buffer_store_dwordx4 v[216:219], v1, s[24:27], s72 offen sc1\n"
  "s_add_u32 s72, s72, s73\n"
  "s_add_u32 m0, s38, 39936\n"
  "s_waitcnt lgkmcnt(7)\n"
  "v_mfma_f32_32x32x16_bf16 v[32:47], a[136:139], a[128:131], v[32:47]\n"
  "ds_read_b64_tr_b16 a[160:161], v25 offset:12288\n"
  "ds_read_b64_tr_b16 a[162:163], v25 offset:12544\n"
  "buffer_load_dwordx4 v11, s[16:19], s36 offen lds\n"
  "s_add_u32 s36, s36, 128\n"
  "s_waitcnt lgkmcnt(7)\n"
  "v_mfma_f32_32x32x16_bf16 v[96:111], a[136:139], a[132:135], v[96:111]\n"
  "ds_read_b128 a[152:155], v23 offset:0\n"
  "ds_read_b128 a[156:159], v23 offset:4096\n"
  "v_accvgpr_write_b32 a83, 0\n"
  "s_waitcnt lgkmcnt(8)\n"
  "v_mfma_f32_32x32x16_bf16 v[48:63], a[140:143], a[128:131], v[48:63]\n"
  "ds_read_b64_tr_b16 a[164:165], v25 offset:12800\n"
  "v_accvgpr_write_b32 a84, 0\n"
  "v_mfma_f32_32x32x16_bf16 v[112:127], a[140:143], a[132:135], v[112:127]\n"
  "ds_read_b64_tr_b16 a[166:167], v25 offset:13056\n"
  "v_accvgpr_write_b32 a85, 0\n"
  "s_waitcnt lgkmcnt(8)\n"
  "v_mfma_f32_32x32x16_bf16 v[64:79], a[144:147], a[128:131], v[64:79]\n"
  "ds_read_b64_tr_b16 a[168:169], v25 offset:13312\n"
  "v_accvgpr_write_b32 a86, 0\n"
  "v_mfma_f32_32x32x16_bf16 v[128:143], a[144:147], a[132:135], v[128:143]\n"
  "ds_read_b64_tr_b16 a[170:171], v25 offset:13568\n"
  "v_accvgpr_write_b32 a87, 0\n"
  "s_waitcnt lgkmcnt(8)\n"
  "v_mfma_f32_32x32x16_bf16 v[80:95], a[148:151], a[128:131], v[80:95]\n"
  "ds_read_b64_tr_b16 a[172:173], v25 offset:13824\n"
  "v_accvgpr_write_b32 a88, 0\n"
  "v_mfma_f32_32x32x16_bf16 v[144:159], a[148:151], a[132:135], v[144:159]\n"
  "ds_read_b64_tr_b16 a[174:175], v25 offset:14080\n"
  "buffer_store_dwordx4 v[220:223], v1, s[24:27], s72 offen sc1\n"
  "s_add_u32 s72, s72, s75\n"
  "s_waitcnt vmcnt(17)\n"
  "s_waitcnt lgkmcnt(0)\n"
  "s_barrier\n"
  "s_add_u32 m0, s39, 131072\n"
  "v_mfma_f32_32x32x16_bf16 v[32:47], a[160:163], a[152:155], v[32:47]\n"
  "ds_read_b64_tr_b16 a[136:137], v24 offset:0\n"
  "ds_read_b64_tr_b16 a[138:139], v24 offset:256\n"
  "buffer_load_dwordx4 v12, s[20:23], s37 offen lds\n"
  "v_mfma_f32_32x32x16_bf16 v[96:111], a[160:163], a[156:159], v[96:111]\n"
  "ds_read_b128 a[128:131], v16 offset:0\n"
  "ds_read_b128 a[132:135], v16 offset:4096\n"
  "v_accvgpr_write_b32 a89, 0\n"
  "s_add_u32 m0, s39, 132096\n"
  "v_mfma_f32_32x32x16_bf16 v[48:63], a[164:167], a[152:155], v[48:63]\n"
  "ds_read_b64_tr_b16 a[140:141], v24 offset:512\n"
  "buffer_load_dwordx4 v13, s[20:23], s37 offen lds\n"
  "v_mfma_f32_32x32x16_bf16 v[112:127], a[164:167], a[156:159], v[112:127]\n"
  "ds_read_b64_tr_b16 a[142:143], v24 offset:768\n"
  "v_accvgpr_write_b32 a90, 0\n"
  "s_add_u32 m0, s39, 133120\n"
  "v_mfma_f32_32x32x16_bf16 v[64:79], a[168:171], a[152:155], v[64:79]\n"
  "ds_read_b64_tr_b16 a[144:145], v24 offset:1024\n"
  "buffer_load_dwordx4 v14, s[20:23], s37 offen lds\n"
  "v_mfma_f32_32x32x16_bf16 v[128:143], a[168:171], a[156:159], v[128:143]\n"
  "ds_read_b64_tr_b16 a[146:147], v24 offset:1280\n"
  "v_accvgpr_write_b32 a91, 0\n"
  "s_add_u32 m0, s39, 134144\n"
  "v_mfma_f32_32x32x16_bf16 v[80:95], a[172:175], a[152:155], v[80:95]\n"
  "ds_read_b64_tr_b16 a[148:149], v24 offset:1536\n"
  "buffer_load_dwordx4 v15, s[20:23], s37 offen lds\n"
  "s_add_u32 s37, s37, 524288\n"
  "v_mfma_f32_32x32x16_bf16 v[144:159], a[172:175], a[156:159], v[144:159]\n"
  "ds_read_b64_tr_b16 a[150:151], v24 offset:1792\n"
  "v_accvgpr_write_b32 a92, 0\n"
  "s_add_u32 m0, s38, 98304\n"
  "s_waitcnt lgkmcnt(7)\n"
  "v_mfma_f32_32x32x16_bf16 v[32:47], a[136:139], a[128:131], v[32:47]\n"
  "ds_read_b64_tr_b16 a[160:161], v24 offset:4096\n"
  "ds_read_b64_tr_b16 a[162:163], v24 offset:4352\n"
  "buffer_load_dwordx4 v4, s[16:19], s36 offen lds\n"
  "s_waitcnt lgkmcnt(8)\n"
  "v_mfma_f32_32x32x16_bf16 v[96:111], a[136:139], a[132:135], v[96:111]\n"
  "ds_read_b128 a[152:155], v18 offset:0\n"
  "ds_read_b128 a[156:159], v18 offset:4096\n"
  "v_accvgpr_write_b32 a93, 0\n"
  "s_add_u32 m0, s38, 99328\n"
  "s_waitcnt lgkmcnt(8)\n"
  "v_mfma_f32_32x32x16_bf16 v[48:63], a[140:143], a[128:131], v[48:63]\n"
  "ds_read_b64_tr_b16 a[164:165], v24 offset:4608\n"
  "buffer_load_dwordx4 v5, s[16:19], s36 offen lds\n"
  "s_add_u32 m0, s38, 100352\n"
  "v_mfma_f32_32x32x16_bf16 v[112:127], a[140:143], a[132:135], v[112:127]\n"
  "ds_read_b64_tr_b16 a[166:167], v24 offset:4864\n"
  "buffer_load_dwordx4 v6, s[16:19], s36 offen lds\n"
  "s_waitcnt lgkmcnt(8)\n"
  "v_mfma_f32_32x32x16_bf16 v[64:79], a[144:147], a[128:131], v[64:79]\n"
  "ds_read_b64_tr_b16 a[168:169], v24 offset:5120\n"
  "v_accvgpr_write_b32 a94, 0\n"
  "s_add_u32 m0, s38, 101376\n"
  "v_mfma_f32_32x32x16_bf16 v[128:143], a[144:147], a[132:135], v[128:143]\n"
  "ds_read_b64_tr_b16 a[170:171], v24 offset:5376\n"
  "buffer_load_dwordx4 v7, s[16:19], s36 offen lds\n"
  "s_waitcnt lgkmcnt(8)\n"
  "v_mfma_f32_32x32x16_bf16 v[80:95], a[148:151], a[128:131], v[80:95]\n"
  "ds_read_b64_tr_b16 a[172:173], v24 offset:5632\n"
  "v_accvgpr_write_b32 a95, 0\n"
  "v_mfma_f32_32x32x16_bf16 v[144:159], a[148:151], a[132:135], v[144:159]\n"
  "ds_read_b64_tr_b16 a[174:175], v24 offset:5888\n"
  "v_accvgpr_write_b32 a96, 0\n"
  "s_add_u32 m0, s38, 102400\n"
  "s_waitcnt lgkmcnt(7)\n"
  "v_mfma_f32_32x32x16_bf16 v[32:47], a[160:163], a[152:155], v[32:47]\n"
  "ds_read_b64_tr_b16 a[136:137], v24 offset:8192\n"
  "ds_read_b64_tr_b16 a[138:139], v24 offset:8448\n"
  "buffer_load_dwordx4 v8, s[16:19], s36 offen lds\n"
  "s_waitcnt lgkmcnt(8)\n"
  "v_mfma_f32_32x32x16_bf16 v[96:111], a[160:163], a[156:159], v[96:111]\n"
  "ds_read_b128 a[128:131], v20 offset:0\n"
  "ds_read_b128 a[132:135], v20 offset:4096\n"
  "v_accvgpr_write_b32 a97, 0\n"
  "s_waitcnt lgkmcnt(8)\n"
  "v_mfma_f32_32x32x16_bf16 v[48:63], a[164:167], a[152:155], v[48:63]\n"
  "ds_read_b64_tr_b16 a[140:141], v24 offset:8704\n"
  "v_accvgpr_write_b32 a98, 0\n"
  "s_add_u32 m0, s38, 103424\n"
  "v_mfma_f32_32x32x16_bf16 v[112:127], a[164:167], a[156:159], v[112:127]\n"
  "ds_read_b64_tr_b16 a[142:143], v24 offset:8960\n"
  "buffer_load_dwordx4 v9, s[16:19], s36 offen lds\n"
  "s_waitcnt lgkmcnt(8)\n"
  "v_mfma_f32_32x32x16_bf16 v[64:79], a[168:171], a[152:155], v[64:79]\n"
  "ds_read_b64_tr_b16 a[144:145], v24 offset:9216\n"
  "v_accvgpr_write_b32 a99, 0\n"
  "s_add_u32 m0, s38, 104448\n"
  "v_mfma_f32_32x32x16_bf16 v[128:143], a[168:171], a[156:159], v[128:143]\n"
  "ds_read_b64_tr_b16 a[146:147], v24 offset:9472\n"
  "buffer_load_dwordx4 v10, s[16:19], s36 offen lds\n"
  "s_waitcnt lgkmcnt(8)\n"
  "v_mfma_f32_32x32x16_bf16 v[80:95], a[172:175], a[152:155], v[80:95]\n"
  "ds_read_b64_tr_b16 a[148:149], v24 offset:9728\n"
  "v_accvgpr_write_b32 a100, 0\n"
  "v_mfma_f32_32x32x16_bf16 v[144:159], a[172:175], a[156:159], v[144:159]\n"
  "ds_read_b64_tr_b16 a[150:151], v24 offset:9984\n"
  "v_accvgpr_write_b32 a101, 0\n"
  "s_add_u32 m0, s38, 105472\n"
  "s_waitcnt lgkmcnt(7)\n"
  "v_mfma_f32_32x32x16_bf16 v[32:47], a[136:139], a[128:131], v[32:47]\n"
  "ds_read_b64_tr_b16 a[160:161], v24 offset:12288\n"
  "ds_read_b64_tr_b16 a[162:163], v24 offset:12544\n"
  "buffer_load_dwordx4 v11, s[16:19], s36 offen lds\n"
  "s_add_u32 s36, s36, 128\n"
  "s_waitcnt lgkmcnt(8)\n"
  "v_mfma_f32_32x32x16_bf16 v[96:111], a[136:139], a[132:135], v[96:111]\n"
  "ds_read_b128 a[152:155], v22 offset:0\n"
  "ds_read_b128 a[156:159], v22 offset:4096\n"
  "v_accvgpr_write_b32 a102, 0\n"
  "s_waitcnt lgkmcnt(8)\n"
  "v_mfma_f32_32x32x16_bf16 v[48:63], a[140:143], a[128:131], v[48:63]\n"
  "ds_read_b64_tr_b16 a[164:165], v24 offset:12800\n"
  "v_accvgpr_write_b32 a103, 0\n"
  "v_mfma_f32_32x32x16_bf16 v[112:127], a[140:143], a[132:135], v[112:127]\n"
  "ds_read_b64_tr_b16 a[166:167], v24 offset:13056\n"
  "v_accvgpr_write_b32 a104, 0\n"
  "s_waitcnt lgkmcnt(8)\n"
  "v_mfma_f32_32x32x16_bf16 v[64:79], a[144:147], a[128:131], v[64:79]\n"
  "ds_read_b64_tr_b16 a[168:169], v24 offset:13312\n"
  "v_accvgpr_write_b32 a105, 0\n"
  "v_mfma_f32_32x32x16_bf16 v[128:143], a[144:147], a[132:135], v[128:143]\n"
  "ds_read_b64_tr_b16 a[170:171], v24 offset:13568\n"
  "v_accvgpr_write_b32 a106, 0\n"
  "s_waitcnt lgkmcnt(8)\n"
  "v_mfma_f32_32x32x16_bf16 v[80:95], a[148:151], a[128:131], v[80:95]\n"
  "ds_read_b64_tr_b16 a[172:173], v24 offset:13824\n"
  "v_accvgpr_write_b32 a107, 0\n"
  "v_mfma_f32_32x32x16_bf16 v[144:159], a[148:151], a[132:135], v[144:159]\n"
  "ds_read_b64_tr_b16 a[174:175], v24 offset:14080\n"
  "v_accvgpr_write_b32 a108, 0\n"
  "s_waitcnt vmcnt(13)\n"
  "s_waitcnt vmcnt(12)\n"
  "s_waitcnt lgkmcnt(0)\n"
  "s_barrier\n"
  "s_lshl_b32 s58, s12, 3\n"
  "s_add_u32 s58, s58, s13\n"
  "s_lshl_b32 s58, s58, 2\n"
  "s_add_u32 s58, s58, 8192\n"
  "v_mov_b32 v31, s71\n"
  "s_mov_b64 exec, 1\n"
  "buffer_store_dword v31, off, s[92:95], s58 sc1\n"
  "s_mov_b64 exec, -1\n"
  "v_mfma_f32_32x32x16_bf16 v[32:47], a[160:163], a[152:155], v[32:47]\n"
  "ds_read_b64_tr_b16 a[136:137], v24 offset:16384\n"
  "ds_read_b64_tr_b16 a[138:139], v24 offset:16640\n"
  "v_accvgpr_write_b32 a109, 0\n"
  "v_mfma_f32_32x32x16_bf16 v[96:111], a[160:163], a[156:159], v[96:111]\n"
  "ds_read_b128 a[128:131], v16 offset:32768\n"
  "ds_read_b128 a[132:135], v16 offset:36864\n"
  "v_accvgpr_write_b32 a110, 0\n"
  "v_mfma_f32_32x32x16_bf16 v[48:63], a[164:167], a[152:155], v[48:63]\n"
  "ds_read_b64_tr_b16 a[140:141], v24 offset:16896\n"
  "v_accvgpr_write_b32 a111, 0\n"
  "v_mfma_f32_32x32x16_bf16 v[112:127], a[164:167], a[156:159], v[112:127]\n"
  "ds_read_b64_tr_b16 a[142:143], v24 offset:17152\n"
  "v_accvgpr_write_b32 a112, 0\n"
  "v_mfma_f32_32x32x16_bf16 v[64:79], a[168:171], a[152:155], v[64:79]\n"
  "ds_read_b64_tr_b16 a[144:145], v24 offset:17408\n"
  "v_accvgpr_write_b32 a113, 0\n"
  "v_mfma_f32_32x32x16_bf16 v[128:143], a[168:171], a[156:159], v[128:143]\n"
  "ds_read_b64_tr_b16 a[146:147], v24 offset:17664\n"
  "v_accvgpr_write_b32 a114, 0\n"
  "v_mfma_f32_32x32x16_bf16 v[80:95], a[172:175], a[152:155], v[80:95]\n"
  "ds_read_b64_tr_b16 a[148:149], v24 offset:17920\n"
  "v_accvgpr_write_b32 a115, 0\n"
  "v_mfma_f32_32x32x16_bf16 v[144:159], a[172:175], a[156:159], v[144:159]\n"
  "ds_read_b64_tr_b16 a[150:151], v24 offset:18176\n"
  "v_accvgpr_write_b32 a116, 0\n"
  "s_waitcnt lgkmcnt(7)\n"
  "v_mfma_f32_32x32x16_bf16 v[32:47], a[136:139], a[128:131], v[32:47]\n"
  "ds_read_b64_tr_b16 a[160:161], v24 offset:20480\n"
  "ds_read_b64_tr_b16 a[162:163], v24 offset:20736\n"
  "v_accvgpr_write_b32 a117, 0\n"
  "s_waitcnt lgkmcnt(8)\n"
  "v_mfma_f32_32x32x16_bf16 v[96:111], a[136:139], a[132:135], v[96:111]\n"
  "ds_read_b128 a[152:155], v18 offset:32768\n"
  "ds_read_b128 a[156:159], v18 offset:36864\n"
  "v_accvgpr_write_b32 a118, 0\n"
  "s_waitcnt lgkmcnt(8)\n"
  "v_mfma_f32_32x32x16_bf16 v[48:63], a[140:143], a[128:131], v[48:63]\n"
  "ds_read_b64_tr_b16 a[164:165], v24 offset:20992\n"
  "v_accvgpr_write_b32 a119, 0\n"
  "v_mfma_f32_32x32x16_bf16 v[112:127], a[140:143], a[132:135], v[112:127]\n"
  "ds_read_b64_tr_b16 a[166:167], v24 offset:21248\n"
  "v_accvgpr_write_b32 a120, 0\n"
  "s_waitcnt lgkmcnt(8)\n"
  "v_mfma_f32_32x32x16_bf16 v[64:79], a[144:147], a[128:131], v[64:79]\n"
  "ds_read_b64_tr_b16 a[168:169], v24 offset:21504\n"
  "v_accvgpr_write_b32 a121, 0\n"
  "v_mfma_f32_32x32x16_bf16 v[128:143], a[144:147], a[132:135], v[128:143]\n"
  "ds_read_b64_tr_b16 a[170:171], v24 offset:21760\n"
  "v_accvgpr_write_b32 a122, 0\n"
  "s_waitcnt lgkmcnt(8)\n"
  "v_mfma_f32_32x32x16_bf16 v[80:95], a[148:151], a[128:131], v[80:95]\n"
  "ds_read_b64_tr_b16 a[172:173], v24 offset:22016\n"
  "v_accvgpr_write_b32 a123, 0\n"
  "v_mfma_f32_32x32x16_bf16 v[144:159], a[148:151], a[132:135], v[144:159]\n"
  "ds_read_b64_tr_b16 a[174:175], v24 offset:22272\n"
  "v_accvgpr_write_b32 a124, 0\n"
  "s_waitcnt lgkmcnt(7)\n"
  "v_mfma_f32_32x32x16_bf16 v[32:47], a[160:163], a[152:155], v[32:47]\n"
  "ds_read_b64_tr_b16 a[136:137], v24 offset:24576\n"
  "ds_read_b64_tr_b16 a[138:139], v24 offset:24832\n"
  "v_accvgpr_write_b32 a125, 0\n"
  "s_waitcnt lgkmcnt(8)\n"
  "v_mfma_f32_32x32x16_bf16 v[96:111], a[160:163], a[156:159], v[96:111]\n"
  "ds_read_b128 a[128:131], v20 offset:32768\n"
  "ds_read_b128 a[132:135], v20 offset:36864\n"
  "v_accvgpr_write_b32 a126, 0\n"
  "s_waitcnt lgkmcnt(8)\n"
  "v_mfma_f32_32x32x16_bf16 v[48:63], a[164:167], a[152:155], v[48:63]\n"
  "ds_read_b64_tr_b16 a[140:141], v24 offset:25088\n"
  "v_accvgpr_write_b32 a127, 0\n"
  "v_mfma_f32_32x32x16_bf16 v[112:127], a[164:167], a[156:159], v[112:127]\n"
  "ds_read_b64_tr_b16 a[142:143], v24 offset:25344\n"
  "v_accvgpr_write_b32 a192, 0\n"
  "s_waitcnt lgkmcnt(8)\n"
  "v_mfma_f32_32x32x16_bf16 v[64:79], a[168:171], a[152:155], v[64:79]\n"
  "ds_read_b64_tr_b16 a[144:145], v24 offset:25600\n"
  "v_accvgpr_write_b32 a193, 0\n"
  "v_mfma_f32_32x32x16_bf16 v[128:143], a[168:171], a[156:159], v[128:143]\n"
  "ds_read_b64_tr_b16 a[146:147], v24 offset:25856\n"
  "v_accvgpr_write_b32 a194, 0\n"
  "s_waitcnt lgkmcnt(8)\n"
  "v_mfma_f32_32x32x16_bf16 v[80:95], a[172:175], a[152:155], v[80:95]\n"
  "ds_read_b64_tr_b16 a[148:149], v24 offset:26112\n"
  "v_accvgpr_write_b32 a195, 0\n"
  "v_mfma_f32_32x32x16_bf16 v[144:159], a[172:175], a[156:159], v[144:159]\n"
  "ds_read_b64_tr_b16 a[150:151], v24 offset:26368\n"
  "v_accvgpr_write_b32 a196, 0\n"
  "s_waitcnt lgkmcnt(7)\n"
  "v_mfma_f32_32x32x16_bf16 v[32:47], a[136:139], a[128:131], v[32:47]\n"
  "ds_read_b64_tr_b16 a[160:161], v24 offset:28672\n"
  "ds_read_b64_tr_b16 a[162:163], v24 offset:28928\n"
  "v_accvgpr_write_b32 a197, 0\n"
  "s_waitcnt lgkmcnt(8)\n"
  "v_mfma_f32_32x32x16_bf16 v[96:111], a[136:139], a[132:135], v[96:111]\n"
  "ds_read_b128 a[152:155], v22 offset:32768\n"
  "ds_read_b128 a[156:159], v22 offset:36864\n"
  "v_accvgpr_write_b32 a198, 0\n"
  "s_waitcnt lgkmcnt(8)\n"
  "v_mfma_f32_32x32x16_bf16 v[48:63], a[140:143], a[128:131], v[48:63]\n"
  "ds_read_b64_tr_b16 a[164:165], v24 offset:29184\n"
  "v_accvgpr_write_b32 a199, 0\n"
  "v_mfma_f32_32x32x16_bf16 v[112:127], a[140:143], a[132:135], v[112:127]\n"
  "ds_read_b64_tr_b16 a[166:167], v24 offset:29440\n"
  "v_accvgpr_write_b32 a200, 0\n"
  "s_waitcnt lgkmcnt(8)\n"
  "v_mfma_f32_32x32x16_bf16 v[64:79], a[144:147], a[128:131], v[64:79]\n"
  "ds_read_b64_tr_b16 a[168:169], v24 offset:29696\n"
  "v_accvgpr_write_b32 a201, 0\n"
  "v_mfma_f32_32x32x16_bf16 v[128:143], a[144:147], a[132:135], v[128:143]\n"
  "ds_read_b64_tr_b16 a[170:171], v24 offset:29952\n"
  "v_accvgpr_write_b32 a202, 0\n"
  "s_waitcnt lgkmcnt(8)\n"
  "v_mfma_f32_32x32x16_bf16 v[80:95], a[148:151], a[128:131], v[80:95]\n"
  "ds_read_b64_tr_b16 a[172:173], v24 offset:30208\n"
  "v_accvgpr_write_b32 a203, 0\n"
  "v_mfma_f32_32x32x16_bf16 v[144:159], a[148:151], a[132:135], v[144:159]\n"
  "ds_read_b64_tr_b16 a[174:175], v24 offset:30464\n"
  "v_accvgpr_write_b32 a204, 0\n"
  "s_waitcnt vmcnt(0)\n"
  "s_waitcnt lgkmcnt(0)\n"
  "s_barrier\n"
  "v_mfma_f32_32x32x16_bf16 v[32:47], a[160:163], a[152:155], v[32:47]\n"
  "ds_read_b64_tr_b16 a[136:137], v25 offset:0\n"
  "ds_read_b64_tr_b16 a[138:139], v25 offset:256\n"
  "v_accvgpr_write_b32 a205, 0\n"
  "v_mfma_f32_32x32x16_bf16 v[96:111], a[160:163], a[156:159], v[96:111]\n"
  "ds_read_b128 a[128:131], v17 offset:0\n"
  "ds_read_b128 a[132:135], v17 offset:4096\n"
  "v_accvgpr_write_b32 a206, 0\n"
  "v_mfma_f32_32x32x16_bf16 v[48:63], a[164:167], a[152:155], v[48:63]\n"
  "ds_read_b64_tr_b16 a[140:141], v25 offset:512\n"
  "v_accvgpr_write_b32 a207, 0\n"
  "v_mfma_f32_32x32x16_bf16 v[112:127], a[164:167], a[156:159], v[112:127]\n"
  "ds_read_b64_tr_b16 a[142:143], v25 offset:768\n"
  "v_accvgpr_write_b32 a208, 0\n"
  "v_mfma_f32_32x32x16_bf16 v[64:79], a[168:171], a[152:155], v[64:79]\n"
  "ds_read_b64_tr_b16 a[144:145], v25 offset:1024\n"
  "v_accvgpr_write_b32 a209, 0\n"
  "v_mfma_f32_32x32x16_bf16 v[128:143], a[168:171], a[156:159], v[128:143]\n"
  "ds_read_b64_tr_b16 a[146:147], v25 offset:1280\n"
  "v_accvgpr_write_b32 a210, 0\n"
  "v_mfma_f32_32x32x16_bf16 v[80:95], a[172:175], a[152:155], v[80:95]\n"
  "ds_read_b64_tr_b16 a[148:149], v25 offset:1536\n"
  "v_accvgpr_write_b32 a211, 0\n"
  "v_mfma_f32_32x32x16_bf16 v[144:159], a[172:175], a[156:159], v[144:159]\n"
  "ds_read_b64_tr_b16 a[150:151], v25 offset:1792\n"
  "v_accvgpr_write_b32 a212, 0\n"
  "s_waitcnt lgkmcnt(7)\n"
  "v_mfma_f32_32x32x16_bf16 v[32:47], a[136:139], a[128:131], v[32:47]\n"
  "ds_read_b64_tr_b16 a[160:161], v25 offset:4096\n"
  "ds_read_b64_tr_b16 a[162:163], v25 offset:4352\n"
  "v_accvgpr_write_b32 a213, 0\n"
  "s_waitcnt lgkmcnt(8)\n"
  "v_mfma_f32_32x32x16_bf16 v[96:111], a[136:139], a[132:135], v[96:111]\n"
  "ds_read_b128 a[152:155], v19 offset:0\n"
  "ds_read_b128 a[156:159], v19 offset:4096\n"
  "v_accvgpr_write_b32 a214, 0\n"
  "s_waitcnt lgkmcnt(8)\n"
  "v_mfma_f32_32x32x16_bf16 v[48:63], a[140:143], a[128:131], v[48:63]\n"
  "ds_read_b64_tr_b16 a[164:165], v25 offset:4608\n"
  "v_accvgpr_write_b32 a215, 0\n"
  "v_mfma_f32_32x32x16_bf16 v[112:127], a[140:143], a[132:135], v[112:127]\n"
  "ds_read_b64_tr_b16 a[166:167], v25 offset:4864\n"
  "v_accvgpr_write_b32 a216, 0\n"
  "s_waitcnt lgkmcnt(8)\n"
  "v_mfma_f32_32x32x16_bf16 v[64:79], a[144:147], a[128:131], v[64:79]\n"
  "ds_read_b64_tr_b16 a[168:169], v25 offset:5120\n"
  "v_accvgpr_write_b32 a217, 0\n"
  "v_mfma_f32_32x32x16_bf16 v[128:143], a[144:147], a[132:135], v[128:143]\n"
  "ds_read_b64_tr_b16 a[170:171], v25 offset:5376\n"
  "v_accvgpr_write_b32 a218, 0\n"
  "s_waitcnt lgkmcnt(8)\n"
  "v_mfma_f32_32x32x16_bf16 v[80:95], a[148:151], a[128:131], v[80:95]\n"
  "ds_read_b64_tr_b16 a[172:173], v25 offset:5632\n"
  "v_accvgpr_write_b32 a219, 0\n"
  "v_mfma_f32_32x32x16_bf16 v[144:159], a[148:151], a[132:135], v[144:159]\n"
  "ds_read_b64_tr_b16 a[174:175], v25 offset:5888\n"
  "v_accvgpr_write_b32 a220, 0\n"
  "s_waitcnt lgkmcnt(7)\n"
  "v_mfma_f32_32x32x16_bf16 v[32:47], a[160:163], a[152:155], v[32:47]\n"
  "ds_read_b64_tr_b16 a[136:137], v25 offset:8192\n"
  "ds_read_b64_tr_b16 a[138:139], v25 offset:8448\n"
  "v_accvgpr_write_b32 a221, 0\n"
  "s_waitcnt lgkmcnt(8)\n"
  "v_mfma_f32_32x32x16_bf16 v[96:111], a[160:163], a[156:159], v[96:111]\n"
  "ds_read_b128 a[128:131], v21 offset:0\n"
  "ds_read_b128 a[132:135], v21 offset:4096\n"
  "v_accvgpr_write_b32 a222, 0\n"
  "s_waitcnt lgkmcnt(8)\n"
  "v_mfma_f32_32x32x16_bf16 v[48:63], a[164:167], a[152:155], v[48:63]\n"
  "ds_read_b64_tr_b16 a[140:141], v25 offset:8704\n"
  "v_accvgpr_write_b32 a223, 0\n"
  "v_mfma_f32_32x32x16_bf16 v[112:127], a[164:167], a[156:159], v[112:127]\n"
  "ds_read_b64_tr_b16 a[142:143], v25 offset:8960\n"
  "v_accvgpr_write_b32 a224, 0\n"
  "s_waitcnt lgkmcnt(8)\n"
  "v_mfma_f32_32x32x16_bf16 v[64:79], a[168:171], a[152:155], v[64:79]\n"
  "ds_read_b64_tr_b16 a[144:145], v25 offset:9216\n"
  "v_accvgpr_write_b32 a225, 0\n"
  "v_mfma_f32_32x32x16_bf16 v[128:143], a[168:171], a[156:159], v[128:143]\n"
  "ds_read_b64_tr_b16 a[146:147], v25 offset:9472\n"
  "v_accvgpr_write_b32 a226, 0\n"
  "s_waitcnt lgkmcnt(8)\n"
  "v_mfma_f32_32x32x16_bf16 v[80:95], a[172:175], a[152:155], v[80:95]\n"
  "ds_read_b64_tr_b16 a[148:149], v25 offset:9728\n"
  "v_accvgpr_write_b32 a227, 0\n"
  "v_mfma_f32_32x32x16_bf16 v[144:159], a[172:175], a[156:159], v[144:159]\n"
  "ds_read_b64_tr_b16 a[150:151], v25 offset:9984\n"
  "v_accvgpr_write_b32 a228, 0\n"
  "s_waitcnt lgkmcnt(7)\n"
  "v_mfma_f32_32x32x16_bf16 v[32:47], a[136:139], a[128:131], v[32:47]\n"
  "ds_read_b64_tr_b16 a[160:161], v25 offset:12288\n"
  "ds_read_b64_tr_b16 a[162:163], v25 offset:12544\n"
  "v_accvgpr_write_b32 a229, 0\n"
  "s_waitcnt lgkmcnt(8)\n"
  "v_mfma_f32_32x32x16_bf16 v[96:111], a[136:139], a[132:135], v[96:111]\n"
  "ds_read_b128 a[152:155], v23 offset:0\n"
  "ds_read_b128 a[156:159], v23 offset:4096\n"
  "v_accvgpr_write_b32 a230, 0\n"
  "s_waitcnt lgkmcnt(8)\n"
  "v_mfma_f32_32x32x16_bf16 v[48:63], a[140:143], a[128:131], v[48:63]\n"
  "ds_read_b64_tr_b16 a[164:165], v25 offset:12800\n"
  "v_accvgpr_write_b32 a231, 0\n"
  "v_mfma_f32_32x32x16_bf16 v[112:127], a[140:143], a[132:135], v[112:127]\n"
  "ds_read_b64_tr_b16 a[166:167], v25 offset:13056\n"
  "v_accvgpr_write_b32 a232, 0\n"
  "s_waitcnt lgkmcnt(8)\n"
  "v_mfma_f32_32x32x16_bf16 v[64:79], a[144:147], a[128:131], v[64:79]\n"
  "ds_read_b64_tr_b16 a[168:169], v25 offset:13312\n"
  "v_accvgpr_write_b32 a233, 0\n"
  "v_mfma_f32_32x32x16_bf16 v[128:143], a[144:147], a[132:135], v[128:143]\n"
  "ds_read_b64_tr_b16 a[170:171], v25 offset:13568\n"
  "v_accvgpr_write_b32 a234, 0\n"
  "s_waitcnt lgkmcnt(8)\n"
  "v_mfma_f32_32x32x16_bf16 v[80:95], a[148:151], a[128:131], v[80:95]\n"
  "ds_read_b64_tr_b16 a[172:173], v25 offset:13824\n"
  "v_accvgpr_write_b32 a235, 0\n"
  "v_mfma_f32_32x32x16_bf16 v[144:159], a[148:151], a[132:135], v[144:159]\n"
  "ds_read_b64_tr_b16 a[174:175], v25 offset:14080\n"
  "v_accvgpr_write_b32 a236, 0\n"
  "s_waitcnt lgkmcnt(7)\n"
  "v_mfma_f32_32x32x16_bf16 v[32:47], a[160:163], a[152:155], v[32:47]\n"
  "v_accvgpr_write_b32 a237, 0\n"
  "s_waitcnt lgkmcnt(6)\n"
  "v_mfma_f32_32x32x16_bf16 v[96:111], a[160:163], a[156:159], v[96:111]\n"
  "v_accvgpr_write_b32 a238, 0\n"
  "s_waitcnt lgkmcnt(4)\n"
  "v_mfma_f32_32x32x16_bf16 v[48:63], a[164:167], a[152:155], v[48:63]\n"
  "v_accvgpr_write_b32 a239, 0\n"
  "v_mfma_f32_32x32x16_bf16 v[112:127], a[164:167], a[156:159], v[112:127]\n"
  "v_accvgpr_write_b32 a240, 0\n"
  "s_waitcnt lgkmcnt(2)\n"
  "v_mfma_f32_32x32x16_bf16 v[64:79], a[168:171], a[152:155], v[64:79]\n"
  "v_accvgpr_write_b32 a241, 0\n"
  "v_mfma_f32_32x32x16_bf16 v[128:143], a[168:171], a[156:159], v[128:143]\n"
  "v_accvgpr_write_b32 a242, 0\n"
  "s_waitcnt lgkmcnt(0)\n"
  "v_mfma_f32_32x32x16_bf16 v[80:95], a[172:175], a[152:155], v[80:95]\n"
  "v_accvgpr_write_b32 a243, 0\n"
  "v_mfma_f32_32x32x16_bf16 v[144:159], a[172:175], a[156:159], v[144:159]\n"
  "v_accvgpr_write_b32 a244, 0\n"
  "v_accvgpr_write_b32 a245, 0\n"
  "v_accvgpr_write_b32 a246, 0\n"
  "v_accvgpr_write_b32 a247, 0\n"
  "v_accvgpr_write_b32 a248, 0\n"
  "v_accvgpr_write_b32 a249, 0\n"
  "v_accvgpr_write_b32 a250, 0\n"
  "v_accvgpr_write_b32 a251, 0\n"
  "v_accvgpr_write_b32 a252, 0\n"
  "v_accvgpr_write_b32 a253, 0\n"
  "v_accvgpr_write_b32 a254, 0\n"
  "v_accvgpr_write_b32 a255, 0\n"
  "v_pk_mul_f32 v[32:33], v[32:33], s[66:67]\n"
  "v_pk_mul_f32 v[34:35], v[34:35], s[66:67]\n"
  "v_pk_mul_f32 v[36:37], v[36:37], s[66:67]\n"
  "v_pk_mul_f32 v[38:39], v[38:39], s[66:67]\n"
  "v_pk_mul_f32 v[40:41], v[40:41], s[66:67]\n"
  "v_pk_mul_f32 v[42:43], v[42:43], s[66:67]\n"
  "v_pk_mul_f32 v[44:45], v[44:45], s[66:67]\n"
  "v_pk_mul_f32 v[46:47], v[46:47], s[66:67]\n"
  "v_cvt_pk_bf16_f32 v160, v32, v33\n"
  "v_cvt_pk_bf16_f32 v161, v34, v35\n"
  "v_cvt_pk_bf16_f32 v162, v36, v37\n"
  "v_cvt_pk_bf16_f32 v163, v38, v39\n"
  "v_cvt_pk_bf16_f32 v164, v40, v41\n"
  "v_cvt_pk_bf16_f32 v165, v42, v43\n"
  "v_cvt_pk_bf16_f32 v166, v44, v45\n"
  "v_cvt_pk_bf16_f32 v167, v46, v47\n"
  "v_pk_mul_f32 v[48:49], v[48:49], s[66:67]\n"
  "v_pk_mul_f32 v[50:51], v[50:51], s[66:67]\n"
  "v_pk_mul_f32 v[52:53], v[52:53], s[66:67]\n"
  "v_pk_mul_f32 v[54:55], v[54:55], s[66:67]\n"
  "v_pk_mul_f32 v[56:57], v[56:57], s[66:67]\n"
  "v_pk_mul_f32 v[58:59], v[58:59], s[66:67]\n"
  "v_pk_mul_f32 v[60:61], v[60:61], s[66:67]\n"
  "v_pk_mul_f32 v[62:63], v[62:63], s[66:67]\n"
  "v_cvt_pk_bf16_f32 v168, v48, v49\n"
  "v_cvt_pk_bf16_f32 v169, v50, v51\n"
  "v_cvt_pk_bf16_f32 v170, v52, v53\n"
  "v_cvt_pk_bf16_f32 v171, v54, v55\n"
  "v_cvt_pk_bf16_f32 v172, v56, v57\n"
  "v_cvt_pk_bf16_f32 v173, v58, v59\n"
  "v_cvt_pk_bf16_f32 v174, v60, v61\n"
  "v_cvt_pk_bf16_f32 v175, v62, v63\n"
  "v_pk_mul_f32 v[64:65], v[64:65], s[66:67]\n"
  "v_pk_mul_f32 v[66:67], v[66:67], s[66:67]\n"
  "v_pk_mul_f32 v[68:69], v[68:69], s[66:67]\n"
  "v_pk_mul_f32 v[70:71], v[70:71], s[66:67]\n"
  "v_pk_mul_f32 v[72:73], v[72:73], s[66:67]\n"
  "v_pk_mul_f32 v[74:75], v[74:75], s[66:67]\n"
  "v_pk_mul_f32 v[76:77], v[76:77], s[66:67]\n"
  "v_pk_mul_f32 v[78:79], v[78:79], s[66:67]\n"
  "v_cvt_pk_bf16_f32 v176, v64, v65\n"
  "v_cvt_pk_bf16_f32 v177, v66, v67\n"
  "v_cvt_pk_bf16_f32 v178, v68, v69\n"
  "v_cvt_pk_bf16_f32 v179, v70, v71\n"
  "v_cvt_pk_bf16_f32 v180, v72, v73\n"
  "v_cvt_pk_bf16_f32 v181, v74, v75\n"
  "v_cvt_pk_bf16_f32 v182, v76, v77\n"
  "v_cvt_pk_bf16_f32 v183, v78, v79\n"
  "v_pk_mul_f32 v[80:81], v[80:81], s[66:67]\n"
  "v_pk_mul_f32 v[82:83], v[82:83], s[66:67]\n"
  "v_pk_mul_f32 v[84:85], v[84:85], s[66:67]\n"
  "v_pk_mul_f32 v[86:87], v[86:87], s[66:67]\n"
  "v_pk_mul_f32 v[88:89], v[88:89], s[66:67]\n"
  "v_pk_mul_f32 v[90:91], v[90:91], s[66:67]\n"
  "v_pk_mul_f32 v[92:93], v[92:93], s[66:67]\n"
  "v_pk_mul_f32 v[94:95], v[94:95], s[66:67]\n"
  "v_cvt_pk_bf16_f32 v184, v80, v81\n"
  "v_cvt_pk_bf16_f32 v185, v82, v83\n"
  "v_cvt_pk_bf16_f32 v186, v84, v85\n"
  "v_cvt_pk_bf16_f32 v187, v86, v87\n"
  "v_cvt_pk_bf16_f32 v188, v88, v89\n"
  "v_cvt_pk_bf16_f32 v189, v90, v91\n"
  "v_cvt_pk_bf16_f32 v190, v92, v93\n"
  "v_cvt_pk_bf16_f32 v191, v94, v95\n"
  "v_pk_mul_f32 v[96:97], v[96:97], s[66:67]\n"
  "v_pk_mul_f32 v[98:99], v[98:99], s[66:67]\n"
  "v_pk_mul_f32 v[100:101], v[100:101], s[66:67]\n"
  "v_pk_mul_f32 v[102:103], v[102:103], s[66:67]\n"
  "v_pk_mul_f32 v[104:105], v[104:105], s[66:67]\n"
  "v_pk_mul_f32 v[106:107], v[106:107], s[66:67]\n"
  "v_pk_mul_f32 v[108:109], v[108:109], s[66:67]\n"
  "v_pk_mul_f32 v[110:111], v[110:111], s[66:67]\n"
  "v_cvt_pk_bf16_f32 v192, v96, v97\n"
  "v_cvt_pk_bf16_f32 v193, v98, v99\n"
  "v_cvt_pk_bf16_f32 v194, v100, v101\n"
  "v_cvt_pk_bf16_f32 v195, v102, v103\n"
  "v_cvt_pk_bf16_f32 v196, v104, v105\n"
  "v_cvt_pk_bf16_f32 v197, v106, v107\n"
  "v_cvt_pk_bf16_f32 v198, v108, v109\n"
  "v_cvt_pk_bf16_f32 v199, v110, v111\n"
  "v_pk_mul_f32 v[112:113], v[112:113], s[66:67]\n"
  "v_pk_mul_f32 v[114:115], v[114:115], s[66:67]\n"
  "v_pk_mul_f32 v[116:117], v[116:117], s[66:67]\n"
  "v_pk_mul_f32 v[118:119], v[118:119], s[66:67]\n"
  "v_pk_mul_f32 v[120:121], v[120:121], s[66:67]\n"
  "v_pk_mul_f32 v[122:123], v[122:123], s[66:67]\n"
  "v_pk_mul_f32 v[124:125], v[124:125], s[66:67]\n"
  "v_pk_mul_f32 v[126:127], v[126:127], s[66:67]\n"
  "v_cvt_pk_bf16_f32 v200, v112, v113\n"
  "v_cvt_pk_bf16_f32 v201, v114, v115\n"
  "v_cvt_pk_bf16_f32 v202, v116, v117\n"
  "v_cvt_pk_bf16_f32 v203, v118, v119\n"
  "v_cvt_pk_bf16_f32 v204, v120, v121\n"
  "v_cvt_pk_bf16_f32 v205, v122, v123\n"
  "v_cvt_pk_bf16_f32 v206, v124, v125\n"
  "v_cvt_pk_bf16_f32 v207, v126, v127\n"
  "v_pk_mul_f32 v[128:129], v[128:129], s[66:67]\n"
  "v_pk_mul_f32 v[130:131], v[130:131], s[66:67]\n"
  "v_pk_mul_f32 v[132:133], v[132:133], s[66:67]\n"
  "v_pk_mul_f32 v[134:135], v[134:135], s[66:67]\n"
  "v_pk_mul_f32 v[136:137], v[136:137], s[66:67]\n"
  "v_pk_mul_f32 v[138:139], v[138:139], s[66:67]\n"
  "v_pk_mul_f32 v[140:141], v[140:141], s[66:67]\n"
  "v_pk_mul_f32 v[142:143], v[142:143], s[66:67]\n"
  "v_cvt_pk_bf16_f32 v208, v128, v129\n"
  "v_cvt_pk_bf16_f32 v209, v130, v131\n"
  "v_cvt_pk_bf16_f32 v210, v132, v133\n"
  "v_cvt_pk_bf16_f32 v211, v134, v135\n"
  "v_cvt_pk_bf16_f32 v212, v136, v137\n"
  "v_cvt_pk_bf16_f32 v213, v138, v139\n"
  "v_cvt_pk_bf16_f32 v214, v140, v141\n"
  "v_cvt_pk_bf16_f32 v215, v142, v143\n"
  "v_pk_mul_f32 v[144:145], v[144:145], s[66:67]\n"
  "v_pk_mul_f32 v[146:147], v[146:147], s[66:67]\n"
  "v_pk_mul_f32 v[148:149], v[148:149], s[66:67]\n"
  "v_pk_mul_f32 v[150:151], v[150:151], s[66:67]\n"
  "v_pk_mul_f32 v[152:153], v[152:153], s[66:67]\n"
  "v_pk_mul_f32 v[154:155], v[154:155], s[66:67]\n"
  "v_pk_mul_f32 v[156:157], v[156:157], s[66:67]\n"
  "v_pk_mul_f32 v[158:159], v[158:159], s[66:67]\n"
  "v_cvt_pk_bf16_f32 v216, v144, v145\n"
  "v_cvt_pk_bf16_f32 v217, v146, v147\n"
  "v_cvt_pk_bf16_f32 v218, v148, v149\n"
  "v_cvt_pk_bf16_f32 v219, v150, v151\n"
  "v_cvt_pk_bf16_f32 v220, v152, v153\n"
  "v_cvt_pk_bf16_f32 v221, v154, v155\n"
  "v_cvt_pk_bf16_f32 v222, v156, v157\n"
  "v_cvt_pk_bf16_f32 v223, v158, v159\n"
  "s_waitcnt lgkmcnt(0)\n"
  "s_lshl_b32 s58, s12, 5\n"
  "s_add_u32 s58, s58, 8192\n"
  "s_lshr_b32 s59, s10, 5\n"
  "s_lshl_b32 s59, s59, 7\n"
  "s_add_u32 s59, s59, 4096\n"
  "s_cmp_lg_u32 s11, 0\n"
  "s_cbranch_scc1 Lfw_kv_d\n"
  "v_lshlrev_b32 v28, 2, v0\n"
  "v_mov_b32 v30, s58\n"
  "v_mov_b32 v31, s59\n"
  "v_cmp_gt_u32 vcc, 32, v0\n"
  "v_subrev_u32 v31, 128, v31\n"
  "v_cndmask_b32 v30, v31, v30, vcc\n"
  "v_add_u32 v28, v28, v30\n"
  "s_mov_b32 exec_lo, 0xff\n"
  "s_mov_b32 exec_hi, 0xffffffff\n"
  "Lfw_kv:\n"
  "buffer_load_dword v29, v28, s[92:95], 0 offen sc1\n"
  "s_waitcnt vmcnt(0)\n"
  "v_cmp_ne_u32 vcc, s71, v29\n"
  "s_nop 4\n"
  "s_cbranch_vccz Lfw_kv_k\n"
  "s_sleep 2\n"
  "s_branch Lfw_kv\n"
  "Lfw_kv_k:\n"
  "s_mov_b64 exec, -1\n"
  "Lfw_kv_d:\n"
  "s_barrier\n"
  "s_and_b32 m0, s44, 0xffff\n"
  "s_nop 0\n"
  "buffer_load_dwordx4 v1, s[24:27], s40 offen offset:0 sc1 lds\n"
  "buffer_load_dwordx4 v1, s[24:27], s40 offen offset:1024 sc1 lds\n"
  "buffer_load_dwordx4 v1, s[24:27], s40 offen offset:2048 sc1 lds\n"
  "buffer_load_dwordx4 v1, s[24:27], s40 offen offset:3072 sc1 lds\n"
  "s_add_u32 s44, s44, 16384\n"
  "s_add_u32 s40, s40, 16384\n"
  "s_min_u32 s40, s40, s42\n"
  "s_and_b32 s58, s45, 0xffff\n"
  "s_or_b32 m0, s58, 0x10000\n"
  "s_nop 0\n"
  "buffer_load_dwordx4 v1, s[28:31], s41 offen offset:0 sc1 lds\n"
  "buffer_load_dwordx4 v1, s[28:31], s41 offen offset:1024 sc1 lds\n"
  "buffer_load_dwordx4 v1, s[28:31], s41 offen offset:2048 sc1 lds\n"
  "buffer_load_dwordx4 v1, s[28:31], s41 offen offset:3072 sc1 lds\n"
  "s_add_u32 s45, s45, 16384\n"
  "s_add_u32 s41, s41, 16384\n"
  "s_min_u32 s41, s41, s42\n"
  "s_and_b32 m0, s44, 0xffff\n"
  "s_nop 0\n"
  "buffer_load_dwordx4 v1, s[24:27], s40 offen offset:0 sc1 lds\n"
  "buffer_load_dwordx4 v1, s[24:27], s40 offen offset:1024 sc1 lds\n"
  "buffer_load_dwordx4 v1, s[24:27], s40 offen offset:2048 sc1 lds\n"
  "buffer_load_dwordx4 v1, s[24:27], s40 offen offset:3072 sc1 lds\n"
  "s_add_u32 s44, s44, 16384\n"
  "s_add_u32 s40, s40, 16384\n"
  "s_min_u32 s40, s40, s42\n"
  "s_and_b32 s58, s45, 0xffff\n"
  "s_or_b32 m0, s58, 0x10000\n"
  "s_nop 0\n"
  "buffer_load_dwordx4 v1, s[28:31], s41 offen offset:0 sc1 lds\n"
  "buffer_load_dwordx4 v1, s[28:31], s41 offen offset:1024 sc1 lds\n"
  "buffer_load_dwordx4 v1, s[28:31], s41 offen offset:2048 sc1 lds\n"
  "buffer_load_dwordx4 v1, s[28:31], s41 offen offset:3072 sc1 lds\n"
  "s_add_u32 s45, s45, 16384\n"
  "s_add_u32 s41, s41, 16384\n"
  "s_min_u32 s41, s41, s42\n"
  "s_and_b32 m0, s44, 0xffff\n"
  "s_nop 0\n"
  "buffer_load_dwordx4 v1, s[24:27], s40 offen offset:0 sc1 lds\n"
  "buffer_load_dwordx4 v1, s[24:27], s40 offen offset:1024 sc1 lds\n"
  "buffer_load_dwordx4 v1, s[24:27], s40 offen offset:2048 sc1 lds\n"
  "buffer_load_dwordx4 v1, s[24:27], s40 offen offset:3072 sc1 lds\n"
  "s_add_u32 s44, s44, 16384\n"
  "s_add_u32 s40, s40, 16384\n"
  "s_min_u32 s40, s40, s42\n"
  "s_load_dwordx2 s[16:17], s[8:9], 32\n"
  "s_load_dwordx2 s[18:19], s[8:9], 0\n"
  "s_load_dwordx2 s[20:21], s[8:9], 88\n"
  "s_lshl_b32 s58, s10, 2\n"
  "s_add_u32 s58, s58, s11\n"
  "v_min_u32 v4, 17, v0\n"
  "s_mul_i32 s59, s58, 18\n"
  "v_add_u32 v4, s59, v4\n"
  "v_lshlrev_b32 v4, 7, v4\n"
  "v_min_u32 v5, 17, v0\n"
  "s_mul_i32 s59, s58, 18\n"
  "v_add_u32 v5, s59, v5\n"
  "v_lshlrev_b32 v5, 7, v5\n"
  "v_min_u32 v6, 35, v0\n"
  "s_mul_i32 s59, s58, 36\n"
  "v_add_u32 v6, s59, v6\n"
  "v_lshlrev_b32 v6, 7, v6\n"
  "s_waitcnt lgkmcnt(0)\n"
  "global_load_dword v8, v4, s[16:17]\n"
  "global_load_dword v9, v5, s[18:19]\n"
  "global_load_dword v10, v6, s[20:21]\n"
  "v_mov_b32 v236, 0xff800000\n"
  "v_mov_b32 v2, v1\n"
  "v_add_u32 v3, 0x10000, v1\n"
  "v_mov_b32 v112, 0\n"
  "v_mov_b32 v113, 0\n"
  "v_mov_b32 v114, 0\n"
  "v_mov_b32 v115, 0\n"
  "v_mov_b32 v116, 0\n"
  "v_mov_b32 v117, 0\n"
  "v_mov_b32 v118, 0\n"
  "v_mov_b32 v119, 0\n"
  "v_mov_b32 v120, 0\n"
  "v_mov_b32 v121, 0\n"
  "v_mov_b32 v122, 0\n"
  "v_mov_b32 v123, 0\n"
  "v_mov_b32 v124, 0\n"
  "v_mov_b32 v125, 0\n"
  "v_mov_b32 v126, 0\n"
  "v_mov_b32 v127, 0\n"
  "v_mov_b32 v224, 0\n"
  "v_mov_b32 v225, 0\n"
  "v_mov_b32 v226, 0\n"
  "v_mov_b32 v227, 0\n"
  "v_mov_b32 v228, 0\n"
  "v_mov_b32 v229, 0\n"
  "v_mov_b32 v230, 0\n"
  "v_mov_b32 v231, 0\n"
  "v_and_b32 v26, 31, v0\n"
  "v_lshrrev_b32 v27, 5, v0\n"
  "v_lshlrev_b32 v27, 2, v27\n"
  "v_sub_u32 v26, v26, v27\n"
  "s_lshl_b32 s58, s11, 5\n"
  "v_add_u32 v234, s58, v26\n"
  "v_subrev_u32 v235, 64, v234\n"
  "s_waitcnt vmcnt(19)\n"
  "s_barrier\n"
  "ds_read_b128 a[128:131], v2 offset:0\n"
  "ds_read_b128 a[132:135], v2 offset:1024\n"
  "ds_read_b128 a[136:139], v2 offset:2048\n"
  "ds_read_b128 a[140:143], v2 offset:3072\n"
  "ds_read_b128 a[144:147], v2 offset:4096\n"
  "ds_read_b128 a[148:151], v2 offset:5120\n"
  "ds_read_b128 a[152:155], v2 offset:6144\n"
  "ds_read_b128 a[156:159], v2 offset:7168\n"
  "ds_read_b128 a[160:163], v2 offset:8192\n"
  "ds_read_b128 a[164:167], v2 offset:9216\n"
  "ds_read_b128 a[168:171], v2 offset:10240\n"
  "ds_read_b128 a[172:175], v2 offset:11264\n"
  "ds_read_b128 a[176:179], v2 offset:12288\n"
  "ds_read_b128 a[180:183], v2 offset:13312\n"
  "ds_read_b128 a[184:187], v2 offset:14336\n"
  "ds_read_b128 a[188:191], v2 offset:15360\n"
  "v_add_u32 v2, 0x4000, v2\n"
  "v_and_b32 v2, 0xffff, v2\n"
  "s_waitcnt lgkmcnt(15)\n"
  "v_mfma_f32_32x32x16_bf16 v[32:47], a[128:131], v[160:163], 0\n"
  "v_mfma_f32_32x32x16_bf16 v[64:79], a[128:131], v[192:195], 0\n"
  "s_waitcnt lgkmcnt(14)\n"
  "v_mfma_f32_32x32x16_bf16 v[32:47], a[132:135], v[164:167], v[32:47]\n"
  "v_mfma_f32_32x32x16_bf16 v[64:79], a[132:135], v[196:199], v[64:79]\n"
  "s_waitcnt lgkmcnt(13)\n"
  "v_mfma_f32_32x32x16_bf16 v[32:47], a[136:139], v[168:171], v[32:47]\n"
  "v_mfma_f32_32x32x16_bf16 v[64:79], a[136:139], v[200:203], v[64:79]\n"
  "s_waitcnt lgkmcnt(12)\n"
  "v_mfma_f32_32x32x16_bf16 v[32:47], a[140:143], v[172:175], v[32:47]\n"
  "v_mfma_f32_32x32x16_bf16 v[64:79], a[140:143], v[204:207], v[64:79]\n"
  "s_waitcnt lgkmcnt(11)\n"
  "v_mfma_f32_32x32x16_bf16 v[32:47], a[144:147], v[176:179], v[32:47]\n"
  "v_mfma_f32_32x32x16_bf16 v[64:79], a[144:147], v[208:211], v[64:79]\n"
  "s_waitcnt lgkmcnt(10)\n"
  "v_mfma_f32_32x32x16_bf16 v[32:47], a[148:151], v[180:183], v[32:47]\n"
  "v_mfma_f32_32x32x16_bf16 v[64:79], a[148:151], v[212:215], v[64:79]\n"
  "s_waitcnt lgkmcnt(9)\n"
  "v_mfma_f32_32x32x16_bf16 v[32:47], a[152:155], v[184:187], v[32:47]\n"
  "v_mfma_f32_32x32x16_bf16 v[64:79], a[152:155], v[216:219], v[64:79]\n"
  "s_waitcnt lgkmcnt(8)\n"
  "v_mfma_f32_32x32x16_bf16 v[32:47], a[156:159], v[188:191], v[32:47]\n"
  "v_mfma_f32_32x32x16_bf16 v[64:79], a[156:159], v[220:223], v[64:79]\n"
  "s_nop 10\n"
  "v_mov_b32 v232, v32\n"
  "v_mov_b32 v26, v32\n"
  "s_nop 1\n"
  "v_permlane32_swap_b32 v232, v26\n"
  "v_sub_f32 v128, 0, v232\n"
  "v_sub_f32 v129, 0, v232\n"
  "v_sub_f32 v130, 0, v232\n"
  "v_sub_f32 v131, 0, v232\n"
  "v_sub_f32 v132, 0, v232\n"
  "v_sub_f32 v133, 0, v232\n"
  "v_sub_f32 v134, 0, v232\n"
  "v_sub_f32 v135, 0, v232\n"
  "v_sub_f32 v136, 0, v232\n"
  "v_sub_f32 v137, 0, v232\n"
  "v_sub_f32 v138, 0, v232\n"
  "v_sub_f32 v139, 0, v232\n"
  "v_sub_f32 v140, 0, v232\n"
  "v_sub_f32 v141, 0, v232\n"
  "v_sub_f32 v142, 0, v232\n"
  "v_sub_f32 v143, 0, v232\n"
  "v_mov_b32 v233, v64\n"
  "v_mov_b32 v26, v64\n"
  "s_nop 1\n"
  "v_permlane32_swap_b32 v233, v26\n"
  "v_sub_f32 v144, 0, v233\n"
  "v_sub_f32 v145, 0, v233\n"
  "v_sub_f32 v146, 0, v233\n"
  "v_sub_f32 v147, 0, v233\n"
  "v_sub_f32 v148, 0, v233\n"
  "v_sub_f32 v149, 0, v233\n"
  "v_sub_f32 v150, 0, v233\n"
  "v_sub_f32 v151, 0, v233\n"
  "v_sub_f32 v152, 0, v233\n"
  "v_sub_f32 v153, 0, v233\n"
  "v_sub_f32 v154, 0, v233\n"
  "v_sub_f32 v155, 0, v233\n"
  "v_sub_f32 v156, 0, v233\n"
  "v_sub_f32 v157, 0, v233\n"
  "v_sub_f32 v158, 0, v233\n"
  "v_sub_f32 v159, 0, v233\n"
  "v_mov_b32 v64, v236\n"
  "v_mov_b32 v65, v236\n"
  "v_mov_b32 v66, v236\n"
  "v_mov_b32 v67, v236\n"
  "v_mov_b32 v68, v236\n"
  "v_mov_b32 v69, v236\n"
  "v_mov_b32 v70, v236\n"
  "v_mov_b32 v71, v236\n"
  "v_mov_b32 v72, v236\n"
  "v_mov_b32 v73, v236\n"
  "v_mov_b32 v74, v236\n"
  "v_mov_b32 v75, v236\n"
  "v_mov_b32 v76, v236\n"
  "v_mov_b32 v77, v236\n"
  "v_mov_b32 v78, v236\n"
  "v_mov_b32 v79, v236\n"
  "v_mov_b32 v80, v236\n"
  "v_mov_b32 v81, v236\n"
  "v_mov_b32 v82, v236\n"
  "v_mov_b32 v83, v236\n"
  "v_mov_b32 v84, v236\n"
  "v_mov_b32 v85, v236\n"
  "v_mov_b32 v86, v236\n"
  "v_mov_b32 v87, v236\n"
  "v_mov_b32 v88, v236\n"
  "v_mov_b32 v89, v236\n"
  "v_mov_b32 v90, v236\n"
  "v_mov_b32 v91, v236\n"
  "v_mov_b32 v92, v236\n"
  "v_mov_b32 v93, v236\n"
  "v_mov_b32 v94, v236\n"
  "v_mov_b32 v95, v236\n"
  "s_lshl_b32 s46, s13, 1\n"
  "s_cmp_eq_u32 s46, 0\n"
  "s_cbranch_scc1 Ldm\n"
  "Ld:\n"
  "s_waitcnt lgkmcnt(9)\n"
  "v_mfma_f32_32x32x16_bf16 v[32:47], a[128:131], v[160:163], v[128:143]\n"
  "v_exp_f32 v224, v84\n"
  "v_add_f32 v229, v229, v227\n"
  "v_cvt_pk_bf16_f32 v121, v226, v227\n"
  "v_mfma_f32_32x32x16_bf16 v[32:47], a[132:135], v[164:167], v[32:47]\n"
  "v_exp_f32 v225, v85\n"
  "v_add_f32 v229, v229, v224\n"
  "v_mfma_f32_32x32x16_bf16 v[32:47], a[136:139], v[168:171], v[32:47]\n"
  "v_exp_f32 v226, v86\n"
  "v_add_f32 v229, v229, v225\n"
  "v_cvt_pk_bf16_f32 v122, v224, v225\n"
  "v_mfma_f32_32x32x16_bf16 v[32:47], a[140:143], v[172:175], v[32:47]\n"
  "v_exp_f32 v227, v87\n"
  "v_add_f32 v229, v229, v226\n"
  "v_mfma_f32_32x32x16_bf16 v[32:47], a[144:147], v[176:179], v[32:47]\n"
  "v_exp_f32 v224, v88\n"
  "v_add_f32 v229, v229, v227\n"
  "v_cvt_pk_bf16_f32 v123, v226, v227\n"
  "v_mfma_f32_32x32x16_bf16 v[32:47], a[148:151], v[180:183], v[32:47]\n"
  "v_exp_f32 v225, v89\n"
  "v_add_f32 v229, v229, v224\n"
  "v_mfma_f32_32x32x16_bf16 v[32:47], a[152:155], v[184:187], v[32:47]\n"
  "v_exp_f32 v226, v90\n"
  "v_add_f32 v229, v229, v225\n"
  "v_cvt_pk_bf16_f32 v124, v224, v225\n"
  "s_waitcnt lgkmcnt(2)\n"
  "v_mfma_f32_32x32x16_bf16 v[32:47], a[156:159], v[188:191], v[32:47]\n"
  "v_exp_f32 v227, v91\n"
  "v_add_f32 v229, v229, v226\n"
  "s_waitcnt lgkmcnt(1)\n"
  "v_mfma_f32_32x32x16_bf16 v[48:63], a[160:163], v[160:163], v[128:143]\n"
  "v_exp_f32 v224, v92\n"
  "v_add_f32 v229, v229, v227\n"
  "v_cvt_pk_bf16_f32 v125, v226, v227\n"
  "v_mfma_f32_32x32x16_bf16 v[48:63], a[164:167], v[164:167], v[48:63]\n"
  "v_exp_f32 v225, v93\n"
  "v_add_f32 v229, v229, v224\n"
  "v_mfma_f32_32x32x16_bf16 v[48:63], a[168:171], v[168:171], v[48:63]\n"
  "v_exp_f32 v226, v94\n"
  "v_add_f32 v229, v229, v225\n"
  "v_cvt_pk_bf16_f32 v126, v224, v225\n"
  "v_mfma_f32_32x32x16_bf16 v[48:63], a[172:175], v[172:175], v[48:63]\n"
  "v_exp_f32 v227, v95\n"
  "v_add_f32 v229, v229, v226\n"
  "v_mfma_f32_32x32x16_bf16 v[48:63], a[176:179], v[176:179], v[48:63]\n"
  "v_add_f32 v229, v229, v227\n"
  "v_cvt_pk_bf16_f32 v127, v226, v227\n"
  "v_exp_f32 v224, v32\n"
  "v_mfma_f32_32x32x16_bf16 v[48:63], a[180:183], v[180:183], v[48:63]\n"
  "v_exp_f32 v225, v33\n"
  "s_waitcnt lgkmcnt(0)\n"
  "v_mfma_f32_32x32x16_bf16 v[48:63], a[184:187], v[184:187], v[48:63]\n"
  "v_exp_f32 v226, v34\n"
  "v_add_f32 v228, v224, v225\n"
  "v_cvt_pk_bf16_f32 v96, v224, v225\n"
  "v_cmp_lt_f32 vcc, s47, v229\n"
  "s_waitcnt lgkmcnt(0)\n"
  "v_mfma_f32_32x32x16_bf16 v[48:63], a[188:191], v[188:191], v[48:63]\n"
  "v_exp_f32 v227, v35\n"
  "v_add_f32 v228, v228, v226\n"
  "s_cbranch_vccnz Lfb_DY\n"
  "Lret_DY:\n"
  "v_add_f32 v231, v231, v229\n"
  "v_mfma_f32_32x32x16_bf16 a[64:79], a[192:195], v[112:115], a[64:79]\n"
  "v_exp_f32 v224, v36\n"
  "v_add_f32 v228, v228, v227\n"
  "v_cvt_pk_bf16_f32 v97, v226, v227\n"
  "v_mfma_f32_32x32x16_bf16 a[80:95], a[196:199], v[112:115], a[80:95]\n"
  "v_exp_f32 v225, v37\n"
  "v_add_f32 v228, v228, v224\n"
  "v_mfma_f32_32x32x16_bf16 a[96:111], a[200:203], v[112:115], a[96:111]\n"
  "v_exp_f32 v226, v38\n"
  "v_add_f32 v228, v228, v225\n"
  "v_cvt_pk_bf16_f32 v98, v224, v225\n"
  "v_mfma_f32_32x32x16_bf16 a[112:127], a[204:207], v[112:115], a[112:127]\n"
  "v_exp_f32 v227, v39\n"
  "v_add_f32 v228, v228, v226\n"
  "v_mfma_f32_32x32x16_bf16 a[64:79], a[208:211], v[116:119], a[64:79]\n"
  "v_exp_f32 v224, v40\n"
  "v_add_f32 v228, v228, v227\n"
  "v_cvt_pk_bf16_f32 v99, v226, v227\n"
  "v_mfma_f32_32x32x16_bf16 a[80:95], a[212:215], v[116:119], a[80:95]\n"
  "v_exp_f32 v225, v41\n"
  "v_add_f32 v228, v228, v224\n"
  "v_mfma_f32_32x32x16_bf16 a[96:111], a[216:219], v[116:119], a[96:111]\n"
  "v_exp_f32 v226, v42\n"
  "v_add_f32 v228, v228, v225\n"
  "v_cvt_pk_bf16_f32 v100, v224, v225\n"
  "v_mfma_f32_32x32x16_bf16 a[112:127], a[220:223], v[116:119], a[112:127]\n"
  "v_exp_f32 v227, v43\n"
  "v_add_f32 v228, v228, v226\n"
  "v_mfma_f32_32x32x16_bf16 a[64:79], a[224:227], v[120:123], a[64:79]\n"
  "v_exp_f32 v224, v44\n"
  "v_add_f32 v228, v228, v227\n"
  "v_cvt_pk_bf16_f32 v101, v226, v227\n"
  "v_mfma_f32_32x32x16_bf16 a[80:95], a[228:231], v[120:123], a[80:95]\n"
  "v_exp_f32 v225, v45\n"
  "v_add_f32 v228, v228, v224\n"
  "v_mfma_f32_32x32x16_bf16 a[96:111], a[232:235], v[120:123], a[96:111]\n"
  "v_exp_f32 v226, v46\n"
  "v_add_f32 v228, v228, v225\n"
  "v_cvt_pk_bf16_f32 v102, v224, v225\n"
  "v_mfma_f32_32x32x16_bf16 a[112:127], a[236:239], v[120:123], a[112:127]\n"
  "v_exp_f32 v227, v47\n"
  "v_add_f32 v228, v228, v226\n"
  "v_mfma_f32_32x32x16_bf16 a[64:79], a[240:243], v[124:127], a[64:79]\n"
  "v_exp_f32 v224, v48\n"
  "v_add_f32 v228, v228, v227\n"
  "v_cvt_pk_bf16_f32 v103, v226, v227\n"
  "v_mfma_f32_32x32x16_bf16 a[80:95], a[244:247], v[124:127], a[80:95]\n"
  "v_exp_f32 v225, v49\n"
  "v_add_f32 v228, v228, v224\n"
  "v_mfma_f32_32x32x16_bf16 a[96:111], a[248:251], v[124:127], a[96:111]\n"
  "v_exp_f32 v226, v50\n"
  "v_add_f32 v228, v228, v225\n"
  "v_cvt_pk_bf16_f32 v104, v224, v225\n"
  "v_mfma_f32_32x32x16_bf16 a[112:127], a[252:255], v[124:127], a[112:127]\n"
  "v_exp_f32 v227, v51\n"
  "v_add_f32 v228, v228, v226\n"
  "s_waitcnt vmcnt(8)\n"
  "s_waitcnt vmcnt(11)\n"
  "s_barrier\n"
  "v_mfma_f32_32x32x16_bf16 v[64:79], a[128:131], v[192:195], v[144:159]\n"
  "ds_read_b128 a[192:195], v3 offset:0\n"
  "v_exp_f32 v224, v52\n"
  "v_add_f32 v228, v228, v227\n"
  "v_cvt_pk_bf16_f32 v105, v226, v227\n"
  "v_mfma_f32_32x32x16_bf16 v[64:79], a[132:135], v[196:199], v[64:79]\n"
  "ds_read_b128 a[196:199], v3 offset:1024\n"
  "v_exp_f32 v225, v53\n"
  "v_add_f32 v228, v228, v224\n"
  "s_and_b32 m0, s44, 0xffff\n"
  "v_mfma_f32_32x32x16_bf16 v[64:79], a[136:139], v[200:203], v[64:79]\n"
  "ds_read_b128 a[200:203], v3 offset:2048\n"
  "v_exp_f32 v226, v54\n"
  "v_add_f32 v228, v228, v225\n"
  "v_cvt_pk_bf16_f32 v106, v224, v225\n"
  "v_mfma_f32_32x32x16_bf16 v[64:79], a[140:143], v[204:207], v[64:79]\n"
  "ds_read_b128 a[204:207], v3 offset:3072\n"
  "v_exp_f32 v227, v55\n"
  "v_add_f32 v228, v228, v226\n"
  "buffer_load_dwordx4 v1, s[24:27], s40 offen offset:0 sc1 lds\n"
  "v_mfma_f32_32x32x16_bf16 v[64:79], a[144:147], v[208:211], v[64:79]\n"
  "ds_read_b128 a[208:211], v3 offset:4096\n"
  "v_exp_f32 v224, v56\n"
  "v_add_f32 v228, v228, v227\n"
  "v_cvt_pk_bf16_f32 v107, v226, v227\n"
  "v_mfma_f32_32x32x16_bf16 v[64:79], a[148:151], v[212:215], v[64:79]\n"
  "ds_read_b128 a[212:215], v3 offset:5120\n"
  "v_exp_f32 v225, v57\n"
  "v_add_f32 v228, v228, v224\n"
  "v_mfma_f32_32x32x16_bf16 v[64:79], a[152:155], v[216:219], v[64:79]\n"
  "ds_read_b128 a[216:219], v3 offset:6144\n"
  "v_exp_f32 v226, v58\n"
  "v_add_f32 v228, v228, v225\n"
  "v_cvt_pk_bf16_f32 v108, v224, v225\n"
  "buffer_load_dwordx4 v1, s[24:27], s40 offen offset:1024 sc1 lds\n"
  "v_mfma_f32_32x32x16_bf16 v[64:79], a[156:159], v[220:223], v[64:79]\n"
  "ds_read_b128 a[220:223], v3 offset:7168\n"
  "v_exp_f32 v227, v59\n"
  "v_add_f32 v228, v228, v226\n"
  "v_mfma_f32_32x32x16_bf16 v[80:95], a[160:163], v[192:195], v[144:159]\n"
  "ds_read_b128 a[224:227], v3 offset:8192\n"
  "v_exp_f32 v224, v60\n"
  "v_add_f32 v228, v228, v227\n"
  "v_cvt_pk_bf16_f32 v109, v226, v227\n"
  "v_mfma_f32_32x32x16_bf16 v[80:95], a[164:167], v[196:199], v[80:95]\n"
  "ds_read_b128 a[228:231], v3 offset:9216\n"
  "v_exp_f32 v225, v61\n"
  "v_add_f32 v228, v228, v224\n"
  "buffer_load_dwordx4 v1, s[24:27], s40 offen offset:2048 sc1 lds\n"
  "v_mfma_f32_32x32x16_bf16 v[80:95], a[168:171], v[200:203], v[80:95]\n"
  "ds_read_b128 a[232:235], v3 offset:10240\n"
  "v_exp_f32 v226, v62\n"
  "v_add_f32 v228, v228, v225\n"
  "v_cvt_pk_bf16_f32 v110, v224, v225\n"
  "v_mfma_f32_32x32x16_bf16 v[80:95], a[172:175], v[204:207], v[80:95]\n"
  "ds_read_b128 a[236:239], v3 offset:11264\n"
  "v_exp_f32 v227, v63\n"
  "v_add_f32 v228, v228, v226\n"
  "v_mfma_f32_32x32x16_bf16 v[80:95], a[176:179], v[208:211], v[80:95]\n"
  "ds_read_b128 a[240:243], v3 offset:12288\n"
  "v_add_f32 v228, v228, v227\n"
  "v_cvt_pk_bf16_f32 v111, v226, v227\n"
  "v_exp_f32 v224, v64\n"
  "buffer_load_dwordx4 v1, s[24:27], s40 offen offset:3072 sc1 lds\n"
  "s_and_b32 s58, s45, 0xffff\n"
  "v_mfma_f32_32x32x16_bf16 v[80:95], a[180:183], v[212:215], v[80:95]\n"
  "ds_read_b128 a[244:247], v3 offset:13312\n"
  "v_exp_f32 v225, v65\n"
  "v_mfma_f32_32x32x16_bf16 v[80:95], a[184:187], v[216:219], v[80:95]\n"
  "ds_read_b128 a[248:251], v3 offset:14336\n"
  "v_exp_f32 v226, v66\n"
  "v_add_f32 v229, v224, v225\n"
  "v_cvt_pk_bf16_f32 v112, v224, v225\n"
  "v_cmp_lt_f32 vcc, s47, v228\n"
  "s_add_u32 s44, s44, 16384\n"
  "s_or_b32 m0, s58, 0x10000\n"
  "v_mfma_f32_32x32x16_bf16 v[80:95], a[188:191], v[220:223], v[80:95]\n"
  "ds_read_b128 a[252:255], v3 offset:15360\n"
  "v_exp_f32 v227, v67\n"
  "v_add_f32 v229, v229, v226\n"
  "v_add_u32 v26, 0x4000, v3\n"
  "v_bfi_b32 v3, s68, v26, v3\n"
  "s_cbranch_vccnz Lfb_DX\n"
  "Lret_DX:\n"
  "v_add_f32 v230, v230, v228\n"
  "s_waitcnt lgkmcnt(11)\n"
  "v_mfma_f32_32x32x16_bf16 a[0:15], a[192:195], v[96:99], a[0:15]\n"
  "ds_read_b128 a[128:131], v2 offset:0\n"
  "v_exp_f32 v224, v68\n"
  "v_add_f32 v229, v229, v227\n"
  "v_cvt_pk_bf16_f32 v113, v226, v227\n"
  "v_mfma_f32_32x32x16_bf16 a[16:31], a[196:199], v[96:99], a[16:31]\n"
  "ds_read_b128 a[132:135], v2 offset:1024\n"
  "v_exp_f32 v225, v69\n"
  "v_add_f32 v229, v229, v224\n"
  "buffer_load_dwordx4 v1, s[28:31], s41 offen offset:0 sc1 lds\n"
  "s_add_u32 s40, s40, 16384\n"
  "v_mfma_f32_32x32x16_bf16 a[32:47], a[200:203], v[96:99], a[32:47]\n"
  "ds_read_b128 a[136:139], v2 offset:2048\n"
  "v_exp_f32 v226, v70\n"
  "v_add_f32 v229, v229, v225\n"
  "v_cvt_pk_bf16_f32 v114, v224, v225\n"
  "v_mfma_f32_32x32x16_bf16 a[48:63], a[204:207], v[96:99], a[48:63]\n"
  "ds_read_b128 a[140:143], v2 offset:3072\n"
  "v_exp_f32 v227, v71\n"
  "v_add_f32 v229, v229, v226\n"
  "v_mfma_f32_32x32x16_bf16 a[0:15], a[208:211], v[100:103], a[0:15]\n"
  "ds_read_b128 a[144:147], v2 offset:4096\n"
  "v_exp_f32 v224, v72\n"
  "v_add_f32 v229, v229, v227\n"
  "v_cvt_pk_bf16_f32 v115, v226, v227\n"
  "buffer_load_dwordx4 v1, s[28:31], s41 offen offset:1024 sc1 lds\n"
  "s_min_u32 s40, s40, s42\n"
  "s_waitcnt lgkmcnt(11)\n"
  "v_mfma_f32_32x32x16_bf16 a[16:31], a[212:215], v[100:103], a[16:31]\n"
  "ds_read_b128 a[148:151], v2 offset:5120\n"
  "v_exp_f32 v225, v73\n"
  "v_add_f32 v229, v229, v224\n"
  "v_mfma_f32_32x32x16_bf16 a[32:47], a[216:219], v[100:103], a[32:47]\n"
  "ds_read_b128 a[152:155], v2 offset:6144\n"
  "v_exp_f32 v226, v74\n"
  "v_add_f32 v229, v229, v225\n"
  "v_cvt_pk_bf16_f32 v116, v224, v225\n"
  "v_mfma_f32_32x32x16_bf16 a[48:63], a[220:223], v[100:103], a[48:63]\n"
  "ds_read_b128 a[156:159], v2 offset:7168\n"
  "v_exp_f32 v227, v75\n"
  "v_add_f32 v229, v229, v226\n"
  "buffer_load_dwordx4 v1, s[28:31], s41 offen offset:2048 sc1 lds\n"
  "v_mfma_f32_32x32x16_bf16 a[0:15], a[224:227], v[104:107], a[0:15]\n"
  "ds_read_b128 a[160:163], v2 offset:8192\n"
  "v_exp_f32 v224, v76\n"
  "v_add_f32 v229, v229, v227\n"
  "v_cvt_pk_bf16_f32 v117, v226, v227\n"
  "v_mfma_f32_32x32x16_bf16 a[16:31], a[228:231], v[104:107], a[16:31]\n"
  "ds_read_b128 a[164:167], v2 offset:9216\n"
  "v_exp_f32 v225, v77\n"
  "v_add_f32 v229, v229, v224\n"
  "s_waitcnt lgkmcnt(11)\n"
  "v_mfma_f32_32x32x16_bf16 a[32:47], a[232:235], v[104:107], a[32:47]\n"
  "ds_read_b128 a[168:171], v2 offset:10240\n"
  "v_exp_f32 v226, v78\n"
  "v_add_f32 v229, v229, v225\n"
  "v_cvt_pk_bf16_f32 v118, v224, v225\n"
  "buffer_load_dwordx4 v1, s[28:31], s41 offen offset:3072 sc1 lds\n"
  "s_add_u32 s45, s45, 16384\n"
  "v_mfma_f32_32x32x16_bf16 a[48:63], a[236:239], v[104:107], a[48:63]\n"
  "ds_read_b128 a[172:175], v2 offset:11264\n"
  "v_exp_f32 v227, v79\n"
  "v_add_f32 v229, v229, v226\n"
  "v_mfma_f32_32x32x16_bf16 a[0:15], a[240:243], v[108:111], a[0:15]\n"
  "ds_read_b128 a[176:179], v2 offset:12288\n"
  "v_exp_f32 v224, v80\n"
  "v_add_f32 v229, v229, v227\n"
  "v_cvt_pk_bf16_f32 v119, v226, v227\n"
  "s_add_u32 s41, s41, 16384\n"
  "s_min_u32 s41, s41, s42\n"
  "v_mfma_f32_32x32x16_bf16 a[16:31], a[244:247], v[108:111], a[16:31]\n"
  "ds_read_b128 a[180:183], v2 offset:13312\n"
  "v_exp_f32 v225, v81\n"
  "v_add_f32 v229, v229, v224\n"
  "v_mfma_f32_32x32x16_bf16 a[32:47], a[248:251], v[108:111], a[32:47]\n"
  "ds_read_b128 a[184:187], v2 offset:14336\n"
  "v_exp_f32 v226, v82\n"
  "v_add_f32 v229, v229, v225\n"
  "v_cvt_pk_bf16_f32 v120, v224, v225\n"
  "s_waitcnt lgkmcnt(15)\n"
  "v_mfma_f32_32x32x16_bf16 a[48:63], a[252:255], v[108:111], a[48:63]\n"
  "ds_read_b128 a[188:191], v2 offset:15360\n"
  "v_exp_f32 v227, v83\n"
  "v_add_f32 v229, v229, v226\n"
  "v_add_u32 v2, 0x4000, v2\n"
  "v_and_b32 v2, 0xffff, v2\n"
  "s_sub_u32 s46, s46, 1\n"
  "s_cmp_lg_u32 s46, 0\n"
  "s_cbranch_scc1 Ld\n"
  "Ldm:\n"
  "s_mov_b32 s46, 2\n"
  "Ldm2:\n"
  "s_waitcnt lgkmcnt(9)\n"
  "v_mfma_f32_32x32x16_bf16 v[32:47], a[128:131], v[160:163], v[128:143]\n"
  "v_exp_f32 v224, v84\n"
  "v_add_f32 v229, v229, v227\n"
  "v_cvt_pk_bf16_f32 v121, v226, v227\n"
  "v_mfma_f32_32x32x16_bf16 v[32:47], a[132:135], v[164:167], v[32:47]\n"
  "v_exp_f32 v225, v85\n"
  "v_add_f32 v229, v229, v224\n"
  "v_mfma_f32_32x32x16_bf16 v[32:47], a[136:139], v[168:171], v[32:47]\n"
  "v_exp_f32 v226, v86\n"
  "v_add_f32 v229, v229, v225\n"
  "v_cvt_pk_bf16_f32 v122, v224, v225\n"
  "v_mfma_f32_32x32x16_bf16 v[32:47], a[140:143], v[172:175], v[32:47]\n"
  "v_exp_f32 v227, v87\n"
  "v_add_f32 v229, v229, v226\n"
  "v_mfma_f32_32x32x16_bf16 v[32:47], a[144:147], v[176:179], v[32:47]\n"
  "v_exp_f32 v224, v88\n"
  "v_add_f32 v229, v229, v227\n"
  "v_cvt_pk_bf16_f32 v123, v226, v227\n"
  "v_mfma_f32_32x32x16_bf16 v[32:47], a[148:151], v[180:183], v[32:47]\n"
  "v_exp_f32 v225, v89\n"
  "v_add_f32 v229, v229, v224\n"
  "v_mfma_f32_32x32x16_bf16 v[32:47], a[152:155], v[184:187], v[32:47]\n"
  "v_exp_f32 v226, v90\n"
  "v_add_f32 v229, v229, v225\n"
  "v_cvt_pk_bf16_f32 v124, v224, v225\n"
  "s_waitcnt lgkmcnt(3)\n"
  "v_mfma_f32_32x32x16_bf16 v[32:47], a[156:159], v[188:191], v[32:47]\n"
  "v_exp_f32 v227, v91\n"
  "v_add_f32 v229, v229, v226\n"
  "s_waitcnt lgkmcnt(3)\n"
  "v_mfma_f32_32x32x16_bf16 v[48:63], a[160:163], v[160:163], v[128:143]\n"
  "v_exp_f32 v224, v92\n"
  "v_add_f32 v229, v229, v227\n"
  "v_cvt_pk_bf16_f32 v125, v226, v227\n"
  "v_mfma_f32_32x32x16_bf16 v[48:63], a[164:167], v[164:167], v[48:63]\n"
  "v_exp_f32 v225, v93\n"
  "v_add_f32 v229, v229, v224\n"
  "v_mfma_f32_32x32x16_bf16 v[48:63], a[168:171], v[168:171], v[48:63]\n"
  "v_exp_f32 v226, v94\n"
  "v_add_f32 v229, v229, v225\n"
  "v_cvt_pk_bf16_f32 v126, v224, v225\n"
  "v_cmp_gt_i32 s[50:51], 0, v234\n"
  "v_mfma_f32_32x32x16_bf16 v[48:63], a[172:175], v[172:175], v[48:63]\n"
  "v_exp_f32 v227, v95\n"
  "v_add_f32 v229, v229, v226\n"
  "v_cmp_gt_i32 s[52:53], 1, v234\n"
  "v_cndmask_b32 v32, v32, v236, s[50:51]\n"
  "v_mfma_f32_32x32x16_bf16 v[48:63], a[176:179], v[176:179], v[48:63]\n"
  "v_add_f32 v229, v229, v227\n"
  "v_cvt_pk_bf16_f32 v127, v226, v227\n"
  "v_cmp_gt_i32 s[54:55], 2, v234\n"
  "v_cndmask_b32 v33, v33, v236, s[52:53]\n"
  "v_exp_f32 v224, v32\n"
  "s_waitcnt lgkmcnt(0)\n"
  "v_mfma_f32_32x32x16_bf16 v[48:63], a[180:183], v[180:183], v[48:63]\n"
  "v_cmp_gt_i32 s[56:57], 3, v234\n"
  "v_cndmask_b32 v34, v34, v236, s[54:55]\n"
  "v_exp_f32 v225, v33\n"
  "v_mfma_f32_32x32x16_bf16 v[48:63], a[184:187], v[184:187], v[48:63]\n"
  "v_cmp_gt_i32 s[50:51], 8, v234\n"
  "v_cndmask_b32 v35, v35, v236, s[56:57]\n"
  "v_exp_f32 v226, v34\n"
  "v_add_f32 v228, v224, v225\n"
  "v_cvt_pk_bf16_f32 v96, v224, v225\n"
  "v_cmp_lt_f32 vcc, s47, v229\n"
  "v_mfma_f32_32x32x16_bf16 v[48:63], a[188:191], v[188:191], v[48:63]\n"
  "v_cmp_gt_i32 s[52:53], 9, v234\n"
  "v_cndmask_b32 v36, v36, v236, s[50:51]\n"
  "v_exp_f32 v227, v35\n"
  "v_add_f32 v228, v228, v226\n"
  "s_cbranch_vccnz Lfb_MY\n"
  "Lret_MY:\n"
  "v_add_f32 v231, v231, v229\n"
  "v_mfma_f32_32x32x16_bf16 a[64:79], a[192:195], v[112:115], a[64:79]\n"
  "v_cmp_gt_i32 s[54:55], 10, v234\n"
  "v_cndmask_b32 v37, v37, v236, s[52:53]\n"
  "v_exp_f32 v224, v36\n"
  "v_add_f32 v228, v228, v227\n"
  "v_cvt_pk_bf16_f32 v97, v226, v227\n"
  "v_mfma_f32_32x32x16_bf16 a[80:95], a[196:199], v[112:115], a[80:95]\n"
  "v_cmp_gt_i32 s[56:57], 11, v234\n"
  "v_cndmask_b32 v38, v38, v236, s[54:55]\n"
  "v_exp_f32 v225, v37\n"
  "v_add_f32 v228, v228, v224\n"
  "v_mfma_f32_32x32x16_bf16 a[96:111], a[200:203], v[112:115], a[96:111]\n"
  "v_cmp_gt_i32 s[50:51], 16, v234\n"
  "v_cndmask_b32 v39, v39, v236, s[56:57]\n"
  "v_exp_f32 v226, v38\n"
  "v_add_f32 v228, v228, v225\n"
  "v_cvt_pk_bf16_f32 v98, v224, v225\n"
  "v_mfma_f32_32x32x16_bf16 a[112:127], a[204:207], v[112:115], a[112:127]\n"
  "v_cmp_gt_i32 s[52:53], 17, v234\n"
  "v_cndmask_b32 v40, v40, v236, s[50:51]\n"
  "v_exp_f32 v227, v39\n"
  "v_add_f32 v228, v228, v226\n"
  "v_mfma_f32_32x32x16_bf16 a[64:79], a[208:211], v[116:119], a[64:79]\n"
  "v_cmp_gt_i32 s[54:55], 18, v234\n"
  "v_cndmask_b32 v41, v41, v236, s[52:53]\n"
  "v_exp_f32 v224, v40\n"
  "v_add_f32 v228, v228, v227\n"
  "v_cvt_pk_bf16_f32 v99, v226, v227\n"
  "v_mfma_f32_32x32x16_bf16 a[80:95], a[212:215], v[116:119], a[80:95]\n"
  "v_cmp_gt_i32 s[56:57], 19, v234\n"
  "v_cndmask_b32 v42, v42, v236, s[54:55]\n"
  "v_exp_f32 v225, v41\n"
  "v_add_f32 v228, v228, v224\n"
  "v_mfma_f32_32x32x16_bf16 a[96:111], a[216:219], v[116:119], a[96:111]\n"
  "v_cmp_gt_i32 s[50:51], 24, v234\n"
  "v_cndmask_b32 v43, v43, v236, s[56:57]\n"
  "v_exp_f32 v226, v42\n"
  "v_add_f32 v228, v228, v225\n"
  "v_cvt_pk_bf16_f32 v100, v224, v225\n"
  "v_mfma_f32_32x32x16_bf16 a[112:127], a[220:223], v[116:119], a[112:127]\n"
  "v_cmp_gt_i32 s[52:53], 25, v234\n"
  "v_cndmask_b32 v44, v44, v236, s[50:51]\n"
  "v_exp_f32 v227, v43\n"
  "v_add_f32 v228, v228, v226\n"
  "v_mfma_f32_32x32x16_bf16 a[64:79], a[224:227], v[120:123], a[64:79]\n"
  "v_cmp_gt_i32 s[54:55], 26, v234\n"
  "v_cndmask_b32 v45, v45, v236, s[52:53]\n"
  "v_exp_f32 v224, v44\n"
  "v_add_f32 v228, v228, v227\n"
  "v_cvt_pk_bf16_f32 v101, v226, v227\n"
  "v_mfma_f32_32x32x16_bf16 a[80:95], a[228:231], v[120:123], a[80:95]\n"
  "v_cmp_gt_i32 s[56:57], 27, v234\n"
  "v_cndmask_b32 v46, v46, v236, s[54:55]\n"
  "v_exp_f32 v225, v45\n"
  "v_add_f32 v228, v228, v224\n"
  "v_mfma_f32_32x32x16_bf16 a[96:111], a[232:235], v[120:123], a[96:111]\n"
  "v_cmp_gt_i32 s[50:51], 32, v234\n"
  "v_cndmask_b32 v47, v47, v236, s[56:57]\n"
  "v_exp_f32 v226, v46\n"
  "v_add_f32 v228, v228, v225\n"
  "v_cvt_pk_bf16_f32 v102, v224, v225\n"
  "v_mfma_f32_32x32x16_bf16 a[112:127], a[236:239], v[120:123], a[112:127]\n"
  "v_cmp_gt_i32 s[52:53], 33, v234\n"
  "v_cndmask_b32 v48, v48, v236, s[50:51]\n"
  "v_exp_f32 v227, v47\n"
  "v_add_f32 v228, v228, v226\n"
  "v_mfma_f32_32x32x16_bf16 a[64:79], a[240:243], v[124:127], a[64:79]\n"
  "v_cmp_gt_i32 s[54:55], 34, v234\n"
  "v_cndmask_b32 v49, v49, v236, s[52:53]\n"
  "v_exp_f32 v224, v48\n"
  "v_add_f32 v228, v228, v227\n"
  "v_cvt_pk_bf16_f32 v103, v226, v227\n"
  "v_mfma_f32_32x32x16_bf16 a[80:95], a[244:247], v[124:127], a[80:95]\n"
  "v_cmp_gt_i32 s[56:57], 35, v234\n"
  "v_cndmask_b32 v50, v50, v236, s[54:55]\n"
  "v_exp_f32 v225, v49\n"
  "v_add_f32 v228, v228, v224\n"
  "v_mfma_f32_32x32x16_bf16 a[96:111], a[248:251], v[124:127], a[96:111]\n"
  "v_cmp_gt_i32 s[50:51], 40, v234\n"
  "v_cndmask_b32 v51, v51, v236, s[56:57]\n"
  "v_exp_f32 v226, v50\n"
  "v_add_f32 v228, v228, v225\n"
  "v_cvt_pk_bf16_f32 v104, v224, v225\n"
  "v_mfma_f32_32x32x16_bf16 a[112:127], a[252:255], v[124:127], a[112:127]\n"
  "v_cmp_gt_i32 s[52:53], 41, v234\n"
  "v_cndmask_b32 v52, v52, v236, s[50:51]\n"
  "v_exp_f32 v227, v51\n"
  "v_add_f32 v228, v228, v226\n"
  "s_waitcnt vmcnt(8)\n"
  "s_waitcnt vmcnt(11)\n"
  "s_barrier\n"
  "v_mfma_f32_32x32x16_bf16 v[64:79], a[128:131], v[192:195], v[144:159]\n"
  "ds_read_b128 a[192:195], v3 offset:0\n"
  "v_cmp_gt_i32 s[54:55], 42, v234\n"
  "v_cndmask_b32 v53, v53, v236, s[52:53]\n"
  "v_exp_f32 v224, v52\n"
  "v_add_f32 v228, v228, v227\n"
  "v_cvt_pk_bf16_f32 v105, v226, v227\n"
  "v_mfma_f32_32x32x16_bf16 v[64:79], a[132:135], v[196:199], v[64:79]\n"
  "ds_read_b128 a[196:199], v3 offset:1024\n"
  "v_cmp_gt_i32 s[56:57], 43, v234\n"
  "v_cndmask_b32 v54, v54, v236, s[54:55]\n"
  "v_exp_f32 v225, v53\n"
  "v_add_f32 v228, v228, v224\n"
  "s_and_b32 m0, s44, 0xffff\n"
  "v_mfma_f32_32x32x16_bf16 v[64:79], a[136:139], v[200:203], v[64:79]\n"
  "ds_read_b128 a[200:203], v3 offset:2048\n"
  "v_cmp_gt_i32 s[50:51], 48, v234\n"
  "v_cndmask_b32 v55, v55, v236, s[56:57]\n"
  "v_exp_f32 v226, v54\n"
  "v_add_f32 v228, v228, v225\n"
  "v_cvt_pk_bf16_f32 v106, v224, v225\n"
  "v_mfma_f32_32x32x16_bf16 v[64:79], a[140:143], v[204:207], v[64:79]\n"
  "ds_read_b128 a[204:207], v3 offset:3072\n"
  "v_cmp_gt_i32 s[52:53], 49, v234\n"
  "v_cndmask_b32 v56, v56, v236, s[50:51]\n"
  "v_exp_f32 v227, v55\n"
  "v_add_f32 v228, v228, v226\n"
  "buffer_load_dwordx4 v1, s[24:27], s40 offen offset:0 sc1 lds\n"
  "v_mfma_f32_32x32x16_bf16 v[64:79], a[144:147], v[208:211], v[64:79]\n"
  "ds_read_b128 a[208:211], v3 offset:4096\n"
  "v_cmp_gt_i32 s[54:55], 50, v234\n"
  "v_cndmask_b32 v57, v57, v236, s[52:53]\n"
  "v_exp_f32 v224, v56\n"
  "v_add_f32 v228, v228, v227\n"
  "v_cvt_pk_bf16_f32 v107, v226, v227\n"
  "v_mfma_f32_32x32x16_bf16 v[64:79], a[148:151], v[212:215], v[64:79]\n"
  "ds_read_b128 a[212:215], v3 offset:5120\n"
  "v_cmp_gt_i32 s[56:57], 51, v234\n"
  "v_cndmask_b32 v58, v58, v236, s[54:55]\n"
  "v_exp_f32 v225, v57\n"
  "v_add_f32 v228, v228, v224\n"
  "v_mfma_f32_32x32x16_bf16 v[64:79], a[152:155], v[216:219], v[64:79]\n"
  "ds_read_b128 a[216:219], v3 offset:6144\n"
  "v_cmp_gt_i32 s[50:51], 56, v234\n"
  "v_cndmask_b32 v59, v59, v236, s[56:57]\n"
  "v_exp_f32 v226, v58\n"
  "v_add_f32 v228, v228, v225\n"
  "v_cvt_pk_bf16_f32 v108, v224, v225\n"
  "buffer_load_dwordx4 v1, s[24:27], s40 offen offset:1024 sc1 lds\n"
  "v_mfma_f32_32x32x16_bf16 v[64:79], a[156:159], v[220:223], v[64:79]\n"
  "ds_read_b128 a[220:223], v3 offset:7168\n"
  "v_cmp_gt_i32 s[52:53], 57, v234\n"
  "v_cndmask_b32 v60, v60, v236, s[50:51]\n"
  "v_exp_f32 v227, v59\n"
  "v_add_f32 v228, v228, v226\n"
  "v_mfma_f32_32x32x16_bf16 v[80:95], a[160:163], v[192:195], v[144:159]\n"
  "ds_read_b128 a[224:227], v3 offset:8192\n"
  "v_cmp_gt_i32 s[54:55], 58, v234\n"
  "v_cndmask_b32 v61, v61, v236, s[52:53]\n"
  "v_exp_f32 v224, v60\n"
  "v_add_f32 v228, v228, v227\n"
  "v_cvt_pk_bf16_f32 v109, v226, v227\n"
  "v_mfma_f32_32x32x16_bf16 v[80:95], a[164:167], v[196:199], v[80:95]\n"
  "ds_read_b128 a[228:231], v3 offset:9216\n"
  "v_cmp_gt_i32 s[56:57], 59, v234\n"
  "v_cndmask_b32 v62, v62, v236, s[54:55]\n"
  "v_exp_f32 v225, v61\n"
  "v_add_f32 v228, v228, v224\n"
  "buffer_load_dwordx4 v1, s[24:27], s40 offen offset:2048 sc1 lds\n"
  "v_mfma_f32_32x32x16_bf16 v[80:95], a[168:171], v[200:203], v[80:95]\n"
  "ds_read_b128 a[232:235], v3 offset:10240\n"
  "v_cndmask_b32 v63, v63, v236, s[56:57]\n"
  "v_exp_f32 v226, v62\n"
  "v_add_f32 v228, v228, v225\n"
  "v_cvt_pk_bf16_f32 v110, v224, v225\n"
  "v_mfma_f32_32x32x16_bf16 v[80:95], a[172:175], v[204:207], v[80:95]\n"
  "ds_read_b128 a[236:239], v3 offset:11264\n"
  "v_exp_f32 v227, v63\n"
  "v_add_f32 v228, v228, v226\n"
  "v_mfma_f32_32x32x16_bf16 v[80:95], a[176:179], v[208:211], v[80:95]\n"
  "ds_read_b128 a[240:243], v3 offset:12288\n"
  "v_add_f32 v228, v228, v227\n"
  "v_cvt_pk_bf16_f32 v111, v226, v227\n"
  "v_exp_f32 v224, v64\n"
  "buffer_load_dwordx4 v1, s[24:27], s40 offen offset:3072 sc1 lds\n"
  "s_and_b32 s58, s45, 0xffff\n"
  "v_mfma_f32_32x32x16_bf16 v[80:95], a[180:183], v[212:215], v[80:95]\n"
  "ds_read_b128 a[244:247], v3 offset:13312\n"
  "v_exp_f32 v225, v65\n"
  "v_mfma_f32_32x32x16_bf16 v[80:95], a[184:187], v[216:219], v[80:95]\n"
  "ds_read_b128 a[248:251], v3 offset:14336\n"
  "v_exp_f32 v226, v66\n"
  "v_add_f32 v229, v224, v225\n"
  "v_cvt_pk_bf16_f32 v112, v224, v225\n"
  "v_cmp_lt_f32 vcc, s47, v228\n"
  "s_add_u32 s44, s44, 16384\n"
  "s_or_b32 m0, s58, 0x10000\n"
  "v_mfma_f32_32x32x16_bf16 v[80:95], a[188:191], v[220:223], v[80:95]\n"
  "ds_read_b128 a[252:255], v3 offset:15360\n"
  "v_exp_f32 v227, v67\n"
  "v_add_f32 v229, v229, v226\n"
  "v_add_u32 v26, 0x4000, v3\n"
  "v_bfi_b32 v3, s68, v26, v3\n"
  "s_cbranch_vccnz Lfb_MX\n"
  "Lret_MX:\n"
  "v_add_f32 v230, v230, v228\n"
  "s_waitcnt lgkmcnt(11)\n"
  "v_mfma_f32_32x32x16_bf16 a[0:15], a[192:195], v[96:99], a[0:15]\n"
  "ds_read_b128 a[128:131], v2 offset:0\n"
  "v_exp_f32 v224, v68\n"
  "v_add_f32 v229, v229, v227\n"
  "v_cvt_pk_bf16_f32 v113, v226, v227\n"
  "v_mfma_f32_32x32x16_bf16 a[16:31], a[196:199], v[96:99], a[16:31]\n"
  "ds_read_b128 a[132:135], v2 offset:1024\n"
  "v_exp_f32 v225, v69\n"
  "v_add_f32 v229, v229, v224\n"
  "buffer_load_dwordx4 v1, s[28:31], s41 offen offset:0 sc1 lds\n"
  "s_add_u32 s40, s40, 16384\n"
  "v_mfma_f32_32x32x16_bf16 a[32:47], a[200:203], v[96:99], a[32:47]\n"
  "ds_read_b128 a[136:139], v2 offset:2048\n"
  "v_exp_f32 v226, v70\n"
  "v_add_f32 v229, v229, v225\n"
  "v_cvt_pk_bf16_f32 v114, v224, v225\n"
  "v_subrev_u32 v234, 64, v234\n"
  "v_mfma_f32_32x32x16_bf16 a[48:63], a[204:207], v[96:99], a[48:63]\n"
  "ds_read_b128 a[140:143], v2 offset:3072\n"
  "v_exp_f32 v227, v71\n"
  "v_add_f32 v229, v229, v226\n"
  "v_mfma_f32_32x32x16_bf16 a[0:15], a[208:211], v[100:103], a[0:15]\n"
  "ds_read_b128 a[144:147], v2 offset:4096\n"
  "v_exp_f32 v224, v72\n"
  "v_add_f32 v229, v229, v227\n"
  "v_cvt_pk_bf16_f32 v115, v226, v227\n"
  "buffer_load_dwordx4 v1, s[28:31], s41 offen offset:1024 sc1 lds\n"
  "s_min_u32 s40, s40, s42\n"
  "s_waitcnt lgkmcnt(11)\n"
  "v_mfma_f32_32x32x16_bf16 a[16:31], a[212:215], v[100:103], a[16:31]\n"
  "ds_read_b128 a[148:151], v2 offset:5120\n"
  "v_exp_f32 v225, v73\n"
  "v_add_f32 v229, v229, v224\n"
  "v_mfma_f32_32x32x16_bf16 a[32:47], a[216:219], v[100:103], a[32:47]\n"
  "ds_read_b128 a[152:155], v2 offset:6144\n"
  "v_exp_f32 v226, v74\n"
  "v_add_f32 v229, v229, v225\n"
  "v_cvt_pk_bf16_f32 v116, v224, v225\n"
  "v_mfma_f32_32x32x16_bf16 a[48:63], a[220:223], v[100:103], a[48:63]\n"
  "ds_read_b128 a[156:159], v2 offset:7168\n"
  "v_exp_f32 v227, v75\n"
  "v_add_f32 v229, v229, v226\n"
  "buffer_load_dwordx4 v1, s[28:31], s41 offen offset:2048 sc1 lds\n"
  "v_mfma_f32_32x32x16_bf16 a[0:15], a[224:227], v[104:107], a[0:15]\n"
  "ds_read_b128 a[160:163], v2 offset:8192\n"
  "v_exp_f32 v224, v76\n"
  "v_add_f32 v229, v229, v227\n"
  "v_cvt_pk_bf16_f32 v117, v226, v227\n"
  "v_mfma_f32_32x32x16_bf16 a[16:31], a[228:231], v[104:107], a[16:31]\n"
  "ds_read_b128 a[164:167], v2 offset:9216\n"
  "v_exp_f32 v225, v77\n"
  "v_add_f32 v229, v229, v224\n"
  "s_waitcnt lgkmcnt(11)\n"
  "v_mfma_f32_32x32x16_bf16 a[32:47], a[232:235], v[104:107], a[32:47]\n"
  "ds_read_b128 a[168:171], v2 offset:10240\n"
  "v_exp_f32 v226, v78\n"
  "v_add_f32 v229, v229, v225\n"
  "v_cvt_pk_bf16_f32 v118, v224, v225\n"
  "buffer_load_dwordx4 v1, s[28:31], s41 offen offset:3072 sc1 lds\n"
  "s_add_u32 s45, s45, 16384\n"
  "v_mfma_f32_32x32x16_bf16 a[48:63], a[236:239], v[104:107], a[48:63]\n"
  "ds_read_b128 a[172:175], v2 offset:11264\n"
  "v_exp_f32 v227, v79\n"
  "v_add_f32 v229, v229, v226\n"
  "v_mfma_f32_32x32x16_bf16 a[0:15], a[240:243], v[108:111], a[0:15]\n"
  "ds_read_b128 a[176:179], v2 offset:12288\n"
  "v_exp_f32 v224, v80\n"
  "v_add_f32 v229, v229, v227\n"
  "v_cvt_pk_bf16_f32 v119, v226, v227\n"
  "s_add_u32 s41, s41, 16384\n"
  "s_min_u32 s41, s41, s42\n"
  "v_mfma_f32_32x32x16_bf16 a[16:31], a[244:247], v[108:111], a[16:31]\n"
  "ds_read_b128 a[180:183], v2 offset:13312\n"
  "v_exp_f32 v225, v81\n"
  "v_add_f32 v229, v229, v224\n"
  "v_mfma_f32_32x32x16_bf16 a[32:47], a[248:251], v[108:111], a[32:47]\n"
  "ds_read_b128 a[184:187], v2 offset:14336\n"
  "v_exp_f32 v226, v82\n"
  "v_add_f32 v229, v229, v225\n"
  "v_cvt_pk_bf16_f32 v120, v224, v225\n"
  "s_waitcnt lgkmcnt(15)\n"
  "v_mfma_f32_32x32x16_bf16 a[48:63], a[252:255], v[108:111], a[48:63]\n"
  "ds_read_b128 a[188:191], v2 offset:15360\n"
  "v_exp_f32 v227, v83\n"
  "v_add_f32 v229, v229, v226\n"
  "v_add_u32 v2, 0x4000, v2\n"
  "v_and_b32 v2, 0xffff, v2\n"
  "s_sub_u32 s46, s46, 1\n"
  "s_cmp_lg_u32 s46, 0\n"
  "s_cbranch_scc1 Ldm2\n"
  "v_add_u32 v26, 0xc000, v3\n"
  "v_bfi_b32 v3, s68, v26, v3\n"
  "s_lshl_b32 s46, s13, 1\n"
  "s_sub_u32 s46, 14, s46\n"
  "s_cmp_eq_u32 s46, 0\n"
  "s_cbranch_scc1 Lsm\n"
  "Ls:\n"
  "s_waitcnt vmcnt(16)\n"
  "s_waitcnt vmcnt(12)\n"
  "s_barrier\n"
  "s_waitcnt lgkmcnt(11)\n"
  "v_mfma_f32_32x32x16_bf16 v[32:47], a[128:131], v[192:195], v[144:159]\n"
  "ds_read_b128 a[192:195], v3 offset:0\n"
  "v_exp_f32 v224, v84\n"
  "v_add_f32 v229, v229, v227\n"
  "v_cvt_pk_bf16_f32 v121, v226, v227\n"
  "v_mfma_f32_32x32x16_bf16 v[32:47], a[132:135], v[196:199], v[32:47]\n"
  "ds_read_b128 a[196:199], v3 offset:1024\n"
  "v_exp_f32 v225, v85\n"
  "v_add_f32 v229, v229, v224\n"
  "s_and_b32 m0, s44, 0xffff\n"
  "v_mfma_f32_32x32x16_bf16 v[32:47], a[136:139], v[200:203], v[32:47]\n"
  "ds_read_b128 a[200:203], v3 offset:2048\n"
  "v_exp_f32 v226, v86\n"
  "v_add_f32 v229, v229, v225\n"
  "v_cvt_pk_bf16_f32 v122, v224, v225\n"
  "v_mfma_f32_32x32x16_bf16 v[32:47], a[140:143], v[204:207], v[32:47]\n"
  "ds_read_b128 a[204:207], v3 offset:3072\n"
  "v_exp_f32 v227, v87\n"
  "v_add_f32 v229, v229, v226\n"
  "buffer_load_dwordx4 v1, s[24:27], s40 offen offset:0 sc1 lds\n"
  "v_mfma_f32_32x32x16_bf16 v[32:47], a[144:147], v[208:211], v[32:47]\n"
  "ds_read_b128 a[208:211], v3 offset:4096\n"
  "v_exp_f32 v224, v88\n"
  "v_add_f32 v229, v229, v227\n"
  "v_cvt_pk_bf16_f32 v123, v226, v227\n"
  "s_waitcnt lgkmcnt(11)\n"
  "v_mfma_f32_32x32x16_bf16 v[32:47], a[148:151], v[212:215], v[32:47]\n"
  "ds_read_b128 a[212:215], v3 offset:5120\n"
  "v_exp_f32 v225, v89\n"
  "v_add_f32 v229, v229, v224\n"
  "buffer_load_dwordx4 v1, s[24:27], s40 offen offset:1024 sc1 lds\n"
  "v_mfma_f32_32x32x16_bf16 v[32:47], a[152:155], v[216:219], v[32:47]\n"
  "ds_read_b128 a[216:219], v3 offset:6144\n"
  "v_exp_f32 v226, v90\n"
  "v_add_f32 v229, v229, v225\n"
  "v_cvt_pk_bf16_f32 v124, v224, v225\n"
  "v_mfma_f32_32x32x16_bf16 v[32:47], a[156:159], v[220:223], v[32:47]\n"
  "ds_read_b128 a[220:223], v3 offset:7168\n"
  "v_exp_f32 v227, v91\n"
  "v_add_f32 v229, v229, v226\n"
  "buffer_load_dwordx4 v1, s[24:27], s40 offen offset:2048 sc1 lds\n"
  "v_mfma_f32_32x32x16_bf16 v[48:63], a[160:163], v[192:195], v[144:159]\n"
  "ds_read_b128 a[224:227], v3 offset:8192\n"
  "v_exp_f32 v224, v92\n"
  "v_add_f32 v229, v229, v227\n"
  "v_cvt_pk_bf16_f32 v125, v226, v227\n"
  "v_mfma_f32_32x32x16_bf16 v[48:63], a[164:167], v[196:199], v[48:63]\n"
  "ds_read_b128 a[228:231], v3 offset:9216\n"
  "v_exp_f32 v225, v93\n"
  "v_add_f32 v229, v229, v224\n"
  "buffer_load_dwordx4 v1, s[24:27], s40 offen offset:3072 sc1 lds\n"
  "s_and_b32 s58, s45, 0xffff\n"
  "s_waitcnt lgkmcnt(11)\n"
  "v_mfma_f32_32x32x16_bf16 v[48:63], a[168:171], v[200:203], v[48:63]\n"
  "ds_read_b128 a[232:235], v3 offset:10240\n"
  "v_exp_f32 v226, v94\n"
  "v_add_f32 v229, v229, v225\n"
  "v_cvt_pk_bf16_f32 v126, v224, v225\n"
  "v_mfma_f32_32x32x16_bf16 v[48:63], a[172:175], v[204:207], v[48:63]\n"
  "ds_read_b128 a[236:239], v3 offset:11264\n"
  "v_exp_f32 v227, v95\n"
  "v_add_f32 v229, v229, v226\n"
  "v_mfma_f32_32x32x16_bf16 v[48:63], a[176:179], v[208:211], v[48:63]\n"
  "ds_read_b128 a[240:243], v3 offset:12288\n"
  "v_add_f32 v229, v229, v227\n"
  "v_cvt_pk_bf16_f32 v127, v226, v227\n"
  "v_exp_f32 v224, v32\n"
  "s_add_u32 s44, s44, 16384\n"
  "s_or_b32 m0, s58, 0x10000\n"
  "v_mfma_f32_32x32x16_bf16 v[48:63], a[180:183], v[212:215], v[48:63]\n"
  "ds_read_b128 a[244:247], v3 offset:13312\n"
  "v_exp_f32 v225, v33\n"
  "v_mfma_f32_32x32x16_bf16 v[48:63], a[184:187], v[216:219], v[48:63]\n"
  "ds_read_b128 a[248:251], v3 offset:14336\n"
  "v_exp_f32 v226, v34\n"
  "v_add_f32 v228, v224, v225\n"
  "v_cvt_pk_bf16_f32 v96, v224, v225\n"
  "v_cmp_lt_f32 vcc, s47, v229\n"
  "s_waitcnt lgkmcnt(15)\n"
  "v_mfma_f32_32x32x16_bf16 v[48:63], a[188:191], v[220:223], v[48:63]\n"
  "ds_read_b128 a[252:255], v3 offset:15360\n"
  "v_exp_f32 v227, v35\n"
  "v_add_f32 v228, v228, v226\n"
  "v_add_u32 v26, 0x4000, v3\n"
  "v_bfi_b32 v3, s68, v26, v3\n"
  "s_cbranch_vccnz Lfb_SY\n"
  "Lret_SY:\n"
  "v_add_f32 v231, v231, v229\n"
  "s_waitcnt lgkmcnt(11)\n"
  "v_mfma_f32_32x32x16_bf16 a[64:79], a[192:195], v[112:115], a[64:79]\n"
  "ds_read_b128 a[128:131], v2 offset:0\n"
  "v_exp_f32 v224, v36\n"
  "v_add_f32 v228, v228, v227\n"
  "v_cvt_pk_bf16_f32 v97, v226, v227\n"
  "v_mfma_f32_32x32x16_bf16 a[80:95], a[196:199], v[112:115], a[80:95]\n"
  "ds_read_b128 a[132:135], v2 offset:1024\n"
  "v_exp_f32 v225, v37\n"
  "v_add_f32 v228, v228, v224\n"
  "buffer_load_dwordx4 v1, s[28:31], s41 offen offset:0 sc1 lds\n"
  "s_add_u32 s40, s40, 16384\n"
  "v_mfma_f32_32x32x16_bf16 a[96:111], a[200:203], v[112:115], a[96:111]\n"
  "ds_read_b128 a[136:139], v2 offset:2048\n"
  "v_exp_f32 v226, v38\n"
  "v_add_f32 v228, v228, v225\n"
  "v_cvt_pk_bf16_f32 v98, v224, v225\n"
  "v_mfma_f32_32x32x16_bf16 a[112:127], a[204:207], v[112:115], a[112:127]\n"
  "ds_read_b128 a[140:143], v2 offset:3072\n"
  "v_exp_f32 v227, v39\n"
  "v_add_f32 v228, v228, v226\n"
  "v_mfma_f32_32x32x16_bf16 a[64:79], a[208:211], v[116:119], a[64:79]\n"
  "ds_read_b128 a[144:147], v2 offset:4096\n"
  "v_exp_f32 v224, v40\n"
  "v_add_f32 v228, v228, v227\n"
  "v_cvt_pk_bf16_f32 v99, v226, v227\n"
  "buffer_load_dwordx4 v1, s[28:31], s41 offen offset:1024 sc1 lds\n"
  "s_min_u32 s40, s40, s42\n"
  "s_waitcnt lgkmcnt(11)\n"
  "v_mfma_f32_32x32x16_bf16 a[80:95], a[212:215], v[116:119], a[80:95]\n"
  "ds_read_b128 a[148:151], v2 offset:5120\n"
  "v_exp_f32 v225, v41\n"
  "v_add_f32 v228, v228, v224\n"
  "v_mfma_f32_32x32x16_bf16 a[96:111], a[216:219], v[116:119], a[96:111]\n"
  "ds_read_b128 a[152:155], v2 offset:6144\n"
  "v_exp_f32 v226, v42\n"
  "v_add_f32 v228, v228, v225\n"
  "v_cvt_pk_bf16_f32 v100, v224, v225\n"
  "v_mfma_f32_32x32x16_bf16 a[112:127], a[220:223], v[116:119], a[112:127]\n"
  "ds_read_b128 a[156:159], v2 offset:7168\n"
  "v_exp_f32 v227, v43\n"
  "v_add_f32 v228, v228, v226\n"
  "buffer_load_dwordx4 v1, s[28:31], s41 offen offset:2048 sc1 lds\n"
  "v_mfma_f32_32x32x16_bf16 a[64:79], a[224:227], v[120:123], a[64:79]\n"
  "ds_read_b128 a[160:163], v2 offset:8192\n"
  "v_exp_f32 v224, v44\n"
  "v_add_f32 v228, v228, v227\n"
  "v_cvt_pk_bf16_f32 v101, v226, v227\n"
  "v_mfma_f32_32x32x16_bf16 a[80:95], a[228:231], v[120:123], a[80:95]\n"
  "ds_read_b128 a[164:167], v2 offset:9216\n"
  "v_exp_f32 v225, v45\n"
  "v_add_f32 v228, v228, v224\n"
  "s_waitcnt lgkmcnt(11)\n"
  "v_mfma_f32_32x32x16_bf16 a[96:111], a[232:235], v[120:123], a[96:111]\n"
  "ds_read_b128 a[168:171], v2 offset:10240\n"
  "v_exp_f32 v226, v46\n"
  "v_add_f32 v228, v228, v225\n"
  "v_cvt_pk_bf16_f32 v102, v224, v225\n"
  "buffer_load_dwordx4 v1, s[28:31], s41 offen offset:3072 sc1 lds\n"
  "s_add_u32 s45, s45, 16384\n"
  "v_mfma_f32_32x32x16_bf16 a[112:127], a[236:239], v[120:123], a[112:127]\n"
  "ds_read_b128 a[172:175], v2 offset:11264\n"
  "v_exp_f32 v227, v47\n"
  "v_add_f32 v228, v228, v226\n"
  "v_mfma_f32_32x32x16_bf16 a[64:79], a[240:243], v[124:127], a[64:79]\n"
  "ds_read_b128 a[176:179], v2 offset:12288\n"
  "v_exp_f32 v224, v48\n"
  "v_add_f32 v228, v228, v227\n"
  "v_cvt_pk_bf16_f32 v103, v226, v227\n"
  "s_add_u32 s41, s41, 16384\n"
  "s_min_u32 s41, s41, s42\n"
  "v_mfma_f32_32x32x16_bf16 a[80:95], a[244:247], v[124:127], a[80:95]\n"
  "ds_read_b128 a[180:183], v2 offset:13312\n"
  "v_exp_f32 v225, v49\n"
  "v_add_f32 v228, v228, v224\n"
  "v_mfma_f32_32x32x16_bf16 a[96:111], a[248:251], v[124:127], a[96:111]\n"
  "ds_read_b128 a[184:187], v2 offset:14336\n"
  "v_exp_f32 v226, v50\n"
  "v_add_f32 v228, v228, v225\n"
  "v_cvt_pk_bf16_f32 v104, v224, v225\n"
  "s_waitcnt lgkmcnt(12)\n"
  "v_mfma_f32_32x32x16_bf16 a[112:127], a[252:255], v[124:127], a[112:127]\n"
  "ds_read_b128 a[188:191], v2 offset:15360\n"
  "v_exp_f32 v227, v51\n"
  "v_add_f32 v228, v228, v226\n"
  "v_add_u32 v2, 0x4000, v2\n"
  "v_and_b32 v2, 0xffff, v2\n"
  "s_waitcnt vmcnt(16)\n"
  "s_waitcnt vmcnt(12)\n"
  "s_barrier\n"
  "v_mfma_f32_32x32x16_bf16 v[64:79], a[128:131], v[192:195], v[144:159]\n"
  "ds_read_b128 a[192:195], v3 offset:0\n"
  "v_exp_f32 v224, v52\n"
  "v_add_f32 v228, v228, v227\n"
  "v_cvt_pk_bf16_f32 v105, v226, v227\n"
  "v_mfma_f32_32x32x16_bf16 v[64:79], a[132:135], v[196:199], v[64:79]\n"
  "ds_read_b128 a[196:199], v3 offset:1024\n"
  "v_exp_f32 v225, v53\n"
  "v_add_f32 v228, v228, v224\n"
  "s_and_b32 m0, s44, 0xffff\n"
  "v_mfma_f32_32x32x16_bf16 v[64:79], a[136:139], v[200:203], v[64:79]\n"
  "ds_read_b128 a[200:203], v3 offset:2048\n"
  "v_exp_f32 v226, v54\n"
  "v_add_f32 v228, v228, v225\n"
  "v_cvt_pk_bf16_f32 v106, v224, v225\n"
  "s_waitcnt lgkmcnt(11)\n"
  "v_mfma_f32_32x32x16_bf16 v[64:79], a[140:143], v[204:207], v[64:79]\n"
  "ds_read_b128 a[204:207], v3 offset:3072\n"
  "v_exp_f32 v227, v55\n"
  "v_add_f32 v228, v228, v226\n"
  "buffer_load_dwordx4 v1, s[24:27], s40 offen offset:0 sc1 lds\n"
  "v_mfma_f32_32x32x16_bf16 v[64:79], a[144:147], v[208:211], v[64:79]\n"
  "ds_read_b128 a[208:211], v3 offset:4096\n"
  "v_exp_f32 v224, v56\n"
  "v_add_f32 v228, v228, v227\n"
  "v_cvt_pk_bf16_f32 v107, v226, v227\n"
  "v_mfma_f32_32x32x16_bf16 v[64:79], a[148:151], v[212:215], v[64:79]\n"
  "ds_read_b128 a[212:215], v3 offset:5120\n"
  "v_exp_f32 v225, v57\n"
  "v_add_f32 v228, v228, v224\n"
  "buffer_load_dwordx4 v1, s[24:27], s40 offen offset:1024 sc1 lds\n"
  "v_mfma_f32_32x32x16_bf16 v[64:79], a[152:155], v[216:219], v[64:79]\n"
  "ds_read_b128 a[216:219], v3 offset:6144\n"
  "v_exp_f32 v226, v58\n"
  "v_add_f32 v228, v228, v225\n"
  "v_cvt_pk_bf16_f32 v108, v224, v225\n"
  "v_mfma_f32_32x32x16_bf16 v[64:79], a[156:159], v[220:223], v[64:79]\n"
  "ds_read_b128 a[220:223], v3 offset:7168\n"
  "v_exp_f32 v227, v59\n"
  "v_add_f32 v228, v228, v226\n"
  "buffer_load_dwordx4 v1, s[24:27], s40 offen offset:2048 sc1 lds\n"
  "s_waitcnt lgkmcnt(11)\n"
  "v_mfma_f32_32x32x16_bf16 v[80:95], a[160:163], v[192:195], v[144:159]\n"
  "ds_read_b128 a[224:227], v3 offset:8192\n"
  "v_exp_f32 v224, v60\n"
  "v_add_f32 v228, v228, v227\n"
  "v_cvt_pk_bf16_f32 v109, v226, v227\n"
  "v_mfma_f32_32x32x16_bf16 v[80:95], a[164:167], v[196:199], v[80:95]\n"
  "ds_read_b128 a[228:231], v3 offset:9216\n"
  "v_exp_f32 v225, v61\n"
  "v_add_f32 v228, v228, v224\n"
  "buffer_load_dwordx4 v1, s[24:27], s40 offen offset:3072 sc1 lds\n"
  "s_and_b32 s58, s45, 0xffff\n"
  "v_mfma_f32_32x32x16_bf16 v[80:95], a[168:171], v[200:203], v[80:95]\n"
  "ds_read_b128 a[232:235], v3 offset:10240\n"
  "v_exp_f32 v226, v62\n"
  "v_add_f32 v228, v228, v225\n"
  "v_cvt_pk_bf16_f32 v110, v224, v225\n"
  "v_mfma_f32_32x32x16_bf16 v[80:95], a[172:175], v[204:207], v[80:95]\n"
  "ds_read_b128 a[236:239], v3 offset:11264\n"
  "v_exp_f32 v227, v63\n"
  "v_add_f32 v228, v228, v226\n"
  "v_mfma_f32_32x32x16_bf16 v[80:95], a[176:179], v[208:211], v[80:95]\n"
  "ds_read_b128 a[240:243], v3 offset:12288\n"
  "v_add_f32 v228, v228, v227\n"
  "v_cvt_pk_bf16_f32 v111, v226, v227\n"
  "v_exp_f32 v224, v64\n"
  "s_add_u32 s44, s44, 16384\n"
  "s_or_b32 m0, s58, 0x10000\n"
  "s_waitcnt lgkmcnt(13)\n"
  "v_mfma_f32_32x32x16_bf16 v[80:95], a[180:183], v[212:215], v[80:95]\n"
  "ds_read_b128 a[244:247], v3 offset:13312\n"
  "v_exp_f32 v225, v65\n"
  "v_mfma_f32_32x32x16_bf16 v[80:95], a[184:187], v[216:219], v[80:95]\n"
  "ds_read_b128 a[248:251], v3 offset:14336\n"
  "v_exp_f32 v226, v66\n"
  "v_add_f32 v229, v224, v225\n"
  "v_cvt_pk_bf16_f32 v112, v224, v225\n"
  "v_cmp_lt_f32 vcc, s47, v228\n"
  "v_mfma_f32_32x32x16_bf16 v[80:95], a[188:191], v[220:223], v[80:95]\n"
  "ds_read_b128 a[252:255], v3 offset:15360\n"
  "v_exp_f32 v227, v67\n"
  "v_add_f32 v229, v229, v226\n"
  "v_add_u32 v26, 0x4000, v3\n"
  "v_bfi_b32 v3, s68, v26, v3\n"
  "s_cbranch_vccnz Lfb_SX\n"
  "Lret_SX:\n"
  "v_add_f32 v231, v231, v228\n"
  "s_waitcnt lgkmcnt(11)\n"
  "v_mfma_f32_32x32x16_bf16 a[64:79], a[192:195], v[96:99], a[64:79]\n"
  "ds_read_b128 a[128:131], v2 offset:0\n"
  "v_exp_f32 v224, v68\n"
  "v_add_f32 v229, v229, v227\n"
  "v_cvt_pk_bf16_f32 v113, v226, v227\n"
  "v_mfma_f32_32x32x16_bf16 a[80:95], a[196:199], v[96:99], a[80:95]\n"
  "ds_read_b128 a[132:135], v2 offset:1024\n"
  "v_exp_f32 v225, v69\n"
  "v_add_f32 v229, v229, v224\n"
  "buffer_load_dwordx4 v1, s[28:31], s41 offen offset:0 sc1 lds\n"
  "s_add_u32 s40, s40, 16384\n"
  "v_mfma_f32_32x32x16_bf16 a[96:111], a[200:203], v[96:99], a[96:111]\n"
  "ds_read_b128 a[136:139], v2 offset:2048\n"
  "v_exp_f32 v226, v70\n"
  "v_add_f32 v229, v229, v225\n"
  "v_cvt_pk_bf16_f32 v114, v224, v225\n"
  "v_mfma_f32_32x32x16_bf16 a[112:127], a[204:207], v[96:99], a[112:127]\n"
  "ds_read_b128 a[140:143], v2 offset:3072\n"
  "v_exp_f32 v227, v71\n"
  "v_add_f32 v229, v229, v226\n"
  "v_mfma_f32_32x32x16_bf16 a[64:79], a[208:211], v[100:103], a[64:79]\n"
  "ds_read_b128 a[144:147], v2 offset:4096\n"
  "v_exp_f32 v224, v72\n"
  "v_add_f32 v229, v229, v227\n"
  "v_cvt_pk_bf16_f32 v115, v226, v227\n"
  "buffer_load_dwordx4 v1, s[28:31], s41 offen offset:1024 sc1 lds\n"
  "s_min_u32 s40, s40, s42\n"
  "s_waitcnt lgkmcnt(11)\n"
  "v_mfma_f32_32x32x16_bf16 a[80:95], a[212:215], v[100:103], a[80:95]\n"
  "ds_read_b128 a[148:151], v2 offset:5120\n"
  "v_exp_f32 v225, v73\n"
  "v_add_f32 v229, v229, v224\n"
  "v_mfma_f32_32x32x16_bf16 a[96:111], a[216:219], v[100:103], a[96:111]\n"
  "ds_read_b128 a[152:155], v2 offset:6144\n"
  "v_exp_f32 v226, v74\n"
  "v_add_f32 v229, v229, v225\n"
  "v_cvt_pk_bf16_f32 v116, v224, v225\n"
  "v_mfma_f32_32x32x16_bf16 a[112:127], a[220:223], v[100:103], a[112:127]\n"
  "ds_read_b128 a[156:159], v2 offset:7168\n"
  "v_exp_f32 v227, v75\n"
  "v_add_f32 v229, v229, v226\n"
  "buffer_load_dwordx4 v1, s[28:31], s41 offen offset:2048 sc1 lds\n"
  "v_mfma_f32_32x32x16_bf16 a[64:79], a[224:227], v[104:107], a[64:79]\n"
  "ds_read_b128 a[160:163], v2 offset:8192\n"
  "v_exp_f32 v224, v76\n"
  "v_add_f32 v229, v229, v227\n"
  "v_cvt_pk_bf16_f32 v117, v226, v227\n"
  "v_mfma_f32_32x32x16_bf16 a[80:95], a[228:231], v[104:107], a[80:95]\n"
  "ds_read_b128 a[164:167], v2 offset:9216\n"
  "v_exp_f32 v225, v77\n"
  "v_add_f32 v229, v229, v224\n"
  "s_waitcnt lgkmcnt(11)\n"
  "v_mfma_f32_32x32x16_bf16 a[96:111], a[232:235], v[104:107], a[96:111]\n"
  "ds_read_b128 a[168:171], v2 offset:10240\n"
  "v_exp_f32 v226, v78\n"
  "v_add_f32 v229, v229, v225\n"
  "v_cvt_pk_bf16_f32 v118, v224, v225\n"
  "buffer_load_dwordx4 v1, s[28:31], s41 offen offset:3072 sc1 lds\n"
  "s_add_u32 s45, s45, 16384\n"
  "v_mfma_f32_32x32x16_bf16 a[112:127], a[236:239], v[104:107], a[112:127]\n"
  "ds_read_b128 a[172:175], v2 offset:11264\n"
  "v_exp_f32 v227, v79\n"
  "v_add_f32 v229, v229, v226\n"
  "v_mfma_f32_32x32x16_bf16 a[64:79], a[240:243], v[108:111], a[64:79]\n"
  "ds_read_b128 a[176:179], v2 offset:12288\n"
  "v_exp_f32 v224, v80\n"
  "v_add_f32 v229, v229, v227\n"
  "v_cvt_pk_bf16_f32 v119, v226, v227\n"
  "s_add_u32 s41, s41, 16384\n"
  "s_min_u32 s41, s41, s42\n"
  "v_mfma_f32_32x32x16_bf16 a[80:95], a[244:247], v[108:111], a[80:95]\n"
  "ds_read_b128 a[180:183], v2 offset:13312\n"
  "v_exp_f32 v225, v81\n"
  "v_add_f32 v229, v229, v224\n"
  "v_mfma_f32_32x32x16_bf16 a[96:111], a[248:251], v[108:111], a[96:111]\n"
  "ds_read_b128 a[184:187], v2 offset:14336\n"
  "v_exp_f32 v226, v82\n"
  "v_add_f32 v229, v229, v225\n"
  "v_cvt_pk_bf16_f32 v120, v224, v225\n"
  "s_waitcnt lgkmcnt(15)\n"
  "v_mfma_f32_32x32x16_bf16 a[112:127], a[252:255], v[108:111], a[112:127]\n"
  "ds_read_b128 a[188:191], v2 offset:15360\n"
  "v_exp_f32 v227, v83\n"
  "v_add_f32 v229, v229, v226\n"
  "v_add_u32 v2, 0x4000, v2\n"
  "v_and_b32 v2, 0xffff, v2\n"
  "s_sub_u32 s46, s46, 1\n"
  "s_cmp_lg_u32 s46, 0\n"
  "s_cbranch_scc1 Ls\n"
  "Lsm:\n"
  "v_add_u32 v234, 64, v235\n"
  "s_waitcnt vmcnt(16)\n"
  "s_waitcnt vmcnt(12)\n"
  "s_barrier\n"
  "s_waitcnt lgkmcnt(11)\n"
  "v_mfma_f32_32x32x16_bf16 v[32:47], a[128:131], v[192:195], v[144:159]\n"
  "ds_read_b128 a[192:195], v3 offset:0\n"
  "v_exp_f32 v224, v84\n"
  "v_add_f32 v229, v229, v227\n"
  "v_cvt_pk_bf16_f32 v121, v226, v227\n"
  "v_mfma_f32_32x32x16_bf16 v[32:47], a[132:135], v[196:199], v[32:47]\n"
  "ds_read_b128 a[196:199], v3 offset:1024\n"
  "v_exp_f32 v225, v85\n"
  "v_add_f32 v229, v229, v224\n"
  "s_and_b32 m0, s44, 0xffff\n"
  "v_mfma_f32_32x32x16_bf16 v[32:47], a[136:139], v[200:203], v[32:47]\n"
  "ds_read_b128 a[200:203], v3 offset:2048\n"
  "v_exp_f32 v226, v86\n"
  "v_add_f32 v229, v229, v225\n"
  "v_cvt_pk_bf16_f32 v122, v224, v225\n"
  "v_mfma_f32_32x32x16_bf16 v[32:47], a[140:143], v[204:207], v[32:47]\n"
  "ds_read_b128 a[204:207], v3 offset:3072\n"
  "v_exp_f32 v227, v87\n"
  "v_add_f32 v229, v229, v226\n"
  "buffer_load_dwordx4 v1, s[24:27], s40 offen offset:0 sc1 lds\n"
  "v_mfma_f32_32x32x16_bf16 v[32:47], a[144:147], v[208:211], v[32:47]\n"
  "ds_read_b128 a[208:211], v3 offset:4096\n"
  "v_exp_f32 v224, v88\n"
  "v_add_f32 v229, v229, v227\n"
  "v_cvt_pk_bf16_f32 v123, v226, v227\n"
  "s_waitcnt lgkmcnt(11)\n"
  "v_mfma_f32_32x32x16_bf16 v[32:47], a[148:151], v[212:215], v[32:47]\n"
  "ds_read_b128 a[212:215], v3 offset:5120\n"
  "v_exp_f32 v225, v89\n"
  "v_add_f32 v229, v229, v224\n"
  "buffer_load_dwordx4 v1, s[24:27], s40 offen offset:1024 sc1 lds\n"
  "v_mfma_f32_32x32x16_bf16 v[32:47], a[152:155], v[216:219], v[32:47]\n"
  "ds_read_b128 a[216:219], v3 offset:6144\n"
  "v_exp_f32 v226, v90\n"
  "v_add_f32 v229, v229, v225\n"
  "v_cvt_pk_bf16_f32 v124, v224, v225\n"
  "v_mfma_f32_32x32x16_bf16 v[32:47], a[156:159], v[220:223], v[32:47]\n"
  "ds_read_b128 a[220:223], v3 offset:7168\n"
  "v_exp_f32 v227, v91\n"
  "v_add_f32 v229, v229, v226\n"
  "buffer_load_dwordx4 v1, s[24:27], s40 offen offset:2048 sc1 lds\n"
  "v_mfma_f32_32x32x16_bf16 v[48:63], a[160:163], v[192:195], v[144:159]\n"
  "ds_read_b128 a[224:227], v3 offset:8192\n"
  "v_exp_f32 v224, v92\n"
  "v_add_f32 v229, v229, v227\n"
  "v_cvt_pk_bf16_f32 v125, v226, v227\n"
  "v_mfma_f32_32x32x16_bf16 v[48:63], a[164:167], v[196:199], v[48:63]\n"
  "ds_read_b128 a[228:231], v3 offset:9216\n"
  "v_exp_f32 v225, v93\n"
  "v_add_f32 v229, v229, v224\n"
  "buffer_load_dwordx4 v1, s[24:27], s40 offen offset:3072 sc1 lds\n"
  "s_and_b32 s58, s45, 0xffff\n"
  "s_waitcnt lgkmcnt(12)\n"
  "v_mfma_f32_32x32x16_bf16 v[48:63], a[168:171], v[200:203], v[48:63]\n"
  "ds_read_b128 a[232:235], v3 offset:10240\n"
  "v_exp_f32 v226, v94\n"
  "v_add_f32 v229, v229, v225\n"
  "v_cvt_pk_bf16_f32 v126, v224, v225\n"
  "v_cmp_gt_i32 s[50:51], 0, v234\n"
  "v_mfma_f32_32x32x16_bf16 v[48:63], a[172:175], v[204:207], v[48:63]\n"
  "ds_read_b128 a[236:239], v3 offset:11264\n"
  "v_exp_f32 v227, v95\n"
  "v_add_f32 v229, v229, v226\n"
  "v_cmp_gt_i32 s[52:53], 1, v234\n"
  "v_cndmask_b32 v32, v32, v236, s[50:51]\n"
  "v_mfma_f32_32x32x16_bf16 v[48:63], a[176:179], v[208:211], v[48:63]\n"
  "ds_read_b128 a[240:243], v3 offset:12288\n"
  "v_add_f32 v229, v229, v227\n"
  "v_cvt_pk_bf16_f32 v127, v226, v227\n"
  "v_cmp_gt_i32 s[54:55], 2, v234\n"
  "v_cndmask_b32 v33, v33, v236, s[52:53]\n"
  "v_exp_f32 v224, v32\n"
  "s_add_u32 s44, s44, 16384\n"
  "s_or_b32 m0, s58, 0x10000\n"
  "v_mfma_f32_32x32x16_bf16 v[48:63], a[180:183], v[212:215], v[48:63]\n"
  "ds_read_b128 a[244:247], v3 offset:13312\n"
  "v_cmp_gt_i32 s[56:57], 3, v234\n"
  "v_cndmask_b32 v34, v34, v236, s[54:55]\n"
  "v_exp_f32 v225, v33\n"
  "s_waitcnt lgkmcnt(14)\n"
  "v_mfma_f32_32x32x16_bf16 v[48:63], a[184:187], v[216:219], v[48:63]\n"
  "ds_read_b128 a[248:251], v3 offset:14336\n"
  "v_cmp_gt_i32 s[50:51], 8, v234\n"
  "v_cndmask_b32 v35, v35, v236, s[56:57]\n"
  "v_exp_f32 v226, v34\n"
  "v_add_f32 v228, v224, v225\n"
  "v_cvt_pk_bf16_f32 v96, v224, v225\n"
  "v_cmp_lt_f32 vcc, s47, v229\n"
  "v_mfma_f32_32x32x16_bf16 v[48:63], a[188:191], v[220:223], v[48:63]\n"
  "ds_read_b128 a[252:255], v3 offset:15360\n"
  "v_cmp_gt_i32 s[52:53], 9, v234\n"
  "v_cndmask_b32 v36, v36, v236, s[50:51]\n"
  "v_exp_f32 v227, v35\n"
  "v_add_f32 v228, v228, v226\n"
  "v_add_u32 v26, 0x4000, v3\n"
  "v_bfi_b32 v3, s68, v26, v3\n"
  "s_cbranch_vccnz Lfb_NY\n"
  "Lret_NY:\n"
  "v_add_f32 v231, v231, v229\n"
  "s_waitcnt lgkmcnt(12)\n"
  "v_mfma_f32_32x32x16_bf16 a[64:79], a[192:195], v[112:115], a[64:79]\n"
  "ds_read_b128 a[128:131], v2 offset:0\n"
  "v_cmp_gt_i32 s[54:55], 10, v234\n"
  "v_cndmask_b32 v37, v37, v236, s[52:53]\n"
  "v_exp_f32 v224, v36\n"
  "v_add_f32 v228, v228, v227\n"
  "v_cvt_pk_bf16_f32 v97, v226, v227\n"
  "v_mfma_f32_32x32x16_bf16 a[80:95], a[196:199], v[112:115], a[80:95]\n"
  "ds_read_b128 a[132:135], v2 offset:1024\n"
  "v_cmp_gt_i32 s[56:57], 11, v234\n"
  "v_cndmask_b32 v38, v38, v236, s[54:55]\n"
  "v_exp_f32 v225, v37\n"
  "v_add_f32 v228, v228, v224\n"
  "buffer_load_dwordx4 v1, s[28:31], s41 offen offset:0 sc1 lds\n"
  "s_add_u32 s40, s40, 16384\n"
  "v_mfma_f32_32x32x16_bf16 a[96:111], a[200:203], v[112:115], a[96:111]\n"
  "ds_read_b128 a[136:139], v2 offset:2048\n"
  "v_cmp_gt_i32 s[50:51], 16, v234\n"
  "v_cndmask_b32 v39, v39, v236, s[56:57]\n"
  "v_exp_f32 v226, v38\n"
  "v_add_f32 v228, v228, v225\n"
  "v_cvt_pk_bf16_f32 v98, v224, v225\n"
  "v_mfma_f32_32x32x16_bf16 a[112:127], a[204:207], v[112:115], a[112:127]\n"
  "ds_read_b128 a[140:143], v2 offset:3072\n"
  "v_cmp_gt_i32 s[52:53], 17, v234\n"
  "v_cndmask_b32 v40, v40, v236, s[50:51]\n"
  "v_exp_f32 v227, v39\n"
  "v_add_f32 v228, v228, v226\n"
  "s_waitcnt lgkmcnt(12)\n"
  "v_mfma_f32_32x32x16_bf16 a[64:79], a[208:211], v[116:119], a[64:79]\n"
  "ds_read_b128 a[144:147], v2 offset:4096\n"
  "v_cmp_gt_i32 s[54:55], 18, v234\n"
  "v_cndmask_b32 v41, v41, v236, s[52:53]\n"
  "v_exp_f32 v224, v40\n"
  "v_add_f32 v228, v228, v227\n"
  "v_cvt_pk_bf16_f32 v99, v226, v227\n"
  "buffer_load_dwordx4 v1, s[28:31], s41 offen offset:1024 sc1 lds\n"
  "s_min_u32 s40, s40, s42\n"
  "v_mfma_f32_32x32x16_bf16 a[80:95], a[212:215], v[116:119], a[80:95]\n"
  "ds_read_b128 a[148:151], v2 offset:5120\n"
  "v_cmp_gt_i32 s[56:57], 19, v234\n"
  "v_cndmask_b32 v42, v42, v236, s[54:55]\n"
  "v_exp_f32 v225, v41\n"
  "v_add_f32 v228, v228, v224\n"
  "v_mfma_f32_32x32x16_bf16 a[96:111], a[216:219], v[116:119], a[96:111]\n"
  "ds_read_b128 a[152:155], v2 offset:6144\n"
  "v_cmp_gt_i32 s[50:51], 24, v234\n"
  "v_cndmask_b32 v43, v43, v236, s[56:57]\n"
  "v_exp_f32 v226, v42\n"
  "v_add_f32 v228, v228, v225\n"
  "v_cvt_pk_bf16_f32 v100, v224, v225\n"
  "v_mfma_f32_32x32x16_bf16 a[112:127], a[220:223], v[116:119], a[112:127]\n"
  "ds_read_b128 a[156:159], v2 offset:7168\n"
  "v_cmp_gt_i32 s[52:53], 25, v234\n"
  "v_cndmask_b32 v44, v44, v236, s[50:51]\n"
  "v_exp_f32 v227, v43\n"
  "v_add_f32 v228, v228, v226\n"
  "buffer_load_dwordx4 v1, s[28:31], s41 offen offset:2048 sc1 lds\n"
  "s_waitcnt lgkmcnt(12)\n"
  "v_mfma_f32_32x32x16_bf16 a[64:79], a[224:227], v[120:123], a[64:79]\n"
  "ds_read_b128 a[160:163], v2 offset:8192\n"
  "v_cmp_gt_i32 s[54:55], 26, v234\n"
  "v_cndmask_b32 v45, v45, v236, s[52:53]\n"
  "v_exp_f32 v224, v44\n"
  "v_add_f32 v228, v228, v227\n"
  "v_cvt_pk_bf16_f32 v101, v226, v227\n"
  "v_mfma_f32_32x32x16_bf16 a[80:95], a[228:231], v[120:123], a[80:95]\n"
  "ds_read_b128 a[164:167], v2 offset:9216\n"
  "v_cmp_gt_i32 s[56:57], 27, v234\n"
  "v_cndmask_b32 v46, v46, v236, s[54:55]\n"
  "v_exp_f32 v225, v45\n"
  "v_add_f32 v228, v228, v224\n"
  "v_mfma_f32_32x32x16_bf16 a[96:111], a[232:235], v[120:123], a[96:111]\n"
  "ds_read_b128 a[168:171], v2 offset:10240\n"
  "v_cmp_gt_i32 s[50:51], 32, v234\n"
  "v_cndmask_b32 v47, v47, v236, s[56:57]\n"
  "v_exp_f32 v226, v46\n"
  "v_add_f32 v228, v228, v225\n"
  "v_cvt_pk_bf16_f32 v102, v224, v225\n"
  "buffer_load_dwordx4 v1, s[28:31], s41 offen offset:3072 sc1 lds\n"
  "s_add_u32 s45, s45, 16384\n"
  "v_mfma_f32_32x32x16_bf16 a[112:127], a[236:239], v[120:123], a[112:127]\n"
  "ds_read_b128 a[172:175], v2 offset:11264\n"
  "v_cmp_gt_i32 s[52:53], 33, v234\n"
  "v_cndmask_b32 v48, v48, v236, s[50:51]\n"
  "v_exp_f32 v227, v47\n"
  "v_add_f32 v228, v228, v226\n"
  "s_waitcnt lgkmcnt(12)\n"
  "v_mfma_f32_32x32x16_bf16 a[64:79], a[240:243], v[124:127], a[64:79]\n"
  "ds_read_b128 a[176:179], v2 offset:12288\n"
  "v_cmp_gt_i32 s[54:55], 34, v234\n"
  "v_cndmask_b32 v49, v49, v236, s[52:53]\n"
  "v_exp_f32 v224, v48\n"
  "v_add_f32 v228, v228, v227\n"
  "v_cvt_pk_bf16_f32 v103, v226, v227\n"
  "s_add_u32 s41, s41, 16384\n"
  "s_min_u32 s41, s41, s42\n"
  "v_mfma_f32_32x32x16_bf16 a[80:95], a[244:247], v[124:127], a[80:95]\n"
  "ds_read_b128 a[180:183], v2 offset:13312\n"
  "v_cmp_gt_i32 s[56:57], 35, v234\n"
  "v_cndmask_b32 v50, v50, v236, s[54:55]\n"
  "v_exp_f32 v225, v49\n"
  "v_add_f32 v228, v228, v224\n"
  "v_mfma_f32_32x32x16_bf16 a[96:111], a[248:251], v[124:127], a[96:111]\n"
  "ds_read_b128 a[184:187], v2 offset:14336\n"
  "v_cmp_gt_i32 s[50:51], 40, v234\n"
  "v_cndmask_b32 v51, v51, v236, s[56:57]\n"
  "v_exp_f32 v226, v50\n"
  "v_add_f32 v228, v228, v225\n"
  "v_cvt_pk_bf16_f32 v104, v224, v225\n"
  "v_mfma_f32_32x32x16_bf16 a[112:127], a[252:255], v[124:127], a[112:127]\n"
  "ds_read_b128 a[188:191], v2 offset:15360\n"
  "v_cmp_gt_i32 s[52:53], 41, v234\n"
  "v_cndmask_b32 v52, v52, v236, s[50:51]\n"
  "v_exp_f32 v227, v51\n"
  "v_add_f32 v228, v228, v226\n"
  "v_add_u32 v2, 0x4000, v2\n"
  "v_and_b32 v2, 0xffff, v2\n"
  "s_waitcnt vmcnt(16)\n"
  "s_waitcnt vmcnt(12)\n"
  "s_barrier\n"
  "s_waitcnt lgkmcnt(12)\n"
  "v_mfma_f32_32x32x16_bf16 v[64:79], a[128:131], v[192:195], v[144:159]\n"
  "ds_read_b128 a[192:195], v3 offset:0\n"
  "v_cmp_gt_i32 s[54:55], 42, v234\n"
  "v_cndmask_b32 v53, v53, v236, s[52:53]\n"
  "v_exp_f32 v224, v52\n"
  "v_add_f32 v228, v228, v227\n"
  "v_cvt_pk_bf16_f32 v105, v226, v227\n"
  "v_mfma_f32_32x32x16_bf16 v[64:79], a[132:135], v[196:199], v[64:79]\n"
  "ds_read_b128 a[196:199], v3 offset:1024\n"
  "v_cmp_gt_i32 s[56:57], 43, v234\n"
  "v_cndmask_b32 v54, v54, v236, s[54:55]\n"
  "v_exp_f32 v225, v53\n"
  "v_add_f32 v228, v228, v224\n"
  "s_and_b32 m0, s44, 0xffff\n"
  "v_mfma_f32_32x32x16_bf16 v[64:79], a[136:139], v[200:203], v[64:79]\n"
  "ds_read_b128 a[200:203], v3 offset:2048\n"
  "v_cmp_gt_i32 s[50:51], 48, v234\n"
  "v_cndmask_b32 v55, v55, v236, s[56:57]\n"
  "v_exp_f32 v226, v54\n"
  "v_add_f32 v228, v228, v225\n"
  "v_cvt_pk_bf16_f32 v106, v224, v225\n"
  "v_mfma_f32_32x32x16_bf16 v[64:79], a[140:143], v[204:207], v[64:79]\n"
  "ds_read_b128 a[204:207], v3 offset:3072\n"
  "v_cmp_gt_i32 s[52:53], 49, v234\n"
  "v_cndmask_b32 v56, v56, v236, s[50:51]\n"
  "v_exp_f32 v227, v55\n"
  "v_add_f32 v228, v228, v226\n"
  "buffer_load_dwordx4 v1, s[24:27], s40 offen offset:0 sc1 lds\n"
  "s_waitcnt lgkmcnt(12)\n"
  "v_mfma_f32_32x32x16_bf16 v[64:79], a[144:147], v[208:211], v[64:79]\n"
  "ds_read_b128 a[208:211], v3 offset:4096\n"
  "v_cmp_gt_i32 s[54:55], 50, v234\n"
  "v_cndmask_b32 v57, v57, v236, s[52:53]\n"
  "v_exp_f32 v224, v56\n"
  "v_add_f32 v228, v228, v227\n"
  "v_cvt_pk_bf16_f32 v107, v226, v227\n"
  "v_mfma_f32_32x32x16_bf16 v[64:79], a[148:151], v[212:215], v[64:79]\n"
  "ds_read_b128 a[212:215], v3 offset:5120\n"
  "v_cmp_gt_i32 s[56:57], 51, v234\n"
  "v_cndmask_b32 v58, v58, v236, s[54:55]\n"
  "v_exp_f32 v225, v57\n"
  "v_add_f32 v228, v228, v224\n"
  "buffer_load_dwordx4 v1, s[24:27], s40 offen offset:1024 sc1 lds\n"
  "v_mfma_f32_32x32x16_bf16 v[64:79], a[152:155], v[216:219], v[64:79]\n"
  "ds_read_b128 a[216:219], v3 offset:6144\n"
  "v_cmp_gt_i32 s[50:51], 56, v234\n"
  "v_cndmask_b32 v59, v59, v236, s[56:57]\n"
  "v_exp_f32 v226, v58\n"
  "v_add_f32 v228, v228, v225\n"
  "v_cvt_pk_bf16_f32 v108, v224, v225\n"
  "v_mfma_f32_32x32x16_bf16 v[64:79], a[156:159], v[220:223], v[64:79]\n"
  "ds_read_b128 a[220:223], v3 offset:7168\n"
  "v_cmp_gt_i32 s[52:53], 57, v234\n"
  "v_cndmask_b32 v60, v60, v236, s[50:51]\n"
  "v_exp_f32 v227, v59\n"
  "v_add_f32 v228, v228, v226\n"
  "buffer_load_dwordx4 v1, s[24:27], s40 offen offset:2048 sc1 lds\n"
  "s_waitcnt lgkmcnt(12)\n"
  "v_mfma_f32_32x32x16_bf16 v[80:95], a[160:163], v[192:195], v[144:159]\n"
  "ds_read_b128 a[224:227], v3 offset:8192\n"
  "v_cmp_gt_i32 s[54:55], 58, v234\n"
  "v_cndmask_b32 v61, v61, v236, s[52:53]\n"
  "v_exp_f32 v224, v60\n"
  "v_add_f32 v228, v228, v227\n"
  "v_cvt_pk_bf16_f32 v109, v226, v227\n"
  "v_mfma_f32_32x32x16_bf16 v[80:95], a[164:167], v[196:199], v[80:95]\n"
  "ds_read_b128 a[228:231], v3 offset:9216\n"
  "v_cmp_gt_i32 s[56:57], 59, v234\n"
  "v_cndmask_b32 v62, v62, v236, s[54:55]\n"
  "v_exp_f32 v225, v61\n"
  "v_add_f32 v228, v228, v224\n"
  "buffer_load_dwordx4 v1, s[24:27], s40 offen offset:3072 sc1 lds\n"
  "s_and_b32 s58, s45, 0xffff\n"
  "v_mfma_f32_32x32x16_bf16 v[80:95], a[168:171], v[200:203], v[80:95]\n"
  "ds_read_b128 a[232:235], v3 offset:10240\n"
  "v_cndmask_b32 v63, v63, v236, s[56:57]\n"
  "v_exp_f32 v226, v62\n"
  "v_add_f32 v228, v228, v225\n"
  "v_cvt_pk_bf16_f32 v110, v224, v225\n"
  "v_cmp_gt_i32 s[50:51], 0, v235\n"
  "v_mfma_f32_32x32x16_bf16 v[80:95], a[172:175], v[204:207], v[80:95]\n"
  "ds_read_b128 a[236:239], v3 offset:11264\n"
  "v_exp_f32 v227, v63\n"
  "v_add_f32 v228, v228, v226\n"
  "v_cmp_gt_i32 s[52:53], 1, v235\n"
  "v_cndmask_b32 v64, v64, v236, s[50:51]\n"
  "s_waitcnt lgkmcnt(12)\n"
  "v_mfma_f32_32x32x16_bf16 v[80:95], a[176:179], v[208:211], v[80:95]\n"
  "ds_read_b128 a[240:243], v3 offset:12288\n"
  "v_add_f32 v228, v228, v227\n"
  "v_cvt_pk_bf16_f32 v111, v226, v227\n"
  "v_cmp_gt_i32 s[54:55], 2, v235\n"
  "v_cndmask_b32 v65, v65, v236, s[52:53]\n"
  "v_exp_f32 v224, v64\n"
  "s_add_u32 s44, s44, 16384\n"
  "s_or_b32 m0, s58, 0x10000\n"
  "v_mfma_f32_32x32x16_bf16 v[80:95], a[180:183], v[212:215], v[80:95]\n"
  "ds_read_b128 a[244:247], v3 offset:13312\n"
  "v_cmp_gt_i32 s[56:57], 3, v235\n"
  "v_cndmask_b32 v66, v66, v236, s[54:55]\n"
  "v_exp_f32 v225, v65\n"
  "v_mfma_f32_32x32x16_bf16 v[80:95], a[184:187], v[216:219], v[80:95]\n"
  "ds_read_b128 a[248:251], v3 offset:14336\n"
  "v_cmp_gt_i32 s[50:51], 8, v235\n"
  "v_cndmask_b32 v67, v67, v236, s[56:57]\n"
  "v_exp_f32 v226, v66\n"
  "v_add_f32 v229, v224, v225\n"
  "v_cvt_pk_bf16_f32 v112, v224, v225\n"
  "v_cmp_lt_f32 vcc, s47, v228\n"
  "v_mfma_f32_32x32x16_bf16 v[80:95], a[188:191], v[220:223], v[80:95]\n"
  "ds_read_b128 a[252:255], v3 offset:15360\n"
  "v_cmp_gt_i32 s[52:53], 9, v235\n"
  "v_cndmask_b32 v68, v68, v236, s[50:51]\n"
  "v_exp_f32 v227, v67\n"
  "v_add_f32 v229, v229, v226\n"
  "v_add_u32 v26, 0x4000, v3\n"
  "v_bfi_b32 v3, s68, v26, v3\n"
  "s_cbranch_vccnz Lfb_NX\n"
  "Lret_NX:\n"
  "v_add_f32 v231, v231, v228\n"
  "s_waitcnt lgkmcnt(13)\n"
  "v_mfma_f32_32x32x16_bf16 a[64:79], a[192:195], v[96:99], a[64:79]\n"
  "ds_read_b128 a[128:131], v2 offset:0\n"
  "v_cmp_gt_i32 s[54:55], 10, v235\n"
  "v_cndmask_b32 v69, v69, v236, s[52:53]\n"
  "v_exp_f32 v224, v68\n"
  "v_add_f32 v229, v229, v227\n"
  "v_cvt_pk_bf16_f32 v113, v226, v227\n"
  "v_mfma_f32_32x32x16_bf16 a[80:95], a[196:199], v[96:99], a[80:95]\n"
  "ds_read_b128 a[132:135], v2 offset:1024\n"
  "v_cmp_gt_i32 s[56:57], 11, v235\n"
  "v_cndmask_b32 v70, v70, v236, s[54:55]\n"
  "v_exp_f32 v225, v69\n"
  "v_add_f32 v229, v229, v224\n"
  "buffer_load_dwordx4 v1, s[28:31], s41 offen offset:0 sc1 lds\n"
  "s_add_u32 s40, s40, 16384\n"
  "v_mfma_f32_32x32x16_bf16 a[96:111], a[200:203], v[96:99], a[96:111]\n"
  "ds_read_b128 a[136:139], v2 offset:2048\n"
  "v_cmp_gt_i32 s[50:51], 16, v235\n"
  "v_cndmask_b32 v71, v71, v236, s[56:57]\n"
  "v_exp_f32 v226, v70\n"
  "v_add_f32 v229, v229, v225\n"
  "v_cvt_pk_bf16_f32 v114, v224, v225\n"
  "v_subrev_u32 v234, 128, v234\n"
  "s_waitcnt lgkmcnt(12)\n"
  "v_mfma_f32_32x32x16_bf16 a[112:127], a[204:207], v[96:99], a[112:127]\n"
  "ds_read_b128 a[140:143], v2 offset:3072\n"
  "v_cmp_gt_i32 s[52:53], 17, v235\n"
  "v_cndmask_b32 v72, v72, v236, s[50:51]\n"
  "v_exp_f32 v227, v71\n"
  "v_add_f32 v229, v229, v226\n"
  "v_mfma_f32_32x32x16_bf16 a[64:79], a[208:211], v[100:103], a[64:79]\n"
  "ds_read_b128 a[144:147], v2 offset:4096\n"
  "v_cmp_gt_i32 s[54:55], 18, v235\n"
  "v_cndmask_b32 v73, v73, v236, s[52:53]\n"
  "v_exp_f32 v224, v72\n"
  "v_add_f32 v229, v229, v227\n"
  "v_cvt_pk_bf16_f32 v115, v226, v227\n"
  "buffer_load_dwordx4 v1, s[28:31], s41 offen offset:1024 sc1 lds\n"
  "s_min_u32 s40, s40, s42\n"
  "v_mfma_f32_32x32x16_bf16 a[80:95], a[212:215], v[100:103], a[80:95]\n"
  "ds_read_b128 a[148:151], v2 offset:5120\n"
  "v_cmp_gt_i32 s[56:57], 19, v235\n"
  "v_cndmask_b32 v74, v74, v236, s[54:55]\n"
  "v_exp_f32 v225, v73\n"
  "v_add_f32 v229, v229, v224\n"
  "v_mfma_f32_32x32x16_bf16 a[96:111], a[216:219], v[100:103], a[96:111]\n"
  "ds_read_b128 a[152:155], v2 offset:6144\n"
  "v_cmp_gt_i32 s[50:51], 24, v235\n"
  "v_cndmask_b32 v75, v75, v236, s[56:57]\n"
  "v_exp_f32 v226, v74\n"
  "v_add_f32 v229, v229, v225\n"
  "v_cvt_pk_bf16_f32 v116, v224, v225\n"
  "s_waitcnt lgkmcnt(12)\n"
  "v_mfma_f32_32x32x16_bf16 a[112:127], a[220:223], v[100:103], a[112:127]\n"
  "ds_read_b128 a[156:159], v2 offset:7168\n"
  "v_cmp_gt_i32 s[52:53], 25, v235\n"
  "v_cndmask_b32 v76, v76, v236, s[50:51]\n"
  "v_exp_f32 v227, v75\n"
  "v_add_f32 v229, v229, v226\n"
  "buffer_load_dwordx4 v1, s[28:31], s41 offen offset:2048 sc1 lds\n"
  "v_mfma_f32_32x32x16_bf16 a[64:79], a[224:227], v[104:107], a[64:79]\n"
  "ds_read_b128 a[160:163], v2 offset:8192\n"
  "v_cmp_gt_i32 s[54:55], 26, v235\n"
  "v_cndmask_b32 v77, v77, v236, s[52:53]\n"
  "v_exp_f32 v224, v76\n"
  "v_add_f32 v229, v229, v227\n"
  "v_cvt_pk_bf16_f32 v117, v226, v227\n"
  "v_mfma_f32_32x32x16_bf16 a[80:95], a[228:231], v[104:107], a[80:95]\n"
  "ds_read_b128 a[164:167], v2 offset:9216\n"
  "v_cmp_gt_i32 s[56:57], 27, v235\n"
  "v_cndmask_b32 v78, v78, v236, s[54:55]\n"
  "v_exp_f32 v225, v77\n"
  "v_add_f32 v229, v229, v224\n"
  "v_mfma_f32_32x32x16_bf16 a[96:111], a[232:235], v[104:107], a[96:111]\n"
  "ds_read_b128 a[168:171], v2 offset:10240\n"
  "v_cmp_gt_i32 s[50:51], 32, v235\n"
  "v_cndmask_b32 v79, v79, v236, s[56:57]\n"
  "v_exp_f32 v226, v78\n"
  "v_add_f32 v229, v229, v225\n"
  "v_cvt_pk_bf16_f32 v118, v224, v225\n"
  "buffer_load_dwordx4 v1, s[28:31], s41 offen offset:3072 sc1 lds\n"
  "s_add_u32 s45, s45, 16384\n"
  "s_waitcnt lgkmcnt(12)\n"
  "v_mfma_f32_32x32x16_bf16 a[112:127], a[236:239], v[104:107], a[112:127]\n"
  "ds_read_b128 a[172:175], v2 offset:11264\n"
  "v_cmp_gt_i32 s[52:53], 33, v235\n"
  "v_cndmask_b32 v80, v80, v236, s[50:51]\n"
  "v_exp_f32 v227, v79\n"
  "v_add_f32 v229, v229, v226\n"
  "v_mfma_f32_32x32x16_bf16 a[64:79], a[240:243], v[108:111], a[64:79]\n"
  "ds_read_b128 a[176:179], v2 offset:12288\n"
  "v_cmp_gt_i32 s[54:55], 34, v235\n"
  "v_cndmask_b32 v81, v81, v236, s[52:53]\n"
  "v_exp_f32 v224, v80\n"
  "v_add_f32 v229, v229, v227\n"
  "v_cvt_pk_bf16_f32 v119, v226, v227\n"
  "s_add_u32 s41, s41, 16384\n"
  "s_min_u32 s41, s41, s42\n"
  "v_mfma_f32_32x32x16_bf16 a[80:95], a[244:247], v[108:111], a[80:95]\n"
  "ds_read_b128 a[180:183], v2 offset:13312\n"
  "v_cmp_gt_i32 s[56:57], 35, v235\n"
  "v_cndmask_b32 v82, v82, v236, s[54:55]\n"
  "v_exp_f32 v225, v81\n"
  "v_add_f32 v229, v229, v224\n"
  "v_mfma_f32_32x32x16_bf16 a[96:111], a[248:251], v[108:111], a[96:111]\n"
  "ds_read_b128 a[184:187], v2 offset:14336\n"
  "v_cmp_gt_i32 s[50:51], 40, v235\n"
  "v_cndmask_b32 v83, v83, v236, s[56:57]\n"
  "v_exp_f32 v226, v82\n"
  "v_add_f32 v229, v229, v225\n"
  "v_cvt_pk_bf16_f32 v120, v224, v225\n"
  "s_waitcnt lgkmcnt(15)\n"
  "v_mfma_f32_32x32x16_bf16 a[112:127], a[252:255], v[108:111], a[112:127]\n"
  "ds_read_b128 a[188:191], v2 offset:15360\n"
  "v_cmp_gt_i32 s[52:53], 41, v235\n"
  "v_cndmask_b32 v84, v84, v236, s[50:51]\n"
  "v_exp_f32 v227, v83\n"
  "v_add_f32 v229, v229, v226\n"
  "v_add_u32 v2, 0x4000, v2\n"
  "v_and_b32 v2, 0xffff, v2\n"
  "s_waitcnt vmcnt(16)\n"
  "s_barrier\n"
  "ds_read_b128 a[192:195], v3 offset:0\n"
  "v_cmp_gt_i32 s[54:55], 42, v235\n"
  "v_cndmask_b32 v85, v85, v236, s[52:53]\n"
  "v_exp_f32 v224, v84\n"
  "v_add_f32 v229, v229, v227\n"
  "v_cvt_pk_bf16_f32 v121, v226, v227\n"
  "ds_read_b128 a[196:199], v3 offset:1024\n"
  "v_cmp_gt_i32 s[56:57], 43, v235\n"
  "v_cndmask_b32 v86, v86, v236, s[54:55]\n"
  "v_exp_f32 v225, v85\n"
  "v_add_f32 v229, v229, v224\n"
  "ds_read_b128 a[200:203], v3 offset:2048\n"
  "v_cmp_gt_i32 s[50:51], 48, v235\n"
  "v_cndmask_b32 v87, v87, v236, s[56:57]\n"
  "v_exp_f32 v226, v86\n"
  "v_add_f32 v229, v229, v225\n"
  "v_cvt_pk_bf16_f32 v122, v224, v225\n"
  "ds_read_b128 a[204:207], v3 offset:3072\n"
  "v_cmp_gt_i32 s[52:53], 49, v235\n"
  "v_cndmask_b32 v88, v88, v236, s[50:51]\n"
  "v_exp_f32 v227, v87\n"
  "v_add_f32 v229, v229, v226\n"
  "ds_read_b128 a[208:211], v3 offset:4096\n"
  "v_cmp_gt_i32 s[54:55], 50, v235\n"
  "v_cndmask_b32 v89, v89, v236, s[52:53]\n"
  "v_exp_f32 v224, v88\n"
  "v_add_f32 v229, v229, v227\n"
  "v_cvt_pk_bf16_f32 v123, v226, v227\n"
  "ds_read_b128 a[212:215], v3 offset:5120\n"
  "v_cmp_gt_i32 s[56:57], 51, v235\n"
  "v_cndmask_b32 v90, v90, v236, s[54:55]\n"
  "v_exp_f32 v225, v89\n"
  "v_add_f32 v229, v229, v224\n"
  "ds_read_b128 a[216:219], v3 offset:6144\n"
  "v_cmp_gt_i32 s[50:51], 56, v235\n"
  "v_cndmask_b32 v91, v91, v236, s[56:57]\n"
  "v_exp_f32 v226, v90\n"
  "v_add_f32 v229, v229, v225\n"
  "v_cvt_pk_bf16_f32 v124, v224, v225\n"
  "ds_read_b128 a[220:223], v3 offset:7168\n"
  "v_cmp_gt_i32 s[52:53], 57, v235\n"
  "v_cndmask_b32 v92, v92, v236, s[50:51]\n"
  "v_exp_f32 v227, v91\n"
  "v_add_f32 v229, v229, v226\n"
  "ds_read_b128 a[224:227], v3 offset:8192\n"
  "v_cmp_gt_i32 s[54:55], 58, v235\n"
  "v_cndmask_b32 v93, v93, v236, s[52:53]\n"
  "v_exp_f32 v224, v92\n"
  "v_add_f32 v229, v229, v227\n"
  "v_cvt_pk_bf16_f32 v125, v226, v227\n"
  "ds_read_b128 a[228:231], v3 offset:9216\n"
  "v_cmp_gt_i32 s[56:57], 59, v235\n"
  "v_cndmask_b32 v94, v94, v236, s[54:55]\n"
  "v_exp_f32 v225, v93\n"
  "v_add_f32 v229, v229, v224\n"
  "ds_read_b128 a[232:235], v3 offset:10240\n"
  "v_cndmask_b32 v95, v95, v236, s[56:57]\n"
  "v_exp_f32 v226, v94\n"
  "v_add_f32 v229, v229, v225\n"
  "v_cvt_pk_bf16_f32 v126, v224, v225\n"
  "ds_read_b128 a[236:239], v3 offset:11264\n"
  "v_exp_f32 v227, v95\n"
  "v_add_f32 v229, v229, v226\n"
  "ds_read_b128 a[240:243], v3 offset:12288\n"
  "v_add_f32 v229, v229, v227\n"
  "v_cvt_pk_bf16_f32 v127, v226, v227\n"
  "ds_read_b128 a[244:247], v3 offset:13312\n"
  "ds_read_b128 a[248:251], v3 offset:14336\n"
  "v_cmp_lt_f32 vcc, s47, v229\n"
  "ds_read_b128 a[252:255], v3 offset:15360\n"
  "s_cbranch_vccnz Lfb_drainY\n"
  "Lret_drainY:\n"
  "v_add_f32 v231, v231, v229\n"
  "s_waitcnt lgkmcnt(7)\n"
  "v_mfma_f32_32x32x16_bf16 a[64:79], a[192:195], v[112:115], a[64:79]\n"
  "v_mfma_f32_32x32x16_bf16 a[80:95], a[196:199], v[112:115], a[80:95]\n"
  "v_mfma_f32_32x32x16_bf16 a[96:111], a[200:203], v[112:115], a[96:111]\n"
  "v_mfma_f32_32x32x16_bf16 a[112:127], a[204:207], v[112:115], a[112:127]\n"
  "v_mfma_f32_32x32x16_bf16 a[64:79], a[208:211], v[116:119], a[64:79]\n"
  "v_mfma_f32_32x32x16_bf16 a[80:95], a[212:215], v[116:119], a[80:95]\n"
  "v_mfma_f32_32x32x16_bf16 a[96:111], a[216:219], v[116:119], a[96:111]\n"
  "v_mfma_f32_32x32x16_bf16 a[112:127], a[220:223], v[116:119], a[112:127]\n"
  "v_mfma_f32_32x32x16_bf16 a[64:79], a[224:227], v[120:123], a[64:79]\n"
  "s_waitcnt lgkmcnt(5)\n"
  "v_mfma_f32_32x32x16_bf16 a[80:95], a[228:231], v[120:123], a[80:95]\n"
  "v_mfma_f32_32x32x16_bf16 a[96:111], a[232:235], v[120:123], a[96:111]\n"
  "s_waitcnt lgkmcnt(4)\n"
  "v_mfma_f32_32x32x16_bf16 a[112:127], a[236:239], v[120:123], a[112:127]\n"
  "s_waitcnt lgkmcnt(3)\n"
  "v_mfma_f32_32x32x16_bf16 a[64:79], a[240:243], v[124:127], a[64:79]\n"
  "s_waitcnt lgkmcnt(2)\n"
  "v_mfma_f32_32x32x16_bf16 a[80:95], a[244:247], v[124:127], a[80:95]\n"
  "s_waitcnt lgkmcnt(1)\n"
  "v_mfma_f32_32x32x16_bf16 a[96:111], a[248:251], v[124:127], a[96:111]\n"
  "s_waitcnt lgkmcnt(0)\n"
  "v_mfma_f32_32x32x16_bf16 a[112:127], a[252:255], v[124:127], a[112:127]\n"
  "s_load_dwordx2 s[58:59], s[8:9], 32\n"
  "s_load_dwordx2 s[60:61], s[8:9], 40\n"
  "s_lshr_b32 s36, s10, 2\n"
  "s_mul_i32 s36, s36, 9\n"
  "s_and_b32 s62, s10, 3\n"
  "s_lshl_b32 s63, s62, 2\n"
  "s_lshr_b32 s64, 0x6520, s63\n"
  "s_and_b32 s64, s64, 15\n"
  "s_add_u32 s36, s36, s64\n"
  "s_add_u32 s63, s63, s11\n"
  "s_lshr_b32 s37, 0x1737, s63\n"
  "s_and_b32 s37, s37, 1\n"
  "s_lshr_b32 s38, 0x48, s63\n"
  "s_and_b32 s38, s38, 1\n"
  "s_lshl_b32 s63, s63, 1\n"
  "s_lshr_b32 s39, 0x4A241924, s63\n"
  "s_and_b32 s39, s39, 3\n"
  "s_add_u32 s40, s62, 1\n"
  "s_lshl_b32 s62, s11, 10\n"
  "v_add_u32 v200, s62, v1\n"
  "v_subrev_u32 v216, 16, v200\n"
  "v_mov_b32 v217, 0\n"
  "s_waitcnt lgkmcnt(0)\n"
  "s_mov_b32 s16, s58\n"
  "s_mov_b32 s17, s59\n"
  "s_and_b32 s17, s17, 0xffff\n"
  "s_mov_b32 s18, 2359296\n"
  "s_mov_b32 s19, 131072\n"
  "s_mov_b32 s28, s60\n"
  "s_mov_b32 s29, s61\n"
  "s_and_b32 s29, s29, 0xffff\n"
  "s_mov_b32 s30, 1152\n"
  "s_mov_b32 s31, 131072\n"
  "s_add_u32 s20, s14, 75497472\n"
  "s_addc_u32 s21, s15, 0\n"
  "s_and_b32 s21, s21, 0xffff\n"
  "s_mov_b32 s22, 2359296\n"
  "s_mov_b32 s23, 131072\n"
  "s_add_u32 s24, s14, 79691776\n"
  "s_addc_u32 s25, s15, 0\n"
  "s_and_b32 s25, s25, 0xffff\n"
  "s_mov_b32 s26, 2359296\n"
  "s_mov_b32 s27, 131072\n"
  "s_add_u32 s58, s36, 0\n"
  "s_lshl_b32 s59, s58, 12\n"
  "s_lshl_b32 s60, s58, 1\n"
  "buffer_load_dwordx4 v[160:163], v200, s[16:19], s59 offen\n"
  "buffer_load_dwordx4 v[164:167], v216, s[16:19], s59 offen\n"
  "buffer_load_dwordx4 v[168:171], v200, s[20:23], s59 offen sc1\n"
  "buffer_load_ushort v196, v217, s[28:31], s60 offen\n"
  "s_add_u32 s58, s36, 1\n"
  "s_lshl_b32 s59, s58, 12\n"
  "s_lshl_b32 s60, s58, 1\n"
  "buffer_load_dwordx4 v[172:175], v200, s[16:19], s59 offen\n"
  "buffer_load_dwordx4 v[176:179], v216, s[16:19], s59 offen\n"
  "buffer_load_dwordx4 v[180:183], v200, s[20:23], s59 offen sc1\n"
  "buffer_load_ushort v197, v217, s[28:31], s60 offen\n"
  "s_add_u32 s58, s36, 2\n"
  "s_lshl_b32 s59, s58, 12\n"
  "s_lshl_b32 s60, s58, 1\n"
  "buffer_load_dwordx4 v[184:187], v200, s[16:19], s59 offen\n"
  "buffer_load_dwordx4 v[188:191], v216, s[16:19], s59 offen\n"
  "buffer_load_dwordx4 v[192:195], v200, s[20:23], s59 offen sc1\n"
  "buffer_load_ushort v198, v217, s[28:31], s60 offen\n"
  "s_lshl_b32 s58, s13, 2\n"
  "s_add_u32 s58, s58, s11\n"
  "s_lshl_b32 s59, s12, 6\n"
  "s_add_u32 s58, s58, s59\n"
  "s_lshl_b32 s69, s58, 13\n"
  "v_mov_b32 v238, v230\n"
  "s_nop 1\n"
  "v_permlane32_swap_b32 v230, v238\n"
  "v_add_f32 v230, v230, v238\n"
  "v_rcp_f32 v230, v230\n"
  "v_accvgpr_read_b32 v240, a0\n"
  "v_accvgpr_read_b32 v241, a1\n"
  "v_accvgpr_read_b32 v242, a2\n"
  "v_accvgpr_read_b32 v243, a3\n"
  "v_accvgpr_read_b32 v244, a4\n"
  "v_accvgpr_read_b32 v245, a5\n"
  "v_accvgpr_read_b32 v246, a6\n"
  "v_accvgpr_read_b32 v247, a7\n"
  "v_mul_f32 v240, v240, v230\n"
  "v_mul_f32 v241, v241, v230\n"
  "v_mul_f32 v242, v242, v230\n"
  "v_mul_f32 v243, v243, v230\n"
  "v_mul_f32 v244, v244, v230\n"
  "v_mul_f32 v245, v245, v230\n"
  "v_mul_f32 v246, v246, v230\n"
  "v_mul_f32 v247, v247, v230\n"
  "v_cvt_pk_bf16_f32 v32, v240, v241\n"
  "v_cvt_pk_bf16_f32 v33, v242, v243\n"
  "v_cvt_pk_bf16_f32 v34, v244, v245\n"
  "v_cvt_pk_bf16_f32 v35, v246, v247\n"
  "s_add_u32 s58, s69, 0\n"
  "buffer_store_dwordx4 v[32:35], v1, s[32:35], s58 offen offset:0 sc1\n"
  "v_accvgpr_read_b32 v248, a8\n"
  "v_accvgpr_read_b32 v249, a9\n"
  "v_accvgpr_read_b32 v250, a10\n"
  "v_accvgpr_read_b32 v251, a11\n"
  "v_accvgpr_read_b32 v252, a12\n"
  "v_accvgpr_read_b32 v253, a13\n"
  "v_accvgpr_read_b32 v254, a14\n"
  "v_accvgpr_read_b32 v255, a15\n"
  "v_mul_f32 v248, v248, v230\n"
  "v_mul_f32 v249, v249, v230\n"
  "v_mul_f32 v250, v250, v230\n"
  "v_mul_f32 v251, v251, v230\n"
  "v_mul_f32 v252, v252, v230\n"
  "v_mul_f32 v253, v253, v230\n"
  "v_mul_f32 v254, v254, v230\n"
  "v_mul_f32 v255, v255, v230\n"
  "v_cvt_pk_bf16_f32 v36, v248, v249\n"
  "v_cvt_pk_bf16_f32 v37, v250, v251\n"
  "v_cvt_pk_bf16_f32 v38, v252, v253\n"
  "v_cvt_pk_bf16_f32 v39, v254, v255\n"
  "buffer_store_dwordx4 v[36:39], v1, s[32:35], s58 offen offset:1024 sc1\n"
  "v_accvgpr_read_b32 v240, a16\n"
  "v_accvgpr_read_b32 v241, a17\n"
  "v_accvgpr_read_b32 v242, a18\n"
  "v_accvgpr_read_b32 v243, a19\n"
  "v_accvgpr_read_b32 v244, a20\n"
  "v_accvgpr_read_b32 v245, a21\n"
  "v_accvgpr_read_b32 v246, a22\n"
  "v_accvgpr_read_b32 v247, a23\n"
  "v_mul_f32 v240, v240, v230\n"
  "v_mul_f32 v241, v241, v230\n"
  "v_mul_f32 v242, v242, v230\n"
  "v_mul_f32 v243, v243, v230\n"
  "v_mul_f32 v244, v244, v230\n"
  "v_mul_f32 v245, v245, v230\n"
  "v_mul_f32 v246, v246, v230\n"
  "v_mul_f32 v247, v247, v230\n"
  "v_cvt_pk_bf16_f32 v40, v240, v241\n"
  "v_cvt_pk_bf16_f32 v41, v242, v243\n"
  "v_cvt_pk_bf16_f32 v42, v244, v245\n"
  "v_cvt_pk_bf16_f32 v43, v246, v247\n"
  "buffer_store_dwordx4 v[40:43], v1, s[32:35], s58 offen offset:2048 sc1\n"
  "v_accvgpr_read_b32 v248, a24\n"
  "v_accvgpr_read_b32 v249, a25\n"
  "v_accvgpr_read_b32 v250, a26\n"
  "v_accvgpr_read_b32 v251, a27\n"
  "v_accvgpr_read_b32 v252, a28\n"
  "v_accvgpr_read_b32 v253, a29\n"
  "v_accvgpr_read_b32 v254, a30\n"
  "v_accvgpr_read_b32 v255, a31\n"
  "v_mul_f32 v248, v248, v230\n"
  "v_mul_f32 v249, v249, v230\n"
  "v_mul_f32 v250, v250, v230\n"
  "v_mul_f32 v251, v251, v230\n"
  "v_mul_f32 v252, v252, v230\n"
  "v_mul_f32 v253, v253, v230\n"
  "v_mul_f32 v254, v254, v230\n"
  "v_mul_f32 v255, v255, v230\n"
  "v_cvt_pk_bf16_f32 v44, v248, v249\n"
  "v_cvt_pk_bf16_f32 v45, v250, v251\n"
  "v_cvt_pk_bf16_f32 v46, v252, v253\n"
  "v_cvt_pk_bf16_f32 v47, v254, v255\n"
  "buffer_store_dwordx4 v[44:47], v1, s[32:35], s58 offen offset:3072 sc1\n"
  "v_accvgpr_read_b32 v240, a32\n"
  "v_accvgpr_read_b32 v241, a33\n"
  "v_accvgpr_read_b32 v242, a34\n"
  "v_accvgpr_read_b32 v243, a35\n"
  "v_accvgpr_read_b32 v244, a36\n"
  "v_accvgpr_read_b32 v245, a37\n"
  "v_accvgpr_read_b32 v246, a38\n"
  "v_accvgpr_read_b32 v247, a39\n"
  "v_mul_f32 v240, v240, v230\n"
  "v_mul_f32 v241, v241, v230\n"
  "v_mul_f32 v242, v242, v230\n"
  "v_mul_f32 v243, v243, v230\n"
  "v_mul_f32 v244, v244, v230\n"
  "v_mul_f32 v245, v245, v230\n"
  "v_mul_f32 v246, v246, v230\n"
  "v_mul_f32 v247, v247, v230\n"
  "v_cvt_pk_bf16_f32 v48, v240, v241\n"
  "v_cvt_pk_bf16_f32 v49, v242, v243\n"
  "v_cvt_pk_bf16_f32 v50, v244, v245\n"
  "v_cvt_pk_bf16_f32 v51, v246, v247\n"
  "s_add_u32 s58, s69, 4096\n"
  "buffer_store_dwordx4 v[48:51], v1, s[32:35], s58 offen offset:0 sc1\n"
  "v_accvgpr_read_b32 v248, a40\n"
  "v_accvgpr_read_b32 v249, a41\n"
  "v_accvgpr_read_b32 v250, a42\n"
  "v_accvgpr_read_b32 v251, a43\n"
  "v_accvgpr_read_b32 v252, a44\n"
  "v_accvgpr_read_b32 v253, a45\n"
  "v_accvgpr_read_b32 v254, a46\n"
  "v_accvgpr_read_b32 v255, a47\n"
  "v_mul_f32 v248, v248, v230\n"
  "v_mul_f32 v249, v249, v230\n"
  "v_mul_f32 v250, v250, v230\n"
  "v_mul_f32 v251, v251, v230\n"
  "v_mul_f32 v252, v252, v230\n"
  "v_mul_f32 v253, v253, v230\n"
  "v_mul_f32 v254, v254, v230\n"
  "v_mul_f32 v255, v255, v230\n"
  "v_cvt_pk_bf16_f32 v52, v248, v249\n"
  "v_cvt_pk_bf16_f32 v53, v250, v251\n"
  "v_cvt_pk_bf16_f32 v54, v252, v253\n"
  "v_cvt_pk_bf16_f32 v55, v254, v255\n"
  "buffer_store_dwordx4 v[52:55], v1, s[32:35], s58 offen offset:1024 sc1\n"
  "v_accvgpr_read_b32 v240, a48\n"
  "v_accvgpr_read_b32 v241, a49\n"
  "v_accvgpr_read_b32 v242, a50\n"
  "v_accvgpr_read_b32 v243, a51\n"
  "v_accvgpr_read_b32 v244, a52\n"
  "v_accvgpr_read_b32 v245, a53\n"
  "v_accvgpr_read_b32 v246, a54\n"
  "v_accvgpr_read_b32 v247, a55\n"
  "v_mul_f32 v240, v240, v230\n"
  "v_mul_f32 v241, v241, v230\n"
  "v_mul_f32 v242, v242, v230\n"
  "v_mul_f32 v243, v243, v230\n"
  "v_mul_f32 v244, v244, v230\n"
  "v_mul_f32 v245, v245, v230\n"
  "v_mul_f32 v246, v246, v230\n"
  "v_mul_f32 v247, v247, v230\n"
  "v_cvt_pk_bf16_f32 v56, v240, v241\n"
  "v_cvt_pk_bf16_f32 v57, v242, v243\n"
  "v_cvt_pk_bf16_f32 v58, v244, v245\n"
  "v_cvt_pk_bf16_f32 v59, v246, v247\n"
  "buffer_store_dwordx4 v[56:59], v1, s[32:35], s58 offen offset:2048 sc1\n"
  "v_accvgpr_read_b32 v248, a56\n"
  "v_accvgpr_read_b32 v249, a57\n"
  "v_accvgpr_read_b32 v250, a58\n"
  "v_accvgpr_read_b32 v251, a59\n"
  "v_accvgpr_read_b32 v252, a60\n"
  "v_accvgpr_read_b32 v253, a61\n"
  "v_accvgpr_read_b32 v254, a62\n"
  "v_accvgpr_read_b32 v255, a63\n"
  "v_mul_f32 v248, v248, v230\n"
  "v_mul_f32 v249, v249, v230\n"
  "v_mul_f32 v250, v250, v230\n"
  "v_mul_f32 v251, v251, v230\n"
  "v_mul_f32 v252, v252, v230\n"
  "v_mul_f32 v253, v253, v230\n"
  "v_mul_f32 v254, v254, v230\n"
  "v_mul_f32 v255, v255, v230\n"
  "v_cvt_pk_bf16_f32 v60, v248, v249\n"
  "v_cvt_pk_bf16_f32 v61, v250, v251\n"
  "v_cvt_pk_bf16_f32 v62, v252, v253\n"
  "v_cvt_pk_bf16_f32 v63, v254, v255\n"
  "buffer_store_dwordx4 v[60:63], v1, s[32:35], s58 offen offset:3072 sc1\n"
  "s_lshl_b32 s58, s13, 2\n"
  "s_sub_u32 s58, 60, s58\n"
  "s_add_u32 s58, s58, s11\n"
  "s_lshl_b32 s59, s12, 6\n"
  "s_add_u32 s58, s58, s59\n"
  "s_lshl_b32 s69, s58, 13\n"
  "v_mov_b32 v238, v231\n"
  "s_nop 1\n"
  "v_permlane32_swap_b32 v231, v238\n"
  "v_add_f32 v231, v231, v238\n"
  "v_rcp_f32 v231, v231\n"
  "v_accvgpr_read_b32 v240, a64\n"
  "v_accvgpr_read_b32 v241, a65\n"
  "v_accvgpr_read_b32 v242, a66\n"
  "v_accvgpr_read_b32 v243, a67\n"
  "v_accvgpr_read_b32 v244, a68\n"
  "v_accvgpr_read_b32 v245, a69\n"
  "v_accvgpr_read_b32 v246, a70\n"
  "v_accvgpr_read_b32 v247, a71\n"
  "v_mul_f32 v240, v240, v231\n"
  "v_mul_f32 v241, v241, v231\n"
  "v_mul_f32 v242, v242, v231\n"
  "v_mul_f32 v243, v243, v231\n"
  "v_mul_f32 v244, v244, v231\n"
  "v_mul_f32 v245, v245, v231\n"
  "v_mul_f32 v246, v246, v231\n"
  "v_mul_f32 v247, v247, v231\n"
  "v_cvt_pk_bf16_f32 v64, v240, v241\n"
  "v_cvt_pk_bf16_f32 v65, v242, v243\n"
  "v_cvt_pk_bf16_f32 v66, v244, v245\n"
  "v_cvt_pk_bf16_f32 v67, v246, v247\n"
  "s_add_u32 s58, s69, 0\n"
  "buffer_store_dwordx4 v[64:67], v1, s[32:35], s58 offen offset:0 sc1\n"
  "v_accvgpr_read_b32 v248, a72\n"
  "v_accvgpr_read_b32 v249, a73\n"
  "v_accvgpr_read_b32 v250, a74\n"
  "v_accvgpr_read_b32 v251, a75\n"
  "v_accvgpr_read_b32 v252, a76\n"
  "v_accvgpr_read_b32 v253, a77\n"
  "v_accvgpr_read_b32 v254, a78\n"
  "v_accvgpr_read_b32 v255, a79\n"
  "v_mul_f32 v248, v248, v231\n"
  "v_mul_f32 v249, v249, v231\n"
  "v_mul_f32 v250, v250, v231\n"
  "v_mul_f32 v251, v251, v231\n"
  "v_mul_f32 v252, v252, v231\n"
  "v_mul_f32 v253, v253, v231\n"
  "v_mul_f32 v254, v254, v231\n"
  "v_mul_f32 v255, v255, v231\n"
  "v_cvt_pk_bf16_f32 v68, v248, v249\n"
  "v_cvt_pk_bf16_f32 v69, v250, v251\n"
  "v_cvt_pk_bf16_f32 v70, v252, v253\n"
  "v_cvt_pk_bf16_f32 v71, v254, v255\n"
  "buffer_store_dwordx4 v[68:71], v1, s[32:35], s58 offen offset:1024 sc1\n"
  "v_accvgpr_read_b32 v240, a80\n"
  "v_accvgpr_read_b32 v241, a81\n"
  "v_accvgpr_read_b32 v242, a82\n"
  "v_accvgpr_read_b32 v243, a83\n"
  "v_accvgpr_read_b32 v244, a84\n"
  "v_accvgpr_read_b32 v245, a85\n"
  "v_accvgpr_read_b32 v246, a86\n"
  "v_accvgpr_read_b32 v247, a87\n"
  "v_mul_f32 v240, v240, v231\n"
  "v_mul_f32 v241, v241, v231\n"
  "v_mul_f32 v242, v242, v231\n"
  "v_mul_f32 v243, v243, v231\n"
  "v_mul_f32 v244, v244, v231\n"
  "v_mul_f32 v245, v245, v231\n"
  "v_mul_f32 v246, v246, v231\n"
  "v_mul_f32 v247, v247, v231\n"
  "v_cvt_pk_bf16_f32 v72, v240, v241\n"
  "v_cvt_pk_bf16_f32 v73, v242, v243\n"
  "v_cvt_pk_bf16_f32 v74, v244, v245\n"
  "v_cvt_pk_bf16_f32 v75, v246, v247\n"
  "buffer_store_dwordx4 v[72:75], v1, s[32:35], s58 offen offset:2048 sc1\n"
  "v_accvgpr_read_b32 v248, a88\n"
  "v_accvgpr_read_b32 v249, a89\n"
  "v_accvgpr_read_b32 v250, a90\n"
  "v_accvgpr_read_b32 v251, a91\n"
  "v_accvgpr_read_b32 v252, a92\n"
  "v_accvgpr_read_b32 v253, a93\n"
  "v_accvgpr_read_b32 v254, a94\n"
  "v_accvgpr_read_b32 v255, a95\n"
  "v_mul_f32 v248, v248, v231\n"
  "v_mul_f32 v249, v249, v231\n"
  "v_mul_f32 v250, v250, v231\n"
  "v_mul_f32 v251, v251, v231\n"
  "v_mul_f32 v252, v252, v231\n"
  "v_mul_f32 v253, v253, v231\n"
  "v_mul_f32 v254, v254, v231\n"
  "v_mul_f32 v255, v255, v231\n"
  "v_cvt_pk_bf16_f32 v76, v248, v249\n"
  "v_cvt_pk_bf16_f32 v77, v250, v251\n"
  "v_cvt_pk_bf16_f32 v78, v252, v253\n"
  "v_cvt_pk_bf16_f32 v79, v254, v255\n"
  "buffer_store_dwordx4 v[76:79], v1, s[32:35], s58 offen offset:3072 sc1\n"
  "v_accvgpr_read_b32 v240, a96\n"
  "v_accvgpr_read_b32 v241, a97\n"
  "v_accvgpr_read_b32 v242, a98\n"
  "v_accvgpr_read_b32 v243, a99\n"
  "v_accvgpr_read_b32 v244, a100\n"
  "v_accvgpr_read_b32 v245, a101\n"
  "v_accvgpr_read_b32 v246, a102\n"
  "v_accvgpr_read_b32 v247, a103\n"
  "v_mul_f32 v240, v240, v231\n"
  "v_mul_f32 v241, v241, v231\n"
  "v_mul_f32 v242, v242, v231\n"
  "v_mul_f32 v243, v243, v231\n"
  "v_mul_f32 v244, v244, v231\n"
  "v_mul_f32 v245, v245, v231\n"
  "v_mul_f32 v246, v246, v231\n"
  "v_mul_f32 v247, v247, v231\n"
  "v_cvt_pk_bf16_f32 v80, v240, v241\n"
  "v_cvt_pk_bf16_f32 v81, v242, v243\n"
  "v_cvt_pk_bf16_f32 v82, v244, v245\n"
  "v_cvt_pk_bf16_f32 v83, v246, v247\n"
  "s_add_u32 s58, s69, 4096\n"
  "buffer_store_dwordx4 v[80:83], v1, s[32:35], s58 offen offset:0 sc1\n"
  "v_accvgpr_read_b32 v248, a104\n"
  "v_accvgpr_read_b32 v249, a105\n"
  "v_accvgpr_read_b32 v250, a106\n"
  "v_accvgpr_read_b32 v251, a107\n"
  "v_accvgpr_read_b32 v252, a108\n"
  "v_accvgpr_read_b32 v253, a109\n"
  "v_accvgpr_read_b32 v254, a110\n"
  "v_accvgpr_read_b32 v255, a111\n"
  "v_mul_f32 v248, v248, v231\n"
  "v_mul_f32 v249, v249, v231\n"
  "v_mul_f32 v250, v250, v231\n"
  "v_mul_f32 v251, v251, v231\n"
  "v_mul_f32 v252, v252, v231\n"
  "v_mul_f32 v253, v253, v231\n"
  "v_mul_f32 v254, v254, v231\n"
  "v_mul_f32 v255, v255, v231\n"
  "v_cvt_pk_bf16_f32 v84, v248, v249\n"
  "v_cvt_pk_bf16_f32 v85, v250, v251\n"
  "v_cvt_pk_bf16_f32 v86, v252, v253\n"
  "v_cvt_pk_bf16_f32 v87, v254, v255\n"
  "buffer_store_dwordx4 v[84:87], v1, s[32:35], s58 offen offset:1024 sc1\n"
  "v_accvgpr_read_b32 v240, a112\n"
  "v_accvgpr_read_b32 v241, a113\n"
  "v_accvgpr_read_b32 v242, a114\n"
  "v_accvgpr_read_b32 v243, a115\n"
  "v_accvgpr_read_b32 v244, a116\n"
  "v_accvgpr_read_b32 v245, a117\n"
  "v_accvgpr_read_b32 v246, a118\n"
  "v_accvgpr_read_b32 v247, a119\n"
  "v_mul_f32 v240, v240, v231\n"
  "v_mul_f32 v241, v241, v231\n"
  "v_mul_f32 v242, v242, v231\n"
  "v_mul_f32 v243, v243, v231\n"
  "v_mul_f32 v244, v244, v231\n"
  "v_mul_f32 v245, v245, v231\n"
  "v_mul_f32 v246, v246, v231\n"
  "v_mul_f32 v247, v247, v231\n"
  "v_cvt_pk_bf16_f32 v88, v240, v241\n"
  "v_cvt_pk_bf16_f32 v89, v242, v243\n"
  "v_cvt_pk_bf16_f32 v90, v244, v245\n"
  "v_cvt_pk_bf16_f32 v91, v246, v247\n"
  "buffer_store_dwordx4 v[88:91], v1, s[32:35], s58 offen offset:2048 sc1\n"
  "v_accvgpr_read_b32 v248, a120\n"
  "v_accvgpr_read_b32 v249, a121\n"
  "v_accvgpr_read_b32 v250, a122\n"
  "v_accvgpr_read_b32 v251, a123\n"
  "v_accvgpr_read_b32 v252, a124\n"
  "v_accvgpr_read_b32 v253, a125\n"
  "v_accvgpr_read_b32 v254, a126\n"
  "v_accvgpr_read_b32 v255, a127\n"
  "v_mul_f32 v248, v248, v231\n"
  "v_mul_f32 v249, v249, v231\n"
  "v_mul_f32 v250, v250, v231\n"
  "v_mul_f32 v251, v251, v231\n"
  "v_mul_f32 v252, v252, v231\n"
  "v_mul_f32 v253, v253, v231\n"
  "v_mul_f32 v254, v254, v231\n"
  "v_mul_f32 v255, v255, v231\n"
  "v_cvt_pk_bf16_f32 v92, v248, v249\n"
  "v_cvt_pk_bf16_f32 v93, v250, v251\n"
  "v_cvt_pk_bf16_f32 v94, v252, v253\n"
  "v_cvt_pk_bf16_f32 v95, v254, v255\n"
  "buffer_store_dwordx4 v[92:95], v1, s[32:35], s58 offen offset:3072 sc1\n"
  "v_sub_u32 v201, 4096, v200\n"
  "v_sub_u32 v202, 46848, v200\n"
  "v_sub_u32 v203, 89600, v200\n"
  "v_lshrrev_b32 v216, 6, v200\n"
  "v_mul_u32_u24 v216, 80, v216\n"
  "v_and_b32 v217, 48, v200\n"
  "v_add_u32 v216, v216, v217\n"
  "v_add_u32 v204, 37632, v216\n"
  "v_add_u32 v205, 80384, v216\n"
  "v_add_u32 v206, 123136, v216\n"
  "v_and_b32 v216, 7, v0\n"
  "v_mul_u32_u24 v216, 4384, v216\n"
  "v_and_b32 v217, 24, v0\n"
  "v_lshlrev_b32 v217, 1, v217\n"
  "v_sub_u32 v216, v216, v217\n"
  "v_lshrrev_b32 v218, 5, v0\n"
  "v_lshlrev_b32 v218, 4, v218\n"
  "v_add_u32 v216, v216, v218\n"
  "v_add_u32 v207, 2112, v216\n"
  "v_and_b32 v216, 31, v0\n"
  "v_mul_u32_u24 v216, 80, v216\n"
  "v_add_u32 v216, v216, v218\n"
  "v_add_u32 v208, 35152, v216\n"
  "v_mov_b32 v212, 0\n"
  "v_mov_b32 v213, 0\n"
  "v_mov_b32 v214, 0\n"
  "v_mov_b32 v215, 0\n"
  "v_mov_b32 v96, 0\n"
  "v_mov_b32 v97, 0\n"
  "v_mov_b32 v98, 0\n"
  "v_mov_b32 v99, 0\n"
  "v_mov_b32 v100, 0\n"
  "v_mov_b32 v101, 0\n"
  "v_mov_b32 v102, 0\n"
  "v_mov_b32 v103, 0\n"
  "v_mov_b32 v104, 0\n"
  "v_mov_b32 v105, 0\n"
  "v_mov_b32 v106, 0\n"
  "v_mov_b32 v107, 0\n"
  "v_mov_b32 v108, 0\n"
  "v_mov_b32 v109, 0\n"
  "v_mov_b32 v110, 0\n"
  "v_mov_b32 v111, 0\n"
  "s_barrier\n"
  "s_waitcnt vmcnt(27)\n"
  "v_alignbit_b32 v228, v163, v163, 16\n"
  "s_waitcnt vmcnt(26)\n"
  "v_alignbit_b32 v232, v167, v167, 16\n"
  "v_alignbit_b32 v229, v162, v162, 16\n"
  "v_alignbit_b32 v233, v166, v166, 16\n"
  "v_alignbit_b32 v230, v161, v161, 16\n"
  "v_alignbit_b32 v234, v165, v165, 16\n"
  "v_alignbit_b32 v231, v160, v160, 16\n"
  "v_alignbit_b32 v235, v164, v164, 16\n"
  "v_alignbit_b32 v216, v232, v231, 16\n"
  "v_alignbit_b32 v217, v233, v232, 16\n"
  "v_alignbit_b32 v218, v234, v233, 16\n"
  "v_alignbit_b32 v219, v235, v234, 16\n"
  "ds_write_b128 v201, v[216:219] offset:0\n"
  "v_mov_b32 v224, v231\n"
  "v_mov_b32 v225, v232\n"
  "v_mov_b32 v226, v233\n"
  "v_mov_b32 v227, v234\n"
  "ds_write_b128 v201, v[224:227] offset:4384\n"
  "v_alignbit_b32 v220, v231, v230, 16\n"
  "v_alignbit_b32 v221, v232, v231, 16\n"
  "v_alignbit_b32 v222, v233, v232, 16\n"
  "v_alignbit_b32 v223, v234, v233, 16\n"
  "ds_write_b128 v201, v[220:223] offset:8768\n"
  "ds_write_b128 v201, v[230:233] offset:13152\n"
  "v_alignbit_b32 v216, v230, v229, 16\n"
  "v_alignbit_b32 v217, v231, v230, 16\n"
  "v_alignbit_b32 v218, v232, v231, 16\n"
  "v_alignbit_b32 v219, v233, v232, 16\n"
  "ds_write_b128 v201, v[216:219] offset:17536\n"
  "v_mov_b32 v224, v229\n"
  "v_mov_b32 v225, v230\n"
  "v_mov_b32 v226, v231\n"
  "v_mov_b32 v227, v232\n"
  "ds_write_b128 v201, v[224:227] offset:21920\n"
  "v_alignbit_b32 v220, v229, v228, 16\n"
  "v_alignbit_b32 v221, v230, v229, 16\n"
  "v_alignbit_b32 v222, v231, v230, 16\n"
  "v_alignbit_b32 v223, v232, v231, 16\n"
  "ds_write_b128 v201, v[220:223] offset:26304\n"
  "ds_write_b128 v201, v[228:231] offset:30688\n"
  "s_waitcnt vmcnt(25)\n"
  "ds_write_b128 v204, v[168:171]\n"
  "s_waitcnt vmcnt(23)\n"
  "v_alignbit_b32 v228, v175, v175, 16\n"
  "s_waitcnt vmcnt(22)\n"
  "v_alignbit_b32 v232, v179, v179, 16\n"
  "v_alignbit_b32 v229, v174, v174, 16\n"
  "v_alignbit_b32 v233, v178, v178, 16\n"
  "v_alignbit_b32 v230, v173, v173, 16\n"
  "v_alignbit_b32 v234, v177, v177, 16\n"
  "v_alignbit_b32 v231, v172, v172, 16\n"
  "v_alignbit_b32 v235, v176, v176, 16\n"
  "v_alignbit_b32 v216, v232, v231, 16\n"
  "v_alignbit_b32 v217, v233, v232, 16\n"
  "v_alignbit_b32 v218, v234, v233, 16\n"
  "v_alignbit_b32 v219, v235, v234, 16\n"
  "ds_write_b128 v202, v[216:219] offset:0\n"
  "v_mov_b32 v224, v231\n"
  "v_mov_b32 v225, v232\n"
  "v_mov_b32 v226, v233\n"
  "v_mov_b32 v227, v234\n"
  "ds_write_b128 v202, v[224:227] offset:4384\n"
  "v_alignbit_b32 v220, v231, v230, 16\n"
  "v_alignbit_b32 v221, v232, v231, 16\n"
  "v_alignbit_b32 v222, v233, v232, 16\n"
  "v_alignbit_b32 v223, v234, v233, 16\n"
  "ds_write_b128 v202, v[220:223] offset:8768\n"
  "ds_write_b128 v202, v[230:233] offset:13152\n"
  "v_alignbit_b32 v216, v230, v229, 16\n"
  "v_alignbit_b32 v217, v231, v230, 16\n"
  "v_alignbit_b32 v218, v232, v231, 16\n"
  "v_alignbit_b32 v219, v233, v232, 16\n"
  "ds_write_b128 v202, v[216:219] offset:17536\n"
  "v_mov_b32 v224, v229\n"
  "v_mov_b32 v225, v230\n"
  "v_mov_b32 v226, v231\n"
  "v_mov_b32 v227, v232\n"
  "ds_write_b128 v202, v[224:227] offset:21920\n"
  "v_alignbit_b32 v220, v229, v228, 16\n"
  "v_alignbit_b32 v221, v230, v229, 16\n"
  "v_alignbit_b32 v222, v231, v230, 16\n"
  "v_alignbit_b32 v223, v232, v231, 16\n"
  "ds_write_b128 v202, v[220:223] offset:26304\n"
  "ds_write_b128 v202, v[228:231] offset:30688\n"
  "s_waitcnt vmcnt(21)\n"
  "ds_write_b128 v205, v[180:183]\n"
  "s_waitcnt vmcnt(19)\n"
  "v_alignbit_b32 v228, v187, v187, 16\n"
  "s_waitcnt vmcnt(18)\n"
  "v_alignbit_b32 v232, v191, v191, 16\n"
  "v_alignbit_b32 v229, v186, v186, 16\n"
  "v_alignbit_b32 v233, v190, v190, 16\n"
  "v_alignbit_b32 v230, v185, v185, 16\n"
  "v_alignbit_b32 v234, v189, v189, 16\n"
  "v_alignbit_b32 v231, v184, v184, 16\n"
  "v_alignbit_b32 v235, v188, v188, 16\n"
  "v_alignbit_b32 v216, v232, v231, 16\n"
  "v_alignbit_b32 v217, v233, v232, 16\n"
  "v_alignbit_b32 v218, v234, v233, 16\n"
  "v_alignbit_b32 v219, v235, v234, 16\n"
  "ds_write_b128 v203, v[216:219] offset:0\n"
  "v_mov_b32 v224, v231\n"
  "v_mov_b32 v225, v232\n"
  "v_mov_b32 v226, v233\n"
  "v_mov_b32 v227, v234\n"
  "ds_write_b128 v203, v[224:227] offset:4384\n"
  "v_alignbit_b32 v220, v231, v230, 16\n"
  "v_alignbit_b32 v221, v232, v231, 16\n"
  "v_alignbit_b32 v222, v233, v232, 16\n"
  "v_alignbit_b32 v223, v234, v233, 16\n"
  "ds_write_b128 v203, v[220:223] offset:8768\n"
  "ds_write_b128 v203, v[230:233] offset:13152\n"
  "v_alignbit_b32 v216, v230, v229, 16\n"
  "v_alignbit_b32 v217, v231, v230, 16\n"
  "v_alignbit_b32 v218, v232, v231, 16\n"
  "v_alignbit_b32 v219, v233, v232, 16\n"
  "ds_write_b128 v203, v[216:219] offset:17536\n"
  "v_mov_b32 v224, v229\n"
  "v_mov_b32 v225, v230\n"
  "v_mov_b32 v226, v231\n"
  "v_mov_b32 v227, v232\n"
  "ds_write_b128 v203, v[224:227] offset:21920\n"
  "v_alignbit_b32 v220, v229, v228, 16\n"
  "v_alignbit_b32 v221, v230, v229, 16\n"
  "v_alignbit_b32 v222, v231, v230, 16\n"
  "v_alignbit_b32 v223, v232, v231, 16\n"
  "ds_write_b128 v203, v[220:223] offset:26304\n"
  "ds_write_b128 v203, v[228:231] offset:30688\n"
  "s_waitcnt vmcnt(17)\n"
  "ds_write_b128 v206, v[192:195]\n"
  "v_lshrrev_b32 v216, 6, v200\n"
  "v_mul_u32_u24 v216, 4384, v216\n"
  "v_and_b32 v217, 48, v200\n"
  "v_add_u32 v216, v216, v217\n"
  "v_cmp_gt_u32 vcc, 512, v200\n"
  "s_mov_b64 exec, vcc\n"
  "v_add_u32 v217, 4112, v216\n"
  "ds_write_b128 v217, v[212:215]\n"
  "v_add_u32 v217, 46864, v216\n"
  "ds_write_b128 v217, v[212:215]\n"
  "v_add_u32 v217, 89616, v216\n"
  "ds_write_b128 v217, v[212:215]\n"
  "s_mov_b64 exec, -1\n"
  "v_cmp_gt_u32 vcc, 2560, v200\n"
  "s_mov_b64 exec, vcc\n"
  "v_add_u32 v217, 35072, v200\n"
  "ds_write_b128 v217, v[212:215]\n"
  "v_add_u32 v217, 77824, v200\n"
  "ds_write_b128 v217, v[212:215]\n"
  "v_add_u32 v217, 120576, v200\n"
  "ds_write_b128 v217, v[212:215]\n"
  "s_mov_b64 exec, -1\n"
  "v_readfirstlane_b32 s44, v196\n"
  "s_lshl_b32 s44, s44, 16\n"
  "v_readfirstlane_b32 s45, v197\n"
  "s_lshl_b32 s45, s45, 16\n"
  "s_waitcnt vmcnt(16)\n"
  "v_readfirstlane_b32 s46, v198\n"
  "s_lshl_b32 s46, s46, 16\n"
  "s_waitcnt lgkmcnt(0)\n"
  "s_barrier\n"
  "s_mov_b32 s41, 0\n"
  "La2_loop:\n"
  "s_and_b32 s58, s41, s38\n"
  "s_cmp_lg_u32 s58, 0\n"
  "s_cselect_b32 s42, s40, s39\n"
  "s_and_b32 s58, s41, s37\n"
  "s_mul_i32 s59, s42, 42752\n"
  "s_lshl_b32 s60, s58, 11\n"
  "s_sub_u32 s60, s59, s60\n"
  "v_add_u32 v209, s60, v207\n"
  "s_sub_u32 s60, s37, s58\n"
  "s_mul_i32 s60, s60, 2560\n"
  "s_add_u32 s60, s60, s59\n"
  "v_add_u32 v210, s60, v208\n"
  "ds_read_b128 v[32:35], v209 offset:1984\n"
  "ds_read_b128 v[40:43], v210 offset:2480\n"
  "ds_read_b128 v[36:39], v209 offset:2016\n"
  "ds_read_b128 v[44:47], v210 offset:2512\n"
  "ds_read_b128 v[48:51], v209 offset:1920\n"
  "ds_read_b128 v[56:59], v210 offset:2400\n"
  "ds_read_b128 v[52:55], v209 offset:1952\n"
  "ds_read_b128 v[60:63], v210 offset:2432\n"
  "ds_read_b128 v[64:67], v209 offset:1856\n"
  "ds_read_b128 v[72:75], v210 offset:2320\n"
  "ds_read_b128 v[68:71], v209 offset:1888\n"
  "ds_read_b128 v[76:79], v210 offset:2352\n"
  "s_waitcnt lgkmcnt(10)\n"
  "v_mfma_f32_32x32x16_bf16 v[96:111], v[32:35], v[40:43], v[96:111]\n"
  "ds_read_b128 v[80:83], v209 offset:1792\n"
  "ds_read_b128 v[88:91], v210 offset:2240\n"
  "s_waitcnt lgkmcnt(10)\n"
  "v_mfma_f32_32x32x16_bf16 v[96:111], v[36:39], v[44:47], v[96:111]\n"
  "ds_read_b128 v[84:87], v209 offset:1824\n"
  "ds_read_b128 v[92:95], v210 offset:2272\n"
  "s_waitcnt lgkmcnt(10)\n"
  "v_mfma_f32_32x32x16_bf16 v[96:111], v[48:51], v[56:59], v[96:111]\n"
  "ds_read_b128 v[32:35], v209 offset:1728\n"
  "ds_read_b128 v[40:43], v210 offset:2160\n"
  "s_waitcnt lgkmcnt(10)\n"
  "v_mfma_f32_32x32x16_bf16 v[96:111], v[52:55], v[60:63], v[96:111]\n"
  "ds_read_b128 v[36:39], v209 offset:1760\n"
  "ds_read_b128 v[44:47], v210 offset:2192\n"
  "s_waitcnt lgkmcnt(10)\n"
  "v_mfma_f32_32x32x16_bf16 v[96:111], v[64:67], v[72:75], v[96:111]\n"
  "ds_read_b128 v[48:51], v209 offset:1664\n"
  "ds_read_b128 v[56:59], v210 offset:2080\n"
  "s_waitcnt lgkmcnt(10)\n"
  "v_mfma_f32_32x32x16_bf16 v[96:111], v[68:71], v[76:79], v[96:111]\n"
  "ds_read_b128 v[52:55], v209 offset:1696\n"
  "ds_read_b128 v[60:63], v210 offset:2112\n"
  "s_waitcnt lgkmcnt(10)\n"
  "v_mfma_f32_32x32x16_bf16 v[96:111], v[80:83], v[88:91], v[96:111]\n"
  "ds_read_b128 v[64:67], v209 offset:1600\n"
  "ds_read_b128 v[72:75], v210 offset:2000\n"
  "s_waitcnt lgkmcnt(10)\n"
  "v_mfma_f32_32x32x16_bf16 v[96:111], v[84:87], v[92:95], v[96:111]\n"
  "ds_read_b128 v[68:71], v209 offset:1632\n"
  "ds_read_b128 v[76:79], v210 offset:2032\n"
  "s_waitcnt lgkmcnt(10)\n"
  "v_mfma_f32_32x32x16_bf16 v[96:111], v[32:35], v[40:43], v[96:111]\n"
  "ds_read_b128 v[80:83], v209 offset:1536\n"
  "ds_read_b128 v[88:91], v210 offset:1920\n"
  "s_waitcnt lgkmcnt(10)\n"
  "v_mfma_f32_32x32x16_bf16 v[96:111], v[36:39], v[44:47], v[96:111]\n"
  "ds_read_b128 v[84:87], v209 offset:1568\n"
  "ds_read_b128 v[92:95], v210 offset:1952\n"
  "s_waitcnt lgkmcnt(10)\n"
  "v_mfma_f32_32x32x16_bf16 v[96:111], v[48:51], v[56:59], v[96:111]\n"
  "ds_read_b128 v[32:35], v209 offset:1472\n"
  "ds_read_b128 v[40:43], v210 offset:1840\n"
  "s_waitcnt lgkmcnt(10)\n"
  "v_mfma_f32_32x32x16_bf16 v[96:111], v[52:55], v[60:63], v[96:111]\n"
  "ds_read_b128 v[36:39], v209 offset:1504\n"
  "ds_read_b128 v[44:47], v210 offset:1872\n"
  "s_waitcnt lgkmcnt(10)\n"
  "v_mfma_f32_32x32x16_bf16 v[96:111], v[64:67], v[72:75], v[96:111]\n"
  "ds_read_b128 v[48:51], v209 offset:1408\n"
  "ds_read_b128 v[56:59], v210 offset:1760\n"
  "s_waitcnt lgkmcnt(10)\n"
  "v_mfma_f32_32x32x16_bf16 v[96:111], v[68:71], v[76:79], v[96:111]\n"
  "ds_read_b128 v[52:55], v209 offset:1440\n"
  "ds_read_b128 v[60:63], v210 offset:1792\n"
  "s_waitcnt lgkmcnt(10)\n"
  "v_mfma_f32_32x32x16_bf16 v[96:111], v[80:83], v[88:91], v[96:111]\n"
  "ds_read_b128 v[64:67], v209 offset:1344\n"
  "ds_read_b128 v[72:75], v210 offset:1680\n"
  "s_waitcnt lgkmcnt(10)\n"
  "v_mfma_f32_32x32x16_bf16 v[96:111], v[84:87], v[92:95], v[96:111]\n"
  "ds_read_b128 v[68:71], v209 offset:1376\n"
  "ds_read_b128 v[76:79], v210 offset:1712\n"
  "s_waitcnt lgkmcnt(10)\n"
  "v_mfma_f32_32x32x16_bf16 v[96:111], v[32:35], v[40:43], v[96:111]\n"
  "ds_read_b128 v[80:83], v209 offset:1280\n"
  "ds_read_b128 v[88:91], v210 offset:1600\n"
  "s_waitcnt lgkmcnt(10)\n"
  "v_mfma_f32_32x32x16_bf16 v[96:111], v[36:39], v[44:47], v[96:111]\n"
  "ds_read_b128 v[84:87], v209 offset:1312\n"
  "ds_read_b128 v[92:95], v210 offset:1632\n"
  "s_waitcnt lgkmcnt(10)\n"
  "v_mfma_f32_32x32x16_bf16 v[96:111], v[48:51], v[56:59], v[96:111]\n"
  "ds_read_b128 v[32:35], v209 offset:1216\n"
  "ds_read_b128 v[40:43], v210 offset:1520\n"
  "s_waitcnt lgkmcnt(10)\n"
  "v_mfma_f32_32x32x16_bf16 v[96:111], v[52:55], v[60:63], v[96:111]\n"
  "ds_read_b128 v[36:39], v209 offset:1248\n"
  "ds_read_b128 v[44:47], v210 offset:1552\n"
  "s_waitcnt lgkmcnt(10)\n"
  "v_mfma_f32_32x32x16_bf16 v[96:111], v[64:67], v[72:75], v[96:111]\n"
  "ds_read_b128 v[48:51], v209 offset:1152\n"
  "ds_read_b128 v[56:59], v210 offset:1440\n"
  "s_waitcnt lgkmcnt(10)\n"
  "v_mfma_f32_32x32x16_bf16 v[96:111], v[68:71], v[76:79], v[96:111]\n"
  "ds_read_b128 v[52:55], v209 offset:1184\n"
  "ds_read_b128 v[60:63], v210 offset:1472\n"
  "s_waitcnt lgkmcnt(10)\n"
  "v_mfma_f32_32x32x16_bf16 v[96:111], v[80:83], v[88:91], v[96:111]\n"
  "ds_read_b128 v[64:67], v209 offset:1088\n"
  "ds_read_b128 v[72:75], v210 offset:1360\n"
  "s_waitcnt lgkmcnt(10)\n"
  "v_mfma_f32_32x32x16_bf16 v[96:111], v[84:87], v[92:95], v[96:111]\n"
  "ds_read_b128 v[68:71], v209 offset:1120\n"
  "ds_read_b128 v[76:79], v210 offset:1392\n"
  "s_waitcnt lgkmcnt(10)\n"
  "v_mfma_f32_32x32x16_bf16 v[96:111], v[32:35], v[40:43], v[96:111]\n"
  "ds_read_b128 v[80:83], v209 offset:1024\n"
  "ds_read_b128 v[88:91], v210 offset:1280\n"
  "s_waitcnt lgkmcnt(10)\n"
  "v_mfma_f32_32x32x16_bf16 v[96:111], v[36:39], v[44:47], v[96:111]\n"
  "ds_read_b128 v[84:87], v209 offset:1056\n"
  "ds_read_b128 v[92:95], v210 offset:1312\n"
  "s_waitcnt lgkmcnt(10)\n"
  "v_mfma_f32_32x32x16_bf16 v[96:111], v[48:51], v[56:59], v[96:111]\n"
  "ds_read_b128 v[32:35], v209 offset:960\n"
  "ds_read_b128 v[40:43], v210 offset:1200\n"
  "s_waitcnt lgkmcnt(10)\n"
  "v_mfma_f32_32x32x16_bf16 v[96:111], v[52:55], v[60:63], v[96:111]\n"
  "ds_read_b128 v[36:39], v209 offset:992\n"
  "ds_read_b128 v[44:47], v210 offset:1232\n"
  "s_waitcnt lgkmcnt(10)\n"
  "v_mfma_f32_32x32x16_bf16 v[96:111], v[64:67], v[72:75], v[96:111]\n"
  "ds_read_b128 v[48:51], v209 offset:896\n"
  "ds_read_b128 v[56:59], v210 offset:1120\n"
  "s_waitcnt lgkmcnt(10)\n"
  "v_mfma_f32_32x32x16_bf16 v[96:111], v[68:71], v[76:79], v[96:111]\n"
  "ds_read_b128 v[52:55], v209 offset:928\n"
  "ds_read_b128 v[60:63], v210 offset:1152\n"
  "s_waitcnt lgkmcnt(10)\n"
  "v_mfma_f32_32x32x16_bf16 v[96:111], v[80:83], v[88:91], v[96:111]\n"
  "ds_read_b128 v[64:67], v209 offset:832\n"
  "ds_read_b128 v[72:75], v210 offset:1040\n"
  "s_waitcnt lgkmcnt(10)\n"
  "v_mfma_f32_32x32x16_bf16 v[96:111], v[84:87], v[92:95], v[96:111]\n"
  "ds_read_b128 v[68:71], v209 offset:864\n"
  "ds_read_b128 v[76:79], v210 offset:1072\n"
  "s_waitcnt lgkmcnt(10)\n"
  "v_mfma_f32_32x32x16_bf16 v[96:111], v[32:35], v[40:43], v[96:111]\n"
  "ds_read_b128 v[80:83], v209 offset:768\n"
  "ds_read_b128 v[88:91], v210 offset:960\n"
  "s_waitcnt lgkmcnt(10)\n"
  "v_mfma_f32_32x32x16_bf16 v[96:111], v[36:39], v[44:47], v[96:111]\n"
  "ds_read_b128 v[84:87], v209 offset:800\n"
  "ds_read_b128 v[92:95], v210 offset:992\n"
  "s_waitcnt lgkmcnt(10)\n"
  "v_mfma_f32_32x32x16_bf16 v[96:111], v[48:51], v[56:59], v[96:111]\n"
  "ds_read_b128 v[32:35], v209 offset:704\n"
  "ds_read_b128 v[40:43], v210 offset:880\n"
  "s_waitcnt lgkmcnt(10)\n"
  "v_mfma_f32_32x32x16_bf16 v[96:111], v[52:55], v[60:63], v[96:111]\n"
  "ds_read_b128 v[36:39], v209 offset:736\n"
  "ds_read_b128 v[44:47], v210 offset:912\n"
  "s_waitcnt lgkmcnt(10)\n"
  "v_mfma_f32_32x32x16_bf16 v[96:111], v[64:67], v[72:75], v[96:111]\n"
  "ds_read_b128 v[48:51], v209 offset:640\n"
  "ds_read_b128 v[56:59], v210 offset:800\n"
  "s_waitcnt lgkmcnt(10)\n"
  "v_mfma_f32_32x32x16_bf16 v[96:111], v[68:71], v[76:79], v[96:111]\n"
  "ds_read_b128 v[52:55], v209 offset:672\n"
  "ds_read_b128 v[60:63], v210 offset:832\n"
  "s_waitcnt lgkmcnt(10)\n"
  "v_mfma_f32_32x32x16_bf16 v[96:111], v[80:83], v[88:91], v[96:111]\n"
  "ds_read_b128 v[64:67], v209 offset:576\n"
  "ds_read_b128 v[72:75], v210 offset:720\n"
  "s_waitcnt lgkmcnt(10)\n"
  "v_mfma_f32_32x32x16_bf16 v[96:111], v[84:87], v[92:95], v[96:111]\n"
  "ds_read_b128 v[68:71], v209 offset:608\n"
  "ds_read_b128 v[76:79], v210 offset:752\n"
  "s_waitcnt lgkmcnt(10)\n"
  "v_mfma_f32_32x32x16_bf16 v[96:111], v[32:35], v[40:43], v[96:111]\n"
  "ds_read_b128 v[80:83], v209 offset:512\n"
  "ds_read_b128 v[88:91], v210 offset:640\n"
  "s_waitcnt lgkmcnt(10)\n"
  "v_mfma_f32_32x32x16_bf16 v[96:111], v[36:39], v[44:47], v[96:111]\n"
  "ds_read_b128 v[84:87], v209 offset:544\n"
  "ds_read_b128 v[92:95], v210 offset:672\n"
  "s_waitcnt lgkmcnt(10)\n"
  "v_mfma_f32_32x32x16_bf16 v[96:111], v[48:51], v[56:59], v[96:111]\n"
  "ds_read_b128 v[32:35], v209 offset:448\n"
  "ds_read_b128 v[40:43], v210 offset:560\n"
  "s_waitcnt lgkmcnt(10)\n"
  "v_mfma_f32_32x32x16_bf16 v[96:111], v[52:55], v[60:63], v[96:111]\n"
  "ds_read_b128 v[36:39], v209 offset:480\n"
  "ds_read_b128 v[44:47], v210 offset:592\n"
  "s_waitcnt lgkmcnt(10)\n"
  "v_mfma_f32_32x32x16_bf16 v[96:111], v[64:67], v[72:75], v[96:111]\n"
  "ds_read_b128 v[48:51], v209 offset:384\n"
  "ds_read_b128 v[56:59], v210 offset:480\n"
  "s_waitcnt lgkmcnt(10)\n"
  "v_mfma_f32_32x32x16_bf16 v[96:111], v[68:71], v[76:79], v[96:111]\n"
  "ds_read_b128 v[52:55], v209 offset:416\n"
  "ds_read_b128 v[60:63], v210 offset:512\n"
  "s_waitcnt lgkmcnt(10)\n"
  "v_mfma_f32_32x32x16_bf16 v[96:111], v[80:83], v[88:91], v[96:111]\n"
  "ds_read_b128 v[64:67], v209 offset:320\n"
  "ds_read_b128 v[72:75], v210 offset:400\n"
  "s_waitcnt lgkmcnt(10)\n"
  "v_mfma_f32_32x32x16_bf16 v[96:111], v[84:87], v[92:95], v[96:111]\n"
  "ds_read_b128 v[68:71], v209 offset:352\n"
  "ds_read_b128 v[76:79], v210 offset:432\n"
  "s_waitcnt lgkmcnt(10)\n"
  "v_mfma_f32_32x32x16_bf16 v[96:111], v[32:35], v[40:43], v[96:111]\n"
  "ds_read_b128 v[80:83], v209 offset:256\n"
  "ds_read_b128 v[88:91], v210 offset:320\n"
  "s_waitcnt lgkmcnt(10)\n"
  "v_mfma_f32_32x32x16_bf16 v[96:111], v[36:39], v[44:47], v[96:111]\n"
  "ds_read_b128 v[84:87], v209 offset:288\n"
  "ds_read_b128 v[92:95], v210 offset:352\n"
  "s_waitcnt lgkmcnt(10)\n"
  "v_mfma_f32_32x32x16_bf16 v[96:111], v[48:51], v[56:59], v[96:111]\n"
  "ds_read_b128 v[32:35], v209 offset:192\n"
  "ds_read_b128 v[40:43], v210 offset:240\n"
  "s_waitcnt lgkmcnt(10)\n"
  "v_mfma_f32_32x32x16_bf16 v[96:111], v[52:55], v[60:63], v[96:111]\n"
  "ds_read_b128 v[36:39], v209 offset:224\n"
  "ds_read_b128 v[44:47], v210 offset:272\n"
  "s_waitcnt lgkmcnt(10)\n"
  "v_mfma_f32_32x32x16_bf16 v[96:111], v[64:67], v[72:75], v[96:111]\n"
  "ds_read_b128 v[48:51], v209 offset:128\n"
  "ds_read_b128 v[56:59], v210 offset:160\n"
  "s_waitcnt lgkmcnt(10)\n"
  "v_mfma_f32_32x32x16_bf16 v[96:111], v[68:71], v[76:79], v[96:111]\n"
  "ds_read_b128 v[52:55], v209 offset:160\n"
  "ds_read_b128 v[60:63], v210 offset:192\n"
  "s_waitcnt lgkmcnt(10)\n"
  "v_mfma_f32_32x32x16_bf16 v[96:111], v[80:83], v[88:91], v[96:111]\n"
  "ds_read_b128 v[64:67], v209 offset:64\n"
  "ds_read_b128 v[72:75], v210 offset:80\n"
  "s_waitcnt lgkmcnt(10)\n"
  "v_mfma_f32_32x32x16_bf16 v[96:111], v[84:87], v[92:95], v[96:111]\n"
  "ds_read_b128 v[68:71], v209 offset:96\n"
  "ds_read_b128 v[76:79], v210 offset:112\n"
  "s_waitcnt lgkmcnt(10)\n"
  "v_mfma_f32_32x32x16_bf16 v[96:111], v[32:35], v[40:43], v[96:111]\n"
  "ds_read_b128 v[80:83], v209 offset:0\n"
  "ds_read_b128 v[88:91], v210 offset:0\n"
  "s_waitcnt lgkmcnt(10)\n"
  "v_mfma_f32_32x32x16_bf16 v[96:111], v[36:39], v[44:47], v[96:111]\n"
  "ds_read_b128 v[84:87], v209 offset:32\n"
  "ds_read_b128 v[92:95], v210 offset:32\n"
  "s_waitcnt lgkmcnt(10)\n"
  "v_mfma_f32_32x32x16_bf16 v[96:111], v[48:51], v[56:59], v[96:111]\n"
  "s_waitcnt lgkmcnt(8)\n"
  "v_mfma_f32_32x32x16_bf16 v[96:111], v[52:55], v[60:63], v[96:111]\n"
  "s_waitcnt lgkmcnt(6)\n"
  "v_mfma_f32_32x32x16_bf16 v[96:111], v[64:67], v[72:75], v[96:111]\n"
  "s_waitcnt lgkmcnt(4)\n"
  "v_mfma_f32_32x32x16_bf16 v[96:111], v[68:71], v[76:79], v[96:111]\n"
  "s_waitcnt lgkmcnt(2)\n"
  "v_mfma_f32_32x32x16_bf16 v[96:111], v[80:83], v[88:91], v[96:111]\n"
  "s_waitcnt lgkmcnt(0)\n"
  "v_mfma_f32_32x32x16_bf16 v[96:111], v[84:87], v[92:95], v[96:111]\n"
  "s_cmp_lg_u32 s41, 0\n"
  "s_cbranch_scc1 La2_nomid\n"
  "s_waitcnt vmcnt(0)\n"
  "s_barrier\n"
  "s_lshl_b32 s58, s13, 5\n"
  "s_add_u32 s58, s58, s12\n"
  "s_lshl_b32 s58, s58, 2\n"
  "s_add_u32 s58, s58, 12288\n"
  "v_mov_b32 v216, s71\n"
  "s_mov_b64 exec, 1\n"
  "buffer_store_dword v216, off, s[92:95], s58 sc1\n"
  "s_mov_b64 exec, -1\n"
  "La2_nomid:\n"
  "s_xor_b32 s58, s37, 1\n"
  "s_or_b32 s58, s58, s41\n"
  "s_cmp_eq_u32 s58, 0\n"
  "s_cbranch_scc1 La2_noflush\n"
  "s_lshl_b32 s58, s37, 5\n"
  "s_add_u32 s58, s58, 32\n"
  "s_mul_i32 s58, s58, 80\n"
  "s_mul_i32 s59, s42, 42752\n"
  "s_add_u32 s58, s58, s59\n"
  "s_add_u32 s58, s58, 35072\n"
  "v_and_b32 v216, 31, v0\n"
  "v_mul_u32_u24 v217, 80, v216\n"
  "v_lshrrev_b32 v218, 5, v0\n"
  "v_lshlrev_b32 v218, 3, v218\n"
  "v_add3_u32 v217, v217, v218, s58\n"
  "ds_read_b64 v[32:33], v217 offset:0\n"
  "ds_read_b64 v[34:35], v217 offset:16\n"
  "ds_read_b64 v[36:37], v217 offset:32\n"
  "ds_read_b64 v[38:39], v217 offset:48\n"
  "v_lshlrev_b32 v216, 6, v216\n"
  "v_add_u32 v216, v216, v218\n"
  "s_cmp_eq_u32 s42, 1\n"
  "s_cselect_b32 s60, s45, s44\n"
  "s_cmp_eq_u32 s42, 2\n"
  "s_cselect_b32 s60, s46, s60\n"
  "s_add_u32 s61, s36, s42\n"
  "s_lshl_b32 s61, s61, 12\n"
  "s_lshl_b32 s62, s37, 11\n"
  "s_add_u32 s61, s61, s62\n"
  "v_cvt_pk_bf16_f32 v220, v96, v97\n"
  "s_waitcnt lgkmcnt(3)\n"
  "v_lshlrev_b32 v222, 16, v32\n"
  "v_and_b32 v223, 0xffff0000, v32\n"
  "v_mul_f32 v222, s60, v222\n"
  "v_mul_f32 v223, s60, v223\n"
  "v_cvt_pk_bf16_f32 v221, v222, v223\n"
  "v_lshlrev_b32 v222, 16, v220\n"
  "v_and_b32 v223, 0xffff0000, v220\n"
  "v_lshlrev_b32 v224, 16, v221\n"
  "v_and_b32 v225, 0xffff0000, v221\n"
  "v_add_f32 v222, v222, v224\n"
  "v_add_f32 v223, v223, v225\n"
  "v_cvt_pk_bf16_f32 v48, v222, v223\n"
  "v_cvt_pk_bf16_f32 v220, v98, v99\n"
  "v_lshlrev_b32 v222, 16, v33\n"
  "v_and_b32 v223, 0xffff0000, v33\n"
  "v_mul_f32 v222, s60, v222\n"
  "v_mul_f32 v223, s60, v223\n"
  "v_cvt_pk_bf16_f32 v221, v222, v223\n"
  "v_lshlrev_b32 v222, 16, v220\n"
  "v_and_b32 v223, 0xffff0000, v220\n"
  "v_lshlrev_b32 v224, 16, v221\n"
  "v_and_b32 v225, 0xffff0000, v221\n"
  "v_add_f32 v222, v222, v224\n"
  "v_add_f32 v223, v223, v225\n"
  "v_cvt_pk_bf16_f32 v49, v222, v223\n"
  "buffer_store_dwordx2 v[48:49], v216, s[24:27], s61 offen offset:0 sc1\n"
  "v_cvt_pk_bf16_f32 v220, v100, v101\n"
  "s_waitcnt lgkmcnt(2)\n"
  "v_lshlrev_b32 v222, 16, v34\n"
  "v_and_b32 v223, 0xffff0000, v34\n"
  "v_mul_f32 v222, s60, v222\n"
  "v_mul_f32 v223, s60, v223\n"
  "v_cvt_pk_bf16_f32 v221, v222, v223\n"
  "v_lshlrev_b32 v222, 16, v220\n"
  "v_and_b32 v223, 0xffff0000, v220\n"
  "v_lshlrev_b32 v224, 16, v221\n"
  "v_and_b32 v225, 0xffff0000, v221\n"
  "v_add_f32 v222, v222, v224\n"
  "v_add_f32 v223, v223, v225\n"
  "v_cvt_pk_bf16_f32 v50, v222, v223\n"
  "v_cvt_pk_bf16_f32 v220, v102, v103\n"
  "v_lshlrev_b32 v222, 16, v35\n"
  "v_and_b32 v223, 0xffff0000, v35\n"
  "v_mul_f32 v222, s60, v222\n"
  "v_mul_f32 v223, s60, v223\n"
  "v_cvt_pk_bf16_f32 v221, v222, v223\n"
  "v_lshlrev_b32 v222, 16, v220\n"
  "v_and_b32 v223, 0xffff0000, v220\n"
  "v_lshlrev_b32 v224, 16, v221\n"
  "v_and_b32 v225, 0xffff0000, v221\n"
  "v_add_f32 v222, v222, v224\n"
  "v_add_f32 v223, v223, v225\n"
  "v_cvt_pk_bf16_f32 v51, v222, v223\n"
  "buffer_store_dwordx2 v[50:51], v216, s[24:27], s61 offen offset:16 sc1\n"
  "v_cvt_pk_bf16_f32 v220, v104, v105\n"
  "s_waitcnt lgkmcnt(1)\n"
  "v_lshlrev_b32 v222, 16, v36\n"
  "v_and_b32 v223, 0xffff0000, v36\n"
  "v_mul_f32 v222, s60, v222\n"
  "v_mul_f32 v223, s60, v223\n"
  "v_cvt_pk_bf16_f32 v221, v222, v223\n"
  "v_lshlrev_b32 v222, 16, v220\n"
  "v_and_b32 v223, 0xffff0000, v220\n"
  "v_lshlrev_b32 v224, 16, v221\n"
  "v_and_b32 v225, 0xffff0000, v221\n"
  "v_add_f32 v222, v222, v224\n"
  "v_add_f32 v223, v223, v225\n"
  "v_cvt_pk_bf16_f32 v52, v222, v223\n"
  "v_cvt_pk_bf16_f32 v220, v106, v107\n"
  "v_lshlrev_b32 v222, 16, v37\n"
  "v_and_b32 v223, 0xffff0000, v37\n"
  "v_mul_f32 v222, s60, v222\n"
  "v_mul_f32 v223, s60, v223\n"
  "v_cvt_pk_bf16_f32 v221, v222, v223\n"
  "v_lshlrev_b32 v222, 16, v220\n"
  "v_and_b32 v223, 0xffff0000, v220\n"
  "v_lshlrev_b32 v224, 16, v221\n"
  "v_and_b32 v225, 0xffff0000, v221\n"
  "v_add_f32 v222, v222, v224\n"
  "v_add_f32 v223, v223, v225\n"
  "v_cvt_pk_bf16_f32 v53, v222, v223\n"
  "buffer_store_dwordx2 v[52:53], v216, s[24:27], s61 offen offset:32 sc1\n"
  "v_cvt_pk_bf16_f32 v220, v108, v109\n"
  "s_waitcnt lgkmcnt(0)\n"
  "v_lshlrev_b32 v222, 16, v38\n"
  "v_and_b32 v223, 0xffff0000, v38\n"
  "v_mul_f32 v222, s60, v222\n"
  "v_mul_f32 v223, s60, v223\n"
  "v_cvt_pk_bf16_f32 v221, v222, v223\n"
  "v_lshlrev_b32 v222, 16, v220\n"
  "v_and_b32 v223, 0xffff0000, v220\n"
  "v_lshlrev_b32 v224, 16, v221\n"
  "v_and_b32 v225, 0xffff0000, v221\n"
  "v_add_f32 v222, v222, v224\n"
  "v_add_f32 v223, v223, v225\n"
  "v_cvt_pk_bf16_f32 v54, v222, v223\n"
  "v_cvt_pk_bf16_f32 v220, v110, v111\n"
  "v_lshlrev_b32 v222, 16, v39\n"
  "v_and_b32 v223, 0xffff0000, v39\n"
  "v_mul_f32 v222, s60, v222\n"
  "v_mul_f32 v223, s60, v223\n"
  "v_cvt_pk_bf16_f32 v221, v222, v223\n"
  "v_lshlrev_b32 v222, 16, v220\n"
  "v_and_b32 v223, 0xffff0000, v220\n"
  "v_lshlrev_b32 v224, 16, v221\n"
  "v_and_b32 v225, 0xffff0000, v221\n"
  "v_add_f32 v222, v222, v224\n"
  "v_add_f32 v223, v223, v225\n"
  "v_cvt_pk_bf16_f32 v55, v222, v223\n"
  "buffer_store_dwordx2 v[54:55], v216, s[24:27], s61 offen offset:48 sc1\n"
  "v_mov_b32 v96, 0\n"
  "v_mov_b32 v97, 0\n"
  "v_mov_b32 v98, 0\n"
  "v_mov_b32 v99, 0\n"
  "v_mov_b32 v100, 0\n"
  "v_mov_b32 v101, 0\n"
  "v_mov_b32 v102, 0\n"
  "v_mov_b32 v103, 0\n"
  "v_mov_b32 v104, 0\n"
  "v_mov_b32 v105, 0\n"
  "v_mov_b32 v106, 0\n"
  "v_mov_b32 v107, 0\n"
  "v_mov_b32 v108, 0\n"
  "v_mov_b32 v109, 0\n"
  "v_mov_b32 v110, 0\n"
  "v_mov_b32 v111, 0\n"
  "La2_noflush:\n"
  "s_or_b32 s58, s37, s38\n"
  "s_xor_b32 s59, s41, 1\n"
  "s_and_b32 s58, s58, s59\n"
  "s_add_u32 s41, s41, 1\n"
  "s_cmp_lg_u32 s58, 0\n"
  "s_cbranch_scc1 La2_loop\n"
  "s_branch Lend\n"
  "Lfbv_Xd:\n"
  "s_nop 15\n"
  "s_nop 15\n"
  "v_max_f32 v238, v32, v33\n"
  "v_max3_f32 v238, v238, v34, v35\n"
  "v_max3_f32 v238, v238, v36, v37\n"
  "v_max3_f32 v238, v238, v38, v39\n"
  "v_max3_f32 v238, v238, v40, v41\n"
  "v_max3_f32 v238, v238, v42, v43\n"
  "v_max3_f32 v238, v238, v44, v45\n"
  "v_max3_f32 v238, v238, v46, v47\n"
  "v_max3_f32 v238, v238, v48, v49\n"
  "v_max3_f32 v238, v238, v50, v51\n"
  "v_max3_f32 v238, v238, v52, v53\n"
  "v_max3_f32 v238, v238, v54, v55\n"
  "v_max3_f32 v238, v238, v56, v57\n"
  "v_max3_f32 v238, v238, v58, v59\n"
  "v_max3_f32 v238, v238, v60, v61\n"
  "v_max3_f32 v238, v238, v62, v63\n"
  "v_mov_b32 v239, v238\n"
  "s_nop 1\n"
  "v_permlane32_swap_b32 v238, v239\n"
  "v_max3_f32 v238, v238, v239, 0\n"
  "v_sub_f32 v239, 0, v238\n"
  "v_exp_f32 v239, v239\n"
  "v_add_f32 v232, v232, v238\n"
  "v_mul_f32 v230, v230, v239\n"
  "v_accvgpr_read_b32 v240, a0\n"
  "v_mul_f32 v240, v240, v239\n"
  "v_accvgpr_write_b32 a0, v240\n"
  "v_accvgpr_read_b32 v240, a1\n"
  "v_mul_f32 v240, v240, v239\n"
  "v_accvgpr_write_b32 a1, v240\n"
  "v_accvgpr_read_b32 v240, a2\n"
  "v_mul_f32 v240, v240, v239\n"
  "v_accvgpr_write_b32 a2, v240\n"
  "v_accvgpr_read_b32 v240, a3\n"
  "v_mul_f32 v240, v240, v239\n"
  "v_accvgpr_write_b32 a3, v240\n"
  "v_accvgpr_read_b32 v240, a4\n"
  "v_mul_f32 v240, v240, v239\n"
  "v_accvgpr_write_b32 a4, v240\n"
  "v_accvgpr_read_b32 v240, a5\n"
  "v_mul_f32 v240, v240, v239\n"
  "v_accvgpr_write_b32 a5, v240\n"
  "v_accvgpr_read_b32 v240, a6\n"
  "v_mul_f32 v240, v240, v239\n"
  "v_accvgpr_write_b32 a6, v240\n"
  "v_accvgpr_read_b32 v240, a7\n"
  "v_mul_f32 v240, v240, v239\n"
  "v_accvgpr_write_b32 a7, v240\n"
  "v_accvgpr_read_b32 v240, a8\n"
  "v_mul_f32 v240, v240, v239\n"
  "v_accvgpr_write_b32 a8, v240\n"
  "v_accvgpr_read_b32 v240, a9\n"
  "v_mul_f32 v240, v240, v239\n"
  "v_accvgpr_write_b32 a9, v240\n"
  "v_accvgpr_read_b32 v240, a10\n"
  "v_mul_f32 v240, v240, v239\n"
  "v_accvgpr_write_b32 a10, v240\n"
  "v_accvgpr_read_b32 v240, a11\n"
  "v_mul_f32 v240, v240, v239\n"
  "v_accvgpr_write_b32 a11, v240\n"
  "v_accvgpr_read_b32 v240, a12\n"
  "v_mul_f32 v240, v240, v239\n"
  "v_accvgpr_write_b32 a12, v240\n"
  "v_accvgpr_read_b32 v240, a13\n"
  "v_mul_f32 v240, v240, v239\n"
  "v_accvgpr_write_b32 a13, v240\n"
  "v_accvgpr_read_b32 v240, a14\n"
  "v_mul_f32 v240, v240, v239\n"
  "v_accvgpr_write_b32 a14, v240\n"
  "v_accvgpr_read_b32 v240, a15\n"
  "v_mul_f32 v240, v240, v239\n"
  "v_accvgpr_write_b32 a15, v240\n"
  "v_accvgpr_read_b32 v240, a16\n"
  "v_mul_f32 v240, v240, v239\n"
  "v_accvgpr_write_b32 a16, v240\n"
  "v_accvgpr_read_b32 v240, a17\n"
  "v_mul_f32 v240, v240, v239\n"
  "v_accvgpr_write_b32 a17, v240\n"
  "v_accvgpr_read_b32 v240, a18\n"
  "v_mul_f32 v240, v240, v239\n"
  "v_accvgpr_write_b32 a18, v240\n"
  "v_accvgpr_read_b32 v240, a19\n"
  "v_mul_f32 v240, v240, v239\n"
  "v_accvgpr_write_b32 a19, v240\n"
  "v_accvgpr_read_b32 v240, a20\n"
  "v_mul_f32 v240, v240, v239\n"
  "v_accvgpr_write_b32 a20, v240\n"
  "v_accvgpr_read_b32 v240, a21\n"
  "v_mul_f32 v240, v240, v239\n"
  "v_accvgpr_write_b32 a21, v240\n"
  "v_accvgpr_read_b32 v240, a22\n"
  "v_mul_f32 v240, v240, v239\n"
  "v_accvgpr_write_b32 a22, v240\n"
  "v_accvgpr_read_b32 v240, a23\n"
  "v_mul_f32 v240, v240, v239\n"
  "v_accvgpr_write_b32 a23, v240\n"
  "v_accvgpr_read_b32 v240, a24\n"
  "v_mul_f32 v240, v240, v239\n"
  "v_accvgpr_write_b32 a24, v240\n"
  "v_accvgpr_read_b32 v240, a25\n"
  "v_mul_f32 v240, v240, v239\n"
  "v_accvgpr_write_b32 a25, v240\n"
  "v_accvgpr_read_b32 v240, a26\n"
  "v_mul_f32 v240, v240, v239\n"
  "v_accvgpr_write_b32 a26, v240\n"
  "v_accvgpr_read_b32 v240, a27\n"
  "v_mul_f32 v240, v240, v239\n"
  "v_accvgpr_write_b32 a27, v240\n"
  "v_accvgpr_read_b32 v240, a28\n"
  "v_mul_f32 v240, v240, v239\n"
  "v_accvgpr_write_b32 a28, v240\n"
  "v_accvgpr_read_b32 v240, a29\n"
  "v_mul_f32 v240, v240, v239\n"
  "v_accvgpr_write_b32 a29, v240\n"
  "v_accvgpr_read_b32 v240, a30\n"
  "v_mul_f32 v240, v240, v239\n"
  "v_accvgpr_write_b32 a30, v240\n"
  "v_accvgpr_read_b32 v240, a31\n"
  "v_mul_f32 v240, v240, v239\n"
  "v_accvgpr_write_b32 a31, v240\n"
  "v_accvgpr_read_b32 v240, a32\n"
  "v_mul_f32 v240, v240, v239\n"
  "v_accvgpr_write_b32 a32, v240\n"
  "v_accvgpr_read_b32 v240, a33\n"
  "v_mul_f32 v240, v240, v239\n"
  "v_accvgpr_write_b32 a33, v240\n"
  "v_accvgpr_read_b32 v240, a34\n"
  "v_mul_f32 v240, v240, v239\n"
  "v_accvgpr_write_b32 a34, v240\n"
  "v_accvgpr_read_b32 v240, a35\n"
  "v_mul_f32 v240, v240, v239\n"
  "v_accvgpr_write_b32 a35, v240\n"
  "v_accvgpr_read_b32 v240, a36\n"
  "v_mul_f32 v240, v240, v239\n"
  "v_accvgpr_write_b32 a36, v240\n"
  "v_accvgpr_read_b32 v240, a37\n"
  "v_mul_f32 v240, v240, v239\n"
  "v_accvgpr_write_b32 a37, v240\n"
  "v_accvgpr_read_b32 v240, a38\n"
  "v_mul_f32 v240, v240, v239\n"
  "v_accvgpr_write_b32 a38, v240\n"
  "v_accvgpr_read_b32 v240, a39\n"
  "v_mul_f32 v240, v240, v239\n"
  "v_accvgpr_write_b32 a39, v240\n"
  "v_accvgpr_read_b32 v240, a40\n"
  "v_mul_f32 v240, v240, v239\n"
  "v_accvgpr_write_b32 a40, v240\n"
  "v_accvgpr_read_b32 v240, a41\n"
  "v_mul_f32 v240, v240, v239\n"
  "v_accvgpr_write_b32 a41, v240\n"
  "v_accvgpr_read_b32 v240, a42\n"
  "v_mul_f32 v240, v240, v239\n"
  "v_accvgpr_write_b32 a42, v240\n"
  "v_accvgpr_read_b32 v240, a43\n"
  "v_mul_f32 v240, v240, v239\n"
  "v_accvgpr_write_b32 a43, v240\n"
  "v_accvgpr_read_b32 v240, a44\n"
  "v_mul_f32 v240, v240, v239\n"
  "v_accvgpr_write_b32 a44, v240\n"
  "v_accvgpr_read_b32 v240, a45\n"
  "v_mul_f32 v240, v240, v239\n"
  "v_accvgpr_write_b32 a45, v240\n"
  "v_accvgpr_read_b32 v240, a46\n"
  "v_mul_f32 v240, v240, v239\n"
  "v_accvgpr_write_b32 a46, v240\n"
  "v_accvgpr_read_b32 v240, a47\n"
  "v_mul_f32 v240, v240, v239\n"
  "v_accvgpr_write_b32 a47, v240\n"
  "v_accvgpr_read_b32 v240, a48\n"
  "v_mul_f32 v240, v240, v239\n"
  "v_accvgpr_write_b32 a48, v240\n"
  "v_accvgpr_read_b32 v240, a49\n"
  "v_mul_f32 v240, v240, v239\n"
  "v_accvgpr_write_b32 a49, v240\n"
  "v_accvgpr_read_b32 v240, a50\n"
  "v_mul_f32 v240, v240, v239\n"
  "v_accvgpr_write_b32 a50, v240\n"
  "v_accvgpr_read_b32 v240, a51\n"
  "v_mul_f32 v240, v240, v239\n"
  "v_accvgpr_write_b32 a51, v240\n"
  "v_accvgpr_read_b32 v240, a52\n"
  "v_mul_f32 v240, v240, v239\n"
  "v_accvgpr_write_b32 a52, v240\n"
  "v_accvgpr_read_b32 v240, a53\n"
  "v_mul_f32 v240, v240, v239\n"
  "v_accvgpr_write_b32 a53, v240\n"
  "v_accvgpr_read_b32 v240, a54\n"
  "v_mul_f32 v240, v240, v239\n"
  "v_accvgpr_write_b32 a54, v240\n"
  "v_accvgpr_read_b32 v240, a55\n"
  "v_mul_f32 v240, v240, v239\n"
  "v_accvgpr_write_b32 a55, v240\n"
  "v_accvgpr_read_b32 v240, a56\n"
  "v_mul_f32 v240, v240, v239\n"
  "v_accvgpr_write_b32 a56, v240\n"
  "v_accvgpr_read_b32 v240, a57\n"
  "v_mul_f32 v240, v240, v239\n"
  "v_accvgpr_write_b32 a57, v240\n"
  "v_accvgpr_read_b32 v240, a58\n"
  "v_mul_f32 v240, v240, v239\n"
  "v_accvgpr_write_b32 a58, v240\n"
  "v_accvgpr_read_b32 v240, a59\n"
  "v_mul_f32 v240, v240, v239\n"
  "v_accvgpr_write_b32 a59, v240\n"
  "v_accvgpr_read_b32 v240, a60\n"
  "v_mul_f32 v240, v240, v239\n"
  "v_accvgpr_write_b32 a60, v240\n"
  "v_accvgpr_read_b32 v240, a61\n"
  "v_mul_f32 v240, v240, v239\n"
  "v_accvgpr_write_b32 a61, v240\n"
  "v_accvgpr_read_b32 v240, a62\n"
  "v_mul_f32 v240, v240, v239\n"
  "v_accvgpr_write_b32 a62, v240\n"
  "v_accvgpr_read_b32 v240, a63\n"
  "v_mul_f32 v240, v240, v239\n"
  "v_accvgpr_write_b32 a63, v240\n"
  "v_sub_f32 v128, 0, v232\n"
  "v_sub_f32 v129, 0, v232\n"
  "v_sub_f32 v130, 0, v232\n"
  "v_sub_f32 v131, 0, v232\n"
  "v_sub_f32 v132, 0, v232\n"
  "v_sub_f32 v133, 0, v232\n"
  "v_sub_f32 v134, 0, v232\n"
  "v_sub_f32 v135, 0, v232\n"
  "v_sub_f32 v136, 0, v232\n"
  "v_sub_f32 v137, 0, v232\n"
  "v_sub_f32 v138, 0, v232\n"
  "v_sub_f32 v139, 0, v232\n"
  "v_sub_f32 v140, 0, v232\n"
  "v_sub_f32 v141, 0, v232\n"
  "v_sub_f32 v142, 0, v232\n"
  "v_sub_f32 v143, 0, v232\n"
  "v_sub_f32 v32, v32, v238\n"
  "v_sub_f32 v33, v33, v238\n"
  "v_sub_f32 v34, v34, v238\n"
  "v_sub_f32 v35, v35, v238\n"
  "v_sub_f32 v36, v36, v238\n"
  "v_sub_f32 v37, v37, v238\n"
  "v_sub_f32 v38, v38, v238\n"
  "v_sub_f32 v39, v39, v238\n"
  "v_sub_f32 v40, v40, v238\n"
  "v_sub_f32 v41, v41, v238\n"
  "v_sub_f32 v42, v42, v238\n"
  "v_sub_f32 v43, v43, v238\n"
  "v_sub_f32 v44, v44, v238\n"
  "v_sub_f32 v45, v45, v238\n"
  "v_sub_f32 v46, v46, v238\n"
  "v_sub_f32 v47, v47, v238\n"
  "v_sub_f32 v48, v48, v238\n"
  "v_sub_f32 v49, v49, v238\n"
  "v_sub_f32 v50, v50, v238\n"
  "v_sub_f32 v51, v51, v238\n"
  "v_sub_f32 v52, v52, v238\n"
  "v_sub_f32 v53, v53, v238\n"
  "v_sub_f32 v54, v54, v238\n"
  "v_sub_f32 v55, v55, v238\n"
  "v_sub_f32 v56, v56, v238\n"
  "v_sub_f32 v57, v57, v238\n"
  "v_sub_f32 v58, v58, v238\n"
  "v_sub_f32 v59, v59, v238\n"
  "v_sub_f32 v60, v60, v238\n"
  "v_sub_f32 v61, v61, v238\n"
  "v_sub_f32 v62, v62, v238\n"
  "v_sub_f32 v63, v63, v238\n"
  "v_exp_f32 v240, v32\n"
  "v_exp_f32 v241, v33\n"
  "s_nop 0\n"
  "v_add_f32 v228, v240, v241\n"
  "v_cvt_pk_bf16_f32 v96, v240, v241\n"
  "v_exp_f32 v240, v34\n"
  "v_exp_f32 v241, v35\n"
  "s_nop 0\n"
  "v_add_f32 v228, v228, v240\n"
  "v_add_f32 v228, v228, v241\n"
  "v_cvt_pk_bf16_f32 v97, v240, v241\n"
  "v_exp_f32 v240, v36\n"
  "v_exp_f32 v241, v37\n"
  "s_nop 0\n"
  "v_add_f32 v228, v228, v240\n"
  "v_add_f32 v228, v228, v241\n"
  "v_cvt_pk_bf16_f32 v98, v240, v241\n"
  "v_exp_f32 v240, v38\n"
  "v_exp_f32 v241, v39\n"
  "s_nop 0\n"
  "v_add_f32 v228, v228, v240\n"
  "v_add_f32 v228, v228, v241\n"
  "v_cvt_pk_bf16_f32 v99, v240, v241\n"
  "v_exp_f32 v240, v40\n"
  "v_exp_f32 v241, v41\n"
  "s_nop 0\n"
  "v_add_f32 v228, v228, v240\n"
  "v_add_f32 v228, v228, v241\n"
  "v_cvt_pk_bf16_f32 v100, v240, v241\n"
  "v_exp_f32 v240, v42\n"
  "v_exp_f32 v241, v43\n"
  "s_nop 0\n"
  "v_add_f32 v228, v228, v240\n"
  "v_add_f32 v228, v228, v241\n"
  "v_cvt_pk_bf16_f32 v101, v240, v241\n"
  "v_exp_f32 v240, v44\n"
  "v_exp_f32 v241, v45\n"
  "s_nop 0\n"
  "v_add_f32 v228, v228, v240\n"
  "v_add_f32 v228, v228, v241\n"
  "v_cvt_pk_bf16_f32 v102, v240, v241\n"
  "v_exp_f32 v240, v46\n"
  "v_exp_f32 v241, v47\n"
  "s_nop 0\n"
  "v_add_f32 v228, v228, v240\n"
  "v_add_f32 v228, v228, v241\n"
  "v_cvt_pk_bf16_f32 v103, v240, v241\n"
  "v_exp_f32 v240, v48\n"
  "v_exp_f32 v241, v49\n"
  "s_nop 0\n"
  "v_add_f32 v228, v228, v240\n"
  "v_add_f32 v228, v228, v241\n"
  "v_cvt_pk_bf16_f32 v104, v240, v241\n"
  "v_exp_f32 v240, v50\n"
  "v_exp_f32 v241, v51\n"
  "s_nop 0\n"
  "v_add_f32 v228, v228, v240\n"
  "v_add_f32 v228, v228, v241\n"
  "v_cvt_pk_bf16_f32 v105, v240, v241\n"
  "v_exp_f32 v240, v52\n"
  "v_exp_f32 v241, v53\n"
  "s_nop 0\n"
  "v_add_f32 v228, v228, v240\n"
  "v_add_f32 v228, v228, v241\n"
  "v_cvt_pk_bf16_f32 v106, v240, v241\n"
  "v_exp_f32 v240, v54\n"
  "v_exp_f32 v241, v55\n"
  "s_nop 0\n"
  "v_add_f32 v228, v228, v240\n"
  "v_add_f32 v228, v228, v241\n"
  "v_cvt_pk_bf16_f32 v107, v240, v241\n"
  "v_exp_f32 v240, v56\n"
  "v_exp_f32 v241, v57\n"
  "s_nop 0\n"
  "v_add_f32 v228, v228, v240\n"
  "v_add_f32 v228, v228, v241\n"
  "v_cvt_pk_bf16_f32 v108, v240, v241\n"
  "v_exp_f32 v240, v58\n"
  "v_exp_f32 v241, v59\n"
  "s_nop 0\n"
  "v_add_f32 v228, v228, v240\n"
  "v_add_f32 v228, v228, v241\n"
  "v_cvt_pk_bf16_f32 v109, v240, v241\n"
  "v_exp_f32 v240, v60\n"
  "v_exp_f32 v241, v61\n"
  "s_nop 0\n"
  "v_add_f32 v228, v228, v240\n"
  "v_add_f32 v228, v228, v241\n"
  "v_cvt_pk_bf16_f32 v110, v240, v241\n"
  "v_exp_f32 v240, v62\n"
  "v_exp_f32 v241, v63\n"
  "s_nop 0\n"
  "v_add_f32 v228, v228, v240\n"
  "v_add_f32 v228, v228, v241\n"
  "v_cvt_pk_bf16_f32 v111, v240, v241\n"
  "s_nop 15\n"
  "s_nop 15\n"
  "s_cmp_eq_u32 s48, 1\n"
  "s_cbranch_scc1 Lret_DX\n"
  "s_cmp_eq_u32 s48, 3\n"
  "s_cbranch_scc1 Lret_MX\n"
  "s_endpgm\n"
  "Lfbv_Yd:\n"
  "s_nop 15\n"
  "s_nop 15\n"
  "v_max_f32 v238, v64, v65\n"
  "v_max3_f32 v238, v238, v66, v67\n"
  "v_max3_f32 v238, v238, v68, v69\n"
  "v_max3_f32 v238, v238, v70, v71\n"
  "v_max3_f32 v238, v238, v72, v73\n"
  "v_max3_f32 v238, v238, v74, v75\n"
  "v_max3_f32 v238, v238, v76, v77\n"
  "v_max3_f32 v238, v238, v78, v79\n"
  "v_max3_f32 v238, v238, v80, v81\n"
  "v_max3_f32 v238, v238, v82, v83\n"
  "v_max3_f32 v238, v238, v84, v85\n"
  "v_max3_f32 v238, v238, v86, v87\n"
  "v_max3_f32 v238, v238, v88, v89\n"
  "v_max3_f32 v238, v238, v90, v91\n"
  "v_max3_f32 v238, v238, v92, v93\n"
  "v_max3_f32 v238, v238, v94, v95\n"
  "v_mov_b32 v239, v238\n"
  "s_nop 1\n"
  "v_permlane32_swap_b32 v238, v239\n"
  "v_max3_f32 v238, v238, v239, 0\n"
  "v_sub_f32 v239, 0, v238\n"
  "v_exp_f32 v239, v239\n"
  "v_add_f32 v233, v233, v238\n"
  "v_mul_f32 v231, v231, v239\n"
  "v_accvgpr_read_b32 v240, a64\n"
  "v_mul_f32 v240, v240, v239\n"
  "v_accvgpr_write_b32 a64, v240\n"
  "v_accvgpr_read_b32 v240, a65\n"
  "v_mul_f32 v240, v240, v239\n"
  "v_accvgpr_write_b32 a65, v240\n"
  "v_accvgpr_read_b32 v240, a66\n"
  "v_mul_f32 v240, v240, v239\n"
  "v_accvgpr_write_b32 a66, v240\n"
  "v_accvgpr_read_b32 v240, a67\n"
  "v_mul_f32 v240, v240, v239\n"
  "v_accvgpr_write_b32 a67, v240\n"
  "v_accvgpr_read_b32 v240, a68\n"
  "v_mul_f32 v240, v240, v239\n"
  "v_accvgpr_write_b32 a68, v240\n"
  "v_accvgpr_read_b32 v240, a69\n"
  "v_mul_f32 v240, v240, v239\n"
  "v_accvgpr_write_b32 a69, v240\n"
  "v_accvgpr_read_b32 v240, a70\n"
  "v_mul_f32 v240, v240, v239\n"
  "v_accvgpr_write_b32 a70, v240\n"
  "v_accvgpr_read_b32 v240, a71\n"
  "v_mul_f32 v240, v240, v239\n"
  "v_accvgpr_write_b32 a71, v240\n"
  "v_accvgpr_read_b32 v240, a72\n"
  "v_mul_f32 v240, v240, v239\n"
  "v_accvgpr_write_b32 a72, v240\n"
  "v_accvgpr_read_b32 v240, a73\n"
  "v_mul_f32 v240, v240, v239\n"
  "v_accvgpr_write_b32 a73, v240\n"
  "v_accvgpr_read_b32 v240, a74\n"
  "v_mul_f32 v240, v240, v239\n"
  "v_accvgpr_write_b32 a74, v240\n"
  "v_accvgpr_read_b32 v240, a75\n"
  "v_mul_f32 v240, v240, v239\n"
  "v_accvgpr_write_b32 a75, v240\n"
  "v_accvgpr_read_b32 v240, a76\n"
  "v_mul_f32 v240, v240, v239\n"
  "v_accvgpr_write_b32 a76, v240\n"
  "v_accvgpr_read_b32 v240, a77\n"
  "v_mul_f32 v240, v240, v239\n"
  "v_accvgpr_write_b32 a77, v240\n"
  "v_accvgpr_read_b32 v240, a78\n"
  "v_mul_f32 v240, v240, v239\n"
  "v_accvgpr_write_b32 a78, v240\n"
  "v_accvgpr_read_b32 v240, a79\n"
  "v_mul_f32 v240, v240, v239\n"
  "v_accvgpr_write_b32 a79, v240\n"
  "v_accvgpr_read_b32 v240, a80\n"
  "v_mul_f32 v240, v240, v239\n"
  "v_accvgpr_write_b32 a80, v240\n"
  "v_accvgpr_read_b32 v240, a81\n"
  "v_mul_f32 v240, v240, v239\n"
  "v_accvgpr_write_b32 a81, v240\n"
  "v_accvgpr_read_b32 v240, a82\n"
  "v_mul_f32 v240, v240, v239\n"
  "v_accvgpr_write_b32 a82, v240\n"
  "v_accvgpr_read_b32 v240, a83\n"
  "v_mul_f32 v240, v240, v239\n"
  "v_accvgpr_write_b32 a83, v240\n"
  "v_accvgpr_read_b32 v240, a84\n"
  "v_mul_f32 v240, v240, v239\n"
  "v_accvgpr_write_b32 a84, v240\n"
  "v_accvgpr_read_b32 v240, a85\n"
  "v_mul_f32 v240, v240, v239\n"
  "v_accvgpr_write_b32 a85, v240\n"
  "v_accvgpr_read_b32 v240, a86\n"
  "v_mul_f32 v240, v240, v239\n"
  "v_accvgpr_write_b32 a86, v240\n"
  "v_accvgpr_read_b32 v240, a87\n"
  "v_mul_f32 v240, v240, v239\n"
  "v_accvgpr_write_b32 a87, v240\n"
  "v_accvgpr_read_b32 v240, a88\n"
  "v_mul_f32 v240, v240, v239\n"
  "v_accvgpr_write_b32 a88, v240\n"
  "v_accvgpr_read_b32 v240, a89\n"
  "v_mul_f32 v240, v240, v239\n"
  "v_accvgpr_write_b32 a89, v240\n"
  "v_accvgpr_read_b32 v240, a90\n"
  "v_mul_f32 v240, v240, v239\n"
  "v_accvgpr_write_b32 a90, v240\n"
  "v_accvgpr_read_b32 v240, a91\n"
  "v_mul_f32 v240, v240, v239\n"
  "v_accvgpr_write_b32 a91, v240\n"
  "v_accvgpr_read_b32 v240, a92\n"
  "v_mul_f32 v240, v240, v239\n"
  "v_accvgpr_write_b32 a92, v240\n"
  "v_accvgpr_read_b32 v240, a93\n"
  "v_mul_f32 v240, v240, v239\n"
  "v_accvgpr_write_b32 a93, v240\n"
  "v_accvgpr_read_b32 v240, a94\n"
  "v_mul_f32 v240, v240, v239\n"
  "v_accvgpr_write_b32 a94, v240\n"
  "v_accvgpr_read_b32 v240, a95\n"
  "v_mul_f32 v240, v240, v239\n"
  "v_accvgpr_write_b32 a95, v240\n"
  "v_accvgpr_read_b32 v240, a96\n"
  "v_mul_f32 v240, v240, v239\n"
  "v_accvgpr_write_b32 a96, v240\n"
  "v_accvgpr_read_b32 v240, a97\n"
  "v_mul_f32 v240, v240, v239\n"
  "v_accvgpr_write_b32 a97, v240\n"
  "v_accvgpr_read_b32 v240, a98\n"
  "v_mul_f32 v240, v240, v239\n"
  "v_accvgpr_write_b32 a98, v240\n"
  "v_accvgpr_read_b32 v240, a99\n"
  "v_mul_f32 v240, v240, v239\n"
  "v_accvgpr_write_b32 a99, v240\n"
  "v_accvgpr_read_b32 v240, a100\n"
  "v_mul_f32 v240, v240, v239\n"
  "v_accvgpr_write_b32 a100, v240\n"
  "v_accvgpr_read_b32 v240, a101\n"
  "v_mul_f32 v240, v240, v239\n"
  "v_accvgpr_write_b32 a101, v240\n"
  "v_accvgpr_read_b32 v240, a102\n"
  "v_mul_f32 v240, v240, v239\n"
  "v_accvgpr_write_b32 a102, v240\n"
  "v_accvgpr_read_b32 v240, a103\n"
  "v_mul_f32 v240, v240, v239\n"
  "v_accvgpr_write_b32 a103, v240\n"
  "v_accvgpr_read_b32 v240, a104\n"
  "v_mul_f32 v240, v240, v239\n"
  "v_accvgpr_write_b32 a104, v240\n"
  "v_accvgpr_read_b32 v240, a105\n"
  "v_mul_f32 v240, v240, v239\n"
  "v_accvgpr_write_b32 a105, v240\n"
  "v_accvgpr_read_b32 v240, a106\n"
  "v_mul_f32 v240, v240, v239\n"
  "v_accvgpr_write_b32 a106, v240\n"
  "v_accvgpr_read_b32 v240, a107\n"
  "v_mul_f32 v240, v240, v239\n"
  "v_accvgpr_write_b32 a107, v240\n"
  "v_accvgpr_read_b32 v240, a108\n"
  "v_mul_f32 v240, v240, v239\n"
  "v_accvgpr_write_b32 a108, v240\n"
  "v_accvgpr_read_b32 v240, a109\n"
  "v_mul_f32 v240, v240, v239\n"
  "v_accvgpr_write_b32 a109, v240\n"
  "v_accvgpr_read_b32 v240, a110\n"
  "v_mul_f32 v240, v240, v239\n"
  "v_accvgpr_write_b32 a110, v240\n"
  "v_accvgpr_read_b32 v240, a111\n"
  "v_mul_f32 v240, v240, v239\n"
  "v_accvgpr_write_b32 a111, v240\n"
  "v_accvgpr_read_b32 v240, a112\n"
  "v_mul_f32 v240, v240, v239\n"
  "v_accvgpr_write_b32 a112, v240\n"
  "v_accvgpr_read_b32 v240, a113\n"
  "v_mul_f32 v240, v240, v239\n"
  "v_accvgpr_write_b32 a113, v240\n"
  "v_accvgpr_read_b32 v240, a114\n"
  "v_mul_f32 v240, v240, v239\n"
  "v_accvgpr_write_b32 a114, v240\n"
  "v_accvgpr_read_b32 v240, a115\n"
  "v_mul_f32 v240, v240, v239\n"
  "v_accvgpr_write_b32 a115, v240\n"
  "v_accvgpr_read_b32 v240, a116\n"
  "v_mul_f32 v240, v240, v239\n"
  "v_accvgpr_write_b32 a116, v240\n"
  "v_accvgpr_read_b32 v240, a117\n"
  "v_mul_f32 v240, v240, v239\n"
  "v_accvgpr_write_b32 a117, v240\n"
  "v_accvgpr_read_b32 v240, a118\n"
  "v_mul_f32 v240, v240, v239\n"
  "v_accvgpr_write_b32 a118, v240\n"
  "v_accvgpr_read_b32 v240, a119\n"
  "v_mul_f32 v240, v240, v239\n"
  "v_accvgpr_write_b32 a119, v240\n"
  "v_accvgpr_read_b32 v240, a120\n"
  "v_mul_f32 v240, v240, v239\n"
  "v_accvgpr_write_b32 a120, v240\n"
  "v_accvgpr_read_b32 v240, a121\n"
  "v_mul_f32 v240, v240, v239\n"
  "v_accvgpr_write_b32 a121, v240\n"
  "v_accvgpr_read_b32 v240, a122\n"
  "v_mul_f32 v240, v240, v239\n"
  "v_accvgpr_write_b32 a122, v240\n"
  "v_accvgpr_read_b32 v240, a123\n"
  "v_mul_f32 v240, v240, v239\n"
  "v_accvgpr_write_b32 a123, v240\n"
  "v_accvgpr_read_b32 v240, a124\n"
  "v_mul_f32 v240, v240, v239\n"
  "v_accvgpr_write_b32 a124, v240\n"
  "v_accvgpr_read_b32 v240, a125\n"
  "v_mul_f32 v240, v240, v239\n"
  "v_accvgpr_write_b32 a125, v240\n"
  "v_accvgpr_read_b32 v240, a126\n"
  "v_mul_f32 v240, v240, v239\n"
  "v_accvgpr_write_b32 a126, v240\n"
  "v_accvgpr_read_b32 v240, a127\n"
  "v_mul_f32 v240, v240, v239\n"
  "v_accvgpr_write_b32 a127, v240\n"
  "v_sub_f32 v144, 0, v233\n"
  "v_sub_f32 v145, 0, v233\n"
  "v_sub_f32 v146, 0, v233\n"
  "v_sub_f32 v147, 0, v233\n"
  "v_sub_f32 v148, 0, v233\n"
  "v_sub_f32 v149, 0, v233\n"
  "v_sub_f32 v150, 0, v233\n"
  "v_sub_f32 v151, 0, v233\n"
  "v_sub_f32 v152, 0, v233\n"
  "v_sub_f32 v153, 0, v233\n"
  "v_sub_f32 v154, 0, v233\n"
  "v_sub_f32 v155, 0, v233\n"
  "v_sub_f32 v156, 0, v233\n"
  "v_sub_f32 v157, 0, v233\n"
  "v_sub_f32 v158, 0, v233\n"
  "v_sub_f32 v159, 0, v233\n"
  "v_sub_f32 v64, v64, v238\n"
  "v_sub_f32 v65, v65, v238\n"
  "v_sub_f32 v66, v66, v238\n"
  "v_sub_f32 v67, v67, v238\n"
  "v_sub_f32 v68, v68, v238\n"
  "v_sub_f32 v69, v69, v238\n"
  "v_sub_f32 v70, v70, v238\n"
  "v_sub_f32 v71, v71, v238\n"
  "v_sub_f32 v72, v72, v238\n"
  "v_sub_f32 v73, v73, v238\n"
  "v_sub_f32 v74, v74, v238\n"
  "v_sub_f32 v75, v75, v238\n"
  "v_sub_f32 v76, v76, v238\n"
  "v_sub_f32 v77, v77, v238\n"
  "v_sub_f32 v78, v78, v238\n"
  "v_sub_f32 v79, v79, v238\n"
  "v_sub_f32 v80, v80, v238\n"
  "v_sub_f32 v81, v81, v238\n"
  "v_sub_f32 v82, v82, v238\n"
  "v_sub_f32 v83, v83, v238\n"
  "v_sub_f32 v84, v84, v238\n"
  "v_sub_f32 v85, v85, v238\n"
  "v_sub_f32 v86, v86, v238\n"
  "v_sub_f32 v87, v87, v238\n"
  "v_sub_f32 v88, v88, v238\n"
  "v_sub_f32 v89, v89, v238\n"
  "v_sub_f32 v90, v90, v238\n"
  "v_sub_f32 v91, v91, v238\n"
  "v_sub_f32 v92, v92, v238\n"
  "v_sub_f32 v93, v93, v238\n"
  "v_sub_f32 v94, v94, v238\n"
  "v_sub_f32 v95, v95, v238\n"
  "v_exp_f32 v240, v64\n"
  "v_exp_f32 v241, v65\n"
  "s_nop 0\n"
  "v_add_f32 v229, v240, v241\n"
  "v_cvt_pk_bf16_f32 v112, v240, v241\n"
  "v_exp_f32 v240, v66\n"
  "v_exp_f32 v241, v67\n"
  "s_nop 0\n"
  "v_add_f32 v229, v229, v240\n"
  "v_add_f32 v229, v229, v241\n"
  "v_cvt_pk_bf16_f32 v113, v240, v241\n"
  "v_exp_f32 v240, v68\n"
  "v_exp_f32 v241, v69\n"
  "s_nop 0\n"
  "v_add_f32 v229, v229, v240\n"
  "v_add_f32 v229, v229, v241\n"
  "v_cvt_pk_bf16_f32 v114, v240, v241\n"
  "v_exp_f32 v240, v70\n"
  "v_exp_f32 v241, v71\n"
  "s_nop 0\n"
  "v_add_f32 v229, v229, v240\n"
  "v_add_f32 v229, v229, v241\n"
  "v_cvt_pk_bf16_f32 v115, v240, v241\n"
  "v_exp_f32 v240, v72\n"
  "v_exp_f32 v241, v73\n"
  "s_nop 0\n"
  "v_add_f32 v229, v229, v240\n"
  "v_add_f32 v229, v229, v241\n"
  "v_cvt_pk_bf16_f32 v116, v240, v241\n"
  "v_exp_f32 v240, v74\n"
  "v_exp_f32 v241, v75\n"
  "s_nop 0\n"
  "v_add_f32 v229, v229, v240\n"
  "v_add_f32 v229, v229, v241\n"
  "v_cvt_pk_bf16_f32 v117, v240, v241\n"
  "v_exp_f32 v240, v76\n"
  "v_exp_f32 v241, v77\n"
  "s_nop 0\n"
  "v_add_f32 v229, v229, v240\n"
  "v_add_f32 v229, v229, v241\n"
  "v_cvt_pk_bf16_f32 v118, v240, v241\n"
  "v_exp_f32 v240, v78\n"
  "v_exp_f32 v241, v79\n"
  "s_nop 0\n"
  "v_add_f32 v229, v229, v240\n"
  "v_add_f32 v229, v229, v241\n"
  "v_cvt_pk_bf16_f32 v119, v240, v241\n"
  "v_exp_f32 v240, v80\n"
  "v_exp_f32 v241, v81\n"
  "s_nop 0\n"
  "v_add_f32 v229, v229, v240\n"
  "v_add_f32 v229, v229, v241\n"
  "v_cvt_pk_bf16_f32 v120, v240, v241\n"
  "v_exp_f32 v240, v82\n"
  "v_exp_f32 v241, v83\n"
  "s_nop 0\n"
  "v_add_f32 v229, v229, v240\n"
  "v_add_f32 v229, v229, v241\n"
  "v_cvt_pk_bf16_f32 v121, v240, v241\n"
  "v_exp_f32 v240, v84\n"
  "v_exp_f32 v241, v85\n"
  "s_nop 0\n"
  "v_add_f32 v229, v229, v240\n"
  "v_add_f32 v229, v229, v241\n"
  "v_cvt_pk_bf16_f32 v122, v240, v241\n"
  "v_exp_f32 v240, v86\n"
  "v_exp_f32 v241, v87\n"
  "s_nop 0\n"
  "v_add_f32 v229, v229, v240\n"
  "v_add_f32 v229, v229, v241\n"
  "v_cvt_pk_bf16_f32 v123, v240, v241\n"
  "v_exp_f32 v240, v88\n"
  "v_exp_f32 v241, v89\n"
  "s_nop 0\n"
  "v_add_f32 v229, v229, v240\n"
  "v_add_f32 v229, v229, v241\n"
  "v_cvt_pk_bf16_f32 v124, v240, v241\n"
  "v_exp_f32 v240, v90\n"
  "v_exp_f32 v241, v91\n"
  "s_nop 0\n"
  "v_add_f32 v229, v229, v240\n"
  "v_add_f32 v229, v229, v241\n"
  "v_cvt_pk_bf16_f32 v125, v240, v241\n"
  "v_exp_f32 v240, v92\n"
  "v_exp_f32 v241, v93\n"
  "s_nop 0\n"
  "v_add_f32 v229, v229, v240\n"
  "v_add_f32 v229, v229, v241\n"
  "v_cvt_pk_bf16_f32 v126, v240, v241\n"
  "v_exp_f32 v240, v94\n"
  "v_exp_f32 v241, v95\n"
  "s_nop 0\n"
  "v_add_f32 v229, v229, v240\n"
  "v_add_f32 v229, v229, v241\n"
  "v_cvt_pk_bf16_f32 v127, v240, v241\n"
  "s_nop 15\n"
  "s_nop 15\n"
  "s_cmp_eq_u32 s48, 0\n"
  "s_cbranch_scc1 Lret_DY\n"
  "s_cmp_eq_u32 s48, 2\n"
  "s_cbranch_scc1 Lret_MY\n"
  "s_endpgm\n"
  "Lfbv_Xs:\n"
  "s_nop 15\n"
  "s_nop 15\n"
  "v_max_f32 v238, v32, v33\n"
  "v_max3_f32 v238, v238, v34, v35\n"
  "v_max3_f32 v238, v238, v36, v37\n"
  "v_max3_f32 v238, v238, v38, v39\n"
  "v_max3_f32 v238, v238, v40, v41\n"
  "v_max3_f32 v238, v238, v42, v43\n"
  "v_max3_f32 v238, v238, v44, v45\n"
  "v_max3_f32 v238, v238, v46, v47\n"
  "v_max3_f32 v238, v238, v48, v49\n"
  "v_max3_f32 v238, v238, v50, v51\n"
  "v_max3_f32 v238, v238, v52, v53\n"
  "v_max3_f32 v238, v238, v54, v55\n"
  "v_max3_f32 v238, v238, v56, v57\n"
  "v_max3_f32 v238, v238, v58, v59\n"
  "v_max3_f32 v238, v238, v60, v61\n"
  "v_max3_f32 v238, v238, v62, v63\n"
  "v_mov_b32 v239, v238\n"
  "s_nop 1\n"
  "v_permlane32_swap_b32 v238, v239\n"
  "v_max3_f32 v238, v238, v239, 0\n"
  "v_sub_f32 v239, 0, v238\n"
  "v_exp_f32 v239, v239\n"
  "v_add_f32 v233, v233, v238\n"
  "v_mul_f32 v231, v231, v239\n"
  "v_accvgpr_read_b32 v240, a64\n"
  "v_mul_f32 v240, v240, v239\n"
  "v_accvgpr_write_b32 a64, v240\n"
  "v_accvgpr_read_b32 v240, a65\n"
  "v_mul_f32 v240, v240, v239\n"
  "v_accvgpr_write_b32 a65, v240\n"
  "v_accvgpr_read_b32 v240, a66\n"
  "v_mul_f32 v240, v240, v239\n"
  "v_accvgpr_write_b32 a66, v240\n"
  "v_accvgpr_read_b32 v240, a67\n"
  "v_mul_f32 v240, v240, v239\n"
  "v_accvgpr_write_b32 a67, v240\n"
  "v_accvgpr_read_b32 v240, a68\n"
  "v_mul_f32 v240, v240, v239\n"
  "v_accvgpr_write_b32 a68, v240\n"
  "v_accvgpr_read_b32 v240, a69\n"
  "v_mul_f32 v240, v240, v239\n"
  "v_accvgpr_write_b32 a69, v240\n"
  "v_accvgpr_read_b32 v240, a70\n"
  "v_mul_f32 v240, v240, v239\n"
  "v_accvgpr_write_b32 a70, v240\n"
  "v_accvgpr_read_b32 v240, a71\n"
  "v_mul_f32 v240, v240, v239\n"
  "v_accvgpr_write_b32 a71, v240\n"
  "v_accvgpr_read_b32 v240, a72\n"
  "v_mul_f32 v240, v240, v239\n"
  "v_accvgpr_write_b32 a72, v240\n"
  "v_accvgpr_read_b32 v240, a73\n"
  "v_mul_f32 v240, v240, v239\n"
  "v_accvgpr_write_b32 a73, v240\n"
  "v_accvgpr_read_b32 v240, a74\n"
  "v_mul_f32 v240, v240, v239\n"
  "v_accvgpr_write_b32 a74, v240\n"
  "v_accvgpr_read_b32 v240, a75\n"
  "v_mul_f32 v240, v240, v239\n"
  "v_accvgpr_write_b32 a75, v240\n"
  "v_accvgpr_read_b32 v240, a76\n"
  "v_mul_f32 v240, v240, v239\n"
  "v_accvgpr_write_b32 a76, v240\n"
  "v_accvgpr_read_b32 v240, a77\n"
  "v_mul_f32 v240, v240, v239\n"
  "v_accvgpr_write_b32 a77, v240\n"
  "v_accvgpr_read_b32 v240, a78\n"
  "v_mul_f32 v240, v240, v239\n"
  "v_accvgpr_write_b32 a78, v240\n"
  "v_accvgpr_read_b32 v240, a79\n"
  "v_mul_f32 v240, v240, v239\n"
  "v_accvgpr_write_b32 a79, v240\n"
  "v_accvgpr_read_b32 v240, a80\n"
  "v_mul_f32 v240, v240, v239\n"
  "v_accvgpr_write_b32 a80, v240\n"
  "v_accvgpr_read_b32 v240, a81\n"
  "v_mul_f32 v240, v240, v239\n"
  "v_accvgpr_write_b32 a81, v240\n"
  "v_accvgpr_read_b32 v240, a82\n"
  "v_mul_f32 v240, v240, v239\n"
  "v_accvgpr_write_b32 a82, v240\n"
  "v_accvgpr_read_b32 v240, a83\n"
  "v_mul_f32 v240, v240, v239\n"
  "v_accvgpr_write_b32 a83, v240\n"
  "v_accvgpr_read_b32 v240, a84\n"
  "v_mul_f32 v240, v240, v239\n"
  "v_accvgpr_write_b32 a84, v240\n"
  "v_accvgpr_read_b32 v240, a85\n"
  "v_mul_f32 v240, v240, v239\n"
  "v_accvgpr_write_b32 a85, v240\n"
  "v_accvgpr_read_b32 v240, a86\n"
  "v_mul_f32 v240, v240, v239\n"
  "v_accvgpr_write_b32 a86, v240\n"
  "v_accvgpr_read_b32 v240, a87\n"
  "v_mul_f32 v240, v240, v239\n"
  "v_accvgpr_write_b32 a87, v240\n"
  "v_accvgpr_read_b32 v240, a88\n"
  "v_mul_f32 v240, v240, v239\n"
  "v_accvgpr_write_b32 a88, v240\n"
  "v_accvgpr_read_b32 v240, a89\n"
  "v_mul_f32 v240, v240, v239\n"
  "v_accvgpr_write_b32 a89, v240\n"
  "v_accvgpr_read_b32 v240, a90\n"
  "v_mul_f32 v240, v240, v239\n"
  "v_accvgpr_write_b32 a90, v240\n"
  "v_accvgpr_read_b32 v240, a91\n"
  "v_mul_f32 v240, v240, v239\n"
  "v_accvgpr_write_b32 a91, v240\n"
  "v_accvgpr_read_b32 v240, a92\n"
  "v_mul_f32 v240, v240, v239\n"
  "v_accvgpr_write_b32 a92, v240\n"
  "v_accvgpr_read_b32 v240, a93\n"
  "v_mul_f32 v240, v240, v239\n"
  "v_accvgpr_write_b32 a93, v240\n"
  "v_accvgpr_read_b32 v240, a94\n"
  "v_mul_f32 v240, v240, v239\n"
  "v_accvgpr_write_b32 a94, v240\n"
  "v_accvgpr_read_b32 v240, a95\n"
  "v_mul_f32 v240, v240, v239\n"
  "v_accvgpr_write_b32 a95, v240\n"
  "v_accvgpr_read_b32 v240, a96\n"
  "v_mul_f32 v240, v240, v239\n"
  "v_accvgpr_write_b32 a96, v240\n"
  "v_accvgpr_read_b32 v240, a97\n"
  "v_mul_f32 v240, v240, v239\n"
  "v_accvgpr_write_b32 a97, v240\n"
  "v_accvgpr_read_b32 v240, a98\n"
  "v_mul_f32 v240, v240, v239\n"
  "v_accvgpr_write_b32 a98, v240\n"
  "v_accvgpr_read_b32 v240, a99\n"
  "v_mul_f32 v240, v240, v239\n"
  "v_accvgpr_write_b32 a99, v240\n"
  "v_accvgpr_read_b32 v240, a100\n"
  "v_mul_f32 v240, v240, v239\n"
  "v_accvgpr_write_b32 a100, v240\n"
  "v_accvgpr_read_b32 v240, a101\n"
  "v_mul_f32 v240, v240, v239\n"
  "v_accvgpr_write_b32 a101, v240\n"
  "v_accvgpr_read_b32 v240, a102\n"
  "v_mul_f32 v240, v240, v239\n"
  "v_accvgpr_write_b32 a102, v240\n"
  "v_accvgpr_read_b32 v240, a103\n"
  "v_mul_f32 v240, v240, v239\n"
  "v_accvgpr_write_b32 a103, v240\n"
  "v_accvgpr_read_b32 v240, a104\n"
  "v_mul_f32 v240, v240, v239\n"
  "v_accvgpr_write_b32 a104, v240\n"
  "v_accvgpr_read_b32 v240, a105\n"
  "v_mul_f32 v240, v240, v239\n"
  "v_accvgpr_write_b32 a105, v240\n"
  "v_accvgpr_read_b32 v240, a106\n"
  "v_mul_f32 v240, v240, v239\n"
  "v_accvgpr_write_b32 a106, v240\n"
  "v_accvgpr_read_b32 v240, a107\n"
  "v_mul_f32 v240, v240, v239\n"
  "v_accvgpr_write_b32 a107, v240\n"
  "v_accvgpr_read_b32 v240, a108\n"
  "v_mul_f32 v240, v240, v239\n"
  "v_accvgpr_write_b32 a108, v240\n"
  "v_accvgpr_read_b32 v240, a109\n"
  "v_mul_f32 v240, v240, v239\n"
  "v_accvgpr_write_b32 a109, v240\n"
  "v_accvgpr_read_b32 v240, a110\n"
  "v_mul_f32 v240, v240, v239\n"
  "v_accvgpr_write_b32 a110, v240\n"
  "v_accvgpr_read_b32 v240, a111\n"
  "v_mul_f32 v240, v240, v239\n"
  "v_accvgpr_write_b32 a111, v240\n"
  "v_accvgpr_read_b32 v240, a112\n"
  "v_mul_f32 v240, v240, v239\n"
  "v_accvgpr_write_b32 a112, v240\n"
  "v_accvgpr_read_b32 v240, a113\n"
  "v_mul_f32 v240, v240, v239\n"
  "v_accvgpr_write_b32 a113, v240\n"
  "v_accvgpr_read_b32 v240, a114\n"
  "v_mul_f32 v240, v240, v239\n"
  "v_accvgpr_write_b32 a114, v240\n"
  "v_accvgpr_read_b32 v240, a115\n"
  "v_mul_f32 v240, v240, v239\n"
  "v_accvgpr_write_b32 a115, v240\n"
  "v_accvgpr_read_b32 v240, a116\n"
  "v_mul_f32 v240, v240, v239\n"
  "v_accvgpr_write_b32 a116, v240\n"
  "v_accvgpr_read_b32 v240, a117\n"
  "v_mul_f32 v240, v240, v239\n"
  "v_accvgpr_write_b32 a117, v240\n"
  "v_accvgpr_read_b32 v240, a118\n"
  "v_mul_f32 v240, v240, v239\n"
  "v_accvgpr_write_b32 a118, v240\n"
  "v_accvgpr_read_b32 v240, a119\n"
  "v_mul_f32 v240, v240, v239\n"
  "v_accvgpr_write_b32 a119, v240\n"
  "v_accvgpr_read_b32 v240, a120\n"
  "v_mul_f32 v240, v240, v239\n"
  "v_accvgpr_write_b32 a120, v240\n"
  "v_accvgpr_read_b32 v240, a121\n"
  "v_mul_f32 v240, v240, v239\n"
  "v_accvgpr_write_b32 a121, v240\n"
  "v_accvgpr_read_b32 v240, a122\n"
  "v_mul_f32 v240, v240, v239\n"
  "v_accvgpr_write_b32 a122, v240\n"
  "v_accvgpr_read_b32 v240, a123\n"
  "v_mul_f32 v240, v240, v239\n"
  "v_accvgpr_write_b32 a123, v240\n"
  "v_accvgpr_read_b32 v240, a124\n"
  "v_mul_f32 v240, v240, v239\n"
  "v_accvgpr_write_b32 a124, v240\n"
  "v_accvgpr_read_b32 v240, a125\n"
  "v_mul_f32 v240, v240, v239\n"
  "v_accvgpr_write_b32 a125, v240\n"
  "v_accvgpr_read_b32 v240, a126\n"
  "v_mul_f32 v240, v240, v239\n"
  "v_accvgpr_write_b32 a126, v240\n"
  "v_accvgpr_read_b32 v240, a127\n"
  "v_mul_f32 v240, v240, v239\n"
  "v_accvgpr_write_b32 a127, v240\n"
  "v_sub_f32 v144, 0, v233\n"
  "v_sub_f32 v145, 0, v233\n"
  "v_sub_f32 v146, 0, v233\n"
  "v_sub_f32 v147, 0, v233\n"
  "v_sub_f32 v148, 0, v233\n"
  "v_sub_f32 v149, 0, v233\n"
  "v_sub_f32 v150, 0, v233\n"
  "v_sub_f32 v151, 0, v233\n"
  "v_sub_f32 v152, 0, v233\n"
  "v_sub_f32 v153, 0, v233\n"
  "v_sub_f32 v154, 0, v233\n"
  "v_sub_f32 v155, 0, v233\n"
  "v_sub_f32 v156, 0, v233\n"
  "v_sub_f32 v157, 0, v233\n"
  "v_sub_f32 v158, 0, v233\n"
  "v_sub_f32 v159, 0, v233\n"
  "v_sub_f32 v32, v32, v238\n"
  "v_sub_f32 v33, v33, v238\n"
  "v_sub_f32 v34, v34, v238\n"
  "v_sub_f32 v35, v35, v238\n"
  "v_sub_f32 v36, v36, v238\n"
  "v_sub_f32 v37, v37, v238\n"
  "v_sub_f32 v38, v38, v238\n"
  "v_sub_f32 v39, v39, v238\n"
  "v_sub_f32 v40, v40, v238\n"
  "v_sub_f32 v41, v41, v238\n"
  "v_sub_f32 v42, v42, v238\n"
  "v_sub_f32 v43, v43, v238\n"
  "v_sub_f32 v44, v44, v238\n"
  "v_sub_f32 v45, v45, v238\n"
  "v_sub_f32 v46, v46, v238\n"
  "v_sub_f32 v47, v47, v238\n"
  "v_sub_f32 v48, v48, v238\n"
  "v_sub_f32 v49, v49, v238\n"
  "v_sub_f32 v50, v50, v238\n"
  "v_sub_f32 v51, v51, v238\n"
  "v_sub_f32 v52, v52, v238\n"
  "v_sub_f32 v53, v53, v238\n"
  "v_sub_f32 v54, v54, v238\n"
  "v_sub_f32 v55, v55, v238\n"
  "v_sub_f32 v56, v56, v238\n"
  "v_sub_f32 v57, v57, v238\n"
  "v_sub_f32 v58, v58, v238\n"
  "v_sub_f32 v59, v59, v238\n"
  "v_sub_f32 v60, v60, v238\n"
  "v_sub_f32 v61, v61, v238\n"
  "v_sub_f32 v62, v62, v238\n"
  "v_sub_f32 v63, v63, v238\n"
  "v_sub_f32 v64, v64, v238\n"
  "v_sub_f32 v65, v65, v238\n"
  "v_sub_f32 v66, v66, v238\n"
  "v_sub_f32 v67, v67, v238\n"
  "v_sub_f32 v68, v68, v238\n"
  "v_sub_f32 v69, v69, v238\n"
  "v_sub_f32 v70, v70, v238\n"
  "v_sub_f32 v71, v71, v238\n"
  "v_sub_f32 v72, v72, v238\n"
  "v_sub_f32 v73, v73, v238\n"
  "v_sub_f32 v74, v74, v238\n"
  "v_sub_f32 v75, v75, v238\n"
  "v_sub_f32 v76, v76, v238\n"
  "v_sub_f32 v77, v77, v238\n"
  "v_sub_f32 v78, v78, v238\n"
  "v_sub_f32 v79, v79, v238\n"
  "v_sub_f32 v80, v80, v238\n"
  "v_sub_f32 v81, v81, v238\n"
  "v_sub_f32 v82, v82, v238\n"
  "v_sub_f32 v83, v83, v238\n"
  "v_sub_f32 v84, v84, v238\n"
  "v_sub_f32 v85, v85, v238\n"
  "v_sub_f32 v86, v86, v238\n"
  "v_sub_f32 v87, v87, v238\n"
  "v_sub_f32 v88, v88, v238\n"
  "v_sub_f32 v89, v89, v238\n"
  "v_sub_f32 v90, v90, v238\n"
  "v_sub_f32 v91, v91, v238\n"
  "v_sub_f32 v92, v92, v238\n"
  "v_sub_f32 v93, v93, v238\n"
  "v_sub_f32 v94, v94, v238\n"
  "v_sub_f32 v95, v95, v238\n"
  "v_exp_f32 v240, v32\n"
  "v_exp_f32 v241, v33\n"
  "s_nop 0\n"
  "v_add_f32 v228, v240, v241\n"
  "v_cvt_pk_bf16_f32 v96, v240, v241\n"
  "v_exp_f32 v240, v34\n"
  "v_exp_f32 v241, v35\n"
  "s_nop 0\n"
  "v_add_f32 v228, v228, v240\n"
  "v_add_f32 v228, v228, v241\n"
  "v_cvt_pk_bf16_f32 v97, v240, v241\n"
  "v_exp_f32 v240, v36\n"
  "v_exp_f32 v241, v37\n"
  "s_nop 0\n"
  "v_add_f32 v228, v228, v240\n"
  "v_add_f32 v228, v228, v241\n"
  "v_cvt_pk_bf16_f32 v98, v240, v241\n"
  "v_exp_f32 v240, v38\n"
  "v_exp_f32 v241, v39\n"
  "s_nop 0\n"
  "v_add_f32 v228, v228, v240\n"
  "v_add_f32 v228, v228, v241\n"
  "v_cvt_pk_bf16_f32 v99, v240, v241\n"
  "v_exp_f32 v240, v40\n"
  "v_exp_f32 v241, v41\n"
  "s_nop 0\n"
  "v_add_f32 v228, v228, v240\n"
  "v_add_f32 v228, v228, v241\n"
  "v_cvt_pk_bf16_f32 v100, v240, v241\n"
  "v_exp_f32 v240, v42\n"
  "v_exp_f32 v241, v43\n"
  "s_nop 0\n"
  "v_add_f32 v228, v228, v240\n"
  "v_add_f32 v228, v228, v241\n"
  "v_cvt_pk_bf16_f32 v101, v240, v241\n"
  "v_exp_f32 v240, v44\n"
  "v_exp_f32 v241, v45\n"
  "s_nop 0\n"
  "v_add_f32 v228, v228, v240\n"
  "v_add_f32 v228, v228, v241\n"
  "v_cvt_pk_bf16_f32 v102, v240, v241\n"
  "v_exp_f32 v240, v46\n"
  "v_exp_f32 v241, v47\n"
  "s_nop 0\n"
  "v_add_f32 v228, v228, v240\n"
  "v_add_f32 v228, v228, v241\n"
  "v_cvt_pk_bf16_f32 v103, v240, v241\n"
  "v_exp_f32 v240, v48\n"
  "v_exp_f32 v241, v49\n"
  "s_nop 0\n"
  "v_add_f32 v228, v228, v240\n"
  "v_add_f32 v228, v228, v241\n"
  "v_cvt_pk_bf16_f32 v104, v240, v241\n"
  "v_exp_f32 v240, v50\n"
  "v_exp_f32 v241, v51\n"
  "s_nop 0\n"
  "v_add_f32 v228, v228, v240\n"
  "v_add_f32 v228, v228, v241\n"
  "v_cvt_pk_bf16_f32 v105, v240, v241\n"
  "v_exp_f32 v240, v52\n"
  "v_exp_f32 v241, v53\n"
  "s_nop 0\n"
  "v_add_f32 v228, v228, v240\n"
  "v_add_f32 v228, v228, v241\n"
  "v_cvt_pk_bf16_f32 v106, v240, v241\n"
  "v_exp_f32 v240, v54\n"
  "v_exp_f32 v241, v55\n"
  "s_nop 0\n"
  "v_add_f32 v228, v228, v240\n"
  "v_add_f32 v228, v228, v241\n"
  "v_cvt_pk_bf16_f32 v107, v240, v241\n"
  "v_exp_f32 v240, v56\n"
  "v_exp_f32 v241, v57\n"
  "s_nop 0\n"
  "v_add_f32 v228, v228, v240\n"
  "v_add_f32 v228, v228, v241\n"
  "v_cvt_pk_bf16_f32 v108, v240, v241\n"
  "v_exp_f32 v240, v58\n"
  "v_exp_f32 v241, v59\n"
  "s_nop 0\n"
  "v_add_f32 v228, v228, v240\n"
  "v_add_f32 v228, v228, v241\n"
  "v_cvt_pk_bf16_f32 v109, v240, v241\n"
  "v_exp_f32 v240, v60\n"
  "v_exp_f32 v241, v61\n"
  "s_nop 0\n"
  "v_add_f32 v228, v228, v240\n"
  "v_add_f32 v228, v228, v241\n"
  "v_cvt_pk_bf16_f32 v110, v240, v241\n"
  "v_exp_f32 v240, v62\n"
  "v_exp_f32 v241, v63\n"
  "s_nop 0\n"
  "v_add_f32 v228, v228, v240\n"
  "v_add_f32 v228, v228, v241\n"
  "v_cvt_pk_bf16_f32 v111, v240, v241\n"
  "s_nop 15\n"
  "s_nop 15\n"
  "s_cmp_eq_u32 s48, 5\n"
  "s_cbranch_scc1 Lret_SX\n"
  "s_cmp_eq_u32 s48, 7\n"
  "s_cbranch_scc1 Lret_NX\n"
  "s_endpgm\n"
  "Lfbv_Ys:\n"
  "s_nop 15\n"
  "s_nop 15\n"
  "v_max_f32 v238, v64, v65\n"
  "v_max3_f32 v238, v238, v66, v67\n"
  "v_max3_f32 v238, v238, v68, v69\n"
  "v_max3_f32 v238, v238, v70, v71\n"
  "v_max3_f32 v238, v238, v72, v73\n"
  "v_max3_f32 v238, v238, v74, v75\n"
  "v_max3_f32 v238, v238, v76, v77\n"
  "v_max3_f32 v238, v238, v78, v79\n"
  "v_max3_f32 v238, v238, v80, v81\n"
  "v_max3_f32 v238, v238, v82, v83\n"
  "v_max3_f32 v238, v238, v84, v85\n"
  "v_max3_f32 v238, v238, v86, v87\n"
  "v_max3_f32 v238, v238, v88, v89\n"
  "v_max3_f32 v238, v238, v90, v91\n"
  "v_max3_f32 v238, v238, v92, v93\n"
  "v_max3_f32 v238, v238, v94, v95\n"
  "v_mov_b32 v239, v238\n"
  "s_nop 1\n"
  "v_permlane32_swap_b32 v238, v239\n"
  "v_max3_f32 v238, v238, v239, 0\n"
  "v_sub_f32 v239, 0, v238\n"
  "v_exp_f32 v239, v239\n"
  "v_add_f32 v233, v233, v238\n"
  "v_mul_f32 v231, v231, v239\n"
  "v_accvgpr_read_b32 v240, a64\n"
  "v_mul_f32 v240, v240, v239\n"
  "v_accvgpr_write_b32 a64, v240\n"
  "v_accvgpr_read_b32 v240, a65\n"
  "v_mul_f32 v240, v240, v239\n"
  "v_accvgpr_write_b32 a65, v240\n"
  "v_accvgpr_read_b32 v240, a66\n"
  "v_mul_f32 v240, v240, v239\n"
  "v_accvgpr_write_b32 a66, v240\n"
  "v_accvgpr_read_b32 v240, a67\n"
  "v_mul_f32 v240, v240, v239\n"
  "v_accvgpr_write_b32 a67, v240\n"
  "v_accvgpr_read_b32 v240, a68\n"
  "v_mul_f32 v240, v240, v239\n"
  "v_accvgpr_write_b32 a68, v240\n"
  "v_accvgpr_read_b32 v240, a69\n"
  "v_mul_f32 v240, v240, v239\n"
  "v_accvgpr_write_b32 a69, v240\n"
  "v_accvgpr_read_b32 v240, a70\n"
  "v_mul_f32 v240, v240, v239\n"
  "v_accvgpr_write_b32 a70, v240\n"
  "v_accvgpr_read_b32 v240, a71\n"
  "v_mul_f32 v240, v240, v239\n"
  "v_accvgpr_write_b32 a71, v240\n"
  "v_accvgpr_read_b32 v240, a72\n"
  "v_mul_f32 v240, v240, v239\n"
  "v_accvgpr_write_b32 a72, v240\n"
  "v_accvgpr_read_b32 v240, a73\n"
  "v_mul_f32 v240, v240, v239\n"
  "v_accvgpr_write_b32 a73, v240\n"
  "v_accvgpr_read_b32 v240, a74\n"
  "v_mul_f32 v240, v240, v239\n"
  "v_accvgpr_write_b32 a74, v240\n"
  "v_accvgpr_read_b32 v240, a75\n"
  "v_mul_f32 v240, v240, v239\n"
  "v_accvgpr_write_b32 a75, v240\n"
  "v_accvgpr_read_b32 v240, a76\n"
  "v_mul_f32 v240, v240, v239\n"
  "v_accvgpr_write_b32 a76, v240\n"
  "v_accvgpr_read_b32 v240, a77\n"
  "v_mul_f32 v240, v240, v239\n"
  "v_accvgpr_write_b32 a77, v240\n"
  "v_accvgpr_read_b32 v240, a78\n"
  "v_mul_f32 v240, v240, v239\n"
  "v_accvgpr_write_b32 a78, v240\n"
  "v_accvgpr_read_b32 v240, a79\n"
  "v_mul_f32 v240, v240, v239\n"
  "v_accvgpr_write_b32 a79, v240\n"
  "v_accvgpr_read_b32 v240, a80\n"
  "v_mul_f32 v240, v240, v239\n"
  "v_accvgpr_write_b32 a80, v240\n"
  "v_accvgpr_read_b32 v240, a81\n"
  "v_mul_f32 v240, v240, v239\n"
  "v_accvgpr_write_b32 a81, v240\n"
  "v_accvgpr_read_b32 v240, a82\n"
  "v_mul_f32 v240, v240, v239\n"
  "v_accvgpr_write_b32 a82, v240\n"
  "v_accvgpr_read_b32 v240, a83\n"
  "v_mul_f32 v240, v240, v239\n"
  "v_accvgpr_write_b32 a83, v240\n"
  "v_accvgpr_read_b32 v240, a84\n"
  "v_mul_f32 v240, v240, v239\n"
  "v_accvgpr_write_b32 a84, v240\n"
  "v_accvgpr_read_b32 v240, a85\n"
  "v_mul_f32 v240, v240, v239\n"
  "v_accvgpr_write_b32 a85, v240\n"
  "v_accvgpr_read_b32 v240, a86\n"
  "v_mul_f32 v240, v240, v239\n"
  "v_accvgpr_write_b32 a86, v240\n"
  "v_accvgpr_read_b32 v240, a87\n"
  "v_mul_f32 v240, v240, v239\n"
  "v_accvgpr_write_b32 a87, v240\n"
  "v_accvgpr_read_b32 v240, a88\n"
  "v_mul_f32 v240, v240, v239\n"
  "v_accvgpr_write_b32 a88, v240\n"
  "v_accvgpr_read_b32 v240, a89\n"
  "v_mul_f32 v240, v240, v239\n"
  "v_accvgpr_write_b32 a89, v240\n"
  "v_accvgpr_read_b32 v240, a90\n"
  "v_mul_f32 v240, v240, v239\n"
  "v_accvgpr_write_b32 a90, v240\n"
  "v_accvgpr_read_b32 v240, a91\n"
  "v_mul_f32 v240, v240, v239\n"
  "v_accvgpr_write_b32 a91, v240\n"
  "v_accvgpr_read_b32 v240, a92\n"
  "v_mul_f32 v240, v240, v239\n"
  "v_accvgpr_write_b32 a92, v240\n"
  "v_accvgpr_read_b32 v240, a93\n"
  "v_mul_f32 v240, v240, v239\n"
  "v_accvgpr_write_b32 a93, v240\n"
  "v_accvgpr_read_b32 v240, a94\n"
  "v_mul_f32 v240, v240, v239\n"
  "v_accvgpr_write_b32 a94, v240\n"
  "v_accvgpr_read_b32 v240, a95\n"
  "v_mul_f32 v240, v240, v239\n"
  "v_accvgpr_write_b32 a95, v240\n"
  "v_accvgpr_read_b32 v240, a96\n"
  "v_mul_f32 v240, v240, v239\n"
  "v_accvgpr_write_b32 a96, v240\n"
  "v_accvgpr_read_b32 v240, a97\n"
  "v_mul_f32 v240, v240, v239\n"
  "v_accvgpr_write_b32 a97, v240\n"
  "v_accvgpr_read_b32 v240, a98\n"
  "v_mul_f32 v240, v240, v239\n"
  "v_accvgpr_write_b32 a98, v240\n"
  "v_accvgpr_read_b32 v240, a99\n"
  "v_mul_f32 v240, v240, v239\n"
  "v_accvgpr_write_b32 a99, v240\n"
  "v_accvgpr_read_b32 v240, a100\n"
  "v_mul_f32 v240, v240, v239\n"
  "v_accvgpr_write_b32 a100, v240\n"
  "v_accvgpr_read_b32 v240, a101\n"
  "v_mul_f32 v240, v240, v239\n"
  "v_accvgpr_write_b32 a101, v240\n"
  "v_accvgpr_read_b32 v240, a102\n"
  "v_mul_f32 v240, v240, v239\n"
  "v_accvgpr_write_b32 a102, v240\n"
  "v_accvgpr_read_b32 v240, a103\n"
  "v_mul_f32 v240, v240, v239\n"
  "v_accvgpr_write_b32 a103, v240\n"
  "v_accvgpr_read_b32 v240, a104\n"
  "v_mul_f32 v240, v240, v239\n"
  "v_accvgpr_write_b32 a104, v240\n"
  "v_accvgpr_read_b32 v240, a105\n"
  "v_mul_f32 v240, v240, v239\n"
  "v_accvgpr_write_b32 a105, v240\n"
  "v_accvgpr_read_b32 v240, a106\n"
  "v_mul_f32 v240, v240, v239\n"
  "v_accvgpr_write_b32 a106, v240\n"
  "v_accvgpr_read_b32 v240, a107\n"
  "v_mul_f32 v240, v240, v239\n"
  "v_accvgpr_write_b32 a107, v240\n"
  "v_accvgpr_read_b32 v240, a108\n"
  "v_mul_f32 v240, v240, v239\n"
  "v_accvgpr_write_b32 a108, v240\n"
  "v_accvgpr_read_b32 v240, a109\n"
  "v_mul_f32 v240, v240, v239\n"
  "v_accvgpr_write_b32 a109, v240\n"
  "v_accvgpr_read_b32 v240, a110\n"
  "v_mul_f32 v240, v240, v239\n"
  "v_accvgpr_write_b32 a110, v240\n"
  "v_accvgpr_read_b32 v240, a111\n"
  "v_mul_f32 v240, v240, v239\n"
  "v_accvgpr_write_b32 a111, v240\n"
  "v_accvgpr_read_b32 v240, a112\n"
  "v_mul_f32 v240, v240, v239\n"
  "v_accvgpr_write_b32 a112, v240\n"
  "v_accvgpr_read_b32 v240, a113\n"
  "v_mul_f32 v240, v240, v239\n"
  "v_accvgpr_write_b32 a113, v240\n"
  "v_accvgpr_read_b32 v240, a114\n"
  "v_mul_f32 v240, v240, v239\n"
  "v_accvgpr_write_b32 a114, v240\n"
  "v_accvgpr_read_b32 v240, a115\n"
  "v_mul_f32 v240, v240, v239\n"
  "v_accvgpr_write_b32 a115, v240\n"
  "v_accvgpr_read_b32 v240, a116\n"
  "v_mul_f32 v240, v240, v239\n"
  "v_accvgpr_write_b32 a116, v240\n"
  "v_accvgpr_read_b32 v240, a117\n"
  "v_mul_f32 v240, v240, v239\n"
  "v_accvgpr_write_b32 a117, v240\n"
  "v_accvgpr_read_b32 v240, a118\n"
  "v_mul_f32 v240, v240, v239\n"
  "v_accvgpr_write_b32 a118, v240\n"
  "v_accvgpr_read_b32 v240, a119\n"
  "v_mul_f32 v240, v240, v239\n"
  "v_accvgpr_write_b32 a119, v240\n"
  "v_accvgpr_read_b32 v240, a120\n"
  "v_mul_f32 v240, v240, v239\n"
  "v_accvgpr_write_b32 a120, v240\n"
  "v_accvgpr_read_b32 v240, a121\n"
  "v_mul_f32 v240, v240, v239\n"
  "v_accvgpr_write_b32 a121, v240\n"
  "v_accvgpr_read_b32 v240, a122\n"
  "v_mul_f32 v240, v240, v239\n"
  "v_accvgpr_write_b32 a122, v240\n"
  "v_accvgpr_read_b32 v240, a123\n"
  "v_mul_f32 v240, v240, v239\n"
  "v_accvgpr_write_b32 a123, v240\n"
  "v_accvgpr_read_b32 v240, a124\n"
  "v_mul_f32 v240, v240, v239\n"
  "v_accvgpr_write_b32 a124, v240\n"
  "v_accvgpr_read_b32 v240, a125\n"
  "v_mul_f32 v240, v240, v239\n"
  "v_accvgpr_write_b32 a125, v240\n"
  "v_accvgpr_read_b32 v240, a126\n"
  "v_mul_f32 v240, v240, v239\n"
  "v_accvgpr_write_b32 a126, v240\n"
  "v_accvgpr_read_b32 v240, a127\n"
  "v_mul_f32 v240, v240, v239\n"
  "v_accvgpr_write_b32 a127, v240\n"
  "v_sub_f32 v144, 0, v233\n"
  "v_sub_f32 v145, 0, v233\n"
  "v_sub_f32 v146, 0, v233\n"
  "v_sub_f32 v147, 0, v233\n"
  "v_sub_f32 v148, 0, v233\n"
  "v_sub_f32 v149, 0, v233\n"
  "v_sub_f32 v150, 0, v233\n"
  "v_sub_f32 v151, 0, v233\n"
  "v_sub_f32 v152, 0, v233\n"
  "v_sub_f32 v153, 0, v233\n"
  "v_sub_f32 v154, 0, v233\n"
  "v_sub_f32 v155, 0, v233\n"
  "v_sub_f32 v156, 0, v233\n"
  "v_sub_f32 v157, 0, v233\n"
  "v_sub_f32 v158, 0, v233\n"
  "v_sub_f32 v159, 0, v233\n"
  "v_sub_f32 v64, v64, v238\n"
  "v_sub_f32 v65, v65, v238\n"
  "v_sub_f32 v66, v66, v238\n"
  "v_sub_f32 v67, v67, v238\n"
  "v_sub_f32 v68, v68, v238\n"
  "v_sub_f32 v69, v69, v238\n"
  "v_sub_f32 v70, v70, v238\n"
  "v_sub_f32 v71, v71, v238\n"
  "v_sub_f32 v72, v72, v238\n"
  "v_sub_f32 v73, v73, v238\n"
  "v_sub_f32 v74, v74, v238\n"
  "v_sub_f32 v75, v75, v238\n"
  "v_sub_f32 v76, v76, v238\n"
  "v_sub_f32 v77, v77, v238\n"
  "v_sub_f32 v78, v78, v238\n"
  "v_sub_f32 v79, v79, v238\n"
  "v_sub_f32 v80, v80, v238\n"
  "v_sub_f32 v81, v81, v238\n"
  "v_sub_f32 v82, v82, v238\n"
  "v_sub_f32 v83, v83, v238\n"
  "v_sub_f32 v84, v84, v238\n"
  "v_sub_f32 v85, v85, v238\n"
  "v_sub_f32 v86, v86, v238\n"
  "v_sub_f32 v87, v87, v238\n"
  "v_sub_f32 v88, v88, v238\n"
  "v_sub_f32 v89, v89, v238\n"
  "v_sub_f32 v90, v90, v238\n"
  "v_sub_f32 v91, v91, v238\n"
  "v_sub_f32 v92, v92, v238\n"
  "v_sub_f32 v93, v93, v238\n"
  "v_sub_f32 v94, v94, v238\n"
  "v_sub_f32 v95, v95, v238\n"
  "v_sub_f32 v32, v32, v238\n"
  "v_sub_f32 v33, v33, v238\n"
  "v_sub_f32 v34, v34, v238\n"
  "v_sub_f32 v35, v35, v238\n"
  "v_sub_f32 v36, v36, v238\n"
  "v_sub_f32 v37, v37, v238\n"
  "v_sub_f32 v38, v38, v238\n"
  "v_sub_f32 v39, v39, v238\n"
  "v_sub_f32 v40, v40, v238\n"
  "v_sub_f32 v41, v41, v238\n"
  "v_sub_f32 v42, v42, v238\n"
  "v_sub_f32 v43, v43, v238\n"
  "v_sub_f32 v44, v44, v238\n"
  "v_sub_f32 v45, v45, v238\n"
  "v_sub_f32 v46, v46, v238\n"
  "v_sub_f32 v47, v47, v238\n"
  "v_sub_f32 v48, v48, v238\n"
  "v_sub_f32 v49, v49, v238\n"
  "v_sub_f32 v50, v50, v238\n"
  "v_sub_f32 v51, v51, v238\n"
  "v_sub_f32 v52, v52, v238\n"
  "v_sub_f32 v53, v53, v238\n"
  "v_sub_f32 v54, v54, v238\n"
  "v_sub_f32 v55, v55, v238\n"
  "v_sub_f32 v56, v56, v238\n"
  "v_sub_f32 v57, v57, v238\n"
  "v_sub_f32 v58, v58, v238\n"
  "v_sub_f32 v59, v59, v238\n"
  "v_sub_f32 v60, v60, v238\n"
  "v_sub_f32 v61, v61, v238\n"
  "v_sub_f32 v62, v62, v238\n"
  "v_sub_f32 v63, v63, v238\n"
  "v_exp_f32 v240, v64\n"
  "v_exp_f32 v241, v65\n"
  "s_nop 0\n"
  "v_add_f32 v229, v240, v241\n"
  "v_cvt_pk_bf16_f32 v112, v240, v241\n"
  "v_exp_f32 v240, v66\n"
  "v_exp_f32 v241, v67\n"
  "s_nop 0\n"
  "v_add_f32 v229, v229, v240\n"
  "v_add_f32 v229, v229, v241\n"
  "v_cvt_pk_bf16_f32 v113, v240, v241\n"
  "v_exp_f32 v240, v68\n"
  "v_exp_f32 v241, v69\n"
  "s_nop 0\n"
  "v_add_f32 v229, v229, v240\n"
  "v_add_f32 v229, v229, v241\n"
  "v_cvt_pk_bf16_f32 v114, v240, v241\n"
  "v_exp_f32 v240, v70\n"
  "v_exp_f32 v241, v71\n"
  "s_nop 0\n"
  "v_add_f32 v229, v229, v240\n"
  "v_add_f32 v229, v229, v241\n"
  "v_cvt_pk_bf16_f32 v115, v240, v241\n"
  "v_exp_f32 v240, v72\n"
  "v_exp_f32 v241, v73\n"
  "s_nop 0\n"
  "v_add_f32 v229, v229, v240\n"
  "v_add_f32 v229, v229, v241\n"
  "v_cvt_pk_bf16_f32 v116, v240, v241\n"
  "v_exp_f32 v240, v74\n"
  "v_exp_f32 v241, v75\n"
  "s_nop 0\n"
  "v_add_f32 v229, v229, v240\n"
  "v_add_f32 v229, v229, v241\n"
  "v_cvt_pk_bf16_f32 v117, v240, v241\n"
  "v_exp_f32 v240, v76\n"
  "v_exp_f32 v241, v77\n"
  "s_nop 0\n"
  "v_add_f32 v229, v229, v240\n"
  "v_add_f32 v229, v229, v241\n"
  "v_cvt_pk_bf16_f32 v118, v240, v241\n"
  "v_exp_f32 v240, v78\n"
  "v_exp_f32 v241, v79\n"
  "s_nop 0\n"
  "v_add_f32 v229, v229, v240\n"
  "v_add_f32 v229, v229, v241\n"
  "v_cvt_pk_bf16_f32 v119, v240, v241\n"
  "v_exp_f32 v240, v80\n"
  "v_exp_f32 v241, v81\n"
  "s_nop 0\n"
  "v_add_f32 v229, v229, v240\n"
  "v_add_f32 v229, v229, v241\n"
  "v_cvt_pk_bf16_f32 v120, v240, v241\n"
  "v_exp_f32 v240, v82\n"
  "v_exp_f32 v241, v83\n"
  "s_nop 0\n"
  "v_add_f32 v229, v229, v240\n"
  "v_add_f32 v229, v229, v241\n"
  "v_cvt_pk_bf16_f32 v121, v240, v241\n"
  "v_exp_f32 v240, v84\n"
  "v_exp_f32 v241, v85\n"
  "s_nop 0\n"
  "v_add_f32 v229, v229, v240\n"
  "v_add_f32 v229, v229, v241\n"
  "v_cvt_pk_bf16_f32 v122, v240, v241\n"
  "v_exp_f32 v240, v86\n"
  "v_exp_f32 v241, v87\n"
  "s_nop 0\n"
  "v_add_f32 v229, v229, v240\n"
  "v_add_f32 v229, v229, v241\n"
  "v_cvt_pk_bf16_f32 v123, v240, v241\n"
  "v_exp_f32 v240, v88\n"
  "v_exp_f32 v241, v89\n"
  "s_nop 0\n"
  "v_add_f32 v229, v229, v240\n"
  "v_add_f32 v229, v229, v241\n"
  "v_cvt_pk_bf16_f32 v124, v240, v241\n"
  "v_exp_f32 v240, v90\n"
  "v_exp_f32 v241, v91\n"
  "s_nop 0\n"
  "v_add_f32 v229, v229, v240\n"
  "v_add_f32 v229, v229, v241\n"
  "v_cvt_pk_bf16_f32 v125, v240, v241\n"
  "v_exp_f32 v240, v92\n"
  "v_exp_f32 v241, v93\n"
  "s_nop 0\n"
  "v_add_f32 v229, v229, v240\n"
  "v_add_f32 v229, v229, v241\n"
  "v_cvt_pk_bf16_f32 v126, v240, v241\n"
  "v_exp_f32 v240, v94\n"
  "v_exp_f32 v241, v95\n"
  "s_nop 0\n"
  "v_add_f32 v229, v229, v240\n"
  "v_add_f32 v229, v229, v241\n"
  "v_cvt_pk_bf16_f32 v127, v240, v241\n"
  "s_nop 15\n"
  "s_nop 15\n"
  "s_cmp_eq_u32 s48, 4\n"
  "s_cbranch_scc1 Lret_SY\n"
  "s_cmp_eq_u32 s48, 6\n"
  "s_cbranch_scc1 Lret_NY\n"
  "s_cmp_eq_u32 s48, 8\n"
  "s_cbranch_scc1 Lret_drainY\n"
  "s_endpgm\n"
  "Lfb_DY:\n"
  "s_mov_b32 s48, 0\n"
  "s_branch Lfbv_Yd\n"
  "Lfb_DX:\n"
  "s_mov_b32 s48, 1\n"
  "s_branch Lfbv_Xd\n"
  "Lfb_MY:\n"
  "s_mov_b32 s48, 2\n"
  "s_branch Lfbv_Yd\n"
  "Lfb_MX:\n"
  "s_mov_b32 s48, 3\n"
  "s_branch Lfbv_Xd\n"
  "Lfb_SY:\n"
  "s_mov_b32 s48, 4\n"
  "s_branch Lfbv_Ys\n"
  "Lfb_SX:\n"
  "s_mov_b32 s48, 5\n"
  "s_branch Lfbv_Xs\n"
  "Lfb_NY:\n"
  "s_mov_b32 s48, 6\n"
  "s_branch Lfbv_Ys\n"
  "Lfb_NX:\n"
  "s_mov_b32 s48, 7\n"
  "s_branch Lfbv_Xs\n"
  "Lfb_drainY:\n"
  "s_mov_b32 s48, 8\n"
  "s_branch Lfbv_Ys\n"
  "Lend:\n"
  : : "s"(kp), "s"(bid), "s"(wid), "s"(tag) : "v0", "v1", "v2", "v3", "v4", "v5", "v6", "v7", "v8", "v9", "v10", "v11", "v12", "v13", "v14", "v15", "v16", "v17", "v18", "v19", "v20", "v21", "v22", "v23", "v24", "v25", "v26", "v27", "v28", "v29", "v30", "v31", "v32", "v33", "v34", "v35", "v36", "v37", "v38", "v39", "v40", "v41", "v42", "v43", "v44", "v45", "v46", "v47", "v48", "v49", "v50", "v51", "v52", "v53", "v54", "v55", "v56", "v57", "v58", "v59", "v60", "v61", "v62", "v63", "v64", "v65", "v66", "v67", "v68", "v69", "v70", "v71", "v72", "v73", "v74", "v75", "v76", "v77", "v78", "v79", "v80", "v81", "v82", "v83", "v84", "v85", "v86", "v87", "v88", "v89", "v90", "v91", "v92", "v93", "v94", "v95", "v96", "v97", "v98", "v99", "v100", "v101", "v102", "v103", "v104", "v105", "v106", "v107", "v108", "v109", "v110", "v111", "v112", "v113", "v114", "v115", "v116", "v117", "v118", "v119", "v120", "v121", "v122", "v123", "v124", "v125", "v126", "v127", "v128", "v129", "v130", "v131", "v132", "v133", "v134", "v135", "v136", "v137", "v138", "v139", "v140", "v141", "v142", "v143", "v144", "v145", "v146", "v147", "v148", "v149", "v150", "v151", "v152", "v153", "v154", "v155", "v156", "v157", "v158", "v159", "v160", "v161", "v162", "v163", "v164", "v165", "v166", "v167", "v168", "v169", "v170", "v171", "v172", "v173", "v174", "v175", "v176", "v177", "v178", "v179", "v180", "v181", "v182", "v183", "v184", "v185", "v186", "v187", "v188", "v189", "v190", "v191", "v192", "v193", "v194", "v195", "v196", "v197", "v198", "v199", "v200", "v201", "v202", "v203", "v204", "v205", "v206", "v207", "v208", "v209", "v210", "v211", "v212", "v213", "v214", "v215", "v216", "v217", "v218", "v219", "v220", "v221", "v222", "v223", "v224", "v225", "v226", "v227", "v228", "v229", "v230", "v231", "v232", "v233", "v234", "v235", "v236", "v237", "v238", "v239", "v240", "v241", "v242", "v243", "v244", "v245", "v246", "v247", "v248", "v249", "v250", "v251", "v252", "v253", "v254", "v255", "a0", "a1", "a2", "a3", "a4", "a5", "a6", "a7", "a8", "a9", "a10", "a11", "a12", "a13", "a14", "a15", "a16", "a17", "a18", "a19", "a20", "a21", "a22", "a23", "a24", "a25", "a26", "a27", "a28", "a29", "a30", "a31", "a32", "a33", "a34", "a35", "a36", "a37", "a38", "a39", "a40", "a41", "a42", "a43", "a44", "a45", "a46", "a47", "a48", "a49", "a50", "a51", "a52", "a53", "a54", "a55", "a56", "a57", "a58", "a59", "a60", "a61", "a62", "a63", "a64", "a65", "a66", "a67", "a68", "a69", "a70", "a71", "a72", "a73", "a74", "a75", "a76", "a77", "a78", "a79", "a80", "a81", "a82", "a83", "a84", "a85", "a86", "a87", "a88", "a89", "a90", "a91", "a92", "a93", "a94", "a95", "a96", "a97", "a98", "a99", "a100", "a101", "a102", "a103", "a104", "a105", "a106", "a107", "a108", "a109", "a110", "a111", "a112", "a113", "a114", "a115", "a116", "a117", "a118", "a119", "a120", "a121", "a122", "a123", "a124", "a125", "a126", "a127", "a128", "a129", "a130", "a131", "a132", "a133", "a134", "a135", "a136", "a137", "a138", "a139", "a140", "a141", "a142", "a143", "a144", "a145", "a146", "a147", "a148", "a149", "a150", "a151", "a152", "a153", "a154", "a155", "a156", "a157", "a158", "a159", "a160", "a161", "a162", "a163", "a164", "a165", "a166", "a167", "a168", "a169", "a170", "a171", "a172", "a173", "a174", "a175", "a176", "a177", "a178", "a179", "a180", "a181", "a182", "a183", "a184", "a185", "a186", "a187", "a188", "a189", "a190", "a191", "a192", "a193", "a194", "a195", "a196", "a197", "a198", "a199", "a200", "a201", "a202", "a203", "a204", "a205", "a206", "a207", "a208", "a209", "a210", "a211", "a212", "a213", "a214", "a215", "a216", "a217", "a218", "a219", "a220", "a221", "a222", "a223", "a224", "a225", "a226", "a227", "a228", "a229", "a230", "a231", "a232", "a233", "a234", "a235", "a236", "a237", "a238", "a239", "a240", "a241", "a242", "a243", "a244", "a245", "a246", "a247", "a248", "a249", "a250", "a251", "a252", "a253", "a254", "a255", "s8", "s9", "s10", "s11", "s12", "s13", "s14", "s15", "s16", "s17", "s18", "s19", "s20", "s21", "s22", "s23", "s24", "s25", "s26", "s27", "s28", "s29", "s30", "s31", "s32", "s33", "s34", "s35", "s36", "s37", "s38", "s39", "s40", "s41", "s42", "s43", "s44", "s45", "s46", "s47", "s48", "s49", "s50", "s51", "s52", "s53", "s54", "s55", "s56", "s57", "s58", "s59", "s60", "s61", "s62", "s63", "s64", "s65", "s66", "s67", "s68", "s69", "s70", "s71", "s72", "s73", "s74", "s75", "s92", "s93", "s94", "s95", "vcc", "scc", "memory");
  u64 gy[5]; u32x4 gq[3];
  asm volatile(
  "s_mov_b64 s[8:9], %8\n"
  "s_mov_b32 s10, %9\n"
  "s_mov_b32 s11, %10\n"
  "s_and_b32 s12, s10, 7\n"
  "s_lshl_b32 s12, s12, 2\n"
  "s_lshr_b32 s13, s10, 3\n"
  "s_and_b32 s13, s13, 3\n"
  "s_add_u32 s12, s12, s13\n"
  "s_lshr_b32 s13, s10, 5\n"
  "s_load_dwordx2 s[14:15], s[8:9], 104\n"
  "s_mov_b32 s71, %11\n"
  "s_waitcnt lgkmcnt(0)\n"
  "s_add_u32 s92, s14, 83886080\n"
  "s_addc_u32 s93, s15, 0\n"
  "s_and_b32 s93, s93, 0xffff\n"
  "s_mov_b32 s94, 65536\n"
  "s_mov_b32 s95, 131072\n"
  "s_load_dwordx2 s[58:59], s[8:9], 88\n"
  "s_load_dwordx2 s[60:61], s[8:9], 96\n"
  "v_mbcnt_lo_u32_b32 v0, -1, 0\n"
  "v_mbcnt_hi_u32_b32 v0, -1, v0\n"
  "s_and_b32 s40, s10, 3\n"
  "s_bfe_u32 s41, s10, 0x10002\n"
  "s_lshl_b32 s41, s41, 3\n"
  "s_bfe_u32 s62, s10, 0x30003\n"
  "s_add_u32 s41, s41, s62\n"
  "s_lshr_b32 s42, s10, 6\n"
  "v_lshlrev_b32 v1, 4, v0\n"
  "s_mul_i32 s62, s11, 288\n"
  "v_add_u32 v12, s62, v0\n"
  "v_mul_u32_u24 v13, 3641, v12\n"
  "v_lshrrev_b32 v13, 16, v13\n"
  "v_mul_u32_u24 v14, 18, v13\n"
  "v_sub_u32 v14, v12, v14\n"
  "v_lshlrev_b32 v14, 4, v14\n"
  "v_mul_u32_u24 v13, 1152, v13\n"
  "v_add_u32 v2, v13, v14\n"
  "v_add_u32 v12, s62, v0\n"
  "v_add_u32 v12, 64, v12\n"
  "v_mul_u32_u24 v13, 3641, v12\n"
  "v_lshrrev_b32 v13, 16, v13\n"
  "v_mul_u32_u24 v14, 18, v13\n"
  "v_sub_u32 v14, v12, v14\n"
  "v_lshlrev_b32 v14, 4, v14\n"
  "v_mul_u32_u24 v13, 1152, v13\n"
  "v_add_u32 v3, v13, v14\n"
  "v_add_u32 v12, s62, v0\n"
  "v_add_u32 v12, 128, v12\n"
  "v_mul_u32_u24 v13, 3641, v12\n"
  "v_lshrrev_b32 v13, 16, v13\n"
  "v_mul_u32_u24 v14, 18, v13\n"
  "v_sub_u32 v14, v12, v14\n"
  "v_lshlrev_b32 v14, 4, v14\n"
  "v_mul_u32_u24 v13, 1152, v13\n"
  "v_add_u32 v4, v13, v14\n"
  "v_add_u32 v12, s62, v0\n"
  "v_add_u32 v12, 192, v12\n"
  "v_mul_u32_u24 v13, 3641, v12\n"
  "v_lshrrev_b32 v13, 16, v13\n"
  "v_mul_u32_u24 v14, 18, v13\n"
  "v_sub_u32 v14, v12, v14\n"
  "v_lshlrev_b32 v14, 4, v14\n"
  "v_mul_u32_u24 v13, 1152, v13\n"
  "v_add_u32 v5, v13, v14\n"
  "v_add_u32 v12, s62, v0\n"
  "v_add_u32 v12, 256, v12\n"
  "v_mul_u32_u24 v13, 3641, v12\n"
  "v_lshrrev_b32 v13, 16, v13\n"
  "v_mul_u32_u24 v14, 18, v13\n"
  "v_sub_u32 v14, v12, v14\n"
  "v_lshlrev_b32 v14, 4, v14\n"
  "v_mul_u32_u24 v13, 1152, v13\n"
  "v_add_u32 v6, v13, v14\n"
  "v_lshrrev_b32 v12, 5, v0\n"
  "v_lshlrev_b32 v12, 4, v12\n"
  "v_bfe_u32 v13, v0, 4, 1\n"
  "v_lshlrev_b32 v13, 2, v13\n"
  "v_bfe_u32 v14, v0, 2, 2\n"
  "v_add3_u32 v12, v12, v13, v14\n"
  "s_and_b32 s63, s11, 1\n"
  "s_lshl_b32 s64, s63, 5\n"
  "v_add_u32 v12, s64, v12\n"
  "v_mul_u32_u24 v12, 288, v12\n"
  "v_and_b32 v13, 3, v0\n"
  "v_lshlrev_b32 v13, 3, v13\n"
  "v_add_u32 v8, v12, v13\n"
  "v_lshrrev_b32 v12, 5, v0\n"
  "v_lshlrev_b32 v12, 10, v12\n"
  "v_bfe_u32 v13, v0, 4, 1\n"
  "v_lshlrev_b32 v13, 9, v13\n"
  "v_and_b32 v14, 15, v0\n"
  "v_lshlrev_b32 v14, 4, v14\n"
  "v_add3_u32 v12, v12, v13, v14\n"
  "s_lshl_b32 s64, s63, 11\n"
  "s_lshl_b32 s65, s11, 12\n"
  "s_add_u32 s64, s64, s65\n"
  "s_add_u32 s64, s64, 18432\n"
  "v_add_u32 v9, s64, v12\n"
  "s_xor_b32 s65, s65, 0x1000\n"
  "s_lshl_b32 s64, s63, 11\n"
  "s_add_u32 s64, s64, s65\n"
  "s_add_u32 s64, s64, 18432\n"
  "v_add_u32 v10, s64, v12\n"
  "v_add_u32 v28, 69632, v8\n"
  "v_add_u32 v29, 69632, v9\n"
  "v_add_u32 v30, 69632, v10\n"
  "s_waitcnt lgkmcnt(0)\n"
  "s_mov_b32 s16, s58\n"
  "s_mov_b32 s17, s59\n"
  "s_and_b32 s17, s17, 0xffff\n"
  "s_mov_b32 s18, 4718592\n"
  "s_mov_b32 s19, 131072\n"
  "s_mov_b32 s28, s60\n"
  "s_mov_b32 s29, s61\n"
  "s_and_b32 s29, s29, 0xffff\n"
  "s_mov_b32 s30, 4718592\n"
  "s_mov_b32 s31, 131072\n"
  "s_add_u32 s20, s14, 33554432\n"
  "s_addc_u32 s21, s15, 0\n"
  "s_and_b32 s21, s21, 0xffff\n"
  "s_mov_b32 s22, 16777216\n"
  "s_mov_b32 s23, 131072\n"
  "s_add_u32 s24, s14, 50331648\n"
  "s_addc_u32 s25, s15, 0\n"
  "s_and_b32 s25, s25, 0xffff\n"
  "s_mov_b32 s26, 25165824\n"
  "s_mov_b32 s27, 131072\n"
  "s_mul_i32 s36, s40, 1179648\n"
  "s_mul_i32 s58, s42, 288\n"
  "s_add_u32 s36, s36, s58\n"
  "s_lshl_b32 s37, s40, 9\n"
  "s_lshl_b32 s58, s41, 2\n"
  "s_add_u32 s37, s37, s58\n"
  "s_add_u32 s37, s37, s11\n"
  "s_lshl_b32 s37, s37, 13\n"
  "s_mul_i32 s38, s11, 4608\n"
  "s_lshl_b32 s39, s11, 12\n"
  "s_barrier\n"
  "s_add_u32 m0, s38, 0\n"
  "s_nop 0\n"
  "buffer_load_dwordx4 v2, s[16:19], s36 offen lds\n"
  "s_add_u32 m0, s38, 1024\n"
  "s_nop 0\n"
  "buffer_load_dwordx4 v3, s[16:19], s36 offen lds\n"
  "s_add_u32 m0, s38, 2048\n"
  "s_nop 0\n"
  "buffer_load_dwordx4 v4, s[16:19], s36 offen lds\n"
  "s_add_u32 m0, s38, 3072\n"
  "s_nop 0\n"
  "buffer_load_dwordx4 v5, s[16:19], s36 offen lds\n"
  "s_add_u32 m0, s38, 4096\n"
  "s_mov_b32 exec_hi, 0\n"
  "buffer_load_dwordx4 v6, s[16:19], s36 offen lds\n"
  "s_mov_b32 exec_hi, -1\n"
  "s_add_u32 s36, s36, 73728\n"
  "s_add_u32 m0, s38, 34816\n"
  "s_nop 0\n"
  "buffer_load_dwordx4 v2, s[16:19], s36 offen lds\n"
  "s_add_u32 m0, s38, 35840\n"
  "s_nop 0\n"
  "buffer_load_dwordx4 v3, s[16:19], s36 offen lds\n"
  "s_add_u32 m0, s38, 36864\n"
  "s_nop 0\n"
  "buffer_load_dwordx4 v4, s[16:19], s36 offen lds\n"
  "s_add_u32 m0, s38, 37888\n"
  "s_nop 0\n"
  "buffer_load_dwordx4 v5, s[16:19], s36 offen lds\n"
  "s_add_u32 m0, s38, 38912\n"
  "s_mov_b32 exec_hi, 0\n"
  "buffer_load_dwordx4 v6, s[16:19], s36 offen lds\n"
  "s_mov_b32 exec_hi, -1\n"
  "s_add_u32 s36, s36, 73728\n"
  "s_add_u32 m0, s38, 69632\n"
  "s_nop 0\n"
  "buffer_load_dwordx4 v2, s[16:19], s36 offen lds\n"
  "s_add_u32 m0, s38, 70656\n"
  "s_nop 0\n"
  "buffer_load_dwordx4 v3, s[16:19], s36 offen lds\n"
  "s_add_u32 m0, s38, 71680\n"
  "s_nop 0\n"
  "buffer_load_dwordx4 v4, s[16:19], s36 offen lds\n"
  "s_add_u32 m0, s38, 72704\n"
  "s_nop 0\n"
  "buffer_load_dwordx4 v5, s[16:19], s36 offen lds\n"
  "s_add_u32 m0, s38, 73728\n"
  "s_mov_b32 exec_hi, 0\n"
  "buffer_load_dwordx4 v6, s[16:19], s36 offen lds\n"
  "s_mov_b32 exec_hi, -1\n"
  "s_add_u32 s36, s36, 73728\n"
  "s_add_u32 m0, s38, 104448\n"
  "s_nop 0\n"
  "buffer_load_dwordx4 v2, s[16:19], s36 offen lds\n"
  "s_add_u32 m0, s38, 105472\n"
  "s_nop 0\n"
  "buffer_load_dwordx4 v3, s[16:19], s36 offen lds\n"
  "s_add_u32 m0, s38, 106496\n"
  "s_nop 0\n"
  "buffer_load_dwordx4 v4, s[16:19], s36 offen lds\n"
  "s_add_u32 m0, s38, 107520\n"
  "s_nop 0\n"
  "buffer_load_dwordx4 v5, s[16:19], s36 offen lds\n"
  "s_add_u32 m0, s38, 108544\n"
  "s_mov_b32 exec_hi, 0\n"
  "buffer_load_dwordx4 v6, s[16:19], s36 offen lds\n"
  "s_mov_b32 exec_hi, -1\n"
  "s_add_u32 s36, s36, 73728\n"
  "s_sub_u32 s58, 15, s41\n"
  "s_min_u32 s58, s58, s41\n"
  "s_lshl_b32 s58, s58, 2\n"
  "s_add_u32 s58, s58, s40\n"
  "s_lshl_b32 s58, s58, 5\n"
  "s_add_u32 s58, s58, 12288\n"
  "s_cmp_lg_u32 s11, 0\n"
  "s_cbranch_scc1 Lfw_c_d\n"
  "v_lshlrev_b32 v12, 2, v0\n"
  "s_mov_b64 exec, 0xff\n"
  "Lfw_c:\n"
  "buffer_load_dword v13, v12, s[92:95], s58 offen sc1\n"
  "s_waitcnt vmcnt(0)\n"
  "v_cmp_ne_u32 vcc, s71, v13\n"
  "s_nop 4\n"
  "s_cbranch_vccz Lfw_c_k\n"
  "s_sleep 2\n"
  "s_branch Lfw_c\n"
  "Lfw_c_k:\n"
  "s_mov_b64 exec, -1\n"
  "Lfw_c_d:\n"
  "s_barrier\n"
  "s_add_u32 m0, s39, 18432\n"
  "s_nop 0\n"
  "buffer_load_dwordx4 v1, s[20:23], s37 offen sc1 lds\n"
  "buffer_load_dwordx4 v1, s[20:23], s37 offen offset:1024 sc1 lds\n"
  "buffer_load_dwordx4 v1, s[20:23], s37 offen offset:2048 sc1 lds\n"
  "buffer_load_dwordx4 v1, s[20:23], s37 offen offset:3072 sc1 lds\n"
  "s_add_u32 s37, s37, 4096\n"
  "s_add_u32 m0, s39, 53248\n"
  "s_nop 0\n"
  "buffer_load_dwordx4 v1, s[20:23], s37 offen sc1 lds\n"
  "buffer_load_dwordx4 v1, s[20:23], s37 offen offset:1024 sc1 lds\n"
  "buffer_load_dwordx4 v1, s[20:23], s37 offen offset:2048 sc1 lds\n"
  "buffer_load_dwordx4 v1, s[20:23], s37 offen offset:3072 sc1 lds\n"
  "s_add_u32 s37, s37, 520192\n"
  "s_add_u32 m0, s39, 88064\n"
  "s_nop 0\n"
  "buffer_load_dwordx4 v1, s[20:23], s37 offen sc1 lds\n"
  "buffer_load_dwordx4 v1, s[20:23], s37 offen offset:1024 sc1 lds\n"
  "buffer_load_dwordx4 v1, s[20:23], s37 offen offset:2048 sc1 lds\n"
  "buffer_load_dwordx4 v1, s[20:23], s37 offen offset:3072 sc1 lds\n"
  "s_add_u32 s37, s37, 4096\n"
  "s_add_u32 m0, s39, 122880\n"
  "s_nop 0\n"
  "buffer_load_dwordx4 v1, s[20:23], s37 offen sc1 lds\n"
  "buffer_load_dwordx4 v1, s[20:23], s37 offen offset:1024 sc1 lds\n"
  "buffer_load_dwordx4 v1, s[20:23], s37 offen offset:2048 sc1 lds\n"
  "buffer_load_dwordx4 v1, s[20:23], s37 offen offset:3072 sc1 lds\n"
  "s_add_u32 s37, s37, 520192\n"
  "s_waitcnt vmcnt(12)\n"
  "s_barrier\n"
  "s_lshl_b32 s58, s10, 2\n"
  "s_add_u32 s58, s58, 16384\n"
  "v_mov_b32 v12, s71\n"
  "s_mov_b64 exec, 1\n"
  "buffer_store_dword v12, off, s[92:95], s58 sc1\n"
  "s_mov_b64 exec, -1\n"
  "ds_read_b128 a[36:39], v9 offset:0\n"
  "ds_read_b64_tr_b16 a[0:1], v8 offset:0\n"
  "ds_read_b64_tr_b16 a[2:3], v8 offset:2304\n"
  "ds_read_b128 a[40:43], v9 offset:256\n"
  "ds_read_b128 a[44:47], v10 offset:0\n"
  "ds_read_b128 a[48:51], v10 offset:256\n"
  "ds_read_b64_tr_b16 a[4:5], v8 offset:32\n"
  "ds_read_b64_tr_b16 a[6:7], v8 offset:2336\n"
  "ds_read_b64_tr_b16 a[8:9], v8 offset:64\n"
  "ds_read_b64_tr_b16 a[10:11], v8 offset:2368\n"
  "ds_read_b64_tr_b16 a[12:13], v8 offset:96\n"
  "ds_read_b64_tr_b16 a[14:15], v8 offset:2400\n"
  "ds_read_b64_tr_b16 a[16:17], v8 offset:128\n"
  "ds_read_b64_tr_b16 a[18:19], v8 offset:2432\n"
  "ds_read_b64_tr_b16 a[20:21], v8 offset:160\n"
  "ds_read_b64_tr_b16 a[22:23], v8 offset:2464\n"
  "ds_read_b64_tr_b16 a[24:25], v8 offset:192\n"
  "ds_read_b64_tr_b16 a[26:27], v8 offset:2496\n"
  "ds_read_b64_tr_b16 a[28:29], v8 offset:224\n"
  "ds_read_b64_tr_b16 a[30:31], v8 offset:2528\n"
  "ds_read_b64_tr_b16 a[32:33], v8 offset:256\n"
  "ds_read_b64_tr_b16 a[34:35], v8 offset:2560\n"
  "s_waitcnt vmcnt(8)\n"
  "s_waitcnt lgkmcnt(0)\n"
  "s_barrier\n"
  "v_mfma_f32_16x16x32_bf16 v[64:67], a[0:3], a[36:39], 0\n"
  "ds_read_b128 a[88:91], v9 offset:34816\n"
  "v_mfma_f32_16x16x32_bf16 v[100:103], a[0:3], a[40:43], 0\n"
  "s_add_u32 m0, s38, 0\n"
  "s_nop 0\n"
  "buffer_load_dwordx4 v2, s[16:19], s36 offen lds\n"
  "v_mfma_f32_16x16x32_bf16 v[136:139], a[0:3], a[44:47], 0\n"
  "ds_read_b64_tr_b16 a[52:53], v8 offset:34816\n"
  "v_mfma_f32_16x16x32_bf16 v[172:175], a[0:3], a[48:51], 0\n"
  "ds_read_b64_tr_b16 a[54:55], v8 offset:37120\n"
  "v_mfma_f32_16x16x32_bf16 v[68:71], a[4:7], a[36:39], 0\n"
  "ds_read_b128 a[92:95], v9 offset:35072\n"
  "v_mfma_f32_16x16x32_bf16 v[104:107], a[4:7], a[40:43], 0\n"
  "s_add_u32 m0, s38, 1024\n"
  "s_nop 0\n"
  "buffer_load_dwordx4 v3, s[16:19], s36 offen lds\n"
  "v_mfma_f32_16x16x32_bf16 v[140:143], a[4:7], a[44:47], 0\n"
  "ds_read_b128 a[96:99], v10 offset:34816\n"
  "v_mfma_f32_16x16x32_bf16 v[176:179], a[4:7], a[48:51], 0\n"
  "ds_read_b128 a[100:103], v10 offset:35072\n"
  "v_mfma_f32_16x16x32_bf16 v[72:75], a[8:11], a[36:39], 0\n"
  "ds_read_b64_tr_b16 a[56:57], v8 offset:34848\n"
  "v_mfma_f32_16x16x32_bf16 v[108:111], a[8:11], a[40:43], 0\n"
  "s_add_u32 m0, s38, 2048\n"
  "s_nop 0\n"
  "buffer_load_dwordx4 v4, s[16:19], s36 offen lds\n"
  "v_mfma_f32_16x16x32_bf16 v[144:147], a[8:11], a[44:47], 0\n"
  "ds_read_b64_tr_b16 a[58:59], v8 offset:37152\n"
  "v_mfma_f32_16x16x32_bf16 v[180:183], a[8:11], a[48:51], 0\n"
  "ds_read_b64_tr_b16 a[60:61], v8 offset:34880\n"
  "v_mfma_f32_16x16x32_bf16 v[76:79], a[12:15], a[36:39], 0\n"
  "ds_read_b64_tr_b16 a[62:63], v8 offset:37184\n"
  "v_mfma_f32_16x16x32_bf16 v[112:115], a[12:15], a[40:43], 0\n"
  "s_add_u32 m0, s38, 3072\n"
  "s_nop 0\n"
  "buffer_load_dwordx4 v5, s[16:19], s36 offen lds\n"
  "v_mfma_f32_16x16x32_bf16 v[148:151], a[12:15], a[44:47], 0\n"
  "ds_read_b64_tr_b16 a[64:65], v8 offset:34912\n"
  "v_mfma_f32_16x16x32_bf16 v[184:187], a[12:15], a[48:51], 0\n"
  "ds_read_b64_tr_b16 a[66:67], v8 offset:37216\n"
  "v_mfma_f32_16x16x32_bf16 v[80:83], a[16:19], a[36:39], 0\n"
  "ds_read_b64_tr_b16 a[68:69], v8 offset:34944\n"
  "v_mfma_f32_16x16x32_bf16 v[116:119], a[16:19], a[40:43], 0\n"
  "s_add_u32 m0, s38, 4096\n"
  "s_mov_b32 exec_hi, 0\n"
  "buffer_load_dwordx4 v6, s[16:19], s36 offen lds\n"
  "s_mov_b32 exec_hi, -1\n"
  "s_add_u32 s36, s36, 73728\n"
  "v_mfma_f32_16x16x32_bf16 v[152:155], a[16:19], a[44:47], 0\n"
  "ds_read_b64_tr_b16 a[70:71], v8 offset:37248\n"
  "v_mfma_f32_16x16x32_bf16 v[188:191], a[16:19], a[48:51], 0\n"
  "ds_read_b64_tr_b16 a[72:73], v8 offset:34976\n"
  "v_mfma_f32_16x16x32_bf16 v[84:87], a[20:23], a[36:39], 0\n"
  "ds_read_b64_tr_b16 a[74:75], v8 offset:37280\n"
  "v_mfma_f32_16x16x32_bf16 v[120:123], a[20:23], a[40:43], 0\n"
  "s_add_u32 m0, s39, 18432\n"
  "s_nop 0\n"
  "buffer_load_dwordx4 v1, s[20:23], s37 offen sc1 lds\n"
  "v_mfma_f32_16x16x32_bf16 v[156:159], a[20:23], a[44:47], 0\n"
  "ds_read_b64_tr_b16 a[76:77], v8 offset:35008\n"
  "v_mfma_f32_16x16x32_bf16 v[192:195], a[20:23], a[48:51], 0\n"
  "ds_read_b64_tr_b16 a[78:79], v8 offset:37312\n"
  "v_mfma_f32_16x16x32_bf16 v[88:91], a[24:27], a[36:39], 0\n"
  "ds_read_b64_tr_b16 a[80:81], v8 offset:35040\n"
  "v_mfma_f32_16x16x32_bf16 v[124:127], a[24:27], a[40:43], 0\n"
  "buffer_load_dwordx4 v1, s[20:23], s37 offen offset:1024 sc1 lds\n"
  "v_mfma_f32_16x16x32_bf16 v[160:163], a[24:27], a[44:47], 0\n"
  "ds_read_b64_tr_b16 a[82:83], v8 offset:37344\n"
  "v_mfma_f32_16x16x32_bf16 v[196:199], a[24:27], a[48:51], 0\n"
  "ds_read_b64_tr_b16 a[84:85], v8 offset:35072\n"
  "v_mfma_f32_16x16x32_bf16 v[92:95], a[28:31], a[36:39], 0\n"
  "ds_read_b64_tr_b16 a[86:87], v8 offset:37376\n"
  "v_mfma_f32_16x16x32_bf16 v[128:131], a[28:31], a[40:43], 0\n"
  "buffer_load_dwordx4 v1, s[20:23], s37 offen offset:2048 sc1 lds\n"
  "v_mfma_f32_16x16x32_bf16 v[164:167], a[28:31], a[44:47], 0\n"
  "v_mfma_f32_16x16x32_bf16 v[200:203], a[28:31], a[48:51], 0\n"
  "v_mfma_f32_16x16x32_bf16 v[96:99], a[32:35], a[36:39], 0\n"
  "v_mfma_f32_16x16x32_bf16 v[132:135], a[32:35], a[40:43], 0\n"
  "buffer_load_dwordx4 v1, s[20:23], s37 offen offset:3072 sc1 lds\n"
  "s_add_u32 s37, s37, 4096\n"
  "v_mfma_f32_16x16x32_bf16 v[168:171], a[32:35], a[44:47], 0\n"
  "v_mfma_f32_16x16x32_bf16 v[204:207], a[32:35], a[48:51], 0\n"
  "s_waitcnt vmcnt(13)\n"
  "s_waitcnt lgkmcnt(0)\n"
  "s_barrier\n"
  "v_mfma_f32_16x16x32_bf16 v[64:67], a[52:55], a[88:91], v[64:67]\n"
  "ds_read_b128 a[36:39], v29 offset:0\n"
  "v_mfma_f32_16x16x32_bf16 v[100:103], a[52:55], a[92:95], v[100:103]\n"
  "s_add_u32 m0, s38, 34816\n"
  "s_nop 0\n"
  "buffer_load_dwordx4 v2, s[16:19], s36 offen lds\n"
  "v_mfma_f32_16x16x32_bf16 v[136:139], a[52:55], a[96:99], v[136:139]\n"
  "ds_read_b64_tr_b16 a[0:1], v28 offset:0\n"
  "v_mfma_f32_16x16x32_bf16 v[172:175], a[52:55], a[100:103], v[172:175]\n"
  "ds_read_b64_tr_b16 a[2:3], v28 offset:2304\n"
  "v_mfma_f32_16x16x32_bf16 v[68:71], a[56:59], a[88:91], v[68:71]\n"
  "ds_read_b128 a[40:43], v29 offset:256\n"
  "v_mfma_f32_16x16x32_bf16 v[104:107], a[56:59], a[92:95], v[104:107]\n"
  "s_add_u32 m0, s38, 35840\n"
  "s_nop 0\n"
  "buffer_load_dwordx4 v3, s[16:19], s36 offen lds\n"
  "v_mfma_f32_16x16x32_bf16 v[140:143], a[56:59], a[96:99], v[140:143]\n"
  "ds_read_b128 a[44:47], v30 offset:0\n"
  "v_mfma_f32_16x16x32_bf16 v[176:179], a[56:59], a[100:103], v[176:179]\n"
  "ds_read_b128 a[48:51], v30 offset:256\n"
  "v_mfma_f32_16x16x32_bf16 v[72:75], a[60:63], a[88:91], v[72:75]\n"
  "ds_read_b64_tr_b16 a[4:5], v28 offset:32\n"
  "v_mfma_f32_16x16x32_bf16 v[108:111], a[60:63], a[92:95], v[108:111]\n"
  "s_add_u32 m0, s38, 36864\n"
  "s_nop 0\n"
  "buffer_load_dwordx4 v4, s[16:19], s36 offen lds\n"
  "v_mfma_f32_16x16x32_bf16 v[144:147], a[60:63], a[96:99], v[144:147]\n"
  "ds_read_b64_tr_b16 a[6:7], v28 offset:2336\n"
  "v_mfma_f32_16x16x32_bf16 v[180:183], a[60:63], a[100:103], v[180:183]\n"
  "ds_read_b64_tr_b16 a[8:9], v28 offset:64\n"
  "v_mfma_f32_16x16x32_bf16 v[76:79], a[64:67], a[88:91], v[76:79]\n"
  "ds_read_b64_tr_b16 a[10:11], v28 offset:2368\n"
  "v_mfma_f32_16x16x32_bf16 v[112:115], a[64:67], a[92:95], v[112:115]\n"
  "s_add_u32 m0, s38, 37888\n"
  "s_nop 0\n"
  "buffer_load_dwordx4 v5, s[16:19], s36 offen lds\n"
  "v_mfma_f32_16x16x32_bf16 v[148:151], a[64:67], a[96:99], v[148:151]\n"
  "ds_read_b64_tr_b16 a[12:13], v28 offset:96\n"
  "v_mfma_f32_16x16x32_bf16 v[184:187], a[64:67], a[100:103], v[184:187]\n"
  "ds_read_b64_tr_b16 a[14:15], v28 offset:2400\n"
  "v_mfma_f32_16x16x32_bf16 v[80:83], a[68:71], a[88:91], v[80:83]\n"
  "ds_read_b64_tr_b16 a[16:17], v28 offset:128\n"
  "v_mfma_f32_16x16x32_bf16 v[116:119], a[68:71], a[92:95], v[116:119]\n"
  "s_add_u32 m0, s38, 38912\n"
  "s_mov_b32 exec_hi, 0\n"
  "buffer_load_dwordx4 v6, s[16:19], s36 offen lds\n"
  "s_mov_b32 exec_hi, -1\n"
  "s_add_u32 s36, s36, 73728\n"
  "v_mfma_f32_16x16x32_bf16 v[152:155], a[68:71], a[96:99], v[152:155]\n"
  "ds_read_b64_tr_b16 a[18:19], v28 offset:2432\n"
  "v_mfma_f32_16x16x32_bf16 v[188:191], a[68:71], a[100:103], v[188:191]\n"
  "ds_read_b64_tr_b16 a[20:21], v28 offset:160\n"
  "v_mfma_f32_16x16x32_bf16 v[84:87], a[72:75], a[88:91], v[84:87]\n"
  "ds_read_b64_tr_b16 a[22:23], v28 offset:2464\n"
  "v_mfma_f32_16x16x32_bf16 v[120:123], a[72:75], a[92:95], v[120:123]\n"
  "s_add_u32 m0, s39, 53248\n"
  "s_nop 0\n"
  "buffer_load_dwordx4 v1, s[20:23], s37 offen sc1 lds\n"
  "v_mfma_f32_16x16x32_bf16 v[156:159], a[72:75], a[96:99], v[156:159]\n"
  "ds_read_b64_tr_b16 a[24:25], v28 offset:192\n"
  "v_mfma_f32_16x16x32_bf16 v[192:195], a[72:75], a[100:103], v[192:195]\n"
  "ds_read_b64_tr_b16 a[26:27], v28 offset:2496\n"
  "v_mfma_f32_16x16x32_bf16 v[88:91], a[76:79], a[88:91], v[88:91]\n"
  "ds_read_b64_tr_b16 a[28:29], v28 offset:224\n"
  "v_mfma_f32_16x16x32_bf16 v[124:127], a[76:79], a[92:95], v[124:127]\n"
  "buffer_load_dwordx4 v1, s[20:23], s37 offen offset:1024 sc1 lds\n"
  "v_mfma_f32_16x16x32_bf16 v[160:163], a[76:79], a[96:99], v[160:163]\n"
  "ds_read_b64_tr_b16 a[30:31], v28 offset:2528\n"
  "v_mfma_f32_16x16x32_bf16 v[196:199], a[76:79], a[100:103], v[196:199]\n"
  "ds_read_b64_tr_b16 a[32:33], v28 offset:256\n"
  "v_mfma_f32_16x16x32_bf16 v[92:95], a[80:83], a[88:91], v[92:95]\n"
  "ds_read_b64_tr_b16 a[34:35], v28 offset:2560\n"
  "v_mfma_f32_16x16x32_bf16 v[128:131], a[80:83], a[92:95], v[128:131]\n"
  "buffer_load_dwordx4 v1, s[20:23], s37 offen offset:2048 sc1 lds\n"
  "v_mfma_f32_16x16x32_bf16 v[164:167], a[80:83], a[96:99], v[164:167]\n"
  "v_mfma_f32_16x16x32_bf16 v[200:203], a[80:83], a[100:103], v[200:203]\n"
  "v_mfma_f32_16x16x32_bf16 v[96:99], a[84:87], a[88:91], v[96:99]\n"
  "v_mfma_f32_16x16x32_bf16 v[132:135], a[84:87], a[92:95], v[132:135]\n"
  "buffer_load_dwordx4 v1, s[20:23], s37 offen offset:3072 sc1 lds\n"
  "s_add_u32 s37, s37, 520192\n"
  "v_mfma_f32_16x16x32_bf16 v[168:171], a[84:87], a[96:99], v[168:171]\n"
  "v_mfma_f32_16x16x32_bf16 v[204:207], a[84:87], a[100:103], v[204:207]\n"
  "s_waitcnt vmcnt(18)\n"
  "s_waitcnt lgkmcnt(0)\n"
  "s_barrier\n"
  "v_mfma_f32_16x16x32_bf16 v[64:67], a[0:3], a[36:39], v[64:67]\n"
  "ds_read_b128 a[88:91], v29 offset:34816\n"
  "v_mfma_f32_16x16x32_bf16 v[100:103], a[0:3], a[40:43], v[100:103]\n"
  "s_add_u32 m0, s38, 69632\n"
  "s_nop 0\n"
  "buffer_load_dwordx4 v2, s[16:19], s36 offen lds\n"
  "v_mfma_f32_16x16x32_bf16 v[136:139], a[0:3], a[44:47], v[136:139]\n"
  "ds_read_b64_tr_b16 a[52:53], v28 offset:34816\n"
  "v_mfma_f32_16x16x32_bf16 v[172:175], a[0:3], a[48:51], v[172:175]\n"
  "ds_read_b64_tr_b16 a[54:55], v28 offset:37120\n"
  "v_mfma_f32_16x16x32_bf16 v[68:71], a[4:7], a[36:39], v[68:71]\n"
  "ds_read_b128 a[92:95], v29 offset:35072\n"
  "v_mfma_f32_16x16x32_bf16 v[104:107], a[4:7], a[40:43], v[104:107]\n"
  "s_add_u32 m0, s38, 70656\n"
  "s_nop 0\n"
  "buffer_load_dwordx4 v3, s[16:19], s36 offen lds\n"
  "v_mfma_f32_16x16x32_bf16 v[140:143], a[4:7], a[44:47], v[140:143]\n"
  "ds_read_b128 a[96:99], v30 offset:34816\n"
  "v_mfma_f32_16x16x32_bf16 v[176:179], a[4:7], a[48:51], v[176:179]\n"
  "ds_read_b128 a[100:103], v30 offset:35072\n"
  "v_mfma_f32_16x16x32_bf16 v[72:75], a[8:11], a[36:39], v[72:75]\n"
  "ds_read_b64_tr_b16 a[56:57], v28 offset:34848\n"
  "v_mfma_f32_16x16x32_bf16 v[108:111], a[8:11], a[40:43], v[108:111]\n"
  "s_add_u32 m0, s38, 71680\n"
  "s_nop 0\n"
  "buffer_load_dwordx4 v4, s[16:19], s36 offen lds\n"
  "v_mfma_f32_16x16x32_bf16 v[144:147], a[8:11], a[44:47], v[144:147]\n"
  "ds_read_b64_tr_b16 a[58:59], v28 offset:37152\n"
  "v_mfma_f32_16x16x32_bf16 v[180:183], a[8:11], a[48:51], v[180:183]\n"
  "ds_read_b64_tr_b16 a[60:61], v28 offset:34880\n"
  "v_mfma_f32_16x16x32_bf16 v[76:79], a[12:15], a[36:39], v[76:79]\n"
  "ds_read_b64_tr_b16 a[62:63], v28 offset:37184\n"
  "v_mfma_f32_16x16x32_bf16 v[112:115], a[12:15], a[40:43], v[112:115]\n"
  "s_add_u32 m0, s38, 72704\n"
  "s_nop 0\n"
  "buffer_load_dwordx4 v5, s[16:19], s36 offen lds\n"
  "v_mfma_f32_16x16x32_bf16 v[148:151], a[12:15], a[44:47], v[148:151]\n"
  "ds_read_b64_tr_b16 a[64:65], v28 offset:34912\n"
  "v_mfma_f32_16x16x32_bf16 v[184:187], a[12:15], a[48:51], v[184:187]\n"
  "ds_read_b64_tr_b16 a[66:67], v28 offset:37216\n"
  "v_mfma_f32_16x16x32_bf16 v[80:83], a[16:19], a[36:39], v[80:83]\n"
  "ds_read_b64_tr_b16 a[68:69], v28 offset:34944\n"
  "v_mfma_f32_16x16x32_bf16 v[116:119], a[16:19], a[40:43], v[116:119]\n"
  "s_add_u32 m0, s38, 73728\n"
  "s_mov_b32 exec_hi, 0\n"
  "buffer_load_dwordx4 v6, s[16:19], s36 offen lds\n"
  "s_mov_b32 exec_hi, -1\n"
  "s_add_u32 s36, s36, 73728\n"
  "v_mfma_f32_16x16x32_bf16 v[152:155], a[16:19], a[44:47], v[152:155]\n"
  "ds_read_b64_tr_b16 a[70:71], v28 offset:37248\n"
  "v_mfma_f32_16x16x32_bf16 v[188:191], a[16:19], a[48:51], v[188:191]\n"
  "ds_read_b64_tr_b16 a[72:73], v28 offset:34976\n"
  "v_mfma_f32_16x16x32_bf16 v[84:87], a[20:23], a[36:39], v[84:87]\n"
  "ds_read_b64_tr_b16 a[74:75], v28 offset:37280\n"
  "v_mfma_f32_16x16x32_bf16 v[120:123], a[20:23], a[40:43], v[120:123]\n"
  "s_add_u32 m0, s39, 88064\n"
  "s_nop 0\n"
  "buffer_load_dwordx4 v1, s[20:23], s37 offen sc1 lds\n"
  "v_mfma_f32_16x16x32_bf16 v[156:159], a[20:23], a[44:47], v[156:159]\n"
  "ds_read_b64_tr_b16 a[76:77], v28 offset:35008\n"
  "v_mfma_f32_16x16x32_bf16 v[192:195], a[20:23], a[48:51], v[192:195]\n"
  "ds_read_b64_tr_b16 a[78:79], v28 offset:37312\n"
  "v_mfma_f32_16x16x32_bf16 v[88:91], a[24:27], a[36:39], v[88:91]\n"
  "ds_read_b64_tr_b16 a[80:81], v28 offset:35040\n"
  "v_mfma_f32_16x16x32_bf16 v[124:127], a[24:27], a[40:43], v[124:127]\n"
  "buffer_load_dwordx4 v1, s[20:23], s37 offen offset:1024 sc1 lds\n"
  "v_mfma_f32_16x16x32_bf16 v[160:163], a[24:27], a[44:47], v[160:163]\n"
  "ds_read_b64_tr_b16 a[82:83], v28 offset:37344\n"
  "v_mfma_f32_16x16x32_bf16 v[196:199], a[24:27], a[48:51], v[196:199]\n"
  "ds_read_b64_tr_b16 a[84:85], v28 offset:35072\n"
  "v_mfma_f32_16x16x32_bf16 v[92:95], a[28:31], a[36:39], v[92:95]\n"
  "ds_read_b64_tr_b16 a[86:87], v28 offset:37376\n"
  "v_mfma_f32_16x16x32_bf16 v[128:131], a[28:31], a[40:43], v[128:131]\n"
  "buffer_load_dwordx4 v1, s[20:23], s37 offen offset:2048 sc1 lds\n"
  "v_mfma_f32_16x16x32_bf16 v[164:167], a[28:31], a[44:47], v[164:167]\n"
  "v_mfma_f32_16x16x32_bf16 v[200:203], a[28:31], a[48:51], v[200:203]\n"
  "v_mfma_f32_16x16x32_bf16 v[96:99], a[32:35], a[36:39], v[96:99]\n"
  "v_mfma_f32_16x16x32_bf16 v[132:135], a[32:35], a[40:43], v[132:135]\n"
  "buffer_load_dwordx4 v1, s[20:23], s37 offen offset:3072 sc1 lds\n"
  "s_add_u32 s37, s37, 4096\n"
  "v_mfma_f32_16x16x32_bf16 v[168:171], a[32:35], a[44:47], v[168:171]\n"
  "v_mfma_f32_16x16x32_bf16 v[204:207], a[32:35], a[48:51], v[204:207]\n"
  "s_waitcnt vmcnt(18)\n"
  "s_waitcnt lgkmcnt(0)\n"
  "s_barrier\n"
  "v_mfma_f32_16x16x32_bf16 v[64:67], a[52:55], a[88:91], v[64:67]\n"
  "ds_read_b128 a[36:39], v9 offset:0\n"
  "v_mfma_f32_16x16x32_bf16 v[100:103], a[52:55], a[92:95], v[100:103]\n"
  "s_add_u32 m0, s38, 104448\n"
  "s_nop 0\n"
  "buffer_load_dwordx4 v2, s[16:19], s36 offen lds\n"
  "v_mfma_f32_16x16x32_bf16 v[136:139], a[52:55], a[96:99], v[136:139]\n"
  "ds_read_b64_tr_b16 a[0:1], v8 offset:0\n"
  "v_mfma_f32_16x16x32_bf16 v[172:175], a[52:55], a[100:103], v[172:175]\n"
  "ds_read_b64_tr_b16 a[2:3], v8 offset:2304\n"
  "v_mfma_f32_16x16x32_bf16 v[68:71], a[56:59], a[88:91], v[68:71]\n"
  "ds_read_b128 a[40:43], v9 offset:256\n"
  "v_mfma_f32_16x16x32_bf16 v[104:107], a[56:59], a[92:95], v[104:107]\n"
  "s_add_u32 m0, s38, 105472\n"
  "s_nop 0\n"
  "buffer_load_dwordx4 v3, s[16:19], s36 offen lds\n"
  "v_mfma_f32_16x16x32_bf16 v[140:143], a[56:59], a[96:99], v[140:143]\n"
  "ds_read_b128 a[44:47], v10 offset:0\n"
  "v_mfma_f32_16x16x32_bf16 v[176:179], a[56:59], a[100:103], v[176:179]\n"
  "ds_read_b128 a[48:51], v10 offset:256\n"
  "v_mfma_f32_16x16x32_bf16 v[72:75], a[60:63], a[88:91], v[72:75]\n"
  "ds_read_b64_tr_b16 a[4:5], v8 offset:32\n"
  "v_mfma_f32_16x16x32_bf16 v[108:111], a[60:63], a[92:95], v[108:111]\n"
  "s_add_u32 m0, s38, 106496\n"
  "s_nop 0\n"
  "buffer_load_dwordx4 v4, s[16:19], s36 offen lds\n"
  "v_mfma_f32_16x16x32_bf16 v[144:147], a[60:63], a[96:99], v[144:147]\n"
  "ds_read_b64_tr_b16 a[6:7], v8 offset:2336\n"
  "v_mfma_f32_16x16x32_bf16 v[180:183], a[60:63], a[100:103], v[180:183]\n"
  "ds_read_b64_tr_b16 a[8:9], v8 offset:64\n"
  "v_mfma_f32_16x16x32_bf16 v[76:79], a[64:67], a[88:91], v[76:79]\n"
  "ds_read_b64_tr_b16 a[10:11], v8 offset:2368\n"
  "v_mfma_f32_16x16x32_bf16 v[112:115], a[64:67], a[92:95], v[112:115]\n"
  "s_add_u32 m0, s38, 107520\n"
  "s_nop 0\n"
  "buffer_load_dwordx4 v5, s[16:19], s36 offen lds\n"
  "v_mfma_f32_16x16x32_bf16 v[148:151], a[64:67], a[96:99], v[148:151]\n"
  "ds_read_b64_tr_b16 a[12:13], v8 offset:96\n"
  "v_mfma_f32_16x16x32_bf16 v[184:187], a[64:67], a[100:103], v[184:187]\n"
  "ds_read_b64_tr_b16 a[14:15], v8 offset:2400\n"
  "v_mfma_f32_16x16x32_bf16 v[80:83], a[68:71], a[88:91], v[80:83]\n"
  "ds_read_b64_tr_b16 a[16:17], v8 offset:128\n"
  "v_mfma_f32_16x16x32_bf16 v[116:119], a[68:71], a[92:95], v[116:119]\n"
  "s_add_u32 m0, s38, 108544\n"
  "s_mov_b32 exec_hi, 0\n"
  "buffer_load_dwordx4 v6, s[16:19], s36 offen lds\n"
  "s_mov_b32 exec_hi, -1\n"
  "s_add_u32 s36, s36, 73728\n"
  "v_mfma_f32_16x16x32_bf16 v[152:155], a[68:71], a[96:99], v[152:155]\n"
  "ds_read_b64_tr_b16 a[18:19], v8 offset:2432\n"
  "v_mfma_f32_16x16x32_bf16 v[188:191], a[68:71], a[100:103], v[188:191]\n"
  "ds_read_b64_tr_b16 a[20:21], v8 offset:160\n"
  "v_mfma_f32_16x16x32_bf16 v[84:87], a[72:75], a[88:91], v[84:87]\n"
  "ds_read_b64_tr_b16 a[22:23], v8 offset:2464\n"
  "v_mfma_f32_16x16x32_bf16 v[120:123], a[72:75], a[92:95], v[120:123]\n"
  "s_add_u32 m0, s39, 122880\n"
  "s_nop 0\n"
  "buffer_load_dwordx4 v1, s[20:23], s37 offen sc1 lds\n"
  "v_mfma_f32_16x16x32_bf16 v[156:159], a[72:75], a[96:99], v[156:159]\n"
  "ds_read_b64_tr_b16 a[24:25], v8 offset:192\n"
  "v_mfma_f32_16x16x32_bf16 v[192:195], a[72:75], a[100:103], v[192:195]\n"
  "ds_read_b64_tr_b16 a[26:27], v8 offset:2496\n"
  "v_mfma_f32_16x16x32_bf16 v[88:91], a[76:79], a[88:91], v[88:91]\n"
  "ds_read_b64_tr_b16 a[28:29], v8 offset:224\n"
  "v_mfma_f32_16x16x32_bf16 v[124:127], a[76:79], a[92:95], v[124:127]\n"
  "buffer_load_dwordx4 v1, s[20:23], s37 offen offset:1024 sc1 lds\n"
  "v_mfma_f32_16x16x32_bf16 v[160:163], a[76:79], a[96:99], v[160:163]\n"
  "ds_read_b64_tr_b16 a[30:31], v8 offset:2528\n"
  "v_mfma_f32_16x16x32_bf16 v[196:199], a[76:79], a[100:103], v[196:199]\n"
  "ds_read_b64_tr_b16 a[32:33], v8 offset:256\n"
  "v_mfma_f32_16x16x32_bf16 v[92:95], a[80:83], a[88:91], v[92:95]\n"
  "ds_read_b64_tr_b16 a[34:35], v8 offset:2560\n"
  "v_mfma_f32_16x16x32_bf16 v[128:131], a[80:83], a[92:95], v[128:131]\n"
  "buffer_load_dwordx4 v1, s[20:23], s37 offen offset:2048 sc1 lds\n"
  "v_mfma_f32_16x16x32_bf16 v[164:167], a[80:83], a[96:99], v[164:167]\n"
  "v_mfma_f32_16x16x32_bf16 v[200:203], a[80:83], a[100:103], v[200:203]\n"
  "v_mfma_f32_16x16x32_bf16 v[96:99], a[84:87], a[88:91], v[96:99]\n"
  "v_mfma_f32_16x16x32_bf16 v[132:135], a[84:87], a[92:95], v[132:135]\n"
  "buffer_load_dwordx4 v1, s[20:23], s37 offen offset:3072 sc1 lds\n"
  "s_add_u32 s37, s37, 520192\n"
  "v_mfma_f32_16x16x32_bf16 v[168:171], a[84:87], a[96:99], v[168:171]\n"
  "v_mfma_f32_16x16x32_bf16 v[204:207], a[84:87], a[100:103], v[204:207]\n"
  "s_waitcnt vmcnt(18)\n"
  "s_waitcnt lgkmcnt(0)\n"
  "s_barrier\n"
  "v_mfma_f32_16x16x32_bf16 v[64:67], a[0:3], a[36:39], v[64:67]\n"
  "ds_read_b128 a[88:91], v9 offset:34816\n"
  "v_mfma_f32_16x16x32_bf16 v[100:103], a[0:3], a[40:43], v[100:103]\n"
  "s_add_u32 m0, s38, 0\n"
  "s_nop 0\n"
  "buffer_load_dwordx4 v2, s[16:19], s36 offen lds\n"
  "v_mfma_f32_16x16x32_bf16 v[136:139], a[0:3], a[44:47], v[136:139]\n"
  "ds_read_b64_tr_b16 a[52:53], v8 offset:34816\n"
  "v_mfma_f32_16x16x32_bf16 v[172:175], a[0:3], a[48:51], v[172:175]\n"
  "ds_read_b64_tr_b16 a[54:55], v8 offset:37120\n"
  "v_mfma_f32_16x16x32_bf16 v[68:71], a[4:7], a[36:39], v[68:71]\n"
  "ds_read_b128 a[92:95], v9 offset:35072\n"
  "v_mfma_f32_16x16x32_bf16 v[104:107], a[4:7], a[40:43], v[104:107]\n"
  "s_add_u32 m0, s38, 1024\n"
  "s_nop 0\n"
  "buffer_load_dwordx4 v3, s[16:19], s36 offen lds\n"
  "v_mfma_f32_16x16x32_bf16 v[140:143], a[4:7], a[44:47], v[140:143]\n"
  "ds_read_b128 a[96:99], v10 offset:34816\n"
  "v_mfma_f32_16x16x32_bf16 v[176:179], a[4:7], a[48:51], v[176:179]\n"
  "ds_read_b128 a[100:103], v10 offset:35072\n"
  "v_mfma_f32_16x16x32_bf16 v[72:75], a[8:11], a[36:39], v[72:75]\n"
  "ds_read_b64_tr_b16 a[56:57], v8 offset:34848\n"
  "v_mfma_f32_16x16x32_bf16 v[108:111], a[8:11], a[40:43], v[108:111]\n"
  "s_add_u32 m0, s38, 2048\n"
  "s_nop 0\n"
  "buffer_load_dwordx4 v4, s[16:19], s36 offen lds\n"
  "v_mfma_f32_16x16x32_bf16 v[144:147], a[8:11], a[44:47], v[144:147]\n"
  "ds_read_b64_tr_b16 a[58:59], v8 offset:37152\n"
  "v_mfma_f32_16x16x32_bf16 v[180:183], a[8:11], a[48:51], v[180:183]\n"
  "ds_read_b64_tr_b16 a[60:61], v8 offset:34880\n"
  "v_mfma_f32_16x16x32_bf16 v[76:79], a[12:15], a[36:39], v[76:79]\n"
  "ds_read_b64_tr_b16 a[62:63], v8 offset:37184\n"
  "v_mfma_f32_16x16x32_bf16 v[112:115], a[12:15], a[40:43], v[112:115]\n"
  "s_add_u32 m0, s38, 3072\n"
  "s_nop 0\n"
  "buffer_load_dwordx4 v5, s[16:19], s36 offen lds\n"
  "v_mfma_f32_16x16x32_bf16 v[148:151], a[12:15], a[44:47], v[148:151]\n"
  "ds_read_b64_tr_b16 a[64:65], v8 offset:34912\n"
  "v_mfma_f32_16x16x32_bf16 v[184:187], a[12:15], a[48:51], v[184:187]\n"
  "ds_read_b64_tr_b16 a[66:67], v8 offset:37216\n"
  "v_mfma_f32_16x16x32_bf16 v[80:83], a[16:19], a[36:39], v[80:83]\n"
  "ds_read_b64_tr_b16 a[68:69], v8 offset:34944\n"
  "v_mfma_f32_16x16x32_bf16 v[116:119], a[16:19], a[40:43], v[116:119]\n"
  "s_add_u32 m0, s38, 4096\n"
  "s_mov_b32 exec_hi, 0\n"
  "buffer_load_dwordx4 v6, s[16:19], s36 offen lds\n"
  "s_mov_b32 exec_hi, -1\n"
  "s_add_u32 s36, s36, 73728\n"
  "v_mfma_f32_16x16x32_bf16 v[152:155], a[16:19], a[44:47], v[152:155]\n"
  "ds_read_b64_tr_b16 a[70:71], v8 offset:37248\n"
  "v_mfma_f32_16x16x32_bf16 v[188:191], a[16:19], a[48:51], v[188:191]\n"
  "ds_read_b64_tr_b16 a[72:73], v8 offset:34976\n"
  "v_mfma_f32_16x16x32_bf16 v[84:87], a[20:23], a[36:39], v[84:87]\n"
  "ds_read_b64_tr_b16 a[74:75], v8 offset:37280\n"
  "v_mfma_f32_16x16x32_bf16 v[120:123], a[20:23], a[40:43], v[120:123]\n"
  "s_add_u32 m0, s39, 18432\n"
  "s_nop 0\n"
  "buffer_load_dwordx4 v1, s[20:23], s37 offen sc1 lds\n"
  "v_mfma_f32_16x16x32_bf16 v[156:159], a[20:23], a[44:47], v[156:159]\n"
  "ds_read_b64_tr_b16 a[76:77], v8 offset:35008\n"
  "v_mfma_f32_16x16x32_bf16 v[192:195], a[20:23], a[48:51], v[192:195]\n"
  "ds_read_b64_tr_b16 a[78:79], v8 offset:37312\n"
  "v_mfma_f32_16x16x32_bf16 v[88:91], a[24:27], a[36:39], v[88:91]\n"
  "ds_read_b64_tr_b16 a[80:81], v8 offset:35040\n"
  "v_mfma_f32_16x16x32_bf16 v[124:127], a[24:27], a[40:43], v[124:127]\n"
  "buffer_load_dwordx4 v1, s[20:23], s37 offen offset:1024 sc1 lds\n"
  "v_mfma_f32_16x16x32_bf16 v[160:163], a[24:27], a[44:47], v[160:163]\n"
  "ds_read_b64_tr_b16 a[82:83], v8 offset:37344\n"
  "v_mfma_f32_16x16x32_bf16 v[196:199], a[24:27], a[48:51], v[196:199]\n"
  "ds_read_b64_tr_b16 a[84:85], v8 offset:35072\n"
  "v_mfma_f32_16x16x32_bf16 v[92:95], a[28:31], a[36:39], v[92:95]\n"
  "ds_read_b64_tr_b16 a[86:87], v8 offset:37376\n"
  "v_mfma_f32_16x16x32_bf16 v[128:131], a[28:31], a[40:43], v[128:131]\n"
  "buffer_load_dwordx4 v1, s[20:23], s37 offen offset:2048 sc1 lds\n"
  "v_mfma_f32_16x16x32_bf16 v[164:167], a[28:31], a[44:47], v[164:167]\n"
  "v_mfma_f32_16x16x32_bf16 v[200:203], a[28:31], a[48:51], v[200:203]\n"
  "v_mfma_f32_16x16x32_bf16 v[96:99], a[32:35], a[36:39], v[96:99]\n"
  "v_mfma_f32_16x16x32_bf16 v[132:135], a[32:35], a[40:43], v[132:135]\n"
  "buffer_load_dwordx4 v1, s[20:23], s37 offen offset:3072 sc1 lds\n"
  "s_add_u32 s37, s37, 4096\n"
  "v_mfma_f32_16x16x32_bf16 v[168:171], a[32:35], a[44:47], v[168:171]\n"
  "v_mfma_f32_16x16x32_bf16 v[204:207], a[32:35], a[48:51], v[204:207]\n"
  "s_waitcnt vmcnt(18)\n"
  "s_waitcnt lgkmcnt(0)\n"
  "s_barrier\n"
  "v_mfma_f32_16x16x32_bf16 v[64:67], a[52:55], a[88:91], v[64:67]\n"
  "ds_read_b128 a[36:39], v29 offset:0\n"
  "v_mfma_f32_16x16x32_bf16 v[100:103], a[52:55], a[92:95], v[100:103]\n"
  "s_add_u32 m0, s38, 34816\n"
  "s_nop 0\n"
  "buffer_load_dwordx4 v2, s[16:19], s36 offen lds\n"
  "v_mfma_f32_16x16x32_bf16 v[136:139], a[52:55], a[96:99], v[136:139]\n"
  "ds_read_b64_tr_b16 a[0:1], v28 offset:0\n"
  "v_mfma_f32_16x16x32_bf16 v[172:175], a[52:55], a[100:103], v[172:175]\n"
  "ds_read_b64_tr_b16 a[2:3], v28 offset:2304\n"
  "v_mfma_f32_16x16x32_bf16 v[68:71], a[56:59], a[88:91], v[68:71]\n"
  "ds_read_b128 a[40:43], v29 offset:256\n"
  "v_mfma_f32_16x16x32_bf16 v[104:107], a[56:59], a[92:95], v[104:107]\n"
  "s_add_u32 m0, s38, 35840\n"
  "s_nop 0\n"
  "buffer_load_dwordx4 v3, s[16:19], s36 offen lds\n"
  "v_mfma_f32_16x16x32_bf16 v[140:143], a[56:59], a[96:99], v[140:143]\n"
  "ds_read_b128 a[44:47], v30 offset:0\n"
  "v_mfma_f32_16x16x32_bf16 v[176:179], a[56:59], a[100:103], v[176:179]\n"
  "ds_read_b128 a[48:51], v30 offset:256\n"
  "v_mfma_f32_16x16x32_bf16 v[72:75], a[60:63], a[88:91], v[72:75]\n"
  "ds_read_b64_tr_b16 a[4:5], v28 offset:32\n"
  "v_mfma_f32_16x16x32_bf16 v[108:111], a[60:63], a[92:95], v[108:111]\n"
  "s_add_u32 m0, s38, 36864\n"
  "s_nop 0\n"
  "buffer_load_dwordx4 v4, s[16:19], s36 offen lds\n"
  "v_mfma_f32_16x16x32_bf16 v[144:147], a[60:63], a[96:99], v[144:147]\n"
  "ds_read_b64_tr_b16 a[6:7], v28 offset:2336\n"
  "v_mfma_f32_16x16x32_bf16 v[180:183], a[60:63], a[100:103], v[180:183]\n"
  "ds_read_b64_tr_b16 a[8:9], v28 offset:64\n"
  "v_mfma_f32_16x16x32_bf16 v[76:79], a[64:67], a[88:91], v[76:79]\n"
  "ds_read_b64_tr_b16 a[10:11], v28 offset:2368\n"
  "v_mfma_f32_16x16x32_bf16 v[112:115], a[64:67], a[92:95], v[112:115]\n"
  "s_add_u32 m0, s38, 37888\n"
  "s_nop 0\n"
  "buffer_load_dwordx4 v5, s[16:19], s36 offen lds\n"
  "v_mfma_f32_16x16x32_bf16 v[148:151], a[64:67], a[96:99], v[148:151]\n"
  "ds_read_b64_tr_b16 a[12:13], v28 offset:96\n"
  "v_mfma_f32_16x16x32_bf16 v[184:187], a[64:67], a[100:103], v[184:187]\n"
  "ds_read_b64_tr_b16 a[14:15], v28 offset:2400\n"
  "v_mfma_f32_16x16x32_bf16 v[80:83], a[68:71], a[88:91], v[80:83]\n"
  "ds_read_b64_tr_b16 a[16:17], v28 offset:128\n"
  "v_mfma_f32_16x16x32_bf16 v[116:119], a[68:71], a[92:95], v[116:119]\n"
  "s_add_u32 m0, s38, 38912\n"
  "s_mov_b32 exec_hi, 0\n"
  "buffer_load_dwordx4 v6, s[16:19], s36 offen lds\n"
  "s_mov_b32 exec_hi, -1\n"
  "s_add_u32 s36, s36, 73728\n"
  "v_mfma_f32_16x16x32_bf16 v[152:155], a[68:71], a[96:99], v[152:155]\n"
  "ds_read_b64_tr_b16 a[18:19], v28 offset:2432\n"
  "v_mfma_f32_16x16x32_bf16 v[188:191], a[68:71], a[100:103], v[188:191]\n"
  "ds_read_b64_tr_b16 a[20:21], v28 offset:160\n"
  "v_mfma_f32_16x16x32_bf16 v[84:87], a[72:75], a[88:91], v[84:87]\n"
  "ds_read_b64_tr_b16 a[22:23], v28 offset:2464\n"
  "v_mfma_f32_16x16x32_bf16 v[120:123], a[72:75], a[92:95], v[120:123]\n"
  "s_add_u32 m0, s39, 53248\n"
  "s_nop 0\n"
  "buffer_load_dwordx4 v1, s[20:23], s37 offen sc1 lds\n"
  "v_mfma_f32_16x16x32_bf16 v[156:159], a[72:75], a[96:99], v[156:159]\n"
  "ds_read_b64_tr_b16 a[24:25], v28 offset:192\n"
  "v_mfma_f32_16x16x32_bf16 v[192:195], a[72:75], a[100:103], v[192:195]\n"
  "ds_read_b64_tr_b16 a[26:27], v28 offset:2496\n"
  "v_mfma_f32_16x16x32_bf16 v[88:91], a[76:79], a[88:91], v[88:91]\n"
  "ds_read_b64_tr_b16 a[28:29], v28 offset:224\n"
  "v_mfma_f32_16x16x32_bf16 v[124:127], a[76:79], a[92:95], v[124:127]\n"
  "buffer_load_dwordx4 v1, s[20:23], s37 offen offset:1024 sc1 lds\n"
  "v_mfma_f32_16x16x32_bf16 v[160:163], a[76:79], a[96:99], v[160:163]\n"
  "ds_read_b64_tr_b16 a[30:31], v28 offset:2528\n"
  "v_mfma_f32_16x16x32_bf16 v[196:199], a[76:79], a[100:103], v[196:199]\n"
  "ds_read_b64_tr_b16 a[32:33], v28 offset:256\n"
  "v_mfma_f32_16x16x32_bf16 v[92:95], a[80:83], a[88:91], v[92:95]\n"
  "ds_read_b64_tr_b16 a[34:35], v28 offset:2560\n"
  "v_mfma_f32_16x16x32_bf16 v[128:131], a[80:83], a[92:95], v[128:131]\n"
  "buffer_load_dwordx4 v1, s[20:23], s37 offen offset:2048 sc1 lds\n"
  "v_mfma_f32_16x16x32_bf16 v[164:167], a[80:83], a[96:99], v[164:167]\n"
  "v_mfma_f32_16x16x32_bf16 v[200:203], a[80:83], a[100:103], v[200:203]\n"
  "v_mfma_f32_16x16x32_bf16 v[96:99], a[84:87], a[88:91], v[96:99]\n"
  "v_mfma_f32_16x16x32_bf16 v[132:135], a[84:87], a[92:95], v[132:135]\n"
  "buffer_load_dwordx4 v1, s[20:23], s37 offen offset:3072 sc1 lds\n"
  "s_add_u32 s37, s37, 520192\n"
  "v_mfma_f32_16x16x32_bf16 v[168:171], a[84:87], a[96:99], v[168:171]\n"
  "v_mfma_f32_16x16x32_bf16 v[204:207], a[84:87], a[100:103], v[204:207]\n"
  "s_waitcnt vmcnt(18)\n"
  "s_waitcnt lgkmcnt(0)\n"
  "s_barrier\n"
  "v_mfma_f32_16x16x32_bf16 v[64:67], a[0:3], a[36:39], v[64:67]\n"
  "ds_read_b128 a[88:91], v29 offset:34816\n"
  "v_mfma_f32_16x16x32_bf16 v[100:103], a[0:3], a[40:43], v[100:103]\n"
  "s_add_u32 m0, s38, 69632\n"
  "s_nop 0\n"
  "buffer_load_dwordx4 v2, s[16:19], s36 offen lds\n"
  "v_mfma_f32_16x16x32_bf16 v[136:139], a[0:3], a[44:47], v[136:139]\n"
  "ds_read_b64_tr_b16 a[52:53], v28 offset:34816\n"
  "v_mfma_f32_16x16x32_bf16 v[172:175], a[0:3], a[48:51], v[172:175]\n"
  "ds_read_b64_tr_b16 a[54:55], v28 offset:37120\n"
  "v_mfma_f32_16x16x32_bf16 v[68:71], a[4:7], a[36:39], v[68:71]\n"
  "ds_read_b128 a[92:95], v29 offset:35072\n"
  "v_mfma_f32_16x16x32_bf16 v[104:107], a[4:7], a[40:43], v[104:107]\n"
  "s_add_u32 m0, s38, 70656\n"
  "s_nop 0\n"
  "buffer_load_dwordx4 v3, s[16:19], s36 offen lds\n"
  "v_mfma_f32_16x16x32_bf16 v[140:143], a[4:7], a[44:47], v[140:143]\n"
  "ds_read_b128 a[96:99], v30 offset:34816\n"
  "v_mfma_f32_16x16x32_bf16 v[176:179], a[4:7], a[48:51], v[176:179]\n"
  "ds_read_b128 a[100:103], v30 offset:35072\n"
  "v_mfma_f32_16x16x32_bf16 v[72:75], a[8:11], a[36:39], v[72:75]\n"
  "ds_read_b64_tr_b16 a[56:57], v28 offset:34848\n"
  "v_mfma_f32_16x16x32_bf16 v[108:111], a[8:11], a[40:43], v[108:111]\n"
  "s_add_u32 m0, s38, 71680\n"
  "s_nop 0\n"
  "buffer_load_dwordx4 v4, s[16:19], s36 offen lds\n"
  "v_mfma_f32_16x16x32_bf16 v[144:147], a[8:11], a[44:47], v[144:147]\n"
  "ds_read_b64_tr_b16 a[58:59], v28 offset:37152\n"
  "v_mfma_f32_16x16x32_bf16 v[180:183], a[8:11], a[48:51], v[180:183]\n"
  "ds_read_b64_tr_b16 a[60:61], v28 offset:34880\n"
  "v_mfma_f32_16x16x32_bf16 v[76:79], a[12:15], a[36:39], v[76:79]\n"
  "ds_read_b64_tr_b16 a[62:63], v28 offset:37184\n"
  "v_mfma_f32_16x16x32_bf16 v[112:115], a[12:15], a[40:43], v[112:115]\n"
  "s_add_u32 m0, s38, 72704\n"
  "s_nop 0\n"
  "buffer_load_dwordx4 v5, s[16:19], s36 offen lds\n"
  "v_mfma_f32_16x16x32_bf16 v[148:151], a[12:15], a[44:47], v[148:151]\n"
  "ds_read_b64_tr_b16 a[64:65], v28 offset:34912\n"
  "v_mfma_f32_16x16x32_bf16 v[184:187], a[12:15], a[48:51], v[184:187]\n"
  "ds_read_b64_tr_b16 a[66:67], v28 offset:37216\n"
  "v_mfma_f32_16x16x32_bf16 v[80:83], a[16:19], a[36:39], v[80:83]\n"
  "ds_read_b64_tr_b16 a[68:69], v28 offset:34944\n"
  "v_mfma_f32_16x16x32_bf16 v[116:119], a[16:19], a[40:43], v[116:119]\n"
  "s_add_u32 m0, s38, 73728\n"
  "s_mov_b32 exec_hi, 0\n"
  "buffer_load_dwordx4 v6, s[16:19], s36 offen lds\n"
  "s_mov_b32 exec_hi, -1\n"
  "s_add_u32 s36, s36, 73728\n"
  "v_mfma_f32_16x16x32_bf16 v[152:155], a[16:19], a[44:47], v[152:155]\n"
  "ds_read_b64_tr_b16 a[70:71], v28 offset:37248\n"
  "v_mfma_f32_16x16x32_bf16 v[188:191], a[16:19], a[48:51], v[188:191]\n"
  "ds_read_b64_tr_b16 a[72:73], v28 offset:34976\n"
  "v_mfma_f32_16x16x32_bf16 v[84:87], a[20:23], a[36:39], v[84:87]\n"
  "ds_read_b64_tr_b16 a[74:75], v28 offset:37280\n"
  "v_mfma_f32_16x16x32_bf16 v[120:123], a[20:23], a[40:43], v[120:123]\n"
  "s_add_u32 m0, s39, 88064\n"
  "s_nop 0\n"
  "buffer_load_dwordx4 v1, s[20:23], s37 offen sc1 lds\n"
  "v_mfma_f32_16x16x32_bf16 v[156:159], a[20:23], a[44:47], v[156:159]\n"
  "ds_read_b64_tr_b16 a[76:77], v28 offset:35008\n"
  "v_mfma_f32_16x16x32_bf16 v[192:195], a[20:23], a[48:51], v[192:195]\n"
  "ds_read_b64_tr_b16 a[78:79], v28 offset:37312\n"
  "v_mfma_f32_16x16x32_bf16 v[88:91], a[24:27], a[36:39], v[88:91]\n"
  "ds_read_b64_tr_b16 a[80:81], v28 offset:35040\n"
  "v_mfma_f32_16x16x32_bf16 v[124:127], a[24:27], a[40:43], v[124:127]\n"
  "buffer_load_dwordx4 v1, s[20:23], s37 offen offset:1024 sc1 lds\n"
  "v_mfma_f32_16x16x32_bf16 v[160:163], a[24:27], a[44:47], v[160:163]\n"
  "ds_read_b64_tr_b16 a[82:83], v28 offset:37344\n"
  "v_mfma_f32_16x16x32_bf16 v[196:199], a[24:27], a[48:51], v[196:199]\n"
  "ds_read_b64_tr_b16 a[84:85], v28 offset:35072\n"
  "v_mfma_f32_16x16x32_bf16 v[92:95], a[28:31], a[36:39], v[92:95]\n"
  "ds_read_b64_tr_b16 a[86:87], v28 offset:37376\n"
  "v_mfma_f32_16x16x32_bf16 v[128:131], a[28:31], a[40:43], v[128:131]\n"
  "buffer_load_dwordx4 v1, s[20:23], s37 offen offset:2048 sc1 lds\n"
  "v_mfma_f32_16x16x32_bf16 v[164:167], a[28:31], a[44:47], v[164:167]\n"
  "v_mfma_f32_16x16x32_bf16 v[200:203], a[28:31], a[48:51], v[200:203]\n"
  "v_mfma_f32_16x16x32_bf16 v[96:99], a[32:35], a[36:39], v[96:99]\n"
  "v_mfma_f32_16x16x32_bf16 v[132:135], a[32:35], a[40:43], v[132:135]\n"
  "buffer_load_dwordx4 v1, s[20:23], s37 offen offset:3072 sc1 lds\n"
  "s_add_u32 s37, s37, 4096\n"
  "v_mfma_f32_16x16x32_bf16 v[168:171], a[32:35], a[44:47], v[168:171]\n"
  "v_mfma_f32_16x16x32_bf16 v[204:207], a[32:35], a[48:51], v[204:207]\n"
  "s_waitcnt vmcnt(18)\n"
  "s_waitcnt lgkmcnt(0)\n"
  "s_barrier\n"
  "v_mfma_f32_16x16x32_bf16 v[64:67], a[52:55], a[88:91], v[64:67]\n"
  "ds_read_b128 a[36:39], v9 offset:0\n"
  "v_mfma_f32_16x16x32_bf16 v[100:103], a[52:55], a[92:95], v[100:103]\n"
  "s_add_u32 m0, s38, 104448\n"
  "s_nop 0\n"
  "buffer_load_dwordx4 v2, s[16:19], s36 offen lds\n"
  "v_mfma_f32_16x16x32_bf16 v[136:139], a[52:55], a[96:99], v[136:139]\n"
  "ds_read_b64_tr_b16 a[0:1], v8 offset:0\n"
  "v_mfma_f32_16x16x32_bf16 v[172:175], a[52:55], a[100:103], v[172:175]\n"
  "ds_read_b64_tr_b16 a[2:3], v8 offset:2304\n"
  "v_mfma_f32_16x16x32_bf16 v[68:71], a[56:59], a[88:91], v[68:71]\n"
  "ds_read_b128 a[40:43], v9 offset:256\n"
  "v_mfma_f32_16x16x32_bf16 v[104:107], a[56:59], a[92:95], v[104:107]\n"
  "s_add_u32 m0, s38, 105472\n"
  "s_nop 0\n"
  "buffer_load_dwordx4 v3, s[16:19], s36 offen lds\n"
  "v_mfma_f32_16x16x32_bf16 v[140:143], a[56:59], a[96:99], v[140:143]\n"
  "ds_read_b128 a[44:47], v10 offset:0\n"
  "v_mfma_f32_16x16x32_bf16 v[176:179], a[56:59], a[100:103], v[176:179]\n"
  "ds_read_b128 a[48:51], v10 offset:256\n"
  "v_mfma_f32_16x16x32_bf16 v[72:75], a[60:63], a[88:91], v[72:75]\n"
  "ds_read_b64_tr_b16 a[4:5], v8 offset:32\n"
  "v_mfma_f32_16x16x32_bf16 v[108:111], a[60:63], a[92:95], v[108:111]\n"
  "s_add_u32 m0, s38, 106496\n"
  "s_nop 0\n"
  "buffer_load_dwordx4 v4, s[16:19], s36 offen lds\n"
  "v_mfma_f32_16x16x32_bf16 v[144:147], a[60:63], a[96:99], v[144:147]\n"
  "ds_read_b64_tr_b16 a[6:7], v8 offset:2336\n"
  "v_mfma_f32_16x16x32_bf16 v[180:183], a[60:63], a[100:103], v[180:183]\n"
  "ds_read_b64_tr_b16 a[8:9], v8 offset:64\n"
  "v_mfma_f32_16x16x32_bf16 v[76:79], a[64:67], a[88:91], v[76:79]\n"
  "ds_read_b64_tr_b16 a[10:11], v8 offset:2368\n"
  "v_mfma_f32_16x16x32_bf16 v[112:115], a[64:67], a[92:95], v[112:115]\n"
  "s_add_u32 m0, s38, 107520\n"
  "s_nop 0\n"
  "buffer_load_dwordx4 v5, s[16:19], s36 offen lds\n"
  "v_mfma_f32_16x16x32_bf16 v[148:151], a[64:67], a[96:99], v[148:151]\n"
  "ds_read_b64_tr_b16 a[12:13], v8 offset:96\n"
  "v_mfma_f32_16x16x32_bf16 v[184:187], a[64:67], a[100:103], v[184:187]\n"
  "ds_read_b64_tr_b16 a[14:15], v8 offset:2400\n"
  "v_mfma_f32_16x16x32_bf16 v[80:83], a[68:71], a[88:91], v[80:83]\n"
  "ds_read_b64_tr_b16 a[16:17], v8 offset:128\n"
  "v_mfma_f32_16x16x32_bf16 v[116:119], a[68:71], a[92:95], v[116:119]\n"
  "s_add_u32 m0, s38, 108544\n"
  "s_mov_b32 exec_hi, 0\n"
  "buffer_load_dwordx4 v6, s[16:19], s36 offen lds\n"
  "s_mov_b32 exec_hi, -1\n"
  "s_add_u32 s36, s36, 73728\n"
  "v_mfma_f32_16x16x32_bf16 v[152:155], a[68:71], a[96:99], v[152:155]\n"
  "ds_read_b64_tr_b16 a[18:19], v8 offset:2432\n"
  "v_mfma_f32_16x16x32_bf16 v[188:191], a[68:71], a[100:103], v[188:191]\n"
  "ds_read_b64_tr_b16 a[20:21], v8 offset:160\n"
  "v_mfma_f32_16x16x32_bf16 v[84:87], a[72:75], a[88:91], v[84:87]\n"
  "ds_read_b64_tr_b16 a[22:23], v8 offset:2464\n"
  "v_mfma_f32_16x16x32_bf16 v[120:123], a[72:75], a[92:95], v[120:123]\n"
  "s_add_u32 m0, s39, 122880\n"
  "s_nop 0\n"
  "buffer_load_dwordx4 v1, s[20:23], s37 offen sc1 lds\n"
  "v_mfma_f32_16x16x32_bf16 v[156:159], a[72:75], a[96:99], v[156:159]\n"
  "ds_read_b64_tr_b16 a[24:25], v8 offset:192\n"
  "v_mfma_f32_16x16x32_bf16 v[192:195], a[72:75], a[100:103], v[192:195]\n"
  "ds_read_b64_tr_b16 a[26:27], v8 offset:2496\n"
  "v_mfma_f32_16x16x32_bf16 v[88:91], a[76:79], a[88:91], v[88:91]\n"
  "ds_read_b64_tr_b16 a[28:29], v8 offset:224\n"
  "v_mfma_f32_16x16x32_bf16 v[124:127], a[76:79], a[92:95], v[124:127]\n"
  "buffer_load_dwordx4 v1, s[20:23], s37 offen offset:1024 sc1 lds\n"
  "v_mfma_f32_16x16x32_bf16 v[160:163], a[76:79], a[96:99], v[160:163]\n"
  "ds_read_b64_tr_b16 a[30:31], v8 offset:2528\n"
  "v_mfma_f32_16x16x32_bf16 v[196:199], a[76:79], a[100:103], v[196:199]\n"
  "ds_read_b64_tr_b16 a[32:33], v8 offset:256\n"
  "v_mfma_f32_16x16x32_bf16 v[92:95], a[80:83], a[88:91], v[92:95]\n"
  "ds_read_b64_tr_b16 a[34:35], v8 offset:2560\n"
  "v_mfma_f32_16x16x32_bf16 v[128:131], a[80:83], a[92:95], v[128:131]\n"
  "buffer_load_dwordx4 v1, s[20:23], s37 offen offset:2048 sc1 lds\n"
  "v_mfma_f32_16x16x32_bf16 v[164:167], a[80:83], a[96:99], v[164:167]\n"
  "v_mfma_f32_16x16x32_bf16 v[200:203], a[80:83], a[100:103], v[200:203]\n"
  "v_mfma_f32_16x16x32_bf16 v[96:99], a[84:87], a[88:91], v[96:99]\n"
  "v_mfma_f32_16x16x32_bf16 v[132:135], a[84:87], a[92:95], v[132:135]\n"
  "buffer_load_dwordx4 v1, s[20:23], s37 offen offset:3072 sc1 lds\n"
  "s_add_u32 s37, s37, 520192\n"
  "v_mfma_f32_16x16x32_bf16 v[168:171], a[84:87], a[96:99], v[168:171]\n"
  "v_mfma_f32_16x16x32_bf16 v[204:207], a[84:87], a[100:103], v[204:207]\n"
  "s_waitcnt vmcnt(18)\n"
  "s_waitcnt lgkmcnt(0)\n"
  "s_barrier\n"
  "v_mfma_f32_16x16x32_bf16 v[64:67], a[0:3], a[36:39], v[64:67]\n"
  "ds_read_b128 a[88:91], v9 offset:34816\n"
  "v_mfma_f32_16x16x32_bf16 v[100:103], a[0:3], a[40:43], v[100:103]\n"
  "s_add_u32 m0, s38, 0\n"
  "s_nop 0\n"
  "buffer_load_dwordx4 v2, s[16:19], s36 offen lds\n"
  "v_mfma_f32_16x16x32_bf16 v[136:139], a[0:3], a[44:47], v[136:139]\n"
  "ds_read_b64_tr_b16 a[52:53], v8 offset:34816\n"
  "v_mfma_f32_16x16x32_bf16 v[172:175], a[0:3], a[48:51], v[172:175]\n"
  "ds_read_b64_tr_b16 a[54:55], v8 offset:37120\n"
  "v_mfma_f32_16x16x32_bf16 v[68:71], a[4:7], a[36:39], v[68:71]\n"
  "ds_read_b128 a[92:95], v9 offset:35072\n"
  "v_mfma_f32_16x16x32_bf16 v[104:107], a[4:7], a[40:43], v[104:107]\n"
  "s_add_u32 m0, s38, 1024\n"
  "s_nop 0\n"
  "buffer_load_dwordx4 v3, s[16:19], s36 offen lds\n"
  "v_mfma_f32_16x16x32_bf16 v[140:143], a[4:7], a[44:47], v[140:143]\n"
  "ds_read_b128 a[96:99], v10 offset:34816\n"
  "v_mfma_f32_16x16x32_bf16 v[176:179], a[4:7], a[48:51], v[176:179]\n"
  "ds_read_b128 a[100:103], v10 offset:35072\n"
  "v_mfma_f32_16x16x32_bf16 v[72:75], a[8:11], a[36:39], v[72:75]\n"
  "ds_read_b64_tr_b16 a[56:57], v8 offset:34848\n"
  "v_mfma_f32_16x16x32_bf16 v[108:111], a[8:11], a[40:43], v[108:111]\n"
  "s_add_u32 m0, s38, 2048\n"
  "s_nop 0\n"
  "buffer_load_dwordx4 v4, s[16:19], s36 offen lds\n"
  "v_mfma_f32_16x16x32_bf16 v[144:147], a[8:11], a[44:47], v[144:147]\n"
  "ds_read_b64_tr_b16 a[58:59], v8 offset:37152\n"
  "v_mfma_f32_16x16x32_bf16 v[180:183], a[8:11], a[48:51], v[180:183]\n"
  "ds_read_b64_tr_b16 a[60:61], v8 offset:34880\n"
  "v_mfma_f32_16x16x32_bf16 v[76:79], a[12:15], a[36:39], v[76:79]\n"
  "ds_read_b64_tr_b16 a[62:63], v8 offset:37184\n"
  "v_mfma_f32_16x16x32_bf16 v[112:115], a[12:15], a[40:43], v[112:115]\n"
  "s_add_u32 m0, s38, 3072\n"
  "s_nop 0\n"
  "buffer_load_dwordx4 v5, s[16:19], s36 offen lds\n"
  "v_mfma_f32_16x16x32_bf16 v[148:151], a[12:15], a[44:47], v[148:151]\n"
  "ds_read_b64_tr_b16 a[64:65], v8 offset:34912\n"
  "v_mfma_f32_16x16x32_bf16 v[184:187], a[12:15], a[48:51], v[184:187]\n"
  "ds_read_b64_tr_b16 a[66:67], v8 offset:37216\n"
  "v_mfma_f32_16x16x32_bf16 v[80:83], a[16:19], a[36:39], v[80:83]\n"
  "ds_read_b64_tr_b16 a[68:69], v8 offset:34944\n"
  "v_mfma_f32_16x16x32_bf16 v[116:119], a[16:19], a[40:43], v[116:119]\n"
  "s_add_u32 m0, s38, 4096\n"
  "s_mov_b32 exec_hi, 0\n"
  "buffer_load_dwordx4 v6, s[16:19], s36 offen lds\n"
  "s_mov_b32 exec_hi, -1\n"
  "s_add_u32 s36, s36, 73728\n"
  "v_mfma_f32_16x16x32_bf16 v[152:155], a[16:19], a[44:47], v[152:155]\n"
  "ds_read_b64_tr_b16 a[70:71], v8 offset:37248\n"
  "v_mfma_f32_16x16x32_bf16 v[188:191], a[16:19], a[48:51], v[188:191]\n"
  "ds_read_b64_tr_b16 a[72:73], v8 offset:34976\n"
  "v_mfma_f32_16x16x32_bf16 v[84:87], a[20:23], a[36:39], v[84:87]\n"
  "ds_read_b64_tr_b16 a[74:75], v8 offset:37280\n"
  "v_mfma_f32_16x16x32_bf16 v[120:123], a[20:23], a[40:43], v[120:123]\n"
  "s_add_u32 m0, s39, 18432\n"
  "s_nop 0\n"
  "buffer_load_dwordx4 v1, s[20:23], s37 offen sc1 lds\n"
  "v_mfma_f32_16x16x32_bf16 v[156:159], a[20:23], a[44:47], v[156:159]\n"
  "ds_read_b64_tr_b16 a[76:77], v8 offset:35008\n"
  "v_mfma_f32_16x16x32_bf16 v[192:195], a[20:23], a[48:51], v[192:195]\n"
  "ds_read_b64_tr_b16 a[78:79], v8 offset:37312\n"
  "v_mfma_f32_16x16x32_bf16 v[88:91], a[24:27], a[36:39], v[88:91]\n"
  "ds_read_b64_tr_b16 a[80:81], v8 offset:35040\n"
  "v_mfma_f32_16x16x32_bf16 v[124:127], a[24:27], a[40:43], v[124:127]\n"
  "buffer_load_dwordx4 v1, s[20:23], s37 offen offset:1024 sc1 lds\n"
  "v_mfma_f32_16x16x32_bf16 v[160:163], a[24:27], a[44:47], v[160:163]\n"
  "ds_read_b64_tr_b16 a[82:83], v8 offset:37344\n"
  "v_mfma_f32_16x16x32_bf16 v[196:199], a[24:27], a[48:51], v[196:199]\n"
  "ds_read_b64_tr_b16 a[84:85], v8 offset:35072\n"
  "v_mfma_f32_16x16x32_bf16 v[92:95], a[28:31], a[36:39], v[92:95]\n"
  "ds_read_b64_tr_b16 a[86:87], v8 offset:37376\n"
  "v_mfma_f32_16x16x32_bf16 v[128:131], a[28:31], a[40:43], v[128:131]\n"
  "buffer_load_dwordx4 v1, s[20:23], s37 offen offset:2048 sc1 lds\n"
  "v_mfma_f32_16x16x32_bf16 v[164:167], a[28:31], a[44:47], v[164:167]\n"
  "v_mfma_f32_16x16x32_bf16 v[200:203], a[28:31], a[48:51], v[200:203]\n"
  "v_mfma_f32_16x16x32_bf16 v[96:99], a[32:35], a[36:39], v[96:99]\n"
  "v_mfma_f32_16x16x32_bf16 v[132:135], a[32:35], a[40:43], v[132:135]\n"
  "buffer_load_dwordx4 v1, s[20:23], s37 offen offset:3072 sc1 lds\n"
  "s_add_u32 s37, s37, 4096\n"
  "v_mfma_f32_16x16x32_bf16 v[168:171], a[32:35], a[44:47], v[168:171]\n"
  "v_mfma_f32_16x16x32_bf16 v[204:207], a[32:35], a[48:51], v[204:207]\n"
  "s_waitcnt vmcnt(18)\n"
  "s_waitcnt lgkmcnt(0)\n"
  "s_barrier\n"
  "v_mfma_f32_16x16x32_bf16 v[64:67], a[52:55], a[88:91], v[64:67]\n"
  "ds_read_b128 a[36:39], v29 offset:0\n"
  "v_mfma_f32_16x16x32_bf16 v[100:103], a[52:55], a[92:95], v[100:103]\n"
  "s_add_u32 m0, s38, 34816\n"
  "s_nop 0\n"
  "buffer_load_dwordx4 v2, s[16:19], s36 offen lds\n"
  "v_mfma_f32_16x16x32_bf16 v[136:139], a[52:55], a[96:99], v[136:139]\n"
  "ds_read_b64_tr_b16 a[0:1], v28 offset:0\n"
  "v_mfma_f32_16x16x32_bf16 v[172:175], a[52:55], a[100:103], v[172:175]\n"
  "ds_read_b64_tr_b16 a[2:3], v28 offset:2304\n"
  "v_mfma_f32_16x16x32_bf16 v[68:71], a[56:59], a[88:91], v[68:71]\n"
  "ds_read_b128 a[40:43], v29 offset:256\n"
  "v_mfma_f32_16x16x32_bf16 v[104:107], a[56:59], a[92:95], v[104:107]\n"
  "s_add_u32 m0, s38, 35840\n"
  "s_nop 0\n"
  "buffer_load_dwordx4 v3, s[16:19], s36 offen lds\n"
  "v_mfma_f32_16x16x32_bf16 v[140:143], a[56:59], a[96:99], v[140:143]\n"
  "ds_read_b128 a[44:47], v30 offset:0\n"
  "v_mfma_f32_16x16x32_bf16 v[176:179], a[56:59], a[100:103], v[176:179]\n"
  "ds_read_b128 a[48:51], v30 offset:256\n"
  "v_mfma_f32_16x16x32_bf16 v[72:75], a[60:63], a[88:91], v[72:75]\n"
  "ds_read_b64_tr_b16 a[4:5], v28 offset:32\n"
  "v_mfma_f32_16x16x32_bf16 v[108:111], a[60:63], a[92:95], v[108:111]\n"
  "s_add_u32 m0, s38, 36864\n"
  "s_nop 0\n"
  "buffer_load_dwordx4 v4, s[16:19], s36 offen lds\n"
  "v_mfma_f32_16x16x32_bf16 v[144:147], a[60:63], a[96:99], v[144:147]\n"
  "ds_read_b64_tr_b16 a[6:7], v28 offset:2336\n"
  "v_mfma_f32_16x16x32_bf16 v[180:183], a[60:63], a[100:103], v[180:183]\n"
  "ds_read_b64_tr_b16 a[8:9], v28 offset:64\n"
  "v_mfma_f32_16x16x32_bf16 v[76:79], a[64:67], a[88:91], v[76:79]\n"
  "ds_read_b64_tr_b16 a[10:11], v28 offset:2368\n"
  "v_mfma_f32_16x16x32_bf16 v[112:115], a[64:67], a[92:95], v[112:115]\n"
  "s_add_u32 m0, s38, 37888\n"
  "s_nop 0\n"
  "buffer_load_dwordx4 v5, s[16:19], s36 offen lds\n"
  "v_mfma_f32_16x16x32_bf16 v[148:151], a[64:67], a[96:99], v[148:151]\n"
  "ds_read_b64_tr_b16 a[12:13], v28 offset:96\n"
  "v_mfma_f32_16x16x32_bf16 v[184:187], a[64:67], a[100:103], v[184:187]\n"
  "ds_read_b64_tr_b16 a[14:15], v28 offset:2400\n"
  "v_mfma_f32_16x16x32_bf16 v[80:83], a[68:71], a[88:91], v[80:83]\n"
  "ds_read_b64_tr_b16 a[16:17], v28 offset:128\n"
  "v_mfma_f32_16x16x32_bf16 v[116:119], a[68:71], a[92:95], v[116:119]\n"
  "s_add_u32 m0, s38, 38912\n"
  "s_mov_b32 exec_hi, 0\n"
  "buffer_load_dwordx4 v6, s[16:19], s36 offen lds\n"
  "s_mov_b32 exec_hi, -1\n"
  "s_add_u32 s36, s36, 73728\n"
  "v_mfma_f32_16x16x32_bf16 v[152:155], a[68:71], a[96:99], v[152:155]\n"
  "ds_read_b64_tr_b16 a[18:19], v28 offset:2432\n"
  "v_mfma_f32_16x16x32_bf16 v[188:191], a[68:71], a[100:103], v[188:191]\n"
  "ds_read_b64_tr_b16 a[20:21], v28 offset:160\n"
  "v_mfma_f32_16x16x32_bf16 v[84:87], a[72:75], a[88:91], v[84:87]\n"
  "ds_read_b64_tr_b16 a[22:23], v28 offset:2464\n"
  "v_mfma_f32_16x16x32_bf16 v[120:123], a[72:75], a[92:95], v[120:123]\n"
  "s_add_u32 m0, s39, 53248\n"
  "s_nop 0\n"
  "buffer_load_dwordx4 v1, s[20:23], s37 offen sc1 lds\n"
  "v_mfma_f32_16x16x32_bf16 v[156:159], a[72:75], a[96:99], v[156:159]\n"
  "ds_read_b64_tr_b16 a[24:25], v28 offset:192\n"
  "v_mfma_f32_16x16x32_bf16 v[192:195], a[72:75], a[100:103], v[192:195]\n"
  "ds_read_b64_tr_b16 a[26:27], v28 offset:2496\n"
  "v_mfma_f32_16x16x32_bf16 v[88:91], a[76:79], a[88:91], v[88:91]\n"
  "ds_read_b64_tr_b16 a[28:29], v28 offset:224\n"
  "v_mfma_f32_16x16x32_bf16 v[124:127], a[76:79], a[92:95], v[124:127]\n"
  "buffer_load_dwordx4 v1, s[20:23], s37 offen offset:1024 sc1 lds\n"
  "v_mfma_f32_16x16x32_bf16 v[160:163], a[76:79], a[96:99], v[160:163]\n"
  "ds_read_b64_tr_b16 a[30:31], v28 offset:2528\n"
  "v_mfma_f32_16x16x32_bf16 v[196:199], a[76:79], a[100:103], v[196:199]\n"
  "ds_read_b64_tr_b16 a[32:33], v28 offset:256\n"
  "v_mfma_f32_16x16x32_bf16 v[92:95], a[80:83], a[88:91], v[92:95]\n"
  "ds_read_b64_tr_b16 a[34:35], v28 offset:2560\n"
  "v_mfma_f32_16x16x32_bf16 v[128:131], a[80:83], a[92:95], v[128:131]\n"
  "buffer_load_dwordx4 v1, s[20:23], s37 offen offset:2048 sc1 lds\n"
  "v_mfma_f32_16x16x32_bf16 v[164:167], a[80:83], a[96:99], v[164:167]\n"
  "v_mfma_f32_16x16x32_bf16 v[200:203], a[80:83], a[100:103], v[200:203]\n"
  "v_mfma_f32_16x16x32_bf16 v[96:99], a[84:87], a[88:91], v[96:99]\n"
  "v_mfma_f32_16x16x32_bf16 v[132:135], a[84:87], a[92:95], v[132:135]\n"
  "buffer_load_dwordx4 v1, s[20:23], s37 offen offset:3072 sc1 lds\n"
  "s_add_u32 s37, s37, 520192\n"
  "v_mfma_f32_16x16x32_bf16 v[168:171], a[84:87], a[96:99], v[168:171]\n"
  "v_mfma_f32_16x16x32_bf16 v[204:207], a[84:87], a[100:103], v[204:207]\n"
  "s_waitcnt vmcnt(18)\n"
  "s_waitcnt lgkmcnt(0)\n"
  "s_barrier\n"
  "v_mfma_f32_16x16x32_bf16 v[64:67], a[0:3], a[36:39], v[64:67]\n"
  "ds_read_b128 a[88:91], v29 offset:34816\n"
  "v_mfma_f32_16x16x32_bf16 v[100:103], a[0:3], a[40:43], v[100:103]\n"
  "s_add_u32 m0, s38, 69632\n"
  "s_nop 0\n"
  "buffer_load_dwordx4 v2, s[16:19], s36 offen lds\n"
  "v_mfma_f32_16x16x32_bf16 v[136:139], a[0:3], a[44:47], v[136:139]\n"
  "ds_read_b64_tr_b16 a[52:53], v28 offset:34816\n"
  "v_mfma_f32_16x16x32_bf16 v[172:175], a[0:3], a[48:51], v[172:175]\n"
  "ds_read_b64_tr_b16 a[54:55], v28 offset:37120\n"
  "v_mfma_f32_16x16x32_bf16 v[68:71], a[4:7], a[36:39], v[68:71]\n"
  "ds_read_b128 a[92:95], v29 offset:35072\n"
  "v_mfma_f32_16x16x32_bf16 v[104:107], a[4:7], a[40:43], v[104:107]\n"
  "s_add_u32 m0, s38, 70656\n"
  "s_nop 0\n"
  "buffer_load_dwordx4 v3, s[16:19], s36 offen lds\n"
  "v_mfma_f32_16x16x32_bf16 v[140:143], a[4:7], a[44:47], v[140:143]\n"
  "ds_read_b128 a[96:99], v30 offset:34816\n"
  "v_mfma_f32_16x16x32_bf16 v[176:179], a[4:7], a[48:51], v[176:179]\n"
  "ds_read_b128 a[100:103], v30 offset:35072\n"
  "v_mfma_f32_16x16x32_bf16 v[72:75], a[8:11], a[36:39], v[72:75]\n"
  "ds_read_b64_tr_b16 a[56:57], v28 offset:34848\n"
  "v_mfma_f32_16x16x32_bf16 v[108:111], a[8:11], a[40:43], v[108:111]\n"
  "s_add_u32 m0, s38, 71680\n"
  "s_nop 0\n"
  "buffer_load_dwordx4 v4, s[16:19], s36 offen lds\n"
  "v_mfma_f32_16x16x32_bf16 v[144:147], a[8:11], a[44:47], v[144:147]\n"
  "ds_read_b64_tr_b16 a[58:59], v28 offset:37152\n"
  "v_mfma_f32_16x16x32_bf16 v[180:183], a[8:11], a[48:51], v[180:183]\n"
  "ds_read_b64_tr_b16 a[60:61], v28 offset:34880\n"
  "v_mfma_f32_16x16x32_bf16 v[76:79], a[12:15], a[36:39], v[76:79]\n"
  "ds_read_b64_tr_b16 a[62:63], v28 offset:37184\n"
  "v_mfma_f32_16x16x32_bf16 v[112:115], a[12:15], a[40:43], v[112:115]\n"
  "s_add_u32 m0, s38, 72704\n"
  "s_nop 0\n"
  "buffer_load_dwordx4 v5, s[16:19], s36 offen lds\n"
  "v_mfma_f32_16x16x32_bf16 v[148:151], a[12:15], a[44:47], v[148:151]\n"
  "ds_read_b64_tr_b16 a[64:65], v28 offset:34912\n"
  "v_mfma_f32_16x16x32_bf16 v[184:187], a[12:15], a[48:51], v[184:187]\n"
  "ds_read_b64_tr_b16 a[66:67], v28 offset:37216\n"
  "v_mfma_f32_16x16x32_bf16 v[80:83], a[16:19], a[36:39], v[80:83]\n"
  "ds_read_b64_tr_b16 a[68:69], v28 offset:34944\n"
  "v_mfma_f32_16x16x32_bf16 v[116:119], a[16:19], a[40:43], v[116:119]\n"
  "s_add_u32 m0, s38, 73728\n"
  "s_mov_b32 exec_hi, 0\n"
  "buffer_load_dwordx4 v6, s[16:19], s36 offen lds\n"
  "s_mov_b32 exec_hi, -1\n"
  "s_add_u32 s36, s36, 73728\n"
  "v_mfma_f32_16x16x32_bf16 v[152:155], a[16:19], a[44:47], v[152:155]\n"
  "ds_read_b64_tr_b16 a[70:71], v28 offset:37248\n"
  "v_mfma_f32_16x16x32_bf16 v[188:191], a[16:19], a[48:51], v[188:191]\n"
  "ds_read_b64_tr_b16 a[72:73], v28 offset:34976\n"
  "v_mfma_f32_16x16x32_bf16 v[84:87], a[20:23], a[36:39], v[84:87]\n"
  "ds_read_b64_tr_b16 a[74:75], v28 offset:37280\n"
  "v_mfma_f32_16x16x32_bf16 v[120:123], a[20:23], a[40:43], v[120:123]\n"
  "s_add_u32 m0, s39, 88064\n"
  "s_nop 0\n"
  "buffer_load_dwordx4 v1, s[20:23], s37 offen sc1 lds\n"
  "v_mfma_f32_16x16x32_bf16 v[156:159], a[20:23], a[44:47], v[156:159]\n"
  "ds_read_b64_tr_b16 a[76:77], v28 offset:35008\n"
  "v_mfma_f32_16x16x32_bf16 v[192:195], a[20:23], a[48:51], v[192:195]\n"
  "ds_read_b64_tr_b16 a[78:79], v28 offset:37312\n"
  "v_mfma_f32_16x16x32_bf16 v[88:91], a[24:27], a[36:39], v[88:91]\n"
  "ds_read_b64_tr_b16 a[80:81], v28 offset:35040\n"
  "v_mfma_f32_16x16x32_bf16 v[124:127], a[24:27], a[40:43], v[124:127]\n"
  "buffer_load_dwordx4 v1, s[20:23], s37 offen offset:1024 sc1 lds\n"
  "v_mfma_f32_16x16x32_bf16 v[160:163], a[24:27], a[44:47], v[160:163]\n"
  "ds_read_b64_tr_b16 a[82:83], v28 offset:37344\n"
  "v_mfma_f32_16x16x32_bf16 v[196:199], a[24:27], a[48:51], v[196:199]\n"
  "ds_read_b64_tr_b16 a[84:85], v28 offset:35072\n"
  "v_mfma_f32_16x16x32_bf16 v[92:95], a[28:31], a[36:39], v[92:95]\n"
  "ds_read_b64_tr_b16 a[86:87], v28 offset:37376\n"
  "v_mfma_f32_16x16x32_bf16 v[128:131], a[28:31], a[40:43], v[128:131]\n"
  "buffer_load_dwordx4 v1, s[20:23], s37 offen offset:2048 sc1 lds\n"
  "v_mfma_f32_16x16x32_bf16 v[164:167], a[28:31], a[44:47], v[164:167]\n"
  "v_mfma_f32_16x16x32_bf16 v[200:203], a[28:31], a[48:51], v[200:203]\n"
  "v_mfma_f32_16x16x32_bf16 v[96:99], a[32:35], a[36:39], v[96:99]\n"
  "v_mfma_f32_16x16x32_bf16 v[132:135], a[32:35], a[40:43], v[132:135]\n"
  "buffer_load_dwordx4 v1, s[20:23], s37 offen offset:3072 sc1 lds\n"
  "s_add_u32 s37, s37, 4096\n"
  "v_mfma_f32_16x16x32_bf16 v[168:171], a[32:35], a[44:47], v[168:171]\n"
  "v_mfma_f32_16x16x32_bf16 v[204:207], a[32:35], a[48:51], v[204:207]\n"
  "s_waitcnt vmcnt(18)\n"
  "s_waitcnt lgkmcnt(0)\n"
  "s_barrier\n"
  "v_mfma_f32_16x16x32_bf16 v[64:67], a[52:55], a[88:91], v[64:67]\n"
  "ds_read_b128 a[36:39], v9 offset:0\n"
  "v_mfma_f32_16x16x32_bf16 v[100:103], a[52:55], a[92:95], v[100:103]\n"
  "s_add_u32 m0, s38, 104448\n"
  "s_nop 0\n"
  "buffer_load_dwordx4 v2, s[16:19], s36 offen lds\n"
  "v_mfma_f32_16x16x32_bf16 v[136:139], a[52:55], a[96:99], v[136:139]\n"
  "ds_read_b64_tr_b16 a[0:1], v8 offset:0\n"
  "v_mfma_f32_16x16x32_bf16 v[172:175], a[52:55], a[100:103], v[172:175]\n"
  "ds_read_b64_tr_b16 a[2:3], v8 offset:2304\n"
  "v_mfma_f32_16x16x32_bf16 v[68:71], a[56:59], a[88:91], v[68:71]\n"
  "ds_read_b128 a[40:43], v9 offset:256\n"
  "v_mfma_f32_16x16x32_bf16 v[104:107], a[56:59], a[92:95], v[104:107]\n"
  "s_add_u32 m0, s38, 105472\n"
  "s_nop 0\n"
  "buffer_load_dwordx4 v3, s[16:19], s36 offen lds\n"
  "v_mfma_f32_16x16x32_bf16 v[140:143], a[56:59], a[96:99], v[140:143]\n"
  "ds_read_b128 a[44:47], v10 offset:0\n"
  "v_mfma_f32_16x16x32_bf16 v[176:179], a[56:59], a[100:103], v[176:179]\n"
  "ds_read_b128 a[48:51], v10 offset:256\n"
  "v_mfma_f32_16x16x32_bf16 v[72:75], a[60:63], a[88:91], v[72:75]\n"
  "ds_read_b64_tr_b16 a[4:5], v8 offset:32\n"
  "v_mfma_f32_16x16x32_bf16 v[108:111], a[60:63], a[92:95], v[108:111]\n"
  "s_add_u32 m0, s38, 106496\n"
  "s_nop 0\n"
  "buffer_load_dwordx4 v4, s[16:19], s36 offen lds\n"
  "v_mfma_f32_16x16x32_bf16 v[144:147], a[60:63], a[96:99], v[144:147]\n"
  "ds_read_b64_tr_b16 a[6:7], v8 offset:2336\n"
  "v_mfma_f32_16x16x32_bf16 v[180:183], a[60:63], a[100:103], v[180:183]\n"
  "ds_read_b64_tr_b16 a[8:9], v8 offset:64\n"
  "v_mfma_f32_16x16x32_bf16 v[76:79], a[64:67], a[88:91], v[76:79]\n"
  "ds_read_b64_tr_b16 a[10:11], v8 offset:2368\n"
  "v_mfma_f32_16x16x32_bf16 v[112:115], a[64:67], a[92:95], v[112:115]\n"
  "s_add_u32 m0, s38, 107520\n"
  "s_nop 0\n"
  "buffer_load_dwordx4 v5, s[16:19], s36 offen lds\n"
  "v_mfma_f32_16x16x32_bf16 v[148:151], a[64:67], a[96:99], v[148:151]\n"
  "ds_read_b64_tr_b16 a[12:13], v8 offset:96\n"
  "v_mfma_f32_16x16x32_bf16 v[184:187], a[64:67], a[100:103], v[184:187]\n"
  "ds_read_b64_tr_b16 a[14:15], v8 offset:2400\n"
  "v_mfma_f32_16x16x32_bf16 v[80:83], a[68:71], a[88:91], v[80:83]\n"
  "ds_read_b64_tr_b16 a[16:17], v8 offset:128\n"
  "v_mfma_f32_16x16x32_bf16 v[116:119], a[68:71], a[92:95], v[116:119]\n"
  "s_add_u32 m0, s38, 108544\n"
  "s_mov_b32 exec_hi, 0\n"
  "buffer_load_dwordx4 v6, s[16:19], s36 offen lds\n"
  "s_mov_b32 exec_hi, -1\n"
  "s_add_u32 s36, s36, 73728\n"
  "v_mfma_f32_16x16x32_bf16 v[152:155], a[68:71], a[96:99], v[152:155]\n"
  "ds_read_b64_tr_b16 a[18:19], v8 offset:2432\n"
  "v_mfma_f32_16x16x32_bf16 v[188:191], a[68:71], a[100:103], v[188:191]\n"
  "ds_read_b64_tr_b16 a[20:21], v8 offset:160\n"
  "v_mfma_f32_16x16x32_bf16 v[84:87], a[72:75], a[88:91], v[84:87]\n"
  "ds_read_b64_tr_b16 a[22:23], v8 offset:2464\n"
  "v_mfma_f32_16x16x32_bf16 v[120:123], a[72:75], a[92:95], v[120:123]\n"
  "s_add_u32 m0, s39, 122880\n"
  "s_nop 0\n"
  "buffer_load_dwordx4 v1, s[20:23], s37 offen sc1 lds\n"
  "v_mfma_f32_16x16x32_bf16 v[156:159], a[72:75], a[96:99], v[156:159]\n"
  "ds_read_b64_tr_b16 a[24:25], v8 offset:192\n"
  "v_mfma_f32_16x16x32_bf16 v[192:195], a[72:75], a[100:103], v[192:195]\n"
  "ds_read_b64_tr_b16 a[26:27], v8 offset:2496\n"
  "v_mfma_f32_16x16x32_bf16 v[88:91], a[76:79], a[88:91], v[88:91]\n"
  "ds_read_b64_tr_b16 a[28:29], v8 offset:224\n"
  "v_mfma_f32_16x16x32_bf16 v[124:127], a[76:79], a[92:95], v[124:127]\n"
  "buffer_load_dwordx4 v1, s[20:23], s37 offen offset:1024 sc1 lds\n"
  "v_mfma_f32_16x16x32_bf16 v[160:163], a[76:79], a[96:99], v[160:163]\n"
  "ds_read_b64_tr_b16 a[30:31], v8 offset:2528\n"
  "v_mfma_f32_16x16x32_bf16 v[196:199], a[76:79], a[100:103], v[196:199]\n"
  "ds_read_b64_tr_b16 a[32:33], v8 offset:256\n"
  "v_mfma_f32_16x16x32_bf16 v[92:95], a[80:83], a[88:91], v[92:95]\n"
  "ds_read_b64_tr_b16 a[34:35], v8 offset:2560\n"
  "v_mfma_f32_16x16x32_bf16 v[128:131], a[80:83], a[92:95], v[128:131]\n"
  "buffer_load_dwordx4 v1, s[20:23], s37 offen offset:2048 sc1 lds\n"
  "v_mfma_f32_16x16x32_bf16 v[164:167], a[80:83], a[96:99], v[164:167]\n"
  "v_mfma_f32_16x16x32_bf16 v[200:203], a[80:83], a[100:103], v[200:203]\n"
  "v_mfma_f32_16x16x32_bf16 v[96:99], a[84:87], a[88:91], v[96:99]\n"
  "v_mfma_f32_16x16x32_bf16 v[132:135], a[84:87], a[92:95], v[132:135]\n"
  "buffer_load_dwordx4 v1, s[20:23], s37 offen offset:3072 sc1 lds\n"
  "s_add_u32 s37, s37, 520192\n"
  "v_mfma_f32_16x16x32_bf16 v[168:171], a[84:87], a[96:99], v[168:171]\n"
  "v_mfma_f32_16x16x32_bf16 v[204:207], a[84:87], a[100:103], v[204:207]\n"
  "s_waitcnt vmcnt(18)\n"
  "s_waitcnt lgkmcnt(0)\n"
  "s_barrier\n"
  "v_mfma_f32_16x16x32_bf16 v[64:67], a[0:3], a[36:39], v[64:67]\n"
  "ds_read_b128 a[88:91], v9 offset:34816\n"
  "v_mfma_f32_16x16x32_bf16 v[100:103], a[0:3], a[40:43], v[100:103]\n"
  "ds_read_b64_tr_b16 a[52:53], v8 offset:34816\n"
  "v_mfma_f32_16x16x32_bf16 v[136:139], a[0:3], a[44:47], v[136:139]\n"
  "ds_read_b64_tr_b16 a[54:55], v8 offset:37120\n"
  "v_mfma_f32_16x16x32_bf16 v[172:175], a[0:3], a[48:51], v[172:175]\n"
  "ds_read_b128 a[92:95], v9 offset:35072\n"
  "v_mfma_f32_16x16x32_bf16 v[68:71], a[4:7], a[36:39], v[68:71]\n"
  "ds_read_b128 a[96:99], v10 offset:34816\n"
  "v_mfma_f32_16x16x32_bf16 v[104:107], a[4:7], a[40:43], v[104:107]\n"
  "ds_read_b128 a[100:103], v10 offset:35072\n"
  "v_mfma_f32_16x16x32_bf16 v[140:143], a[4:7], a[44:47], v[140:143]\n"
  "ds_read_b64_tr_b16 a[56:57], v8 offset:34848\n"
  "v_mfma_f32_16x16x32_bf16 v[176:179], a[4:7], a[48:51], v[176:179]\n"
  "ds_read_b64_tr_b16 a[58:59], v8 offset:37152\n"
  "v_mfma_f32_16x16x32_bf16 v[72:75], a[8:11], a[36:39], v[72:75]\n"
  "ds_read_b64_tr_b16 a[60:61], v8 offset:34880\n"
  "v_mfma_f32_16x16x32_bf16 v[108:111], a[8:11], a[40:43], v[108:111]\n"
  "ds_read_b64_tr_b16 a[62:63], v8 offset:37184\n"
  "v_mfma_f32_16x16x32_bf16 v[144:147], a[8:11], a[44:47], v[144:147]\n"
  "ds_read_b64_tr_b16 a[64:65], v8 offset:34912\n"
  "v_mfma_f32_16x16x32_bf16 v[180:183], a[8:11], a[48:51], v[180:183]\n"
  "ds_read_b64_tr_b16 a[66:67], v8 offset:37216\n"
  "v_mfma_f32_16x16x32_bf16 v[76:79], a[12:15], a[36:39], v[76:79]\n"
  "ds_read_b64_tr_b16 a[68:69], v8 offset:34944\n"
  "v_mfma_f32_16x16x32_bf16 v[112:115], a[12:15], a[40:43], v[112:115]\n"
  "ds_read_b64_tr_b16 a[70:71], v8 offset:37248\n"
  "v_mfma_f32_16x16x32_bf16 v[148:151], a[12:15], a[44:47], v[148:151]\n"
  "ds_read_b64_tr_b16 a[72:73], v8 offset:34976\n"
  "v_mfma_f32_16x16x32_bf16 v[184:187], a[12:15], a[48:51], v[184:187]\n"
  "ds_read_b64_tr_b16 a[74:75], v8 offset:37280\n"
  "v_mfma_f32_16x16x32_bf16 v[80:83], a[16:19], a[36:39], v[80:83]\n"
  "ds_read_b64_tr_b16 a[76:77], v8 offset:35008\n"
  "v_mfma_f32_16x16x32_bf16 v[116:119], a[16:19], a[40:43], v[116:119]\n"
  "ds_read_b64_tr_b16 a[78:79], v8 offset:37312\n"
  "v_mfma_f32_16x16x32_bf16 v[152:155], a[16:19], a[44:47], v[152:155]\n"
  "ds_read_b64_tr_b16 a[80:81], v8 offset:35040\n"
  "v_mfma_f32_16x16x32_bf16 v[188:191], a[16:19], a[48:51], v[188:191]\n"
  "ds_read_b64_tr_b16 a[82:83], v8 offset:37344\n"
  "v_mfma_f32_16x16x32_bf16 v[84:87], a[20:23], a[36:39], v[84:87]\n"
  "ds_read_b64_tr_b16 a[84:85], v8 offset:35072\n"
  "v_mfma_f32_16x16x32_bf16 v[120:123], a[20:23], a[40:43], v[120:123]\n"
  "ds_read_b64_tr_b16 a[86:87], v8 offset:37376\n"
  "v_mfma_f32_16x16x32_bf16 v[156:159], a[20:23], a[44:47], v[156:159]\n"
  "v_mfma_f32_16x16x32_bf16 v[192:195], a[20:23], a[48:51], v[192:195]\n"
  "v_mfma_f32_16x16x32_bf16 v[88:91], a[24:27], a[36:39], v[88:91]\n"
  "v_mfma_f32_16x16x32_bf16 v[124:127], a[24:27], a[40:43], v[124:127]\n"
  "v_mfma_f32_16x16x32_bf16 v[160:163], a[24:27], a[44:47], v[160:163]\n"
  "v_mfma_f32_16x16x32_bf16 v[196:199], a[24:27], a[48:51], v[196:199]\n"
  "v_mfma_f32_16x16x32_bf16 v[92:95], a[28:31], a[36:39], v[92:95]\n"
  "v_mfma_f32_16x16x32_bf16 v[128:131], a[28:31], a[40:43], v[128:131]\n"
  "v_mfma_f32_16x16x32_bf16 v[164:167], a[28:31], a[44:47], v[164:167]\n"
  "v_mfma_f32_16x16x32_bf16 v[200:203], a[28:31], a[48:51], v[200:203]\n"
  "v_mfma_f32_16x16x32_bf16 v[96:99], a[32:35], a[36:39], v[96:99]\n"
  "v_mfma_f32_16x16x32_bf16 v[132:135], a[32:35], a[40:43], v[132:135]\n"
  "v_mfma_f32_16x16x32_bf16 v[168:171], a[32:35], a[44:47], v[168:171]\n"
  "v_mfma_f32_16x16x32_bf16 v[204:207], a[32:35], a[48:51], v[204:207]\n"
  "s_waitcnt vmcnt(9)\n"
  "s_waitcnt lgkmcnt(0)\n"
  "s_barrier\n"
  "v_mfma_f32_16x16x32_bf16 v[64:67], a[52:55], a[88:91], v[64:67]\n"
  "ds_read_b128 a[36:39], v29 offset:0\n"
  "v_mfma_f32_16x16x32_bf16 v[100:103], a[52:55], a[92:95], v[100:103]\n"
  "ds_read_b64_tr_b16 a[0:1], v28 offset:0\n"
  "v_mfma_f32_16x16x32_bf16 v[136:139], a[52:55], a[96:99], v[136:139]\n"
  "ds_read_b64_tr_b16 a[2:3], v28 offset:2304\n"
  "v_mfma_f32_16x16x32_bf16 v[172:175], a[52:55], a[100:103], v[172:175]\n"
  "ds_read_b128 a[40:43], v29 offset:256\n"
  "v_mfma_f32_16x16x32_bf16 v[68:71], a[56:59], a[88:91], v[68:71]\n"
  "ds_read_b128 a[44:47], v30 offset:0\n"
  "v_mfma_f32_16x16x32_bf16 v[104:107], a[56:59], a[92:95], v[104:107]\n"
  "ds_read_b128 a[48:51], v30 offset:256\n"
  "v_mfma_f32_16x16x32_bf16 v[140:143], a[56:59], a[96:99], v[140:143]\n"
  "ds_read_b64_tr_b16 a[4:5], v28 offset:32\n"
  "v_mfma_f32_16x16x32_bf16 v[176:179], a[56:59], a[100:103], v[176:179]\n"
  "ds_read_b64_tr_b16 a[6:7], v28 offset:2336\n"
  "v_mfma_f32_16x16x32_bf16 v[72:75], a[60:63], a[88:91], v[72:75]\n"
  "ds_read_b64_tr_b16 a[8:9], v28 offset:64\n"
  "v_mfma_f32_16x16x32_bf16 v[108:111], a[60:63], a[92:95], v[108:111]\n"
  "ds_read_b64_tr_b16 a[10:11], v28 offset:2368\n"
  "v_mfma_f32_16x16x32_bf16 v[144:147], a[60:63], a[96:99], v[144:147]\n"
  "ds_read_b64_tr_b16 a[12:13], v28 offset:96\n"
  "v_mfma_f32_16x16x32_bf16 v[180:183], a[60:63], a[100:103], v[180:183]\n"
  "ds_read_b64_tr_b16 a[14:15], v28 offset:2400\n"
  "v_mfma_f32_16x16x32_bf16 v[76:79], a[64:67], a[88:91], v[76:79]\n"
  "ds_read_b64_tr_b16 a[16:17], v28 offset:128\n"
  "v_mfma_f32_16x16x32_bf16 v[112:115], a[64:67], a[92:95], v[112:115]\n"
  "ds_read_b64_tr_b16 a[18:19], v28 offset:2432\n"
  "v_mfma_f32_16x16x32_bf16 v[148:151], a[64:67], a[96:99], v[148:151]\n"
  "ds_read_b64_tr_b16 a[20:21], v28 offset:160\n"
  "v_mfma_f32_16x16x32_bf16 v[184:187], a[64:67], a[100:103], v[184:187]\n"
  "ds_read_b64_tr_b16 a[22:23], v28 offset:2464\n"
  "v_mfma_f32_16x16x32_bf16 v[80:83], a[68:71], a[88:91], v[80:83]\n"
  "ds_read_b64_tr_b16 a[24:25], v28 offset:192\n"
  "v_mfma_f32_16x16x32_bf16 v[116:119], a[68:71], a[92:95], v[116:119]\n"
  "ds_read_b64_tr_b16 a[26:27], v28 offset:2496\n"
  "v_mfma_f32_16x16x32_bf16 v[152:155], a[68:71], a[96:99], v[152:155]\n"
  "ds_read_b64_tr_b16 a[28:29], v28 offset:224\n"
  "v_mfma_f32_16x16x32_bf16 v[188:191], a[68:71], a[100:103], v[188:191]\n"
  "ds_read_b64_tr_b16 a[30:31], v28 offset:2528\n"
  "v_mfma_f32_16x16x32_bf16 v[84:87], a[72:75], a[88:91], v[84:87]\n"
  "ds_read_b64_tr_b16 a[32:33], v28 offset:256\n"
  "v_mfma_f32_16x16x32_bf16 v[120:123], a[72:75], a[92:95], v[120:123]\n"
  "ds_read_b64_tr_b16 a[34:35], v28 offset:2560\n"
  "v_mfma_f32_16x16x32_bf16 v[156:159], a[72:75], a[96:99], v[156:159]\n"
  "v_mfma_f32_16x16x32_bf16 v[192:195], a[72:75], a[100:103], v[192:195]\n"
  "v_mfma_f32_16x16x32_bf16 v[88:91], a[76:79], a[88:91], v[88:91]\n"
  "v_mfma_f32_16x16x32_bf16 v[124:127], a[76:79], a[92:95], v[124:127]\n"
  "v_mfma_f32_16x16x32_bf16 v[160:163], a[76:79], a[96:99], v[160:163]\n"
  "v_mfma_f32_16x16x32_bf16 v[196:199], a[76:79], a[100:103], v[196:199]\n"
  "v_mfma_f32_16x16x32_bf16 v[92:95], a[80:83], a[88:91], v[92:95]\n"
  "v_mfma_f32_16x16x32_bf16 v[128:131], a[80:83], a[92:95], v[128:131]\n"
  "v_mfma_f32_16x16x32_bf16 v[164:167], a[80:83], a[96:99], v[164:167]\n"
  "v_mfma_f32_16x16x32_bf16 v[200:203], a[80:83], a[100:103], v[200:203]\n"
  "v_mfma_f32_16x16x32_bf16 v[96:99], a[84:87], a[88:91], v[96:99]\n"
  "v_mfma_f32_16x16x32_bf16 v[132:135], a[84:87], a[92:95], v[132:135]\n"
  "v_mfma_f32_16x16x32_bf16 v[168:171], a[84:87], a[96:99], v[168:171]\n"
  "v_mfma_f32_16x16x32_bf16 v[204:207], a[84:87], a[100:103], v[204:207]\n"
  "s_waitcnt vmcnt(0)\n"
  "s_waitcnt lgkmcnt(0)\n"
  "s_barrier\n"
  "s_and_b32 s62, s10, 7\n"
  "s_lshl_b32 s62, s62, 7\n"
  "s_add_u32 s62, s62, 16384\n"
  "v_and_b32 v231, 31, v0\n"
  "v_lshlrev_b32 v231, 2, v231\n"
  "s_load_dwordx2 s[60:61], s[8:9], 0\n"
  "buffer_load_dword v230, v231, s[92:95], s62 offen sc1\n"
  "v_mfma_f32_16x16x32_bf16 v[64:67], a[0:3], a[36:39], v[64:67]\n"
  "ds_read_b128 a[88:91], v29 offset:34816\n"
  "v_mfma_f32_16x16x32_bf16 v[100:103], a[0:3], a[40:43], v[100:103]\n"
  "ds_read_b64_tr_b16 a[52:53], v28 offset:34816\n"
  "v_mfma_f32_16x16x32_bf16 v[136:139], a[0:3], a[44:47], v[136:139]\n"
  "ds_read_b64_tr_b16 a[54:55], v28 offset:37120\n"
  "v_mfma_f32_16x16x32_bf16 v[172:175], a[0:3], a[48:51], v[172:175]\n"
  "ds_read_b128 a[92:95], v29 offset:35072\n"
  "v_mfma_f32_16x16x32_bf16 v[68:71], a[4:7], a[36:39], v[68:71]\n"
  "ds_read_b128 a[96:99], v30 offset:34816\n"
  "v_mfma_f32_16x16x32_bf16 v[104:107], a[4:7], a[40:43], v[104:107]\n"
  "ds_read_b128 a[100:103], v30 offset:35072\n"
  "v_mfma_f32_16x16x32_bf16 v[140:143], a[4:7], a[44:47], v[140:143]\n"
  "ds_read_b64_tr_b16 a[56:57], v28 offset:34848\n"
  "v_mfma_f32_16x16x32_bf16 v[176:179], a[4:7], a[48:51], v[176:179]\n"
  "ds_read_b64_tr_b16 a[58:59], v28 offset:37152\n"
  "v_mfma_f32_16x16x32_bf16 v[72:75], a[8:11], a[36:39], v[72:75]\n"
  "ds_read_b64_tr_b16 a[60:61], v28 offset:34880\n"
  "v_mfma_f32_16x16x32_bf16 v[108:111], a[8:11], a[40:43], v[108:111]\n"
  "ds_read_b64_tr_b16 a[62:63], v28 offset:37184\n"
  "v_mfma_f32_16x16x32_bf16 v[144:147], a[8:11], a[44:47], v[144:147]\n"
  "ds_read_b64_tr_b16 a[64:65], v28 offset:34912\n"
  "v_mfma_f32_16x16x32_bf16 v[180:183], a[8:11], a[48:51], v[180:183]\n"
  "ds_read_b64_tr_b16 a[66:67], v28 offset:37216\n"
  "v_mfma_f32_16x16x32_bf16 v[76:79], a[12:15], a[36:39], v[76:79]\n"
  "ds_read_b64_tr_b16 a[68:69], v28 offset:34944\n"
  "v_mfma_f32_16x16x32_bf16 v[112:115], a[12:15], a[40:43], v[112:115]\n"
  "ds_read_b64_tr_b16 a[70:71], v28 offset:37248\n"
  "v_mfma_f32_16x16x32_bf16 v[148:151], a[12:15], a[44:47], v[148:151]\n"
  "ds_read_b64_tr_b16 a[72:73], v28 offset:34976\n"
  "v_mfma_f32_16x16x32_bf16 v[184:187], a[12:15], a[48:51], v[184:187]\n"
  "ds_read_b64_tr_b16 a[74:75], v28 offset:37280\n"
  "v_mfma_f32_16x16x32_bf16 v[80:83], a[16:19], a[36:39], v[80:83]\n"
  "ds_read_b64_tr_b16 a[76:77], v28 offset:35008\n"
  "v_mfma_f32_16x16x32_bf16 v[116:119], a[16:19], a[40:43], v[116:119]\n"
  "ds_read_b64_tr_b16 a[78:79], v28 offset:37312\n"
  "v_mfma_f32_16x16x32_bf16 v[152:155], a[16:19], a[44:47], v[152:155]\n"
  "ds_read_b64_tr_b16 a[80:81], v28 offset:35040\n"
  "v_mfma_f32_16x16x32_bf16 v[188:191], a[16:19], a[48:51], v[188:191]\n"
  "ds_read_b64_tr_b16 a[82:83], v28 offset:37344\n"
  "v_mfma_f32_16x16x32_bf16 v[84:87], a[20:23], a[36:39], v[84:87]\n"
  "ds_read_b64_tr_b16 a[84:85], v28 offset:35072\n"
  "v_mfma_f32_16x16x32_bf16 v[120:123], a[20:23], a[40:43], v[120:123]\n"
  "ds_read_b64_tr_b16 a[86:87], v28 offset:37376\n"
  "v_mfma_f32_16x16x32_bf16 v[156:159], a[20:23], a[44:47], v[156:159]\n"
  "v_mfma_f32_16x16x32_bf16 v[192:195], a[20:23], a[48:51], v[192:195]\n"
  "v_mfma_f32_16x16x32_bf16 v[88:91], a[24:27], a[36:39], v[88:91]\n"
  "v_mfma_f32_16x16x32_bf16 v[124:127], a[24:27], a[40:43], v[124:127]\n"
  "v_mfma_f32_16x16x32_bf16 v[160:163], a[24:27], a[44:47], v[160:163]\n"
  "v_mfma_f32_16x16x32_bf16 v[196:199], a[24:27], a[48:51], v[196:199]\n"
  "v_mfma_f32_16x16x32_bf16 v[92:95], a[28:31], a[36:39], v[92:95]\n"
  "v_mfma_f32_16x16x32_bf16 v[128:131], a[28:31], a[40:43], v[128:131]\n"
  "v_mfma_f32_16x16x32_bf16 v[164:167], a[28:31], a[44:47], v[164:167]\n"
  "v_mfma_f32_16x16x32_bf16 v[200:203], a[28:31], a[48:51], v[200:203]\n"
  "v_mfma_f32_16x16x32_bf16 v[96:99], a[32:35], a[36:39], v[96:99]\n"
  "v_mfma_f32_16x16x32_bf16 v[132:135], a[32:35], a[40:43], v[132:135]\n"
  "v_mfma_f32_16x16x32_bf16 v[168:171], a[32:35], a[44:47], v[168:171]\n"
  "v_mfma_f32_16x16x32_bf16 v[204:207], a[32:35], a[48:51], v[204:207]\n"
  "s_waitcnt lgkmcnt(0)\n"
  "s_barrier\n"
  "v_mfma_f32_16x16x32_bf16 v[64:67], a[52:55], a[88:91], v[64:67]\n"
  "v_mfma_f32_16x16x32_bf16 v[100:103], a[52:55], a[92:95], v[100:103]\n"
  "v_mfma_f32_16x16x32_bf16 v[136:139], a[52:55], a[96:99], v[136:139]\n"
  "v_mfma_f32_16x16x32_bf16 v[172:175], a[52:55], a[100:103], v[172:175]\n"
  "v_mfma_f32_16x16x32_bf16 v[68:71], a[56:59], a[88:91], v[68:71]\n"
  "v_mfma_f32_16x16x32_bf16 v[104:107], a[56:59], a[92:95], v[104:107]\n"
  "v_mfma_f32_16x16x32_bf16 v[140:143], a[56:59], a[96:99], v[140:143]\n"
  "v_mfma_f32_16x16x32_bf16 v[176:179], a[56:59], a[100:103], v[176:179]\n"
  "v_mfma_f32_16x16x32_bf16 v[72:75], a[60:63], a[88:91], v[72:75]\n"
  "v_mfma_f32_16x16x32_bf16 v[108:111], a[60:63], a[92:95], v[108:111]\n"
  "v_mfma_f32_16x16x32_bf16 v[144:147], a[60:63], a[96:99], v[144:147]\n"
  "v_mfma_f32_16x16x32_bf16 v[180:183], a[60:63], a[100:103], v[180:183]\n"
  "v_mfma_f32_16x16x32_bf16 v[76:79], a[64:67], a[88:91], v[76:79]\n"
  "v_mfma_f32_16x16x32_bf16 v[112:115], a[64:67], a[92:95], v[112:115]\n"
  "v_mfma_f32_16x16x32_bf16 v[148:151], a[64:67], a[96:99], v[148:151]\n"
  "v_mfma_f32_16x16x32_bf16 v[184:187], a[64:67], a[100:103], v[184:187]\n"
  "v_mfma_f32_16x16x32_bf16 v[80:83], a[68:71], a[88:91], v[80:83]\n"
  "v_mfma_f32_16x16x32_bf16 v[116:119], a[68:71], a[92:95], v[116:119]\n"
  "v_mfma_f32_16x16x32_bf16 v[152:155], a[68:71], a[96:99], v[152:155]\n"
  "v_mfma_f32_16x16x32_bf16 v[188:191], a[68:71], a[100:103], v[188:191]\n"
  "v_mfma_f32_16x16x32_bf16 v[84:87], a[72:75], a[88:91], v[84:87]\n"
  "v_mfma_f32_16x16x32_bf16 v[120:123], a[72:75], a[92:95], v[120:123]\n"
  "v_mfma_f32_16x16x32_bf16 v[156:159], a[72:75], a[96:99], v[156:159]\n"
  "v_mfma_f32_16x16x32_bf16 v[192:195], a[72:75], a[100:103], v[192:195]\n"
  "v_mfma_f32_16x16x32_bf16 v[88:91], a[76:79], a[88:91], v[88:91]\n"
  "v_mfma_f32_16x16x32_bf16 v[124:127], a[76:79], a[92:95], v[124:127]\n"
  "v_mfma_f32_16x16x32_bf16 v[160:163], a[76:79], a[96:99], v[160:163]\n"
  "v_mfma_f32_16x16x32_bf16 v[196:199], a[76:79], a[100:103], v[196:199]\n"
  "v_mfma_f32_16x16x32_bf16 v[92:95], a[80:83], a[88:91], v[92:95]\n"
  "v_mfma_f32_16x16x32_bf16 v[128:131], a[80:83], a[92:95], v[128:131]\n"
  "v_mfma_f32_16x16x32_bf16 v[164:167], a[80:83], a[96:99], v[164:167]\n"
  "v_mfma_f32_16x16x32_bf16 v[200:203], a[80:83], a[100:103], v[200:203]\n"
  "v_mfma_f32_16x16x32_bf16 v[96:99], a[84:87], a[88:91], v[96:99]\n"
  "v_mfma_f32_16x16x32_bf16 v[132:135], a[84:87], a[92:95], v[132:135]\n"
  "v_mfma_f32_16x16x32_bf16 v[168:171], a[84:87], a[96:99], v[168:171]\n"
  "v_mfma_f32_16x16x32_bf16 v[204:207], a[84:87], a[100:103], v[204:207]\n"
  "s_waitcnt lgkmcnt(0)\n"
  "s_add_u32 s20, s14, 79691776\n"
  "s_addc_u32 s21, s15, 0\n"
  "s_and_b32 s21, s21, 0xffff\n"
  "s_mov_b32 s22, 2359296\n"
  "s_mov_b32 s23, 131072\n"
  "s_mov_b32 s16, s60\n"
  "s_mov_b32 s17, s61\n"
  "s_and_b32 s17, s17, 0xffff\n"
  "s_mov_b32 s18, 2359296\n"
  "s_mov_b32 s19, 131072\n"
  "v_lshrrev_b32 v232, 4, v0\n"
  "v_lshlrev_b32 v232, 12, v232\n"
  "v_and_b32 v233, 15, v0\n"
  "v_lshl_add_u32 v232, v233, 3, v232\n"
  "s_and_b32 s63, s10, 7\n"
  "s_lshr_b32 s64, s10, 3\n"
  "s_mul_i32 s65, s63, 294912\n"
  "s_lshl_b32 s66, s64, 7\n"
  "s_add_u32 s65, s65, s66\n"
  "s_lshl_b32 s66, s11, 14\n"
  "s_add_u32 s65, s65, s66\n"
  "s_lshl_b32 s66, s11, 6\n"
  "v_add_u32 v233, s66, v0\n"
  "v_mul_u32_u24 v234, 7282, v233\n"
  "v_lshrrev_b32 v234, 16, v234\n"
  "v_mul_u32_u24 v235, 9, v234\n"
  "v_sub_u32 v235, v233, v235\n"
  "v_mul_u32_u24 v234, 1152, v234\n"
  "v_lshl_add_u32 v236, v235, 4, v234\n"
  "v_add_u32 v233, s66, v0\n"
  "v_add_u32 v233, 256, v233\n"
  "v_mul_u32_u24 v234, 7282, v233\n"
  "v_lshrrev_b32 v234, 16, v234\n"
  "v_mul_u32_u24 v235, 9, v234\n"
  "v_sub_u32 v235, v233, v235\n"
  "v_mul_u32_u24 v234, 1152, v234\n"
  "v_lshl_add_u32 v237, v235, 4, v234\n"
  "v_add_u32 v233, s66, v0\n"
  "v_add_u32 v233, 512, v233\n"
  "v_mul_u32_u24 v234, 7282, v233\n"
  "v_lshrrev_b32 v234, 16, v234\n"
  "v_mul_u32_u24 v235, 9, v234\n"
  "v_sub_u32 v235, v233, v235\n"
  "v_mul_u32_u24 v234, 1152, v234\n"
  "v_lshl_add_u32 v238, v235, 4, v234\n"
  "s_mul_i32 s67, s64, 73728\n"
  "s_mul_i32 s66, s63, 144\n"
  "s_add_u32 s67, s67, s66\n"
  "s_waitcnt vmcnt(0)\n"
  "La3_chk:\n"
  "v_cmp_ne_u32 vcc, s71, v230\n"
  "s_nop 4\n"
  "s_cbranch_vccz La3_ok\n"
  "s_sleep 2\n"
  "buffer_load_dword v230, v231, s[92:95], s62 offen sc1\n"
  "s_waitcnt vmcnt(0)\n"
  "s_branch La3_chk\n"
  "La3_ok:\n"
  "buffer_load_dwordx2 v[208:209], v232, s[20:23], s65 offen sc1\n"
  "s_add_u32 s65, s65, 65536\n"
  "buffer_load_dwordx2 v[210:211], v232, s[20:23], s65 offen sc1\n"
  "s_add_u32 s65, s65, 65536\n"
  "buffer_load_dwordx2 v[212:213], v232, s[20:23], s65 offen sc1\n"
  "s_add_u32 s65, s65, 65536\n"
  "buffer_load_dwordx2 v[214:215], v232, s[20:23], s65 offen sc1\n"
  "s_add_u32 s65, s65, 65536\n"
  "buffer_load_dwordx4 v[218:221], v236, s[16:19], s67 offen\n"
  "buffer_load_dwordx4 v[222:225], v237, s[16:19], s67 offen\n"
  "s_cmp_ge_u32 s11, 2\n"
  "s_cbranch_scc1 La3_done\n"
  "buffer_load_dwordx2 v[216:217], v232, s[20:23], s65 offen sc1\n"
  "s_cmp_ge_u32 s11, 1\n"
  "s_cbranch_scc1 La3_done\n"
  "buffer_load_dwordx4 v[226:229], v238, s[16:19], s67 offen\n"
  "La3_done:\n"
  "s_barrier\n"
  "s_mul_i32 s58, s11, 18432\n"
  "v_add_u32 v12, s58, v1\n"
  "s_xor_b32 s59, s11, 1\n"
  "s_mul_i32 s59, s59, 18432\n"
  "v_add_u32 v13, s59, v1\n"
  "ds_write_b128 v12, v[136:139] offset:0\n"
  "ds_write_b128 v12, v[140:143] offset:1024\n"
  "ds_write_b128 v12, v[144:147] offset:2048\n"
  "ds_write_b128 v12, v[148:151] offset:3072\n"
  "ds_write_b128 v12, v[152:155] offset:4096\n"
  "ds_write_b128 v12, v[156:159] offset:5120\n"
  "ds_write_b128 v12, v[160:163] offset:6144\n"
  "ds_write_b128 v12, v[164:167] offset:7168\n"
  "ds_write_b128 v12, v[168:171] offset:8192\n"
  "ds_write_b128 v12, v[172:175] offset:9216\n"
  "ds_write_b128 v12, v[176:179] offset:10240\n"
  "ds_write_b128 v12, v[180:183] offset:11264\n"
  "ds_write_b128 v12, v[184:187] offset:12288\n"
  "ds_write_b128 v12, v[188:191] offset:13312\n"
  "ds_write_b128 v12, v[192:195] offset:14336\n"
  "ds_write_b128 v12, v[196:199] offset:15360\n"
  "ds_write_b128 v12, v[200:203] offset:16384\n"
  "ds_write_b128 v12, v[204:207] offset:17408\n"
  "s_waitcnt lgkmcnt(0)\n"
  "s_barrier\n"
  "ds_read_b128 v[136:139], v13 offset:0\n"
  "ds_read_b128 v[140:143], v13 offset:1024\n"
  "ds_read_b128 v[144:147], v13 offset:2048\n"
  "ds_read_b128 v[148:151], v13 offset:3072\n"
  "ds_read_b128 v[152:155], v13 offset:4096\n"
  "ds_read_b128 v[156:159], v13 offset:5120\n"
  "ds_read_b128 v[160:163], v13 offset:6144\n"
  "ds_read_b128 v[164:167], v13 offset:7168\n"
  "ds_read_b128 v[168:171], v13 offset:8192\n"
  "ds_read_b128 v[172:175], v13 offset:9216\n"
  "ds_read_b128 v[176:179], v13 offset:10240\n"
  "ds_read_b128 v[180:183], v13 offset:11264\n"
  "ds_read_b128 v[184:187], v13 offset:12288\n"
  "ds_read_b128 v[188:191], v13 offset:13312\n"
  "ds_read_b128 v[192:195], v13 offset:14336\n"
  "ds_read_b128 v[196:199], v13 offset:15360\n"
  "ds_read_b128 v[200:203], v13 offset:16384\n"
  "ds_read_b128 v[204:207], v13 offset:17408\n"
  "s_waitcnt lgkmcnt(15)\n"
  "v_add_f32 v64, v64, v136\n"
  "v_add_f32 v65, v65, v137\n"
  "v_add_f32 v66, v66, v138\n"
  "v_add_f32 v67, v67, v139\n"
  "s_waitcnt lgkmcnt(15)\n"
  "v_add_f32 v68, v68, v140\n"
  "v_add_f32 v69, v69, v141\n"
  "v_add_f32 v70, v70, v142\n"
  "v_add_f32 v71, v71, v143\n"
  "s_waitcnt lgkmcnt(15)\n"
  "v_add_f32 v72, v72, v144\n"
  "v_add_f32 v73, v73, v145\n"
  "v_add_f32 v74, v74, v146\n"
  "v_add_f32 v75, v75, v147\n"
  "s_waitcnt lgkmcnt(13)\n"
  "v_add_f32 v76, v76, v148\n"
  "v_add_f32 v77, v77, v149\n"
  "v_add_f32 v78, v78, v150\n"
  "v_add_f32 v79, v79, v151\n"
  "v_add_f32 v80, v80, v152\n"
  "v_add_f32 v81, v81, v153\n"
  "v_add_f32 v82, v82, v154\n"
  "v_add_f32 v83, v83, v155\n"
  "s_waitcnt lgkmcnt(7)\n"
  "v_add_f32 v84, v84, v156\n"
  "v_add_f32 v85, v85, v157\n"
  "v_add_f32 v86, v86, v158\n"
  "v_add_f32 v87, v87, v159\n"
  "v_add_f32 v88, v88, v160\n"
  "v_add_f32 v89, v89, v161\n"
  "v_add_f32 v90, v90, v162\n"
  "v_add_f32 v91, v91, v163\n"
  "v_add_f32 v92, v92, v164\n"
  "v_add_f32 v93, v93, v165\n"
  "v_add_f32 v94, v94, v166\n"
  "v_add_f32 v95, v95, v167\n"
  "v_add_f32 v96, v96, v168\n"
  "v_add_f32 v97, v97, v169\n"
  "v_add_f32 v98, v98, v170\n"
  "v_add_f32 v99, v99, v171\n"
  "v_add_f32 v100, v100, v172\n"
  "v_add_f32 v101, v101, v173\n"
  "v_add_f32 v102, v102, v174\n"
  "v_add_f32 v103, v103, v175\n"
  "v_add_f32 v104, v104, v176\n"
  "v_add_f32 v105, v105, v177\n"
  "v_add_f32 v106, v106, v178\n"
  "v_add_f32 v107, v107, v179\n"
  "s_waitcnt lgkmcnt(1)\n"
  "v_add_f32 v108, v108, v180\n"
  "v_add_f32 v109, v109, v181\n"
  "v_add_f32 v110, v110, v182\n"
  "v_add_f32 v111, v111, v183\n"
  "v_add_f32 v112, v112, v184\n"
  "v_add_f32 v113, v113, v185\n"
  "v_add_f32 v114, v114, v186\n"
  "v_add_f32 v115, v115, v187\n"
  "v_add_f32 v116, v116, v188\n"
  "v_add_f32 v117, v117, v189\n"
  "v_add_f32 v118, v118, v190\n"
  "v_add_f32 v119, v119, v191\n"
  "v_add_f32 v120, v120, v192\n"
  "v_add_f32 v121, v121, v193\n"
  "v_add_f32 v122, v122, v194\n"
  "v_add_f32 v123, v123, v195\n"
  "v_add_f32 v124, v124, v196\n"
  "v_add_f32 v125, v125, v197\n"
  "v_add_f32 v126, v126, v198\n"
  "v_add_f32 v127, v127, v199\n"
  "v_add_f32 v128, v128, v200\n"
  "v_add_f32 v129, v129, v201\n"
  "v_add_f32 v130, v130, v202\n"
  "v_add_f32 v131, v131, v203\n"
  "s_waitcnt lgkmcnt(0)\n"
  "v_add_f32 v132, v132, v204\n"
  "v_add_f32 v133, v133, v205\n"
  "v_add_f32 v134, v134, v206\n"
  "v_add_f32 v135, v135, v207\n"
  "s_lshl_b32 s58, s41, 2\n"
  "s_add_u32 s58, s58, s42\n"
  "s_lshl_b32 s60, s58, 4\n"
  "s_lshl_b32 s61, s11, 2\n"
  "s_add_u32 s61, s61, s60\n"
  "s_add_u32 s61, s61, s40\n"
  "s_mul_i32 s61, s61, 18432\n"
  "s_waitcnt vmcnt(0)\n"
  "s_cmp_eq_u32 s11, s40\n"
  "s_cbranch_scc1 Lp4_own\n"
  "buffer_store_dwordx4 v[64:67], v1, s[24:27], s61 offen offset:0 sc1\n"
  "buffer_store_dwordx4 v[68:71], v1, s[24:27], s61 offen offset:1024 sc1\n"
  "buffer_store_dwordx4 v[72:75], v1, s[24:27], s61 offen offset:2048 sc1\n"
  "buffer_store_dwordx4 v[76:79], v1, s[24:27], s61 offen offset:3072 sc1\n"
  "s_add_u32 s61, s61, 4096\n"
  "buffer_store_dwordx4 v[80:83], v1, s[24:27], s61 offen offset:0 sc1\n"
  "buffer_store_dwordx4 v[84:87], v1, s[24:27], s61 offen offset:1024 sc1\n"
  "buffer_store_dwordx4 v[88:91], v1, s[24:27], s61 offen offset:2048 sc1\n"
  "buffer_store_dwordx4 v[92:95], v1, s[24:27], s61 offen offset:3072 sc1\n"
  "s_add_u32 s61, s61, 4096\n"
  "buffer_store_dwordx4 v[96:99], v1, s[24:27], s61 offen offset:0 sc1\n"
  "buffer_store_dwordx4 v[100:103], v1, s[24:27], s61 offen offset:1024 sc1\n"
  "buffer_store_dwordx4 v[104:107], v1, s[24:27], s61 offen offset:2048 sc1\n"
  "buffer_store_dwordx4 v[108:111], v1, s[24:27], s61 offen offset:3072 sc1\n"
  "s_add_u32 s61, s61, 4096\n"
  "buffer_store_dwordx4 v[112:115], v1, s[24:27], s61 offen offset:0 sc1\n"
  "buffer_store_dwordx4 v[116:119], v1, s[24:27], s61 offen offset:1024 sc1\n"
  "buffer_store_dwordx4 v[120:123], v1, s[24:27], s61 offen offset:2048 sc1\n"
  "buffer_store_dwordx4 v[124:127], v1, s[24:27], s61 offen offset:3072 sc1\n"
  "s_add_u32 s61, s61, 4096\n"
  "buffer_store_dwordx4 v[128:131], v1, s[24:27], s61 offen offset:0 sc1\n"
  "buffer_store_dwordx4 v[132:135], v1, s[24:27], s61 offen offset:1024 sc1\n"
  "s_branch Lp4_sent\n"
  "Lp4_own:\n"
  "v_add_u32 v12, 73728, v1\n"
  "ds_write_b128 v12, v[64:67] offset:0\n"
  "ds_write_b128 v12, v[68:71] offset:1024\n"
  "ds_write_b128 v12, v[72:75] offset:2048\n"
  "ds_write_b128 v12, v[76:79] offset:3072\n"
  "ds_write_b128 v12, v[80:83] offset:4096\n"
  "ds_write_b128 v12, v[84:87] offset:5120\n"
  "ds_write_b128 v12, v[88:91] offset:6144\n"
  "ds_write_b128 v12, v[92:95] offset:7168\n"
  "ds_write_b128 v12, v[96:99] offset:8192\n"
  "ds_write_b128 v12, v[100:103] offset:9216\n"
  "ds_write_b128 v12, v[104:107] offset:10240\n"
  "ds_write_b128 v12, v[108:111] offset:11264\n"
  "ds_write_b128 v12, v[112:115] offset:12288\n"
  "ds_write_b128 v12, v[116:119] offset:13312\n"
  "ds_write_b128 v12, v[120:123] offset:14336\n"
  "ds_write_b128 v12, v[124:127] offset:15360\n"
  "ds_write_b128 v12, v[128:131] offset:16384\n"
  "ds_write_b128 v12, v[132:135] offset:17408\n"
  "Lp4_sent:\n"
  "s_waitcnt lgkmcnt(0)\n"
  : "={v[208:209]}"(gy[0]), "={v[210:211]}"(gy[1]), "={v[212:213]}"(gy[2]), "={v[214:215]}"(gy[3]), "={v[216:217]}"(gy[4]), "={v[218:221]}"(gq[0]), "={v[222:225]}"(gq[1]), "={v[226:229]}"(gq[2]) : "s"(kp), "s"(bid), "s"(wid), "s"(tag) : "v0", "v1", "v2", "v3", "v4", "v5", "v6", "v7", "v8", "v9", "v10", "v11", "v12", "v13", "v14", "v15", "v16", "v17", "v18", "v19", "v20", "v21", "v22", "v23", "v24", "v25", "v26", "v27", "v28", "v29", "v30", "v31", "v32", "v33", "v34", "v35", "v36", "v37", "v38", "v39", "v40", "v41", "v42", "v43", "v44", "v45", "v46", "v47", "v48", "v49", "v50", "v51", "v52", "v53", "v54", "v55", "v56", "v57", "v58", "v59", "v60", "v61", "v62", "v63", "v64", "v65", "v66", "v67", "v68", "v69", "v70", "v71", "v72", "v73", "v74", "v75", "v76", "v77", "v78", "v79", "v80", "v81", "v82", "v83", "v84", "v85", "v86", "v87", "v88", "v89", "v90", "v91", "v92", "v93", "v94", "v95", "v96", "v97", "v98", "v99", "v100", "v101", "v102", "v103", "v104", "v105", "v106", "v107", "v108", "v109", "v110", "v111", "v112", "v113", "v114", "v115", "v116", "v117", "v118", "v119", "v120", "v121", "v122", "v123", "v124", "v125", "v126", "v127", "v128", "v129", "v130", "v131", "v132", "v133", "v134", "v135", "v136", "v137", "v138", "v139", "v140", "v141", "v142", "v143", "v144", "v145", "v146", "v147", "v148", "v149", "v150", "v151", "v152", "v153", "v154", "v155", "v156", "v157", "v158", "v159", "v160", "v161", "v162", "v163", "v164", "v165", "v166", "v167", "v168", "v169", "v170", "v171", "v172", "v173", "v174", "v175", "v176", "v177", "v178", "v179", "v180", "v181", "v182", "v183", "v184", "v185", "v186", "v187", "v188", "v189", "v190", "v191", "v192", "v193", "v194", "v195", "v196", "v197", "v198", "v199", "v200", "v201", "v202", "v203", "v204", "v205", "v206", "v207", "v230", "v231", "v232", "v233", "v234", "v235", "v236", "v237", "v238", "v239", "v240", "v241", "v242", "v243", "v244", "v245", "v246", "v247", "v248", "v249", "v250", "v251", "v252", "v253", "v254", "v255", "a0", "a1", "a2", "a3", "a4", "a5", "a6", "a7", "a8", "a9", "a10", "a11", "a12", "a13", "a14", "a15", "a16", "a17", "a18", "a19", "a20", "a21", "a22", "a23", "a24", "a25", "a26", "a27", "a28", "a29", "a30", "a31", "a32", "a33", "a34", "a35", "a36", "a37", "a38", "a39", "a40", "a41", "a42", "a43", "a44", "a45", "a46", "a47", "a48", "a49", "a50", "a51", "a52", "a53", "a54", "a55", "a56", "a57", "a58", "a59", "a60", "a61", "a62", "a63", "a64", "a65", "a66", "a67", "a68", "a69", "a70", "a71", "a72", "a73", "a74", "a75", "a76", "a77", "a78", "a79", "a80", "a81", "a82", "a83", "a84", "a85", "a86", "a87", "a88", "a89", "a90", "a91", "a92", "a93", "a94", "a95", "a96", "a97", "a98", "a99", "a100", "a101", "a102", "a103", "a104", "a105", "a106", "a107", "a108", "a109", "a110", "a111", "a112", "a113", "a114", "a115", "a116", "a117", "a118", "a119", "a120", "a121", "a122", "a123", "a124", "a125", "a126", "a127", "a128", "a129", "a130", "a131", "a132", "a133", "a134", "a135", "a136", "a137", "a138", "a139", "a140", "a141", "a142", "a143", "a144", "a145", "a146", "a147", "a148", "a149", "a150", "a151", "a152", "a153", "a154", "a155", "a156", "a157", "a158", "a159", "a160", "a161", "a162", "a163", "a164", "a165", "a166", "a167", "a168", "a169", "a170", "a171", "a172", "a173", "a174", "a175", "a176", "a177", "a178", "a179", "a180", "a181", "a182", "a183", "a184", "a185", "a186", "a187", "a188", "a189", "a190", "a191", "a192", "a193", "a194", "a195", "a196", "a197", "a198", "a199", "a200", "a201", "a202", "a203", "a204", "a205", "a206", "a207", "a208", "a209", "a210", "a211", "a212", "a213", "a214", "a215", "a216", "a217", "a218", "a219", "a220", "a221", "a222", "a223", "a224", "a225", "a226", "a227", "a228", "a229", "a230", "a231", "a232", "a233", "a234", "a235", "a236", "a237", "a238", "a239", "a240", "a241", "a242", "a243", "a244", "a245", "a246", "a247", "a248", "a249", "a250", "a251", "a252", "a253", "a254", "a255", "s8", "s9", "s10", "s11", "s12", "s13", "s14", "s15", "s16", "s17", "s18", "s19", "s20", "s21", "s22", "s23", "s24", "s25", "s26", "s27", "s28", "s29", "s30", "s31", "s32", "s33", "s34", "s35", "s36", "s37", "s38", "s39", "s40", "s41", "s42", "s43", "s44", "s45", "s46", "s47", "s48", "s49", "s50", "s51", "s52", "s53", "s54", "s55", "s56", "s57", "s58", "s59", "s60", "s61", "s62", "s63", "s64", "s65", "s66", "s67", "s68", "s69", "s70", "s71", "s72", "s73", "s74", "s75", "s92", "s93", "s94", "s95", "vcc", "scc", "memory");
  { KP ap = kp; unsigned bb = bid, ww = wid; asm volatile("" : "+s"(ap), "+s"(bb), "+s"(ww)); int ln;
    asm volatile("v_mbcnt_lo_u32_b32 %0, -1, 0\n v_mbcnt_hi_u32_b32 %0, -1, %0" : "=v"(ln));
    phase_a3p(ap, lds_ + 98304, bb, ww * 64 + ln, tag, gy, gq); }
  asm volatile(
  "s_mov_b64 s[8:9], %0\n"
  "s_mov_b32 s10, %1\n"
  "s_mov_b32 s11, %2\n"
  "s_and_b32 s12, s10, 7\n"
  "s_lshl_b32 s12, s12, 2\n"
  "s_lshr_b32 s13, s10, 3\n"
  "s_and_b32 s13, s13, 3\n"
  "s_add_u32 s12, s12, s13\n"
  "s_lshr_b32 s13, s10, 5\n"
  "s_load_dwordx2 s[14:15], s[8:9], 104\n"
  "s_mov_b32 s71, %3\n"
  "s_load_dwordx2 s[60:61], s[8:9], 96\n"
  "v_mbcnt_lo_u32_b32 v0, -1, 0\n"
  "v_mbcnt_hi_u32_b32 v0, -1, v0\n"
  "v_lshlrev_b32 v1, 4, v0\n"
  "s_and_b32 s40, s10, 3\n"
  "s_bfe_u32 s41, s10, 0x10002\n"
  "s_lshl_b32 s41, s41, 3\n"
  "s_bfe_u32 s62, s10, 0x30003\n"
  "s_add_u32 s41, s41, s62\n"
  "s_lshr_b32 s42, s10, 6\n"
  "s_waitcnt lgkmcnt(0)\n"
  "s_add_u32 s92, s14, 83886080\n"
  "s_addc_u32 s93, s15, 0\n"
  "s_and_b32 s93, s93, 0xffff\n"
  "s_mov_b32 s94, 65536\n"
  "s_mov_b32 s95, 131072\n"
  "s_mov_b32 s28, s60\n"
  "s_mov_b32 s29, s61\n"
  "s_and_b32 s29, s29, 0xffff\n"
  "s_mov_b32 s30, 4718592\n"
  "s_mov_b32 s31, 131072\n"
  "s_add_u32 s24, s14, 50331648\n"
  "s_addc_u32 s25, s15, 0\n"
  "s_and_b32 s25, s25, 0xffff\n"
  "s_mov_b32 s26, 25165824\n"
  "s_mov_b32 s27, 131072\n"
  "s_lshl_b32 s58, s41, 2\n"
  "s_add_u32 s58, s58, s42\n"
  "s_lshl_b32 s58, s58, 4\n"
  "s_add_u32 s58, s58, 20480\n"
  "s_cmp_lg_u32 s11, 0\n"
  "s_cbranch_scc1 Lfw_p_d\n"
  "v_lshlrev_b32 v12, 2, v0\n"
  "s_mov_b64 exec, 0xf\n"
  "Lfw_p:\n"
  "buffer_load_dword v13, v12, s[92:95], s58 offen sc1\n"
  "s_waitcnt vmcnt(0)\n"
  "v_cmp_ne_u32 vcc, s71, v13\n"
  "s_nop 4\n"
  "s_cbranch_vccz Lfw_p_k\n"
  "s_sleep 2\n"
  "s_branch Lfw_p\n"
  "Lfw_p_k:\n"
  "s_mov_b64 exec, -1\n"
  "Lfw_p_d:\n"
  "s_barrier\n"
  "s_lshl_b32 s58, s41, 2\n"
  "s_add_u32 s58, s58, s42\n"
  "s_lshl_b32 s60, s58, 2\n"
  "s_add_u32 s60, s60, s40\n"
  "s_lshl_b32 s60, s60, 2\n"
  "s_add_u32 s61, s40, 1\n"
  "s_and_b32 s61, s61, 3\n"
  "s_add_u32 s61, s61, s60\n"
  "s_mul_i32 s61, s61, 18432\n"
  "s_add_u32 s62, s40, 2\n"
  "s_and_b32 s62, s62, 3\n"
  "s_add_u32 s62, s62, s60\n"
  "s_mul_i32 s62, s62, 18432\n"
  "s_add_u32 s63, s40, 3\n"
  "s_and_b32 s63, s63, 3\n"
  "s_add_u32 s63, s63, s60\n"
  "s_mul_i32 s63, s63, 18432\n"
  "s_add_u32 s64, s11, 0\n"
  "s_min_u32 s64, s64, 17\n"
  "s_lshl_b32 s65, s64, 10\n"
  "v_add_u32 v14, s65, v1\n"
  "buffer_load_dwordx4 v[64:67], v14, s[24:27], s61 offen sc1\n"
  "buffer_load_dwordx4 v[68:71], v14, s[24:27], s62 offen sc1\n"
  "buffer_load_dwordx4 v[72:75], v14, s[24:27], s63 offen sc1\n"
  "v_add_u32 v20, 73728, v14\n"
  "ds_read_b128 v[76:79], v20\n"
  "s_add_u32 s64, s11, 4\n"
  "s_min_u32 s64, s64, 17\n"
  "s_lshl_b32 s65, s64, 10\n"
  "v_add_u32 v15, s65, v1\n"
  "buffer_load_dwordx4 v[80:83], v15, s[24:27], s61 offen sc1\n"
  "buffer_load_dwordx4 v[84:87], v15, s[24:27], s62 offen sc1\n"
  "buffer_load_dwordx4 v[88:91], v15, s[24:27], s63 offen sc1\n"
  "v_add_u32 v20, 73728, v15\n"
  "ds_read_b128 v[92:95], v20\n"
  "s_add_u32 s64, s11, 8\n"
  "s_min_u32 s64, s64, 17\n"
  "s_lshl_b32 s65, s64, 10\n"
  "v_add_u32 v16, s65, v1\n"
  "buffer_load_dwordx4 v[96:99], v16, s[24:27], s61 offen sc1\n"
  "buffer_load_dwordx4 v[100:103], v16, s[24:27], s62 offen sc1\n"
  "buffer_load_dwordx4 v[104:107], v16, s[24:27], s63 offen sc1\n"
  "v_add_u32 v20, 73728, v16\n"
  "ds_read_b128 v[108:111], v20\n"
  "s_add_u32 s64, s11, 12\n"
  "s_min_u32 s64, s64, 17\n"
  "s_lshl_b32 s65, s64, 10\n"
  "v_add_u32 v17, s65, v1\n"
  "buffer_load_dwordx4 v[112:115], v17, s[24:27], s61 offen sc1\n"
  "buffer_load_dwordx4 v[116:119], v17, s[24:27], s62 offen sc1\n"
  "buffer_load_dwordx4 v[120:123], v17, s[24:27], s63 offen sc1\n"
  "v_add_u32 v20, 73728, v17\n"
  "ds_read_b128 v[124:127], v20\n"
  "s_add_u32 s64, s11, 16\n"
  "s_min_u32 s64, s64, 17\n"
  "s_lshl_b32 s65, s64, 10\n"
  "v_add_u32 v18, s65, v1\n"
  "buffer_load_dwordx4 v[128:131], v18, s[24:27], s61 offen sc1\n"
  "buffer_load_dwordx4 v[132:135], v18, s[24:27], s62 offen sc1\n"
  "buffer_load_dwordx4 v[136:139], v18, s[24:27], s63 offen sc1\n"
  "v_add_u32 v20, 73728, v18\n"
  "ds_read_b128 v[140:143], v20\n"
  "v_and_b32 v21, 15, v0\n"
  "v_mul_u32_u24 v21, 2304, v21\n"
  "v_lshrrev_b32 v22, 4, v0\n"
  "v_lshlrev_b32 v22, 3, v22\n"
  "v_add_u32 v21, v21, v22\n"
  "s_lshl_b32 s66, s41, 2\n"
  "s_add_u32 s66, s66, s40\n"
  "s_mul_i32 s66, s66, 73728\n"
  "s_mul_i32 s67, s42, 288\n"
  "s_add_u32 s66, s66, s67\n"
  "s_add_u32 s66, s66, 1152\n"
  "s_waitcnt vmcnt(14) lgkmcnt(3)\n"
  "v_add_f32 v76, v76, v64\n"
  "s_waitcnt vmcnt(13)\n"
  "v_add_f32 v76, v76, v68\n"
  "s_waitcnt vmcnt(12)\n"
  "v_add_f32 v76, v76, v72\n"
  "v_add_f32 v77, v77, v65\n"
  "v_add_f32 v77, v77, v69\n"
  "v_add_f32 v77, v77, v73\n"
  "v_add_f32 v78, v78, v66\n"
  "v_add_f32 v78, v78, v70\n"
  "v_add_f32 v78, v78, v74\n"
  "v_add_f32 v79, v79, v67\n"
  "v_add_f32 v79, v79, v71\n"
  "v_add_f32 v79, v79, v75\n"
  "v_cvt_pk_bf16_f32 v64, v76, v77\n"
  "v_cvt_pk_bf16_f32 v65, v78, v79\n"
  "s_add_u32 s64, s11, 0\n"
  "s_min_u32 s64, s64, 17\n"
  "s_cmp_ge_u32 s64, 9\n"
  "s_cselect_b32 s65, 36576, 0\n"
  "s_lshl_b32 s64, s64, 5\n"
  "s_add_u32 s65, s65, s64\n"
  "s_add_u32 s65, s65, s66\n"
  "buffer_store_dwordx2 v[64:65], v21, s[28:31], s65 offen\n"
  "s_waitcnt vmcnt(12)\n"
  "v_add_f32 v92, v92, v80\n"
  "s_waitcnt vmcnt(11)\n"
  "v_add_f32 v92, v92, v84\n"
  "s_waitcnt vmcnt(10)\n"
  "v_add_f32 v92, v92, v88\n"
  "v_add_f32 v93, v93, v81\n"
  "v_add_f32 v93, v93, v85\n"
  "v_add_f32 v93, v93, v89\n"
  "v_add_f32 v94, v94, v82\n"
  "v_add_f32 v94, v94, v86\n"
  "v_add_f32 v94, v94, v90\n"
  "v_add_f32 v95, v95, v83\n"
  "v_add_f32 v95, v95, v87\n"
  "v_add_f32 v95, v95, v91\n"
  "v_cvt_pk_bf16_f32 v80, v92, v93\n"
  "v_cvt_pk_bf16_f32 v81, v94, v95\n"
  "s_add_u32 s64, s11, 4\n"
  "s_min_u32 s64, s64, 17\n"
  "s_cmp_ge_u32 s64, 9\n"
  "s_cselect_b32 s65, 36576, 0\n"
  "s_lshl_b32 s64, s64, 5\n"
  "s_add_u32 s65, s65, s64\n"
  "s_add_u32 s65, s65, s66\n"
  "buffer_store_dwordx2 v[80:81], v21, s[28:31], s65 offen\n"
  "s_waitcnt vmcnt(10) lgkmcnt(1)\n"
  "v_add_f32 v108, v108, v96\n"
  "s_waitcnt vmcnt(9)\n"
  "v_add_f32 v108, v108, v100\n"
  "s_waitcnt vmcnt(8)\n"
  "v_add_f32 v108, v108, v104\n"
  "v_add_f32 v109, v109, v97\n"
  "v_add_f32 v109, v109, v101\n"
  "v_add_f32 v109, v109, v105\n"
  "v_add_f32 v110, v110, v98\n"
  "v_add_f32 v110, v110, v102\n"
  "v_add_f32 v110, v110, v106\n"
  "v_add_f32 v111, v111, v99\n"
  "v_add_f32 v111, v111, v103\n"
  "v_add_f32 v111, v111, v107\n"
  "v_cvt_pk_bf16_f32 v96, v108, v109\n"
  "v_cvt_pk_bf16_f32 v97, v110, v111\n"
  "s_add_u32 s64, s11, 8\n"
  "s_min_u32 s64, s64, 17\n"
  "s_cmp_ge_u32 s64, 9\n"
  "s_cselect_b32 s65, 36576, 0\n"
  "s_lshl_b32 s64, s64, 5\n"
  "s_add_u32 s65, s65, s64\n"
  "s_add_u32 s65, s65, s66\n"
  "buffer_store_dwordx2 v[96:97], v21, s[28:31], s65 offen\n"
  "s_waitcnt vmcnt(8)\n"
  "v_add_f32 v124, v124, v112\n"
  "s_waitcnt vmcnt(7)\n"
  "v_add_f32 v124, v124, v116\n"
  "s_waitcnt vmcnt(6)\n"
  "v_add_f32 v124, v124, v120\n"
  "v_add_f32 v125, v125, v113\n"
  "v_add_f32 v125, v125, v117\n"
  "v_add_f32 v125, v125, v121\n"
  "v_add_f32 v126, v126, v114\n"
  "v_add_f32 v126, v126, v118\n"
  "v_add_f32 v126, v126, v122\n"
  "v_add_f32 v127, v127, v115\n"
  "v_add_f32 v127, v127, v119\n"
  "v_add_f32 v127, v127, v123\n"
  "v_cvt_pk_bf16_f32 v112, v124, v125\n"
  "v_cvt_pk_bf16_f32 v113, v126, v127\n"
  "s_add_u32 s64, s11, 12\n"
  "s_min_u32 s64, s64, 17\n"
  "s_cmp_ge_u32 s64, 9\n"
  "s_cselect_b32 s65, 36576, 0\n"
  "s_lshl_b32 s64, s64, 5\n"
  "s_add_u32 s65, s65, s64\n"
  "s_add_u32 s65, s65, s66\n"
  "buffer_store_dwordx2 v[112:113], v21, s[28:31], s65 offen\n"
  "s_waitcnt vmcnt(6) lgkmcnt(0)\n"
  "v_add_f32 v140, v140, v128\n"
  "s_waitcnt vmcnt(5)\n"
  "v_add_f32 v140, v140, v132\n"
  "s_waitcnt vmcnt(4)\n"
  "v_add_f32 v140, v140, v136\n"
  "v_add_f32 v141, v141, v129\n"
  "v_add_f32 v141, v141, v133\n"
  "v_add_f32 v141, v141, v137\n"
  "v_add_f32 v142, v142, v130\n"
  "v_add_f32 v142, v142, v134\n"
  "v_add_f32 v142, v142, v138\n"
  "v_add_f32 v143, v143, v131\n"
  "v_add_f32 v143, v143, v135\n"
  "v_add_f32 v143, v143, v139\n"
  "v_cvt_pk_bf16_f32 v128, v140, v141\n"
  "v_cvt_pk_bf16_f32 v129, v142, v143\n"
  "s_add_u32 s64, s11, 16\n"
  "s_min_u32 s64, s64, 17\n"
  "s_cmp_ge_u32 s64, 9\n"
  "s_cselect_b32 s65, 36576, 0\n"
  "s_lshl_b32 s64, s64, 5\n"
  "s_add_u32 s65, s65, s64\n"
  "s_add_u32 s65, s65, s66\n"
  "buffer_store_dwordx2 v[128:129], v21, s[28:31], s65 offen\n"
  "Lcode_end:\n"
  : : "s"(kp), "s"(bid), "s"(wid), "s"(tag) : "v0", "v1", "v2", "v3", "v4", "v5", "v6", "v7", "v8", "v9", "v10", "v11", "v12", "v13", "v14", "v15", "v16", "v17", "v18", "v19", "v20", "v21", "v22", "v23", "v24", "v25", "v26", "v27", "v28", "v29", "v30", "v31", "v32", "v33", "v34", "v35", "v36", "v37", "v38", "v39", "v40", "v41", "v42", "v43", "v44", "v45", "v46", "v47", "v48", "v49", "v50", "v51", "v52", "v53", "v54", "v55", "v56", "v57", "v58", "v59", "v60", "v61", "v62", "v63", "v64", "v65", "v66", "v67", "v68", "v69", "v70", "v71", "v72", "v73", "v74", "v75", "v76", "v77", "v78", "v79", "v80", "v81", "v82", "v83", "v84", "v85", "v86", "v87", "v88", "v89", "v90", "v91", "v92", "v93", "v94", "v95", "v96", "v97", "v98", "v99", "v100", "v101", "v102", "v103", "v104", "v105", "v106", "v107", "v108", "v109", "v110", "v111", "v112", "v113", "v114", "v115", "v116", "v117", "v118", "v119", "v120", "v121", "v122", "v123", "v124", "v125", "v126", "v127", "v128", "v129", "v130", "v131", "v132", "v133", "v134", "v135", "v136", "v137", "v138", "v139", "v140", "v141", "v142", "v143", "v144", "v145", "v146", "v147", "v148", "v149", "v150", "v151", "v152", "v153", "v154", "v155", "v156", "v157", "v158", "v159", "v160", "v161", "v162", "v163", "v164", "v165", "v166", "v167", "v168", "v169", "v170", "v171", "v172", "v173", "v174", "v175", "v176", "v177", "v178", "v179", "v180", "v181", "v182", "v183", "v184", "v185", "v186", "v187", "v188", "v189", "v190", "v191", "v192", "v193", "v194", "v195", "v196", "v197", "v198", "v199", "v200", "v201", "v202", "v203", "v204", "v205", "v206", "v207", "v208", "v209", "v210", "v211", "v212", "v213", "v214", "v215", "v216", "v217", "v218", "v219", "v220", "v221", "v222", "v223", "v224", "v225", "v226", "v227", "v228", "v229", "v230", "v231", "v232", "v233", "v234", "v235", "v236", "v237", "v238", "v239", "v240", "v241", "v242", "v243", "v244", "v245", "v246", "v247", "v248", "v249", "v250", "v251", "v252", "v253", "v254", "v255", "a0", "a1", "a2", "a3", "a4", "a5", "a6", "a7", "a8", "a9", "a10", "a11", "a12", "a13", "a14", "a15", "a16", "a17", "a18", "a19", "a20", "a21", "a22", "a23", "a24", "a25", "a26", "a27", "a28", "a29", "a30", "a31", "a32", "a33", "a34", "a35", "a36", "a37", "a38", "a39", "a40", "a41", "a42", "a43", "a44", "a45", "a46", "a47", "a48", "a49", "a50", "a51", "a52", "a53", "a54", "a55", "a56", "a57", "a58", "a59", "a60", "a61", "a62", "a63", "a64", "a65", "a66", "a67", "a68", "a69", "a70", "a71", "a72", "a73", "a74", "a75", "a76", "a77", "a78", "a79", "a80", "a81", "a82", "a83", "a84", "a85", "a86", "a87", "a88", "a89", "a90", "a91", "a92", "a93", "a94", "a95", "a96", "a97", "a98", "a99", "a100", "a101", "a102", "a103", "a104", "a105", "a106", "a107", "a108", "a109", "a110", "a111", "a112", "a113", "a114", "a115", "a116", "a117", "a118", "a119", "a120", "a121", "a122", "a123", "a124", "a125", "a126", "a127", "a128", "a129", "a130", "a131", "a132", "a133", "a134", "a135", "a136", "a137", "a138", "a139", "a140", "a141", "a142", "a143", "a144", "a145", "a146", "a147", "a148", "a149", "a150", "a151", "a152", "a153", "a154", "a155", "a156", "a157", "a158", "a159", "a160", "a161", "a162", "a163", "a164", "a165", "a166", "a167", "a168", "a169", "a170", "a171", "a172", "a173", "a174", "a175", "a176", "a177", "a178", "a179", "a180", "a181", "a182", "a183", "a184", "a185", "a186", "a187", "a188", "a189", "a190", "a191", "a192", "a193", "a194", "a195", "a196", "a197", "a198", "a199", "a200", "a201", "a202", "a203", "a204", "a205", "a206", "a207", "a208", "a209", "a210", "a211", "a212", "a213", "a214", "a215", "a216", "a217", "a218", "a219", "a220", "a221", "a222", "a223", "a224", "a225", "a226", "a227", "a228", "a229", "a230", "a231", "a232", "a233", "a234", "a235", "a236", "a237", "a238", "a239", "a240", "a241", "a242", "a243", "a244", "a245", "a246", "a247", "a248", "a249", "a250", "a251", "a252", "a253", "a254", "a255", "s8", "s9", "s10", "s11", "s12", "s13", "s14", "s15", "s16", "s17", "s18", "s19", "s20", "s21", "s22", "s23", "s24", "s25", "s26", "s27", "s28", "s29", "s30", "s31", "s32", "s33", "s34", "s35", "s36", "s37", "s38", "s39", "s40", "s41", "s42", "s43", "s44", "s45", "s46", "s47", "s48", "s49", "s50", "s51", "s52", "s53", "s54", "s55", "s56", "s57", "s58", "s59", "s60", "s61", "s62", "s63", "s64", "s65", "s66", "s67", "s68", "s69", "s70", "s71", "s72", "s73", "s74", "s75", "s92", "s93", "s94", "s95", "vcc", "scc", "memory");
  { KP ap = kp; unsigned bb = bid, ww = wid; asm volatile("" : "+s"(ap), "+s"(bb), "+s"(ww)); int ln;
    asm volatile("v_mbcnt_lo_u32_b32 %0, -1, 0\n v_mbcnt_hi_u32_b32 %0, -1, %0" : "=v"(ln));
    if (bb == 0) flag_set(gp<g32>(ap->ws, WS_FLAGS), tag, ww * 64 + ln); }
}
extern "C" void kernel_launch(void* const* d_in, const int* in_sizes, int n_in, void* d_out, int out_size,
                              void* d_ws, size_t ws_size, hipStream_t stream) {
  KArgs a;
  for (int i = 0; i < 12; ++i) a.in[i] = d_in[i];
  a.out = d_out; a.ws = d_ws;
  k_fused<<<256, 256, 0, stream>>>(a);
}
```
